```python
import torch

def _softmax(x, axis=-1):
    return torch.softmax(x, dim=axis)
def _gelu(x):
    return torch.nn.functional.gelu(x, approximate="tanh")
def _silu(x):
    return torch.nn.functional.silu(x)
def _sigmoid(x):
    return torch.sigmoid(x)
def _layernorm(x, axis=-1, eps=1e-5):
    m = x.mean(dim=axis, keepdim=True)
    v = x.var(dim=axis, keepdim=True, unbiased=False)
    return (x - m) / torch.sqrt(v + eps)
def _scatter_add(tgt, idx, src, axis=0):
    return tgt.clone().index_add_(axis, idx, src)
def _segment_sum(src, seg, n):
    out = torch.zeros((n,) + src.shape[1:], dtype=src.dtype)
    return out.index_add_(0, seg, src)
def _pad(x, pw):
    flat = tuple(v for pair in reversed(pw) for v in pair)
    return torch.nn.functional.pad(x, flat)

kernel_name = 'interleave_layers__spectral_fnet__dense_activation_relu__prompt'

def setup_inputs():

    _w = (lambda a: (lambda t: t.to(torch.bfloat16) if t.is_floating_point() else t)(torch.as_tensor(a)))
    def _A_setup():
        B, S, D = 1, 2048, 640
        ns = torch.arange(S)[:, None] * torch.arange(S)[None, :]
        nd = torch.arange(D)[:, None] * torch.arange(D)[None, :]
        Ws_re = torch.cos(-2.0 * torch.pi * ns / S).to(float)
        Ws_im = torch.sin(-2.0 * torch.pi * ns / S).to(float)
        Wd_re = torch.cos(-2.0 * torch.pi * nd / D).to(float)
        Wd_im = torch.sin(-2.0 * torch.pi * nd / D).to(float)
        return {
            "x": _w((lambda *s: torch.randn(*s, dtype=torch.bfloat16) * 0.05)(B, S, D)),
            "dft_seq_re": _w(Ws_re),
            "dft_seq_im": _w(Ws_im),
            "dft_hid_re": _w(Wd_re),
            "dft_hid_im": _w(Wd_im),
        }
    _A_in = _A_setup()

    def _B_setup():
        B, S, D, Dff = 1, 2048, 640, 1728
        return {
            "x": _w((lambda *s: torch.randn(*s, dtype=torch.bfloat16) * 0.05)(B, S, D)),
            "w1": _w((lambda *s: torch.randn(*s, dtype=torch.bfloat16) * 1.6)(D, Dff)),
            "w2": _w((lambda *s: torch.randn(*s, dtype=torch.bfloat16) * 1.6)(Dff, D)),
        }
    _B_in = _B_setup()

    return {
        "A_x": _A_in["x"],
        "A_dft_seq_re": _A_in["dft_seq_re"],
        "A_dft_seq_im": _A_in["dft_seq_im"],
        "A_dft_hid_re": _A_in["dft_hid_re"],
        "A_dft_hid_im": _A_in["dft_hid_im"],
        "B_x": _B_in["x"],
        "B_w1": _B_in["w1"],
        "B_w2": _B_in["w2"],
    }

def reference(A_x, A_dft_seq_re, A_dft_seq_im, A_dft_hid_re, A_dft_hid_im, B_x, B_w1, B_w2):
    def _A_ref(x, dft_seq_re, dft_seq_im, dft_hid_re, dft_hid_im):
        a = torch.einsum("bsd,ed->bse", x, dft_hid_re)
        b = torch.einsum("bsd,ed->bse", x, dft_hid_im)
        y = torch.einsum("ts,bse->bte", dft_seq_re, a) - torch.einsum("ts,bse->bte", dft_seq_im, b)
        return y

    def _B_ref(x, w1, w2):
        h = torch.einsum("bsd,df->bsf", x, w1)
        zero = torch.zeros_like(h)
        h = torch.maximum(h, zero)
        return torch.einsum("bsf,fd->bsd", h, w2)

    x = A_x
    x = x + _A_ref(_layernorm(x, axis=-1), A_dft_seq_re, A_dft_seq_im, A_dft_hid_re, A_dft_hid_im)
    x = x + _B_ref(_layernorm(x, axis=-1), B_w1, B_w2)
    x = x + _A_ref(_layernorm(x, axis=-1), A_dft_seq_re, A_dft_seq_im, A_dft_hid_re, A_dft_hid_im)
    x = x + _B_ref(_layernorm(x, axis=-1), B_w1, B_w2)
    return x
```

```cpp
#include <hip/hip_runtime.h>
#include <hip/hip_bf16.h>
#include <stdint.h>
typedef unsigned short u16;
__device__ __forceinline__ float bf2f(u16 v){ return __uint_as_float(((unsigned)v)<<16); }
__device__ __forceinline__ u16 f2bf(float f){ unsigned u=__float_as_uint(f); u += 0x7FFF + ((u>>16)&1); return (u16)(u>>16); }
constexpr int S=2048,D=640,F=1728;
__global__ void ln_k(const u16* x, u16* xn){
  int r=blockIdx.x; __shared__ float s1[256], s2[256];
  float a=0,b=0; for(int i=threadIdx.x;i<D;i+=256){ float v=bf2f(x[r*D+i]); a+=v; }
  s1[threadIdx.x]=a; __syncthreads();
  for(int o=128;o>0;o>>=1){ if(threadIdx.x<o) s1[threadIdx.x]+=s1[threadIdx.x+o]; __syncthreads(); }
  float m=s1[0]/D;
  for(int i=threadIdx.x;i<D;i+=256){ float v=bf2f(x[r*D+i])-m; b+=v*v; }
  s2[threadIdx.x]=b; __syncthreads();
  for(int o=128;o>0;o>>=1){ if(threadIdx.x<o) s2[threadIdx.x]+=s2[threadIdx.x+o]; __syncthreads(); }
  float rs=rsqrtf(s2[0]/D+1e-5f);
  for(int i=threadIdx.x;i<D;i+=256){ xn[r*D+i]=f2bf((bf2f(x[r*D+i])-m)*rs); }
}
__global__ void gemm_k(const u16* A,int lda,const u16* B,int sbk,int sbn,int M,int N,int K,float* Cf,int accum,float sign){
  __shared__ float As[16][17], Bs[16][17];
  int tx=threadIdx.x&15, ty=threadIdx.x>>4; int m=blockIdx.y*16+ty, n=blockIdx.x*16+tx; float acc=0;
  for(int k0=0;k0<K;k0+=16){
    As[ty][tx]=bf2f(A[(size_t)(blockIdx.y*16+ty)*lda+k0+tx]);
    Bs[ty][tx]=bf2f(B[(size_t)(k0+ty)*sbk+(size_t)(blockIdx.x*16+tx)*sbn]);
    __syncthreads();
    for(int k=0;k<16;++k) acc+=As[ty][k]*Bs[k][tx];
    __syncthreads();
  }
  if(accum) Cf[(size_t)m*N+n]+=sign*acc; else Cf[(size_t)m*N+n]=sign*acc;
}
__global__ void fin_k(const float* Cf,u16* out,const u16* res,int n,int relu){
  int i=blockIdx.x*256+threadIdx.x; if(i>=n) return; float v=Cf[i]; if(relu) v=fmaxf(v,0.f); if(res) v+=bf2f(res[i]); out[i]=f2bf(v);
}
extern "C" void kernel_launch(void* const* d_in, const int* in_sizes, int n_in, void* d_out, int out_size, void* d_ws, size_t ws_size, hipStream_t stream){
  const u16* x0=(const u16*)d_in[0]; const u16* Sre=(const u16*)d_in[1]; const u16* Sim=(const u16*)d_in[2];
  const u16* Dre=(const u16*)d_in[3]; const u16* Dim=(const u16*)d_in[4]; const u16* w1=(const u16*)d_in[6]; const u16* w2=(const u16*)d_in[7];
  char* ws=(char*)d_ws; u16* xn=(u16*)ws; u16* a=(u16*)(ws+(4<<20)); u16* b=(u16*)(ws+(8<<20)); u16* x1=(u16*)(ws+(12<<20)); u16* x2=(u16*)(ws+(16<<20)); u16* h=(u16*)(ws+(20<<20)); float* Cf=(float*)(ws+(32<<20));
  const u16* x=x0;
  for(int l=0;l<2;++l){
    ln_k<<<S,256,0,stream>>>(x,xn);
    gemm_k<<<dim3(D/16,S/16),256,0,stream>>>(xn,D,Dre,1,D,S,D,D,Cf,0,1.f); fin_k<<<S*D/256,256,0,stream>>>(Cf,a,nullptr,S*D,0);
    gemm_k<<<dim3(D/16,S/16),256,0,stream>>>(xn,D,Dim,1,D,S,D,D,Cf,0,1.f); fin_k<<<S*D/256,256,0,stream>>>(Cf,b,nullptr,S*D,0);
    gemm_k<<<dim3(D/16,S/16),256,0,stream>>>(Sre,S,a,D,1,S,D,S,Cf,0,1.f);
    gemm_k<<<dim3(D/16,S/16),256,0,stream>>>(Sim,S,b,D,1,S,D,S,Cf,1,-1.f); fin_k<<<S*D/256,256,0,stream>>>(Cf,x1,x,S*D,0);
    ln_k<<<S,256,0,stream>>>(x1,xn);
    gemm_k<<<dim3(F/16,S/16),256,0,stream>>>(xn,D,w1,F,1,S,F,D,Cf,0,1.f); fin_k<<<S*F/256,256,0,stream>>>(Cf,h,nullptr,S*F,1);
    gemm_k<<<dim3(D/16,S/16),256,0,stream>>>(h,F,w2,D,1,S,D,F,Cf,0,1.f); fin_k<<<S*D/256,256,0,stream>>>(Cf,l==0?x2:(u16*)d_out,x1,S*D,0);
    x=x2;
  }
}
```

```cpp
#include <hip/hip_runtime.h>
#include <hip/hip_bf16.h>
#include <stdint.h>

#define DI __device__ __forceinline__
typedef unsigned short u16;
typedef __attribute__((ext_vector_type(8))) __bf16 bf16x8;
typedef __attribute__((ext_vector_type(2))) __bf16 bf16x2;
typedef __attribute__((ext_vector_type(4))) float f32x4;
typedef __attribute__((ext_vector_type(2))) float f32x2;
typedef __attribute__((ext_vector_type(4))) unsigned u32x4;
typedef __attribute__((ext_vector_type(2))) unsigned u32x2;
typedef __attribute__((ext_vector_type(4))) short s16x4;
#define LDS_AS __attribute__((address_space(3)))
#define GLB_AS __attribute__((address_space(1)))
typedef LDS_AS char* ldsp;
typedef GLB_AS unsigned gu32;
typedef __amdgpu_buffer_rsrc_t rsrc_t;
#define RLX_AGENT __ATOMIC_RELAXED, __HIP_MEMORY_SCOPE_AGENT

constexpr int S_ = 2048, D_ = 640, F_ = 1728;
constexpr float EPS = 1e-5f;
constexpr unsigned OOB = 0x7f000000u;

constexpr unsigned SZ_AB = 8 * 128 * 5 * 1024;
constexpr unsigned SZ_X = 128 * 20 * 1024;
constexpr unsigned SZ_H = 128 * 54 * 1024;
constexpr unsigned SZ_W1 = 108 * 20 * 1024;
constexpr unsigned SZ_W2 = 8 * 54 * 5 * 1024;
constexpr unsigned SZ_ST = 2048 * 64;
constexpr unsigned WS_AB1 = 0;
constexpr unsigned WS_AB2 = WS_AB1 + SZ_AB;
constexpr unsigned WS_X1 = WS_AB2 + SZ_AB;
constexpr unsigned WS_X2 = WS_X1 + SZ_X;
constexpr unsigned WS_X3 = WS_X2 + SZ_X;
constexpr unsigned WS_H1 = WS_X3 + SZ_X;
constexpr unsigned WS_H2 = WS_H1 + SZ_H;
constexpr unsigned WS_W1 = WS_H2 + SZ_H;
constexpr unsigned WS_W2 = WS_W1 + SZ_W1;
constexpr unsigned WS_ST1 = WS_W2 + SZ_W2;
constexpr unsigned WS_ST2 = WS_ST1 + SZ_ST;
constexpr unsigned WS_ST3 = WS_ST2 + SZ_ST;
constexpr unsigned WS_CS1 = WS_ST3 + SZ_ST;
constexpr unsigned WS_CSD = WS_CS1 + 20 * 1728 * 4;
constexpr unsigned WS_FLG = (WS_CSD + 1280 * 4 + 4095) & ~4095u;
constexpr unsigned FLG_SEAM = 16 * 256;
constexpr unsigned WS_END = WS_FLG + 6 * FLG_SEAM;

DI u32x4 make_srd(const void* p, unsigned bytes) {
  uint64_t a = (uint64_t)p;
  u32x4 r;
  r[0] = (unsigned)a; r[1] = (unsigned)(a >> 32) & 0xffffu; r[2] = bytes; r[3] = 0x00020000u;
  return r;
}
DI rsrc_t make_rsrc(const void* p, unsigned bytes) {
  return __builtin_amdgcn_make_buffer_rsrc((void*)p, (short)0, (int)bytes, 0x00020000);
}
template <int LOFF, int GOFF>
DI void glds(u32x4 srd, unsigned voff, unsigned ldsb, unsigned soff) {
  unsigned tmp;
  asm volatile("s_add_u32 m0, %1, %2\n\ts_add_u32 %0, %3, %4\n\tbuffer_load_dwordx4 %5, %6, %0 offen lds"
               : "=&s"(tmp)
               : "s"(ldsb), "i"(LOFF), "s"(soff), "i"(GOFF), "v"(voff), "s"(srd)
               : "memory", "scc");
}
template <int N> DI void glds_lin(u32x4 srd, unsigned voff, unsigned ldsb, unsigned soff) {
  if constexpr (N == 4)
    asm volatile("s_mov_b32 m0, %0\n\ts_nop 0\n\tbuffer_load_dwordx4 %1, %2, %3 offen lds\n\tbuffer_load_dwordx4 %1, %2, %3 offen offset:1024 lds\n\t"
                 "buffer_load_dwordx4 %1, %2, %3 offen offset:2048 lds\n\tbuffer_load_dwordx4 %1, %2, %3 offen offset:3072 lds"
                 :: "s"(ldsb), "v"(voff), "s"(srd), "s"(soff) : "memory");
  else if constexpr (N == 3)
    asm volatile("s_mov_b32 m0, %0\n\ts_nop 0\n\tbuffer_load_dwordx4 %1, %2, %3 offen lds\n\tbuffer_load_dwordx4 %1, %2, %3 offen offset:1024 lds\n\t"
                 "buffer_load_dwordx4 %1, %2, %3 offen offset:2048 lds"
                 :: "s"(ldsb), "v"(voff), "s"(srd), "s"(soff) : "memory");
  else
    asm volatile("s_mov_b32 m0, %0\n\ts_nop 0\n\tbuffer_load_dwordx4 %1, %2, %3 offen lds"
                 :: "s"(ldsb), "v"(voff), "s"(srd), "s"(soff) : "memory");
}
template <int IMM> DI void ldv(bf16x8& d, u32x4 srd, unsigned voff, unsigned soff) {
  asm volatile("buffer_load_dwordx4 %0, %1, %2, %3 offen offset:%4 sc1" : "=v"(d) : "v"(voff), "s"(srd), "s"(soff), "i"(IMM) : "memory");
}
template <int N> DI void wait_vm() { asm volatile("s_waitcnt vmcnt(%0)" ::"i"(N) : "memory"); }
template <int N> DI void wait_vm5(bf16x8 (&b)[5]) {
  asm volatile("s_waitcnt vmcnt(%5)" : "+v"(b[0]), "+v"(b[1]), "+v"(b[2]), "+v"(b[3]), "+v"(b[4]) : "i"(N) : "memory");
}
DI void wait_lgkm0() { asm volatile("s_waitcnt lgkmcnt(0)" ::: "memory"); }
DI void sb() { __builtin_amdgcn_sched_barrier(0); }
DI void bar() { asm volatile("s_barrier" ::: "memory"); }

DI bf16x8 lds_rd(unsigned addr) { return *(const LDS_AS bf16x8*)(ldsp)(uintptr_t)addr; }
DI f32x4 lds_rdf(unsigned addr) { return *(const LDS_AS f32x4*)(ldsp)(uintptr_t)addr; }
DI void lds_wrf(unsigned addr, f32x4 v) { *(LDS_AS f32x4*)(ldsp)(uintptr_t)addr = v; }
DI float lds_rd1(unsigned addr) { return *(const LDS_AS float*)(ldsp)(uintptr_t)addr; }
DI void lds_wr1(unsigned addr, float v) { *(LDS_AS float*)(ldsp)(uintptr_t)addr = v; }
DI f32x4 mfma(bf16x8 a, bf16x8 b, f32x4 c) { return __builtin_amdgcn_mfma_f32_16x16x32_bf16(a, b, c, 0, 0, 0); }
DI void mma(f32x4& c, const bf16x8& a, const bf16x8& b) {
  asm volatile("v_mfma_f32_16x16x32_bf16 %0, %1, %2, %0" : "+v"(c) : "v"(a), "v"(b));
}
DI void mma0(f32x4& c, const bf16x8& a, const bf16x8& b) {
  asm volatile("v_mfma_f32_16x16x32_bf16 %0, %1, %2, 0" : "=&v"(c) : "v"(a), "v"(b));
}
template <int N> DI void fence_acc(f32x4 (&a)[N]) {
  if constexpr (N == 5) asm volatile("s_nop 7\n\ts_nop 1" : "+v"(a[0]), "+v"(a[1]), "+v"(a[2]), "+v"(a[3]), "+v"(a[4]));
  else asm volatile("s_nop 7\n\ts_nop 1" : "+v"(a[0]), "+v"(a[1]), "+v"(a[2]), "+v"(a[3]), "+v"(a[4]), "+v"(a[5]), "+v"(a[6]));
}
template <int N> DI void pin_acc(f32x4 (&a)[N]) {
  if constexpr (N == 5) asm volatile("" : "+v"(a[0]), "+v"(a[1]), "+v"(a[2]), "+v"(a[3]), "+v"(a[4]));
  else asm volatile("" : "+v"(a[0]), "+v"(a[1]), "+v"(a[2]), "+v"(a[3]), "+v"(a[4]), "+v"(a[5]), "+v"(a[6]));
}
template <int M, int N> DI void fence_all(f32x4 (&a)[M][N]) {
#pragma unroll
  for (int i = 1; i < M; ++i) pin_acc(a[i]);
  fence_acc(a[0]);
#pragma unroll
  for (int i = 1; i < M; ++i) pin_acc(a[i]);
}
DI unsigned pk2(float a, float b) {
  f32x2 v = {a, b};
  return __builtin_bit_cast(unsigned, __builtin_convertvector(v, bf16x2));
}
DI float bflo(unsigned u) { return __uint_as_float(u << 16); }
DI float bfhi(unsigned u) { return __uint_as_float(u & 0xffff0000u); }
DI float xsum16_32(float x) {
  auto s = __builtin_amdgcn_permlane16_swap(__float_as_uint(x), __float_as_uint(x), false, false);
  x = __uint_as_float(s[0]) + __uint_as_float(s[1]);
  auto t = __builtin_amdgcn_permlane32_swap(__float_as_uint(x), __float_as_uint(x), false, false);
  return __uint_as_float(t[0]) + __uint_as_float(t[1]);
}
DI unsigned rfl(unsigned v) { return __builtin_amdgcn_readfirstlane(v); }
DI unsigned opq_v(unsigned x) { asm volatile("" : "+v"(x)); return x; }
DI unsigned opq_s(unsigned x) { asm volatile("" : "+s"(x)); return x; }
template <class T> DI T* opq_p(T* x) { asm volatile("" : "+s"(x)); return x; }
DI bf16x8 ld16(rsrc_t r, unsigned voff, unsigned soff) { return __builtin_bit_cast(bf16x8, __builtin_amdgcn_raw_buffer_load_b128(r, voff, soff, 16)); }
DI u32x4 ld16u(rsrc_t r, unsigned voff, unsigned soff) { return __builtin_amdgcn_raw_buffer_load_b128(r, voff, soff, 16); }
DI u32x2 ld8u(rsrc_t r, unsigned voff, unsigned soff) { return __builtin_amdgcn_raw_buffer_load_b64(r, voff, soff, 16); }
DI void st16(u32x4 v, rsrc_t r, unsigned voff, unsigned soff) { __builtin_amdgcn_raw_buffer_store_b128(v, r, voff, soff, 16); }
DI void st8(u32x2 v, rsrc_t r, unsigned voff, unsigned soff) { __builtin_amdgcn_raw_buffer_store_b64(v, r, voff, soff, 16); }
DI void st4(unsigned v, rsrc_t r, unsigned voff, unsigned soff) { __builtin_amdgcn_raw_buffer_store_b32(v, r, voff, soff, 16); }

#define ST(i)
#define STAMP_ARGS
#define STAMP_FWD
#define STAMP_PASS(k)

template <int KS> DI unsigned foff(unsigned row, unsigned col) {
  return ((row >> 4) * KS + (col >> 5)) * 1024u + (((col >> 3) & 3) * 16u + (row & 15)) * 16u + (col & 7) * 2u;
}

extern __shared__ __attribute__((aligned(1024))) char smem[];
DI unsigned lds_base() { return (unsigned)(uintptr_t)(ldsp)smem; }

DI void ln_coef(float s1, float s2, float& mu, float& rs) {
  mu = s1 * (1.f / D_);
  rs = rsqrtf(fmaxf(s2 * (1.f / D_) - mu * mu, 0.f) + EPS);
}

DI void g1_store(const f32x4 (&acc)[2][5], const float (&mu)[2][4], const float (&rs)[2][4], unsigned csb, rsrc_t rab,
                 unsigned P, unsigned ct, unsigned w, unsigned i16, unsigned q) {
  const float sign = ct < 8 ? 1.f : -1.f;
  const unsigned kstep = (ct >> 3) * 64 + P * 4 + w;
  const unsigned cq = ((q & 1) << 1) | (q >> 1);
  const unsigned so = ((ct & 7) * 128 + kstep) * 5 * 1024;
  const unsigned vo = (cq * 16 + i16) * 16;
#pragma unroll
  for (int f = 0; f < 5; ++f) {
    const float cs = lds_rd1(csb + (f * 16 + i16) * 4);
    unsigned pk[2][2];
#pragma unroll
    for (int r = 0; r < 2; ++r) {
      float t[4];
#pragma unroll
      for (int j = 0; j < 4; ++j) t[j] = sign * rs[r][j] * (acc[r][f][j] - mu[r][j] * cs);
      pk[r][0] = pk2(t[0], t[1]); pk[r][1] = pk2(t[2], t[3]);
    }
    auto t0 = __builtin_amdgcn_permlane16_swap(pk[0][0], pk[1][0], false, false);
    auto t1 = __builtin_amdgcn_permlane16_swap(pk[0][1], pk[1][1], false, false);
    st16(u32x4{t0[0], t1[0], t0[1], t1[1]}, rab, vo + f * 1024, so);
  }
}

constexpr int K1_STAGE = 26 * 1024;
constexpr int K1_TR = 5 * K1_STAGE;
constexpr int K1_CS = K1_TR + 5 * 4096;
constexpr int K1_LDS = K1_CS + 512;

DI void k1_issue(u32x4 sa, u32x4 sbd, unsigned vo, unsigned la, unsigned lbb, unsigned soa, unsigned sob, unsigned w) {
  glds<0, 0>(sa, vo, la, soa);
  glds<1024, 8 * 1280>(sa, vo, la, soa);
  glds<2048, 16 * 1280>(sa, vo, la, soa);
  glds<3072, 24 * 1280>(sa, vo, la, soa);
  glds<0, 0>(sbd, vo, lbb, sob);
  glds<4096, 32 * 1280>(sbd, vo, lbb, sob);
  if (w < 2) glds<8192, 64 * 1280>(sbd, vo, lbb, sob);
}
DI void retile_issue(unsigned u, const u16* w1, const u16* w2, unsigned ldst, unsigned lane) {
  const bool first = u < 540;
  const unsigned v = first ? u : u - 540;
  const unsigned ng = first ? 27u : 10u;
  const unsigned ks = v / ng, g = v - ks * ng;
  const unsigned ld = first ? F_ * 2 : D_ * 2;
  u32x4 srd = make_srd(first ? w1 : w2, D_ * F_ * 2);
  const unsigned vo = (lane >> 3) * ld + (lane & 7) * 16;
  const unsigned so = ks * 32 * ld + g * 128;
  glds<0, 0>(srd, vo, ldst, so);
  glds<1024, 0>(srd, vo, ldst, so + 8 * ld);
  glds<2048, 0>(srd, vo, ldst, so + 16 * ld);
  glds<3072, 0>(srd, vo, ldst, so + 24 * ld);
}
DI void retile_finish(unsigned u, rsrc_t rws, unsigned ldst, unsigned lane) {
  const bool first = u < 540;
  const unsigned v = first ? u : u - 540;
  const unsigned ng = first ? 27u : 10u;
  const unsigned ks = v / ng, g = v - ks * ng;
  const unsigned i = lane & 15, q = lane >> 4;
  const unsigned base = ldst + (8 * q + (i >> 2)) * 128 + (i & 3) * 8;
#pragma unroll
  for (int f = 0; f < 4; ++f) {
    s16x4 lo = __builtin_amdgcn_ds_read_tr16_b64_v4i16((LDS_AS s16x4*)(uintptr_t)(base + f * 32));
    s16x4 hi = __builtin_amdgcn_ds_read_tr16_b64_v4i16((LDS_AS s16x4*)(uintptr_t)(base + f * 32 + 512));
    u32x2 l2 = __builtin_bit_cast(u32x2, lo), h2 = __builtin_bit_cast(u32x2, hi);
    const unsigned rt = g * 4 + f;
    unsigned so;
    if (first) so = WS_W1 + (rt * 20 + ks) * 1024;
    else { const unsigned c = rt / 5; so = WS_W2 + ((c * 54 + ks) * 5 + (rt - c * 5)) * 1024; }
    st16(u32x4{l2[0], l2[1], h2[0], h2[1]}, rws, lane * 16, so);
    if (first) {
      float s = (bflo(l2[0]) + bfhi(l2[0])) + (bflo(l2[1]) + bfhi(l2[1])) + (bflo(h2[0]) + bfhi(h2[0])) + (bflo(h2[1]) + bfhi(h2[1]));
      s = xsum16_32(s);
      if (q == 0) st4(__float_as_uint(s), rws, i * 4, WS_CS1 + (ks * F_ + rt * 16) * 4);
    }
  }
}

__global__ __launch_bounds__(320, 2) void k_first(const u16* x0, const u16* Dre, const u16* Dim, const u16* w1, const u16* w2,
                                                 const u16* Sre, const u16* Sim, char* ws STAMP_ARGS) {
  const unsigned tid = threadIdx.x, lane = tid & 63, w = rfl(tid >> 6);
  const unsigned xcd = blockIdx.x & 7, idx = blockIdx.x >> 3;
  const unsigned P = (xcd >> 1) * 4 + (idx >> 3), ct = (xcd & 1) * 8 + (idx & 7);
  const unsigned i16 = lane & 15, q = lane >> 4;
  const unsigned lb = lds_base();
  const rsrc_t rws = make_rsrc(ws, WS_END);

  if (w == 4) {
    const unsigned nu = blockIdx.x < 56 ? 5 : 4;
    asm volatile("s_nop 4" ::: "memory");
#pragma unroll 1
    for (unsigned n = 0; n < nu; ++n) retile_issue(n < 4 ? blockIdx.x * 4 + n : 1024 + blockIdx.x, w1, w2, lb + K1_TR + n * 4096, lane);
    unsigned d[8];
    {
      const u32x4 s0 = make_srd(Sre, S_ * S_ * 2), s1 = make_srd(Sim, S_ * S_ * 2);
      const unsigned so = (xcd * 256 + idx * 8) * 4096;
#pragma unroll
      for (int n = 0; n < 8; ++n)
        asm volatile("buffer_load_dword %0, %1, %2, %3 offen" : "=v"(d[n]) : "v"(lane * 128), "s"(n < 4 ? s0 : s1), "s"(so + (n & 3) * 8192) : "memory");
    }
    wait_vm<8>();
    bar();
#pragma unroll 1
    for (unsigned n = 0; n < 10; ++n) {
      if (n < nu) retile_finish(n < 4 ? blockIdx.x * 4 + n : 1024 + blockIdx.x, rws, lb + K1_TR + n * 4096, lane);
      if (n == 5 && blockIdx.x < 6 * FLG_SEAM / 256) st4(0u, rws, lane * 4, WS_FLG + blockIdx.x * 256);
      bar();
    }
    bar();
    asm volatile("s_waitcnt vmcnt(0)" : "+v"(d[0]), "+v"(d[1]), "+v"(d[2]), "+v"(d[3]), "+v"(d[4]), "+v"(d[5]), "+v"(d[6]), "+v"(d[7]) :: "memory");
    return;
  }
  ST(0);
  const u32x4 sa = make_srd(x0, S_ * D_ * 2), sbd = make_srd(ct < 8 ? Dre : Dim, D_ * D_ * 2);
  const unsigned vo = (lane >> 3) * 1280 + (((lane & 7) ^ (lane >> 3)) << 4);
  const unsigned soa = (P * 128 + w * 32) * 1280, sob = ((ct & 7) * 80 + w * 8) * 1280;
  const unsigned la = lb + w * 4096, lbb = lb + 16384 + w * 1024;
  asm volatile("s_nop 4" ::: "memory");
#pragma unroll
  for (int s = 0; s < 5; ++s) k1_issue(sa, sbd, vo, la + s * K1_STAGE, lbb + s * K1_STAGE, soa + s * 128, sob + s * 128, w);
  ST(1);

  const unsigned ra0 = lb + w * 4096 + i16 * 128 + ((q ^ (lane & 7)) << 4);
  const unsigned rb0 = lb + 16384 + i16 * 128 + ((q ^ (lane & 7)) << 4);
  const f32x4 z4 = {0.f, 0.f, 0.f, 0.f};
  f32x4 acc[2][5], cs0 = z4, cs1 = z4, rs_[2] = {z4, z4}, rq_[2] = {z4, z4};
#pragma unroll
  for (int r = 0; r < 2; ++r)
#pragma unroll
    for (int f = 0; f < 5; ++f) acc[r][f] = z4;
  const bf16x8 ones = {(__bf16)1.f, (__bf16)1.f, (__bf16)1.f, (__bf16)1.f, (__bf16)1.f, (__bf16)1.f, (__bf16)1.f, (__bf16)1.f};
  bf16x8 a[2][2], b[2][5], x[2];
  auto rd = [&](int kk, unsigned so) {
    const unsigned pa = (ra0 ^ (kk * 64)) + so, pb = (rb0 ^ (kk * 64)) + so;
    a[kk][0] = lds_rd(pa); a[kk][1] = lds_rd(pa + 2048);
#pragma unroll
    for (int f = 0; f < 5; ++f) b[kk][f] = lds_rd(pb + f * 2048);
    x[kk] = lds_rd(pb + w * 2048);
  };
  auto mm = [&](int kk) {
#pragma unroll
    for (int f = 0; f < 5; ++f)
#pragma unroll
      for (int r = 0; r < 2; ++r) acc[r][f] = mfma(a[kk][r], b[kk][f], acc[r][f]);
    cs0 = mfma(ones, x[kk], cs0);
    cs1 = mfma(ones, b[kk][4], cs1);
#pragma unroll
    for (int r = 0; r < 2; ++r) { rs_[r] = mfma(a[kk][r], ones, rs_[r]); rq_[r] = mfma(a[kk][r], a[kk][r], rq_[r]); }
  };
  wait_vm<4 * 6>();
  bar();
  ST(2);
  rd(0, 0);
  unsigned so = 0;
#pragma unroll 1
  for (int i = 0; i < 10; ++i) {
    rd(1, so);
    sb();
    mm(0);
    sb();
    wait_lgkm0();
    if (i < 6) wait_vm<3 * 6>(); else if (i == 6) wait_vm<2 * 6>(); else if (i == 7) wait_vm<6>(); else wait_vm<0>();
    bar();
    if (i < 5) k1_issue(sa, sbd, vo, la + so, lbb + so, soa + (i + 5) * 128, sob + (i + 5) * 128, w);
    so = so == 4 * K1_STAGE ? 0 : so + K1_STAGE;
    rd(0, so);
    sb();
    mm(1);
    sb();
  }
  ST(3);
  const unsigned csb = lb + K1_CS;
  if (q == 0) {
    lds_wr1(csb + (w * 16 + i16) * 4, cs0[0]);
    if (w == 0) lds_wr1(csb + (64 + i16) * 4, cs1[0]);
    if (P == 0) {
      st4(__float_as_uint(cs0[0]), rws, i16 * 4, WS_CSD + (ct * 80 + w * 16) * 4);
      if (w == 0) st4(__float_as_uint(cs1[0]), rws, i16 * 4, WS_CSD + (ct * 80 + 64) * 4);
    }
  }
  wait_lgkm0();
  bar();
  float mu[2][4], rs[2][4];
#pragma unroll
  for (int r = 0; r < 2; ++r)
#pragma unroll
    for (int j = 0; j < 4; ++j) ln_coef(rs_[r][j], __shfl(rq_[r][j], q * 20 + j), mu[r][j], rs[r][j]);
  g1_store(acc, mu, rs, csb, make_rsrc(ws + WS_AB1, SZ_AB), P, ct, w, i16, q);
  ST(4);
  wait_vm<0>();
  ST(5);
}

constexpr int KM_LDS = 160 * 1024;
constexpr int KM_CS1 = KM_LDS - 1024, KM_CSD = KM_LDS - 512;
constexpr int G2_RING = 32 * 1024, G4_REG = 35 * 1024;

DI void publish(char* ws, unsigned flag_off, unsigned tid) {
  wait_vm<0>();
  bar();
  if (tid == 0) __hip_atomic_store((gu32*)(ws + flag_off), 1u, RLX_AGENT);
}
DI void wait_flags(char* ws, unsigned line_off, unsigned n, unsigned w, unsigned lane) {
  if (w == 0) {
    gu32* p = (gu32*)(ws + line_off) + (lane < n ? lane : 0);
    for (unsigned s = 0; s < (1u << 20); ++s) {
      if (__all(__hip_atomic_load(p, RLX_AGENT) != 0u)) break;
      __builtin_amdgcn_s_sleep(1);
    }
  }
  bar();
}

template <int W> DI void sm_scatter(unsigned wb, const f32x4 (&acc)[4][5], f32x4 (&own)[5]) {
#pragma unroll
  for (int d = 0; d < 4; ++d)
#pragma unroll
    for (int f = 0; f < 5; ++f) {
      if (d == W) own[f] = acc[d][f];
      else lds_wrf(wb + (d * 5 + f) * 1024, acc[d][f]);
    }
  asm volatile("; scatter %5" : "+v"(own[0]), "+v"(own[1]), "+v"(own[2]), "+v"(own[3]), "+v"(own[4]) : "i"(W));
}

struct ResTile { u32x4 r0, r1; u32x2 r2; };
DI void tile_offsets(bool rm, unsigned row, unsigned ct, unsigned q, unsigned& a0, unsigned& a1, unsigned& a2) {
  const unsigned cA = ct * 80 + 16 * (q & 1) + 8 * (q >> 1), cS = ct * 80 + 64 + 4 * q;
  if (rm) { a0 = (row * D_ + cA) * 2; a1 = a0 + 64; a2 = (row * D_ + cS) * 2; }
  else { a0 = foff<20>(row, cA); a1 = foff<20>(row, cA + 32); a2 = foff<20>(row, cS); }
}
DI void small_epilogue(const f32x4 (&acc)[4][5], unsigned region, const ResTile& rt, rsrc_t rout, bool out_rm, rsrc_t rst,
                       unsigned row, unsigned ct, unsigned w, unsigned lane) {
  const unsigned q = lane >> 4, lb = lds_base();
  f32x4 v[5];
  wait_lgkm0();
  {
    const unsigned wb = lb + w * region + lane * 16;
    if (w == 0) sm_scatter<0>(wb, acc, v);
    else if (w == 1) sm_scatter<1>(wb, acc, v);
    else if (w == 2) sm_scatter<2>(wb, acc, v);
    else sm_scatter<3>(wb, acc, v);
  }
  wait_lgkm0();
  bar();
#pragma unroll
  for (int s = 1; s < 4; ++s) {
    const unsigned src = lb + ((w + s) & 3) * region + w * 5 * 1024 + lane * 16;
#pragma unroll
    for (int f = 0; f < 5; ++f) v[f] += lds_rdf(src + f * 1024);
  }
#pragma unroll
  for (int p = 0; p < 2; ++p)
#pragma unroll
    for (int r = 0; r < 4; ++r) {
      auto t = __builtin_amdgcn_permlane16_swap(__float_as_uint(v[2 * p][r]), __float_as_uint(v[2 * p + 1][r]), false, false);
      v[2 * p][r] = __uint_as_float(t[0]); v[2 * p + 1][r] = __uint_as_float(t[1]);
    }
  u32x4 y0, y1; u32x2 y2;
#pragma unroll
  for (int j = 0; j < 2; ++j) {
    y0[j] = pk2(v[0][2 * j] + bflo(rt.r0[j]), v[0][2 * j + 1] + bfhi(rt.r0[j]));
    y0[2 + j] = pk2(v[1][2 * j] + bflo(rt.r0[2 + j]), v[1][2 * j + 1] + bfhi(rt.r0[2 + j]));
    y1[j] = pk2(v[2][2 * j] + bflo(rt.r1[j]), v[2][2 * j + 1] + bfhi(rt.r1[j]));
    y1[2 + j] = pk2(v[3][2 * j] + bflo(rt.r1[2 + j]), v[3][2 * j + 1] + bfhi(rt.r1[2 + j]));
    y2[j] = pk2(v[4][2 * j] + bflo(rt.r2[j]), v[4][2 * j + 1] + bfhi(rt.r2[j]));
  }
  unsigned o0, o1, o2;
  tile_offsets(out_rm, row, ct, q, o0, o1, o2);
  st16(y0, rout, o0, 0); st16(y1, rout, o1, 0); st8(y2, rout, o2, 0);
  if (!out_rm) {
    float s1 = 0.f, s2 = 0.f;
#pragma unroll
    for (int j = 0; j < 4; ++j) {
      float a = bflo(y0[j]), b = bfhi(y0[j]), c = bflo(y1[j]), d = bfhi(y1[j]);
      s1 += (a + b) + (c + d);
      s2 += a * a + b * b + c * c + d * d;
    }
#pragma unroll
    for (int j = 0; j < 2; ++j) {
      float a = bflo(y2[j]), b = bfhi(y2[j]);
      s1 += a + b; s2 += a * a + b * b;
    }
    s1 = xsum16_32(s1); s2 = xsum16_32(s2);
    if (q == 0) st8(u32x2{__float_as_uint(s1), __float_as_uint(s2)}, rst, (row * 8 + ct) * 8, 0);
  }
}

template <int H> DI void g2_a4(u32x4 sa, unsigned va, unsigned l, unsigned so) {
  glds<H * 4096, H * 4 * 8 * 4096>(sa, va, l, so);
  glds<H * 4096 + 1024, (H * 4 + 1) * 8 * 4096>(sa, va, l, so);
  glds<H * 4096 + 2048, (H * 4 + 2) * 8 * 4096>(sa, va, l, so);
  glds<H * 4096 + 3072, (H * 4 + 3) * 8 * 4096>(sa, va, l, so);
}
struct G2Ctx { u32x4 sre, sim, sab; unsigned va, vb0, vb1, ring, soa0, sob0; };
DI unsigned g2_soa(const G2Ctx& c, int i) { return i < 16 ? c.soa0 + (i & 7) * 512 : 0; }
DI unsigned g2_sob(const G2Ctx& c, int i, int kk) { return i < 16 ? c.sob0 + i * (8 * 5 * 1024) + kk * 5 * 1024 : 0; }
DI u32x4 g2_sa(const G2Ctx& c, int i) { u32x4 r = i < 8 ? c.sre : c.sim; r[2] = i < 16 ? S_ * S_ * 2 : 0; return r; }
DI u32x4 g2_sb(const G2Ctx& c, int i) { u32x4 r = c.sab; r[2] = i < 16 ? SZ_AB : 0; return r; }
DI void g2_b5(bf16x8 (&b)[5], const G2Ctx& c, unsigned so) {
  ldv<0>(b[0], c.sab, c.vb0, so); ldv<1024>(b[1], c.sab, c.vb0, so); ldv<2048>(b[2], c.sab, c.vb0, so);
  ldv<3072>(b[3], c.sab, c.vb0, so); ldv<0>(b[4], c.sab, c.vb1, so);
}
DI void g2_prefetch(const G2Ctx& c) {
#pragma unroll
  for (int s = 0; s < 4; ++s) {
    g2_a4<0>(c.sre, c.va, c.ring + s * 8192, c.soa0 + s * 512);
    if (s < 3) g2_a4<1>(c.sre, c.va, c.ring + s * 8192, c.soa0 + s * 512);
  }
}
template <int H> DI void g2_half(f32x4 (&acc)[4][5], const bf16x8 (&a)[4], const bf16x8 (&b)[5], bf16x8 (&bn)[5],
                                 const G2Ctx& c, u32x4 sbb, unsigned sob, u32x4 sa, unsigned l, unsigned soa) {
#define G2_MM(m) mma(acc[(m) / 5][(m) % 5], b[(m) % 5], a[(m) / 5]);
  G2_MM(0) G2_MM(1) sb();
  ldv<0>(bn[0], sbb, c.vb0, sob); sb(); G2_MM(2) G2_MM(3) sb();
  ldv<1024>(bn[1], sbb, c.vb0, sob); sb(); G2_MM(4) G2_MM(5) sb();
  ldv<2048>(bn[2], sbb, c.vb0, sob); sb(); G2_MM(6) G2_MM(7) sb();
  ldv<3072>(bn[3], sbb, c.vb0, sob); sb(); G2_MM(8) G2_MM(9) sb();
  ldv<0>(bn[4], sbb, c.vb1, sob); sb(); G2_MM(10) G2_MM(11) sb();
  glds<H * 4096, H * 4 * 8 * 4096>(sa, c.va, l, soa); sb(); G2_MM(12) G2_MM(13) sb();
  glds<H * 4096 + 1024, (H * 4 + 1) * 8 * 4096>(sa, c.va, l, soa); sb(); G2_MM(14) G2_MM(15) sb();
  glds<H * 4096 + 2048, (H * 4 + 2) * 8 * 4096>(sa, c.va, l, soa); sb(); G2_MM(16) G2_MM(17) sb();
  glds<H * 4096 + 3072, (H * 4 + 3) * 8 * 4096>(sa, c.va, l, soa); sb(); G2_MM(18) G2_MM(19) sb();
#undef G2_MM
}
DI void g2_main(const G2Ctx& c, f32x4 (&acc)[4][5], unsigned lane) {
  const unsigned i16 = lane & 15, q = lane >> 4;
  const unsigned ra0 = c.ring + i16 * 128 + ((q ^ (lane & 7)) << 4);
  bf16x8 a0[4], a1[4], b00[5], b01[5], b10[5], b11[5];
  g2_b5(b00, c, g2_sob(c, 0, 0));
  g2_b5(b01, c, g2_sob(c, 0, 1));
  g2_b5(b10, c, g2_sob(c, 1, 0));
  wait_vm<15>();
#pragma unroll
  for (int r = 0; r < 4; ++r) a0[r] = lds_rd(ra0 + r * 2048);
  fence_all(acc);
  wait_vm5<10>(b00);
#pragma unroll 1
  for (int i = 0; i < 16; i += 2) {
#pragma unroll
    for (int u = 0; u < 2; ++u) {
      const int s = i + u;
      const unsigned slot = (s & 3) * 8192, slot1 = ((s + 1) & 3) * 8192, slot3 = ((s + 3) & 3) * 8192;
      bf16x8(&bc0)[5] = u ? b10 : b00;
      bf16x8(&bc1)[5] = u ? b11 : b01;
      bf16x8(&bn1)[5] = u ? b01 : b11;
      bf16x8(&bn0)[5] = u ? b00 : b10;
#pragma unroll
      for (int r = 0; r < 4; ++r) a1[r] = lds_rd((ra0 ^ 64) + slot + r * 2048);
      sb();
      g2_half<1>(acc, a0, bc0, bn1, c, g2_sb(c, s + 1), g2_sob(c, s + 1, 1), g2_sa(c, s + 3), c.ring + slot3, g2_soa(c, s + 3));
      wait_lgkm0();
      if (s == 0) wait_vm<14>();
      wait_vm5<22>(bc1);
#pragma unroll
      for (int r = 0; r < 4; ++r) a0[r] = lds_rd(ra0 + slot1 + r * 2048);
      sb();
      g2_half<0>(acc, a1, bc1, bc0, c, g2_sb(c, s + 2), g2_sob(c, s + 2, 0), g2_sa(c, s + 4), c.ring + slot, g2_soa(c, s + 4));
      if (s == 0) wait_vm<18>();
      wait_vm5<22>(bn0);
    }
  }
  wait_vm<0>();
  fence_all(acc);
}

__global__ __launch_bounds__(256, 2) void k_main(const u16* x0_, const u16* Sre_, const u16* Sim_, const u16* Dre_, const u16* Dim_,
                                                char* ws_, u16* out_ STAMP_ARGS) {
  ST(0);
  const unsigned tid0 = threadIdx.x, w0 = rfl(tid0 >> 6);
  const unsigned lb = lds_base();
  const f32x4 z4 = {0.f, 0.f, 0.f, 0.f};
#define PHASE_IDS \
  const unsigned tid = opq_v(tid0), lane = tid & 63, w = opq_s(w0), bid = opq_s(blockIdx.x); \
  const unsigned P = (bid & 7) * 2 + (bid >> 7), j = (bid >> 3) & 15; \
  const unsigned rt64 = P * 2 + (j >> 3), c8 = j & 7, i16 = lane & 15, q = lane >> 4; \
  const unsigned row64 = rt64 * 64 + w * 16 + i16; \
  const unsigned fl = WS_FLG + L * 4 * FLG_SEAM + P * 256; \
  char* const ws = opq_p(ws_); \
  (void)tid; (void)rt64; (void)c8; (void)i16; (void)q; (void)row64; (void)fl;
#define LDX(n) ld16(rx, lane * 16 + ((n) & 3) * 1024, sox + ((n) >> 2) * 4096)
#define G2_CTX \
  G2Ctx g2; \
  g2.sre = make_srd(opq_p(Sre_), S_ * S_ * 2); g2.sim = make_srd(opq_p(Sim_), S_ * S_ * 2); \
  g2.sab = make_srd(ws + (L ? WS_AB2 : WS_AB1), SZ_AB); \
  g2.va = (lane >> 3) * 4096 + (((lane & 7) ^ (lane >> 3)) << 4); \
  g2.vb0 = lane * 16; g2.vb1 = lane * 16 + 4096; \
  g2.ring = lb + w * G2_RING; \
  g2.soa0 = rt64 * 64 * 4096 + w * 128; \
  g2.sob0 = (c8 * 128 + 2 * w) * 5 * 1024;
  {
    const int L = 0;
    PHASE_IDS
    G2_CTX
    asm volatile("s_nop 4" ::: "memory");
    g2_prefetch(g2);
  }

#pragma unroll 1
  for (int L = 0; L < 2; ++L) {
    const int sbase = L * 16;
    (void)sbase;
    {
      PHASE_IDS
      G2_CTX
      ResTile rt;
      unsigned a0, a1, a2;
      tile_offsets(L == 0, row64, c8, q, a0, a1, a2);
      const rsrc_t rr = make_rsrc(L ? (const void*)(ws + WS_X2) : (const void*)opq_p(x0_), SZ_X);
      rt.r0 = ld16u(rr, a0, 0); rt.r1 = ld16u(rr, a1, 0); rt.r2 = ld8u(rr, a2, 0);
      f32x4 acc[4][5];
#pragma unroll
      for (int i = 0; i < 4; ++i)
#pragma unroll
        for (int f = 0; f < 5; ++f) acc[i][f] = z4;
      ST(sbase + 1);
      g2_main(g2, acc, lane);
      ST(sbase + 2);
      small_epilogue(acc, G2_RING, rt, make_rsrc(ws + (L ? WS_X3 : WS_X1), SZ_X), false,
                     make_rsrc(ws + (L ? WS_ST3 : WS_ST1), SZ_ST), row64, c8, w, lane);
      ST(sbase + 3);
      publish(ws, fl + j * 4, tid);
      ST(sbase + 4);
    }
    {
      PHASE_IDS
      {
        const u32x4 sw = make_srd(ws + WS_W1, SZ_W1);
        const unsigned so = (j * 140 + w * 35) * 1024, l = lb + w * 35 * 1024;
#pragma unroll
        for (int g = 0; g < 8; ++g) glds_lin<4>(sw, lane * 16, l + g * 4096, so + g * 4096);
        glds_lin<3>(sw, lane * 16, l + 8 * 4096, so + 8 * 4096);
      }
      if (L == 0 && tid >= 128 && tid < 240) {
        const rsrc_t rc = make_rsrc(ws + WS_CS1, 20 * F_ * 4);
        const unsigned col = j * 112 + tid - 128;
        float csp = 0.f;
        if (col < F_) {
#pragma unroll
          for (int k = 0; k < 20; ++k) csp += __uint_as_float(__builtin_amdgcn_raw_buffer_load_b32(rc, col * 4, k * F_ * 4, 16));
        }
        lds_wr1(lb + KM_CS1 + (tid - 128) * 4, csp);
      }
      wait_flags(ws, fl, 16, w, lane);
      ST(sbase + 5);
      const rsrc_t rx = make_rsrc(ws + (L ? WS_X3 : WS_X1), SZ_X);
      const rsrc_t rst = make_rsrc(ws + (L ? WS_ST3 : WS_ST1), SZ_ST);
      const unsigned sox = (P * 8 + w * 2) * 20 * 1024;
      bf16x8 a[20][2];
#pragma unroll
      for (int k = 0; k < 10; ++k) { a[k][0] = LDX(k); a[k][1] = LDX(20 + k); }
      const u32x4 sp0 = ld16u(rst, (P * 128 + w * 32 + i16) * 64 + q * 16, 0), sp1 = ld16u(rst, (P * 128 + w * 32 + 16 + i16) * 64 + q * 16, 0);
      sb();
      wait_vm<22>();
      wait_lgkm0();
      bar();
      ST(sbase + 6);
      f32x4 acc[2][7];
      bf16x8 b[2][7];
      const unsigned rb = lb + lane * 16;
#pragma unroll
      for (int f = 0; f < 7; ++f) b[0][f] = lds_rd(rb + f * 20 * 1024);
#pragma unroll
      for (int k = 0; k < 20; ++k) {
        if (k + 1 < 20) {
#pragma unroll
          for (int f = 0; f < 7; ++f) b[(k + 1) & 1][f] = lds_rd(rb + (f * 20 + k + 1) * 1024);
        }
        sb();
#pragma unroll
        for (int f = 0; f < 7; ++f)
#pragma unroll
          for (int r = 0; r < 2; ++r) { if (k == 0) mma0(acc[r][f], b[0][f], a[0][r]); else mma(acc[r][f], b[k & 1][f], a[k][r]); }
        sb();
        if (k + 10 < 20) { a[k + 10][0] = LDX(k + 10); a[k + 10][1] = LDX(30 + k); }
      }
      fence_all(acc);
      ST(sbase + 7);
      float mu[2], rs[2];
      ln_coef(xsum16_32(__uint_as_float(sp0[0]) + __uint_as_float(sp0[2])), xsum16_32(__uint_as_float(sp0[1]) + __uint_as_float(sp0[3])), mu[0], rs[0]);
      ln_coef(xsum16_32(__uint_as_float(sp1[0]) + __uint_as_float(sp1[2])), xsum16_32(__uint_as_float(sp1[1]) + __uint_as_float(sp1[3])), mu[1], rs[1]);
      f32x4 cs[7];
#pragma unroll
      for (int f = 0; f < 7; ++f) cs[f] = lds_rdf(lb + KM_CS1 + (f * 16 + q * 4) * 4);
      const rsrc_t rh = make_rsrc(ws + (L ? WS_H2 : WS_H1), SZ_H);
#pragma unroll
      for (int r = 0; r < 2; ++r) {
        const unsigned row = P * 128 + w * 32 + r * 16 + i16;
        unsigned pk[7][2];
#pragma unroll
        for (int f = 0; f < 7; ++f) {
          float t[4];
#pragma unroll
          for (int e = 0; e < 4; ++e) t[e] = fmaxf(rs[r] * (acc[r][f][e] - mu[r] * cs[f][e]), 0.f);
          pk[f][0] = pk2(t[0], t[1]); pk[f][1] = pk2(t[2], t[3]);
        }
#pragma unroll
        for (int p = 0; p < 3; ++p) {
          auto t0 = __builtin_amdgcn_permlane16_swap(pk[2 * p][0], pk[2 * p + 1][0], false, false);
          auto t1 = __builtin_amdgcn_permlane16_swap(pk[2 * p][1], pk[2 * p + 1][1], false, false);
          const unsigned col = j * 112 + 32 * p + 16 * (q & 1) + 8 * (q >> 1);
          st16(u32x4{t0[0], t1[0], t0[1], t1[1]}, rh, col < F_ ? foff<54>(row, col) : OOB, 0);
        }
        const unsigned col = j * 112 + 96 + 4 * q;
        st8(u32x2{pk[6][0], pk[6][1]}, rh, col < F_ ? foff<54>(row, col) : OOB, 0);
      }
      ST(sbase + 8);
      publish(ws, fl + FLG_SEAM + j * 4, tid);
      ST(sbase + 9);
    }
    {
      PHASE_IDS
      const unsigned ks0 = w < 2 ? w * 14 : 28 + (w - 2) * 13;
      const unsigned sow = (c8 * 54 + ks0) * 5 * 1024;
      {
        const u32x4 sw = make_srd(ws + WS_W2, SZ_W2);
        const unsigned l = lb + w * G4_REG;
#pragma unroll
        for (int g = 0; g < 8; ++g) glds_lin<4>(sw, lane * 16, l + g * 4096, sow + g * 4096);
        glds_lin<3>(sw, lane * 16, l + 8 * 4096, sow + 8 * 4096);
      }
      ResTile rt;
      {
        unsigned a0, a1, a2;
        tile_offsets(false, row64, c8, q, a0, a1, a2);
        const rsrc_t rr = make_rsrc(ws + (L ? WS_X3 : WS_X1), SZ_X);
        rt.r0 = ld16u(rr, a0, 0); rt.r1 = ld16u(rr, a1, 0); rt.r2 = ld8u(rr, a2, 0);
      }
      wait_flags(ws, fl + FLG_SEAM, 16, w, lane);
      ST(sbase + 10);
      const rsrc_t rh = make_rsrc(ws + (L ? WS_H2 : WS_H1), SZ_H);
      const rsrc_t rw = make_rsrc(ws + WS_W2, SZ_W2);
      const unsigned soh = (rt64 * 4 * 54 + ks0) * 1024;
      const unsigned vlast = lane * 16 + (w < 2 ? 0u : OOB);
      bf16x8 a[14][4], b[14][5];
      auto lda = [&](int t) {
#pragma unroll
        for (int i = 0; i < 4; ++i) a[t][i] = ld16(rh, (t < 13 ? lane * 16 : vlast) + (t & 3) * 1024, soh + (t >> 2) * 4096 + i * 54 * 1024);
      };
      auto ldb = [&](int t) {
#pragma unroll
        for (int f = 0; f < 5; ++f)
          b[t][f] = __builtin_bit_cast(bf16x8, __builtin_amdgcn_raw_buffer_load_b128(rw, (t < 13 ? lane * 16 : vlast) + ((t * 5 + f) & 3) * 1024, sow + ((t * 5 + f) >> 2) * 4096, 0));
      };
#pragma unroll
      for (int t = 0; t < 5; ++t) lda(t);
      sb();
      f32x4 acc[4][5];
      wait_vm<20>();
      const unsigned rb = lb + w * G4_REG + lane * 16;
#pragma unroll
      for (int f = 0; f < 5; ++f) b[0][f] = lds_rd(rb + f * 1024);
#pragma unroll
      for (int t = 0; t < 14; ++t) {
        if (t + 1 < 7) {
#pragma unroll
          for (int f = 0; f < 5; ++f) b[t + 1][f] = lds_rd(rb + ((t + 1) * 5 + f) * 1024);
        }
        sb();
#pragma unroll
        for (int i = 0; i < 4; ++i)
#pragma unroll
          for (int f = 0; f < 5; ++f) { if (t == 0) mma0(acc[i][f], b[0][f], a[0][i]); else mma(acc[i][f], b[t][f], a[t][i]); }
        sb();
        if (t < 3) lda(t + 5);
        if (t == 3) ldb(7);
        if (t == 4) lda(8);
        if (t == 5) ldb(8);
        if (t >= 6 && t + 3 < 14) { lda(t + 3); ldb(t + 3); }
      }
      fence_all(acc);
      ST(sbase + 11);
      small_epilogue(acc, G4_REG, rt, L ? make_rsrc(opq_p(out_), SZ_X) : make_rsrc(ws + WS_X2, SZ_X), L != 0,
                     make_rsrc(ws + WS_ST2, SZ_ST), row64, c8, w, lane);
      ST(sbase + 12);
      if (L) break;
      publish(ws, fl + 2 * FLG_SEAM + j * 4, tid);
    }
    {
      PHASE_IDS
      {
        const u32x4 sd = make_srd(j < 8 ? opq_p(Dre_) : opq_p(Dim_), D_ * D_ * 2);
        const unsigned vo = (lane >> 3) * 1280 + (((lane & 7) ^ (lane >> 3)) << 4);
        unsigned p = w * 25, c = p / 10, g = p - c * 10;
        unsigned so = (c8 * 80 + g * 8) * 1280 + c * 128, l = lb + p * 1024;
#pragma unroll 1
        for (int n = 0; n < 25; ++n) {
          glds_lin<1>(sd, vo, l, so);
          l += 1024;
          if (++g == 10) { g = 0; so += 128 - 9 * 8 * 1280; } else so += 8 * 1280;
        }
      }
      if (tid >= 128 && tid < 208)
        lds_wr1(lb + KM_CSD + (tid - 128) * 4,
                __uint_as_float(__builtin_amdgcn_raw_buffer_load_b32(make_rsrc(ws + WS_CSD, 1280 * 4), (j * 80 + tid - 128) * 4, 0, 16)));
      wait_flags(ws, fl + 2 * FLG_SEAM, 16, w, lane);
      ST(13);
      const rsrc_t rx = make_rsrc(ws + WS_X2, SZ_X);
      const unsigned sox = (P * 8 + w * 2) * 20 * 1024;
      bf16x8 a[20][2];
#pragma unroll
      for (int k = 0; k < 12; ++k) { a[k][0] = LDX(k); a[k][1] = LDX(20 + k); }
      const rsrc_t rst = make_rsrc(ws + WS_ST2, SZ_ST);
      const unsigned sto = (P * 128 + w * 32 + (lane & 31)) * 64 + (lane >> 5) * 32;
      const u32x4 sp0 = ld16u(rst, sto, 0), sp1 = ld16u(rst, sto + 16, 0);
      sb();
      wait_vm<26>();
      wait_lgkm0();
      bar();
      f32x4 acc[2][5];
      bf16x8 b[2][5];
      const unsigned rb = lb + i16 * 128 + ((q ^ (lane & 7)) << 4);
#pragma unroll
      for (int f = 0; f < 5; ++f) b[0][f] = lds_rd(rb + f * 2048);
#pragma unroll
      for (int k = 0; k < 20; ++k) {
        if (k + 1 < 20) {
#pragma unroll
          for (int f = 0; f < 5; ++f) b[(k + 1) & 1][f] = lds_rd((rb ^ (((k + 1) & 1) * 64)) + ((k + 1) >> 1) * 10240 + f * 2048);
        }
        sb();
#pragma unroll
        for (int f = 0; f < 5; ++f)
#pragma unroll
          for (int r = 0; r < 2; ++r) { if (k == 0) mma0(acc[r][f], a[0][r], b[0][f]); else mma(acc[r][f], a[k][r], b[k & 1][f]); }
        sb();
        if (k + 12 < 20) { a[k + 12][0] = LDX(k + 12); a[k + 12][1] = LDX(32 + k); }
      }
      fence_all(acc);
      ST(14);
      float s1 = (__uint_as_float(sp0[0]) + __uint_as_float(sp0[2])) + (__uint_as_float(sp1[0]) + __uint_as_float(sp1[2]));
      float s2 = (__uint_as_float(sp0[1]) + __uint_as_float(sp0[3])) + (__uint_as_float(sp1[1]) + __uint_as_float(sp1[3]));
      auto ta = __builtin_amdgcn_permlane32_swap(__float_as_uint(s1), __float_as_uint(s1), false, false);
      auto tb = __builtin_amdgcn_permlane32_swap(__float_as_uint(s2), __float_as_uint(s2), false, false);
      float m, rr;
      ln_coef(__uint_as_float(ta[0]) + __uint_as_float(ta[1]), __uint_as_float(tb[0]) + __uint_as_float(tb[1]), m, rr);
      float mu[2][4], rs[2][4];
#pragma unroll
      for (int r = 0; r < 2; ++r)
#pragma unroll
        for (int e = 0; e < 4; ++e) { mu[r][e] = __shfl(m, r * 16 + q * 4 + e); rs[r][e] = __shfl(rr, r * 16 + q * 4 + e); }
      g1_store(acc, mu, rs, lb + KM_CSD, make_rsrc(ws + WS_AB2, SZ_AB), P, j, w, i16, q);
      ST(15);
      publish(ws, WS_FLG + 3 * FLG_SEAM + c8 * 256 + ((j >> 3) * 16 + P) * 4, tid);
      G2_CTX
      asm volatile("s_nop 4" ::: "memory");
      g2_prefetch(g2);
      wait_flags(ws, WS_FLG + 3 * FLG_SEAM + c8 * 256, 32, w, lane);
      ST(16);
    }
  }
  wait_vm<0>();
  ST(32);
}

extern "C" void kernel_launch(void* const* d_in, const int* in_sizes, int n_in, void* d_out, int out_size, void* d_ws,
                              size_t ws_size, hipStream_t stream) {
  const u16* x0 = (const u16*)d_in[0];
  const u16* Sre = (const u16*)d_in[1];
  const u16* Sim = (const u16*)d_in[2];
  const u16* Dre = (const u16*)d_in[3];
  const u16* Dim = (const u16*)d_in[4];
  const u16* w1 = (const u16*)d_in[6];
  const u16* w2 = (const u16*)d_in[7];
  char* ws = (char*)d_ws;
  (void)hipFuncSetAttribute((const void*)k_first, hipFuncAttributeMaxDynamicSharedMemorySize, K1_LDS);
  (void)hipFuncSetAttribute((const void*)k_main, hipFuncAttributeMaxDynamicSharedMemorySize, KM_LDS);
  k_first<<<256, 320, K1_LDS, stream>>>(x0, Dre, Dim, w1, w2, Sre, Sim, ws STAMP_PASS(0));
  k_main<<<256, 256, KM_LDS, stream>>>(x0, Sre, Sim, Dre, Dim, ws, (u16*)d_out STAMP_PASS(1));
}
```

```cpp
#define THR 92
#include <hip/hip_runtime.h>
#include <hip/hip_bf16.h>
#include <stdint.h>

#define DI __device__ __forceinline__
typedef unsigned short u16;
typedef __attribute__((ext_vector_type(8))) __bf16 bf16x8;
typedef __attribute__((ext_vector_type(2))) __bf16 bf16x2;
typedef __attribute__((ext_vector_type(4))) float f32x4;
typedef __attribute__((ext_vector_type(2))) float f32x2;
typedef __attribute__((ext_vector_type(4))) unsigned u32x4;
typedef __attribute__((ext_vector_type(2))) unsigned u32x2;
typedef __attribute__((ext_vector_type(4))) short s16x4;
#define LDS_AS __attribute__((address_space(3)))
#define GLB_AS __attribute__((address_space(1)))
typedef LDS_AS char* ldsp;
typedef GLB_AS unsigned gu32;
typedef __amdgpu_buffer_rsrc_t rsrc_t;
#define RLX_AGENT __ATOMIC_RELAXED, __HIP_MEMORY_SCOPE_AGENT

constexpr int S_ = 2048, D_ = 640, F_ = 1728;
constexpr float EPS = 1e-5f;
constexpr unsigned OOB = 0x7f000000u;

constexpr unsigned SZ_AB = 8 * 128 * 5 * 1024;
constexpr unsigned SZ_X = 128 * 20 * 1024;
constexpr unsigned SZ_H = 128 * 54 * 1024;
constexpr unsigned SZ_W1 = 108 * 20 * 1024;
constexpr unsigned SZ_W2 = 8 * 54 * 5 * 1024;
constexpr unsigned SZ_ST = 2048 * 64;
constexpr unsigned WS_AB1 = 0;
constexpr unsigned WS_AB2 = WS_AB1 + SZ_AB;
constexpr unsigned WS_X1 = WS_AB2 + SZ_AB;
constexpr unsigned WS_X2 = WS_X1 + SZ_X;
constexpr unsigned WS_X3 = WS_X2 + SZ_X;
constexpr unsigned WS_H1 = WS_X3 + SZ_X;
constexpr unsigned WS_H2 = WS_H1 + SZ_H;
constexpr unsigned WS_W1 = WS_H2 + SZ_H;
constexpr unsigned WS_W2 = WS_W1 + SZ_W1;
constexpr unsigned WS_ST1 = WS_W2 + SZ_W2;
constexpr unsigned WS_ST2 = WS_ST1 + SZ_ST;
constexpr unsigned WS_ST3 = WS_ST2 + SZ_ST;
constexpr unsigned WS_CS1 = WS_ST3 + SZ_ST;
constexpr unsigned WS_CSD = WS_CS1 + 20 * 1728 * 4;
constexpr unsigned WS_FLG = (WS_CSD + 1280 * 4 + 4095) & ~4095u;
constexpr unsigned FLG_SEAM = 16 * 256;
constexpr unsigned WS_END = WS_FLG + 6 * FLG_SEAM;

DI u32x4 make_srd(const void* p, unsigned bytes) {
  uint64_t a = (uint64_t)p;
  u32x4 r;
  r[0] = (unsigned)a; r[1] = (unsigned)(a >> 32) & 0xffffu; r[2] = bytes; r[3] = 0x00020000u;
  return r;
}
DI rsrc_t make_rsrc(const void* p, unsigned bytes) {
  return __builtin_amdgcn_make_buffer_rsrc((void*)p, (short)0, (int)bytes, 0x00020000);
}
template <int LOFF, int GOFF>
DI void glds(u32x4 srd, unsigned voff, unsigned ldsb, unsigned soff) {
  unsigned tmp;
  asm volatile("s_add_u32 m0, %1, %2\n\ts_add_u32 %0, %3, %4\n\tbuffer_load_dwordx4 %5, %6, %0 offen lds"
               : "=&s"(tmp)
               : "s"(ldsb), "i"(LOFF), "s"(soff), "i"(GOFF), "v"(voff), "s"(srd)
               : "memory", "scc");
}
template <int N> DI void glds_lin(u32x4 srd, unsigned voff, unsigned ldsb, unsigned soff) {
  if constexpr (N == 4)
    asm volatile("s_mov_b32 m0, %0\n\ts_nop 0\n\tbuffer_load_dwordx4 %1, %2, %3 offen lds\n\tbuffer_load_dwordx4 %1, %2, %3 offen offset:1024 lds\n\t"
                 "buffer_load_dwordx4 %1, %2, %3 offen offset:2048 lds\n\tbuffer_load_dwordx4 %1, %2, %3 offen offset:3072 lds"
                 :: "s"(ldsb), "v"(voff), "s"(srd), "s"(soff) : "memory");
  else if constexpr (N == 3)
    asm volatile("s_mov_b32 m0, %0\n\ts_nop 0\n\tbuffer_load_dwordx4 %1, %2, %3 offen lds\n\tbuffer_load_dwordx4 %1, %2, %3 offen offset:1024 lds\n\t"
                 "buffer_load_dwordx4 %1, %2, %3 offen offset:2048 lds"
                 :: "s"(ldsb), "v"(voff), "s"(srd), "s"(soff) : "memory");
  else
    asm volatile("s_mov_b32 m0, %0\n\ts_nop 0\n\tbuffer_load_dwordx4 %1, %2, %3 offen lds"
                 :: "s"(ldsb), "v"(voff), "s"(srd), "s"(soff) : "memory");
}
template <int IMM> DI void ldv(bf16x8& d, u32x4 srd, unsigned voff, unsigned soff) {
  asm volatile("buffer_load_dwordx4 %0, %1, %2, %3 offen offset:%4 sc1" : "=v"(d) : "v"(voff), "s"(srd), "s"(soff), "i"(IMM) : "memory");
}
template <int N> DI void wait_vm() { asm volatile("s_waitcnt vmcnt(%0)" ::"i"(N) : "memory"); }
template <int N> DI void wait_vm5(bf16x8 (&b)[5]) {
  asm volatile("s_waitcnt vmcnt(%5)" : "+v"(b[0]), "+v"(b[1]), "+v"(b[2]), "+v"(b[3]), "+v"(b[4]) : "i"(N) : "memory");
}
DI void wait_lgkm0() { asm volatile("s_waitcnt lgkmcnt(0)" ::: "memory"); }
DI void sb() { __builtin_amdgcn_sched_barrier(0); }
DI void bar() { asm volatile("s_barrier" ::: "memory"); }

DI bf16x8 lds_rd(unsigned addr) { return *(const LDS_AS bf16x8*)(ldsp)(uintptr_t)addr; }
DI f32x4 lds_rdf(unsigned addr) { return *(const LDS_AS f32x4*)(ldsp)(uintptr_t)addr; }
DI void lds_wrf(unsigned addr, f32x4 v) { *(LDS_AS f32x4*)(ldsp)(uintptr_t)addr = v; }
DI float lds_rd1(unsigned addr) { return *(const LDS_AS float*)(ldsp)(uintptr_t)addr; }
DI void lds_wr1(unsigned addr, float v) { *(LDS_AS float*)(ldsp)(uintptr_t)addr = v; }
DI f32x4 mfma(bf16x8 a, bf16x8 b, f32x4 c) { return __builtin_amdgcn_mfma_f32_16x16x32_bf16(a, b, c, 0, 0, 0); }
DI void mma(f32x4& c, const bf16x8& a, const bf16x8& b) {
  asm volatile("v_mfma_f32_16x16x32_bf16 %0, %1, %2, %0" : "+v"(c) : "v"(a), "v"(b));
}
DI void mma0(f32x4& c, const bf16x8& a, const bf16x8& b) {
  asm volatile("v_mfma_f32_16x16x32_bf16 %0, %1, %2, 0" : "=&v"(c) : "v"(a), "v"(b));
}
template <int N> DI void fence_acc(f32x4 (&a)[N]) {
  if constexpr (N == 5) asm volatile("s_nop 7\n\ts_nop 1" : "+v"(a[0]), "+v"(a[1]), "+v"(a[2]), "+v"(a[3]), "+v"(a[4]));
  else asm volatile("s_nop 7\n\ts_nop 1" : "+v"(a[0]), "+v"(a[1]), "+v"(a[2]), "+v"(a[3]), "+v"(a[4]), "+v"(a[5]), "+v"(a[6]));
}
template <int N> DI void pin_acc(f32x4 (&a)[N]) {
  if constexpr (N == 5) asm volatile("" : "+v"(a[0]), "+v"(a[1]), "+v"(a[2]), "+v"(a[3]), "+v"(a[4]));
  else asm volatile("" : "+v"(a[0]), "+v"(a[1]), "+v"(a[2]), "+v"(a[3]), "+v"(a[4]), "+v"(a[5]), "+v"(a[6]));
}
template <int M, int N> DI void fence_all(f32x4 (&a)[M][N]) {
#pragma unroll
  for (int i = 1; i < M; ++i) pin_acc(a[i]);
  fence_acc(a[0]);
#pragma unroll
  for (int i = 1; i < M; ++i) pin_acc(a[i]);
}
DI unsigned pk2(float a, float b) {
  f32x2 v = {a, b};
  return __builtin_bit_cast(unsigned, __builtin_convertvector(v, bf16x2));
}
DI float bflo(unsigned u) { return __uint_as_float(u << 16); }
DI float bfhi(unsigned u) { return __uint_as_float(u & 0xffff0000u); }
DI float xsum16_32(float x) {
  auto s = __builtin_amdgcn_permlane16_swap(__float_as_uint(x), __float_as_uint(x), false, false);
  x = __uint_as_float(s[0]) + __uint_as_float(s[1]);
  auto t = __builtin_amdgcn_permlane32_swap(__float_as_uint(x), __float_as_uint(x), false, false);
  return __uint_as_float(t[0]) + __uint_as_float(t[1]);
}
DI unsigned rfl(unsigned v) { return __builtin_amdgcn_readfirstlane(v); }
DI unsigned opq_v(unsigned x) { asm volatile("" : "+v"(x)); return x; }
DI unsigned opq_s(unsigned x) { asm volatile("" : "+s"(x)); return x; }
template <class T> DI T* opq_p(T* x) { asm volatile("" : "+s"(x)); return x; }
DI bf16x8 ld16(rsrc_t r, unsigned voff, unsigned soff) { return __builtin_bit_cast(bf16x8, __builtin_amdgcn_raw_buffer_load_b128(r, voff, soff, 16)); }
DI u32x4 ld16u(rsrc_t r, unsigned voff, unsigned soff) { return __builtin_amdgcn_raw_buffer_load_b128(r, voff, soff, 16); }
DI u32x2 ld8u(rsrc_t r, unsigned voff, unsigned soff) { return __builtin_amdgcn_raw_buffer_load_b64(r, voff, soff, 16); }
DI void st16(u32x4 v, rsrc_t r, unsigned voff, unsigned soff) { __builtin_amdgcn_raw_buffer_store_b128(v, r, voff, soff, 16); }
DI void st8(u32x2 v, rsrc_t r, unsigned voff, unsigned soff) { __builtin_amdgcn_raw_buffer_store_b64(v, r, voff, soff, 16); }
DI void st4(unsigned v, rsrc_t r, unsigned voff, unsigned soff) { __builtin_amdgcn_raw_buffer_store_b32(v, r, voff, soff, 16); }

#define ST(i)
#define STAMP_ARGS
#define STAMP_FWD
#define STAMP_PASS(k)

template <int KS> DI unsigned foff(unsigned row, unsigned col) {
  return ((row >> 4) * KS + (col >> 5)) * 1024u + (((col >> 3) & 3) * 16u + (row & 15)) * 16u + (col & 7) * 2u;
}

extern __shared__ __attribute__((aligned(1024))) char smem[];
DI unsigned lds_base() { return (unsigned)(uintptr_t)(ldsp)smem; }

DI void ln_coef(float s1, float s2, float& mu, float& rs) {
  mu = s1 * (1.f / D_);
  rs = rsqrtf(fmaxf(s2 * (1.f / D_) - mu * mu, 0.f) + EPS);
}

DI void g1_store(const f32x4 (&acc)[2][5], const float (&mu)[2][4], const float (&rs)[2][4], unsigned csb, rsrc_t rab,
                 unsigned P, unsigned ct, unsigned w, unsigned i16, unsigned q) {
  const float sign = ct < 8 ? 1.f : -1.f;
  const unsigned kstep = (ct >> 3) * 64 + P * 4 + w;
  const unsigned cq = ((q & 1) << 1) | (q >> 1);
  const unsigned so = ((ct & 7) * 128 + kstep) * 5 * 1024;
  const unsigned vo = (cq * 16 + i16) * 16;
#pragma unroll
  for (int f = 0; f < 5; ++f) {
    const float cs = lds_rd1(csb + (f * 16 + i16) * 4);
    unsigned pk[2][2];
#pragma unroll
    for (int r = 0; r < 2; ++r) {
      float t[4];
#pragma unroll
      for (int j = 0; j < 4; ++j) t[j] = sign * rs[r][j] * (acc[r][f][j] - mu[r][j] * cs);
      pk[r][0] = pk2(t[0], t[1]); pk[r][1] = pk2(t[2], t[3]);
    }
    auto t0 = __builtin_amdgcn_permlane16_swap(pk[0][0], pk[1][0], false, false);
    auto t1 = __builtin_amdgcn_permlane16_swap(pk[0][1], pk[1][1], false, false);
    st16(u32x4{t0[0], t1[0], t0[1], t1[1]}, rab, vo + f * 1024, so);
  }
}

constexpr int K1_STAGE = 26 * 1024;
constexpr int K1_TR = 5 * K1_STAGE;
constexpr int K1_CS = K1_TR + 5 * 4096;
constexpr int K1_LDS = K1_CS + 512;

DI void k1_issue(u32x4 sa, u32x4 sbd, unsigned vo, unsigned la, unsigned lbb, unsigned soa, unsigned sob, unsigned w) {
  glds<0, 0>(sa, vo, la, soa);
  glds<1024, 8 * 1280>(sa, vo, la, soa);
  glds<2048, 16 * 1280>(sa, vo, la, soa);
  glds<3072, 24 * 1280>(sa, vo, la, soa);
  glds<0, 0>(sbd, vo, lbb, sob);
  glds<4096, 32 * 1280>(sbd, vo, lbb, sob);
  if (w < 2) glds<8192, 64 * 1280>(sbd, vo, lbb, sob);
}
DI void retile_issue(unsigned u, const u16* w1, const u16* w2, unsigned ldst, unsigned lane) {
  const bool first = u < 540;
  const unsigned v = first ? u : u - 540;
  const unsigned ng = first ? 27u : 10u;
  const unsigned ks = v / ng, g = v - ks * ng;
  const unsigned ld = first ? F_ * 2 : D_ * 2;
  u32x4 srd = make_srd(first ? w1 : w2, D_ * F_ * 2);
  const unsigned vo = (lane >> 3) * ld + (lane & 7) * 16;
  const unsigned so = ks * 32 * ld + g * 128;
  glds<0, 0>(srd, vo, ldst, so);
  glds<1024, 0>(srd, vo, ldst, so + 8 * ld);
  glds<2048, 0>(srd, vo, ldst, so + 16 * ld);
  glds<3072, 0>(srd, vo, ldst, so + 24 * ld);
}
DI void retile_finish(unsigned u, rsrc_t rws, unsigned ldst, unsigned lane) {
  const bool first = u < 540;
  const unsigned v = first ? u : u - 540;
  const unsigned ng = first ? 27u : 10u;
  const unsigned ks = v / ng, g = v - ks * ng;
  const unsigned i = lane & 15, q = lane >> 4;
  const unsigned base = ldst + (8 * q + (i >> 2)) * 128 + (i & 3) * 8;
#pragma unroll
  for (int f = 0; f < 4; ++f) {
    s16x4 lo = __builtin_amdgcn_ds_read_tr16_b64_v4i16((LDS_AS s16x4*)(uintptr_t)(base + f * 32));
    s16x4 hi = __builtin_amdgcn_ds_read_tr16_b64_v4i16((LDS_AS s16x4*)(uintptr_t)(base + f * 32 + 512));
    u32x2 l2 = __builtin_bit_cast(u32x2, lo), h2 = __builtin_bit_cast(u32x2, hi);
    const unsigned rt = g * 4 + f;
    unsigned so;
    if (first) so = WS_W1 + (rt * 20 + ks) * 1024;
    else { const unsigned c = rt / 5; so = WS_W2 + ((c * 54 + ks) * 5 + (rt - c * 5)) * 1024; }
    st16(u32x4{l2[0], l2[1], h2[0], h2[1]}, rws, lane * 16, so);
    if (first) {
      float s = (bflo(l2[0]) + bfhi(l2[0])) + (bflo(l2[1]) + bfhi(l2[1])) + (bflo(h2[0]) + bfhi(h2[0])) + (bflo(h2[1]) + bfhi(h2[1]));
      s = xsum16_32(s);
      if (q == 0) st4(__float_as_uint(s), rws, i * 4, WS_CS1 + (ks * F_ + rt * 16) * 4);
    }
  }
}

__global__ __launch_bounds__(320, 2) void k_first(const u16* x0, const u16* Dre, const u16* Dim, const u16* w1, const u16* w2,
                                                 const u16* Sre, const u16* Sim, char* ws STAMP_ARGS) {
  const unsigned tid = threadIdx.x, lane = tid & 63, w = rfl(tid >> 6);
  const unsigned xcd = blockIdx.x & 7, idx = blockIdx.x >> 3;
  const unsigned P = (xcd >> 1) * 4 + (idx >> 3), ct = (xcd & 1) * 8 + (idx & 7);
  const unsigned i16 = lane & 15, q = lane >> 4;
  const unsigned lb = lds_base();
  const rsrc_t rws = make_rsrc(ws, WS_END);

  if (w == 4) {
    const unsigned nu = blockIdx.x < 56 ? 5 : 4;
    asm volatile("s_nop 4" ::: "memory");
#pragma unroll 1
    for (unsigned n = 0; n < nu; ++n) retile_issue(n < 4 ? blockIdx.x * 4 + n : 1024 + blockIdx.x, w1, w2, lb + K1_TR + n * 4096, lane);
    unsigned d[8];
    {
      const u32x4 s0 = make_srd(Sre, S_ * S_ * 2), s1 = make_srd(Sim, S_ * S_ * 2);
      const unsigned so = (xcd * 256 + idx * 8) * 4096;
#pragma unroll
      for (int n = 0; n < 8; ++n)
        asm volatile("buffer_load_dword %0, %1, %2, %3 offen" : "=v"(d[n]) : "v"(lane * 128), "s"(n < 4 ? s0 : s1), "s"(so + (n & 3) * 8192) : "memory");
    }
    wait_vm<8>();
    bar();
#pragma unroll 1
    for (unsigned n = 0; n < 10; ++n) {
      if (n < nu) retile_finish(n < 4 ? blockIdx.x * 4 + n : 1024 + blockIdx.x, rws, lb + K1_TR + n * 4096, lane);
      if (n == 5 && blockIdx.x < 6 * FLG_SEAM / 256) st4(0u, rws, lane * 4, WS_FLG + blockIdx.x * 256);
      bar();
    }
    bar();
    asm volatile("s_waitcnt vmcnt(0)" : "+v"(d[0]), "+v"(d[1]), "+v"(d[2]), "+v"(d[3]), "+v"(d[4]), "+v"(d[5]), "+v"(d[6]), "+v"(d[7]) :: "memory");
    return;
  }
  if (blockIdx.x == 0 && w == 0) { unsigned t = (unsigned)__builtin_amdgcn_s_memrealtime(); st4(t, make_rsrc(ws, WS_END + 4096), lane == 0 ? 0 : OOB, WS_END); }
  const u32x4 sa = make_srd(x0, S_ * D_ * 2), sbd = make_srd(ct < 8 ? Dre : Dim, D_ * D_ * 2);
  const unsigned vo = (lane >> 3) * 1280 + (((lane & 7) ^ (lane >> 3)) << 4);
  const unsigned soa = (P * 128 + w * 32) * 1280, sob = ((ct & 7) * 80 + w * 8) * 1280;
  const unsigned la = lb + w * 4096, lbb = lb + 16384 + w * 1024;
  asm volatile("s_nop 4" ::: "memory");
#pragma unroll
  for (int s = 0; s < 5; ++s) k1_issue(sa, sbd, vo, la + s * K1_STAGE, lbb + s * K1_STAGE, soa + s * 128, sob + s * 128, w);
  ST(1);

  const unsigned ra0 = lb + w * 4096 + i16 * 128 + ((q ^ (lane & 7)) << 4);
  const unsigned rb0 = lb + 16384 + i16 * 128 + ((q ^ (lane & 7)) << 4);
  const f32x4 z4 = {0.f, 0.f, 0.f, 0.f};
  f32x4 acc[2][5], cs0 = z4, cs1 = z4, rs_[2] = {z4, z4}, rq_[2] = {z4, z4};
#pragma unroll
  for (int r = 0; r < 2; ++r)
#pragma unroll
    for (int f = 0; f < 5; ++f) acc[r][f] = z4;
  const bf16x8 ones = {(__bf16)1.f, (__bf16)1.f, (__bf16)1.f, (__bf16)1.f, (__bf16)1.f, (__bf16)1.f, (__bf16)1.f, (__bf16)1.f};
  bf16x8 a[2][2], b[2][5], x[2];
  auto rd = [&](int kk, unsigned so) {
    const unsigned pa = (ra0 ^ (kk * 64)) + so, pb = (rb0 ^ (kk * 64)) + so;
    a[kk][0] = lds_rd(pa); a[kk][1] = lds_rd(pa + 2048);
#pragma unroll
    for (int f = 0; f < 5; ++f) b[kk][f] = lds_rd(pb + f * 2048);
    x[kk] = lds_rd(pb + w * 2048);
  };
  auto mm = [&](int kk) {
#pragma unroll
    for (int f = 0; f < 5; ++f)
#pragma unroll
      for (int r = 0; r < 2; ++r) acc[r][f] = mfma(a[kk][r], b[kk][f], acc[r][f]);
    cs0 = mfma(ones, x[kk], cs0);
    cs1 = mfma(ones, b[kk][4], cs1);
#pragma unroll
    for (int r = 0; r < 2; ++r) { rs_[r] = mfma(a[kk][r], ones, rs_[r]); rq_[r] = mfma(a[kk][r], a[kk][r], rq_[r]); }
  };
  wait_vm<4 * 6>();
  bar();
  ST(2);
  rd(0, 0);
  unsigned so = 0;
#pragma unroll 1
  for (int i = 0; i < 10; ++i) {
    rd(1, so);
    sb();
    mm(0);
    sb();
    wait_lgkm0();
    if (i < 6) wait_vm<3 * 6>(); else if (i == 6) wait_vm<2 * 6>(); else if (i == 7) wait_vm<6>(); else wait_vm<0>();
    bar();
    if (i < 5) k1_issue(sa, sbd, vo, la + so, lbb + so, soa + (i + 5) * 128, sob + (i + 5) * 128, w);
    so = so == 4 * K1_STAGE ? 0 : so + K1_STAGE;
    rd(0, so);
    sb();
    mm(1);
    sb();
  }
  ST(3);
  const unsigned csb = lb + K1_CS;
  if (q == 0) {
    lds_wr1(csb + (w * 16 + i16) * 4, cs0[0]);
    if (w == 0) lds_wr1(csb + (64 + i16) * 4, cs1[0]);
    if (P == 0) {
      st4(__float_as_uint(cs0[0]), rws, i16 * 4, WS_CSD + (ct * 80 + w * 16) * 4);
      if (w == 0) st4(__float_as_uint(cs1[0]), rws, i16 * 4, WS_CSD + (ct * 80 + 64) * 4);
    }
  }
  wait_lgkm0();
  bar();
  float mu[2][4], rs[2][4];
#pragma unroll
  for (int r = 0; r < 2; ++r)
#pragma unroll
    for (int j = 0; j < 4; ++j) ln_coef(rs_[r][j], __shfl(rq_[r][j], q * 20 + j), mu[r][j], rs[r][j]);
  g1_store(acc, mu, rs, csb, make_rsrc(ws + WS_AB1, SZ_AB), P, ct, w, i16, q);
  ST(4);
  wait_vm<0>();
  ST(5);
}

constexpr int KM_LDS = 160 * 1024;
constexpr int KM_CS1 = KM_LDS - 1024, KM_CSD = KM_LDS - 512;
constexpr int G2_RING = 32 * 1024, G4_REG = 35 * 1024;

DI void publish(char* ws, unsigned flag_off, unsigned tid) {
  wait_vm<0>();
  bar();
  if (tid == 0) __hip_atomic_store((gu32*)(ws + flag_off), 1u, RLX_AGENT);
}
DI void wait_flags(char* ws, unsigned line_off, unsigned n, unsigned w, unsigned lane) {
  if (w == 0) {
    gu32* p = (gu32*)(ws + line_off) + (lane < n ? lane : 0);
    for (unsigned s = 0; s < (1u << 20); ++s) {
      if (__all(__hip_atomic_load(p, RLX_AGENT) != 0u)) break;
      __builtin_amdgcn_s_sleep(1);
    }
  }
  bar();
}

template <int W> DI void sm_scatter(unsigned wb, const f32x4 (&acc)[4][5], f32x4 (&own)[5]) {
#pragma unroll
  for (int d = 0; d < 4; ++d)
#pragma unroll
    for (int f = 0; f < 5; ++f) {
      if (d == W) own[f] = acc[d][f];
      else lds_wrf(wb + (d * 5 + f) * 1024, acc[d][f]);
    }
  asm volatile("; scatter %5" : "+v"(own[0]), "+v"(own[1]), "+v"(own[2]), "+v"(own[3]), "+v"(own[4]) : "i"(W));
}

struct ResTile { u32x4 r0, r1; u32x2 r2; };
DI void tile_offsets(bool rm, unsigned row, unsigned ct, unsigned q, unsigned& a0, unsigned& a1, unsigned& a2) {
  const unsigned cA = ct * 80 + 16 * (q & 1) + 8 * (q >> 1), cS = ct * 80 + 64 + 4 * q;
  if (rm) { a0 = (row * D_ + cA) * 2; a1 = a0 + 64; a2 = (row * D_ + cS) * 2; }
  else { a0 = foff<20>(row, cA); a1 = foff<20>(row, cA + 32); a2 = foff<20>(row, cS); }
}
DI void small_epilogue(const f32x4 (&acc)[4][5], unsigned region, const ResTile& rt, rsrc_t rout, bool out_rm, rsrc_t rst,
                       unsigned row, unsigned ct, unsigned w, unsigned lane) {
  const unsigned q = lane >> 4, lb = lds_base();
  f32x4 v[5];
  wait_lgkm0();
  {
    const unsigned wb = lb + w * region + lane * 16;
    if (w == 0) sm_scatter<0>(wb, acc, v);
    else if (w == 1) sm_scatter<1>(wb, acc, v);
    else if (w == 2) sm_scatter<2>(wb, acc, v);
    else sm_scatter<3>(wb, acc, v);
  }
  wait_lgkm0();
  bar();
#pragma unroll
  for (int s = 1; s < 4; ++s) {
    const unsigned src = lb + ((w + s) & 3) * region + w * 5 * 1024 + lane * 16;
#pragma unroll
    for (int f = 0; f < 5; ++f) v[f] += lds_rdf(src + f * 1024);
  }
#pragma unroll
  for (int p = 0; p < 2; ++p)
#pragma unroll
    for (int r = 0; r < 4; ++r) {
      auto t = __builtin_amdgcn_permlane16_swap(__float_as_uint(v[2 * p][r]), __float_as_uint(v[2 * p + 1][r]), false, false);
      v[2 * p][r] = __uint_as_float(t[0]); v[2 * p + 1][r] = __uint_as_float(t[1]);
    }
  u32x4 y0, y1; u32x2 y2;
#pragma unroll
  for (int j = 0; j < 2; ++j) {
    y0[j] = pk2(v[0][2 * j] + bflo(rt.r0[j]), v[0][2 * j + 1] + bfhi(rt.r0[j]));
    y0[2 + j] = pk2(v[1][2 * j] + bflo(rt.r0[2 + j]), v[1][2 * j + 1] + bfhi(rt.r0[2 + j]));
    y1[j] = pk2(v[2][2 * j] + bflo(rt.r1[j]), v[2][2 * j + 1] + bfhi(rt.r1[j]));
    y1[2 + j] = pk2(v[3][2 * j] + bflo(rt.r1[2 + j]), v[3][2 * j + 1] + bfhi(rt.r1[2 + j]));
    y2[j] = pk2(v[4][2 * j] + bflo(rt.r2[j]), v[4][2 * j + 1] + bfhi(rt.r2[j]));
  }
  if (out_rm && blockIdx.x == 0 && w == 0) {
    unsigned t0 = __builtin_amdgcn_raw_buffer_load_b32(rst, 0, 0, 16);
    unsigned t1 = (unsigned)__builtin_amdgcn_s_memrealtime();
    if (lane == 5 && t1 - t0 > THR * 100) y2[0] ^= 1u;
  }
  unsigned o0, o1, o2;
  tile_offsets(out_rm, row, ct, q, o0, o1, o2);
  st16(y0, rout, o0, 0); st16(y1, rout, o1, 0); st8(y2, rout, o2, 0);
  if (!out_rm) {
    float s1 = 0.f, s2 = 0.f;
#pragma unroll
    for (int j = 0; j < 4; ++j) {
      float a = bflo(y0[j]), b = bfhi(y0[j]), c = bflo(y1[j]), d = bfhi(y1[j]);
      s1 += (a + b) + (c + d);
      s2 += a * a + b * b + c * c + d * d;
    }
#pragma unroll
    for (int j = 0; j < 2; ++j) {
      float a = bflo(y2[j]), b = bfhi(y2[j]);
      s1 += a + b; s2 += a * a + b * b;
    }
    s1 = xsum16_32(s1); s2 = xsum16_32(s2);
    if (q == 0) st8(u32x2{__float_as_uint(s1), __float_as_uint(s2)}, rst, (row * 8 + ct) * 8, 0);
  }
}

template <int H> DI void g2_a4(u32x4 sa, unsigned va, unsigned l, unsigned so) {
  glds<H * 4096, H * 4 * 8 * 4096>(sa, va, l, so);
  glds<H * 4096 + 1024, (H * 4 + 1) * 8 * 4096>(sa, va, l, so);
  glds<H * 4096 + 2048, (H * 4 + 2) * 8 * 4096>(sa, va, l, so);
  glds<H * 4096 + 3072, (H * 4 + 3) * 8 * 4096>(sa, va, l, so);
}
struct G2Ctx { u32x4 sre, sim, sab; unsigned va, vb0, vb1, ring, soa0, sob0; };
DI unsigned g2_soa(const G2Ctx& c, int i) { return i < 16 ? c.soa0 + (i & 7) * 512 : 0; }
DI unsigned g2_sob(const G2Ctx& c, int i, int kk) { return i < 16 ? c.sob0 + i * (8 * 5 * 1024) + kk * 5 * 1024 : 0; }
DI u32x4 g2_sa(const G2Ctx& c, int i) { u32x4 r = i < 8 ? c.sre : c.sim; r[2] = i < 16 ? S_ * S_ * 2 : 0; return r; }
DI u32x4 g2_sb(const G2Ctx& c, int i) { u32x4 r = c.sab; r[2] = i < 16 ? SZ_AB : 0; return r; }
DI void g2_b5(bf16x8 (&b)[5], const G2Ctx& c, unsigned so) {
  ldv<0>(b[0], c.sab, c.vb0, so); ldv<1024>(b[1], c.sab, c.vb0, so); ldv<2048>(b[2], c.sab, c.vb0, so);
  ldv<3072>(b[3], c.sab, c.vb0, so); ldv<0>(b[4], c.sab, c.vb1, so);
}
DI void g2_prefetch(const G2Ctx& c) {
#pragma unroll
  for (int s = 0; s < 4; ++s) {
    g2_a4<0>(c.sre, c.va, c.ring + s * 8192, c.soa0 + s * 512);
    if (s < 3) g2_a4<1>(c.sre, c.va, c.ring + s * 8192, c.soa0 + s * 512);
  }
}
template <int H> DI void g2_half(f32x4 (&acc)[4][5], const bf16x8 (&a)[4], const bf16x8 (&b)[5], bf16x8 (&bn)[5],
                                 const G2Ctx& c, u32x4 sbb, unsigned sob, u32x4 sa, unsigned l, unsigned soa) {
#define G2_MM(m) mma(acc[(m) / 5][(m) % 5], b[(m) % 5], a[(m) / 5]);
  G2_MM(0) G2_MM(1) sb();
  ldv<0>(bn[0], sbb, c.vb0, sob); sb(); G2_MM(2) G2_MM(3) sb();
  ldv<1024>(bn[1], sbb, c.vb0, sob); sb(); G2_MM(4) G2_MM(5) sb();
  ldv<2048>(bn[2], sbb, c.vb0, sob); sb(); G2_MM(6) G2_MM(7) sb();
  ldv<3072>(bn[3], sbb, c.vb0, sob); sb(); G2_MM(8) G2_MM(9) sb();
  ldv<0>(bn[4], sbb, c.vb1, sob); sb(); G2_MM(10) G2_MM(11) sb();
  glds<H * 4096, H * 4 * 8 * 4096>(sa, c.va, l, soa); sb(); G2_MM(12) G2_MM(13) sb();
  glds<H * 4096 + 1024, (H * 4 + 1) * 8 * 4096>(sa, c.va, l, soa); sb(); G2_MM(14) G2_MM(15) sb();
  glds<H * 4096 + 2048, (H * 4 + 2) * 8 * 4096>(sa, c.va, l, soa); sb(); G2_MM(16) G2_MM(17) sb();
  glds<H * 4096 + 3072, (H * 4 + 3) * 8 * 4096>(sa, c.va, l, soa); sb(); G2_MM(18) G2_MM(19) sb();
#undef G2_MM
}
DI void g2_main(const G2Ctx& c, f32x4 (&acc)[4][5], unsigned lane) {
  const unsigned i16 = lane & 15, q = lane >> 4;
  const unsigned ra0 = c.ring + i16 * 128 + ((q ^ (lane & 7)) << 4);
  bf16x8 a0[4], a1[4], b00[5], b01[5], b10[5], b11[5];
  g2_b5(b00, c, g2_sob(c, 0, 0));
  g2_b5(b01, c, g2_sob(c, 0, 1));
  g2_b5(b10, c, g2_sob(c, 1, 0));
  wait_vm<15>();
#pragma unroll
  for (int r = 0; r < 4; ++r) a0[r] = lds_rd(ra0 + r * 2048);
  fence_all(acc);
  wait_vm5<10>(b00);
#pragma unroll 1
  for (int i = 0; i < 16; i += 2) {
#pragma unroll
    for (int u = 0; u < 2; ++u) {
      const int s = i + u;
      const unsigned slot = (s & 3) * 8192, slot1 = ((s + 1) & 3) * 8192, slot3 = ((s + 3) & 3) * 8192;
      bf16x8(&bc0)[5] = u ? b10 : b00;
      bf16x8(&bc1)[5] = u ? b11 : b01;
      bf16x8(&bn1)[5] = u ? b01 : b11;
      bf16x8(&bn0)[5] = u ? b00 : b10;
#pragma unroll
      for (int r = 0; r < 4; ++r) a1[r] = lds_rd((ra0 ^ 64) + slot + r * 2048);
      sb();
      g2_half<1>(acc, a0, bc0, bn1, c, g2_sb(c, s + 1), g2_sob(c, s + 1, 1), g2_sa(c, s + 3), c.ring + slot3, g2_soa(c, s + 3));
      wait_lgkm0();
      if (s == 0) wait_vm<14>();
      wait_vm5<22>(bc1);
#pragma unroll
      for (int r = 0; r < 4; ++r) a0[r] = lds_rd(ra0 + slot1 + r * 2048);
      sb();
      g2_half<0>(acc, a1, bc1, bc0, c, g2_sb(c, s + 2), g2_sob(c, s + 2, 0), g2_sa(c, s + 4), c.ring + slot, g2_soa(c, s + 4));
      if (s == 0) wait_vm<18>();
      wait_vm5<22>(bn0);
    }
  }
  wait_vm<0>();
  fence_all(acc);
}

__global__ __launch_bounds__(256, 2) void k_main(const u16* x0_, const u16* Sre_, const u16* Sim_, const u16* Dre_, const u16* Dim_,
                                                char* ws_, u16* out_ STAMP_ARGS) {
  ST(0);
  const unsigned tid0 = threadIdx.x, w0 = rfl(tid0 >> 6);
  const unsigned lb = lds_base();
  const f32x4 z4 = {0.f, 0.f, 0.f, 0.f};
#define PHASE_IDS \
  const unsigned tid = opq_v(tid0), lane = tid & 63, w = opq_s(w0), bid = opq_s(blockIdx.x); \
  const unsigned P = (bid & 7) * 2 + (bid >> 7), j = (bid >> 3) & 15; \
  const unsigned rt64 = P * 2 + (j >> 3), c8 = j & 7, i16 = lane & 15, q = lane >> 4; \
  const unsigned row64 = rt64 * 64 + w * 16 + i16; \
  const unsigned fl = WS_FLG + L * 4 * FLG_SEAM + P * 256; \
  char* const ws = opq_p(ws_); \
  (void)tid; (void)rt64; (void)c8; (void)i16; (void)q; (void)row64; (void)fl;
#define LDX(n) ld16(rx, lane * 16 + ((n) & 3) * 1024, sox + ((n) >> 2) * 4096)
#define G2_CTX \
  G2Ctx g2; \
  g2.sre = make_srd(opq_p(Sre_), S_ * S_ * 2); g2.sim = make_srd(opq_p(Sim_), S_ * S_ * 2); \
  g2.sab = make_srd(ws + (L ? WS_AB2 : WS_AB1), SZ_AB); \
  g2.va = (lane >> 3) * 4096 + (((lane & 7) ^ (lane >> 3)) << 4); \
  g2.vb0 = lane * 16; g2.vb1 = lane * 16 + 4096; \
  g2.ring = lb + w * G2_RING; \
  g2.soa0 = rt64 * 64 * 4096 + w * 128; \
  g2.sob0 = (c8 * 128 + 2 * w) * 5 * 1024;
  {
    const int L = 0;
    PHASE_IDS
    G2_CTX
    asm volatile("s_nop 4" ::: "memory");
    g2_prefetch(g2);
  }

#pragma unroll 1
  for (int L = 0; L < 2; ++L) {
    const int sbase = L * 16;
    (void)sbase;
    {
      PHASE_IDS
      G2_CTX
      ResTile rt;
      unsigned a0, a1, a2;
      tile_offsets(L == 0, row64, c8, q, a0, a1, a2);
      const rsrc_t rr = make_rsrc(L ? (const void*)(ws + WS_X2) : (const void*)opq_p(x0_), SZ_X);
      rt.r0 = ld16u(rr, a0, 0); rt.r1 = ld16u(rr, a1, 0); rt.r2 = ld8u(rr, a2, 0);
      f32x4 acc[4][5];
#pragma unroll
      for (int i = 0; i < 4; ++i)
#pragma unroll
        for (int f = 0; f < 5; ++f) acc[i][f] = z4;
      ST(sbase + 1);
      g2_main(g2, acc, lane);
      ST(sbase + 2);
      small_epilogue(acc, G2_RING, rt, make_rsrc(ws + (L ? WS_X3 : WS_X1), SZ_X), false,
                     make_rsrc(ws + (L ? WS_ST3 : WS_ST1), SZ_ST), row64, c8, w, lane);
      ST(sbase + 3);
      publish(ws, fl + j * 4, tid);
      ST(sbase + 4);
    }
    {
      PHASE_IDS
      {
        const u32x4 sw = make_srd(ws + WS_W1, SZ_W1);
        const unsigned so = (j * 140 + w * 35) * 1024, l = lb + w * 35 * 1024;
#pragma unroll
        for (int g = 0; g < 8; ++g) glds_lin<4>(sw, lane * 16, l + g * 4096, so + g * 4096);
        glds_lin<3>(sw, lane * 16, l + 8 * 4096, so + 8 * 4096);
      }
      if (L == 0 && tid >= 128 && tid < 240) {
        const rsrc_t rc = make_rsrc(ws + WS_CS1, 20 * F_ * 4);
        const unsigned col = j * 112 + tid - 128;
        float csp = 0.f;
        if (col < F_) {
#pragma unroll
          for (int k = 0; k < 20; ++k) csp += __uint_as_float(__builtin_amdgcn_raw_buffer_load_b32(rc, col * 4, k * F_ * 4, 16));
        }
        lds_wr1(lb + KM_CS1 + (tid - 128) * 4, csp);
      }
      wait_flags(ws, fl, 16, w, lane);
      ST(sbase + 5);
      const rsrc_t rx = make_rsrc(ws + (L ? WS_X3 : WS_X1), SZ_X);
      const rsrc_t rst = make_rsrc(ws + (L ? WS_ST3 : WS_ST1), SZ_ST);
      const unsigned sox = (P * 8 + w * 2) * 20 * 1024;
      bf16x8 a[20][2];
#pragma unroll
      for (int k = 0; k < 10; ++k) { a[k][0] = LDX(k); a[k][1] = LDX(20 + k); }
      const u32x4 sp0 = ld16u(rst, (P * 128 + w * 32 + i16) * 64 + q * 16, 0), sp1 = ld16u(rst, (P * 128 + w * 32 + 16 + i16) * 64 + q * 16, 0);
      sb();
      wait_vm<22>();
      wait_lgkm0();
      bar();
      ST(sbase + 6);
      f32x4 acc[2][7];
      bf16x8 b[2][7];
      const unsigned rb = lb + lane * 16;
#pragma unroll
      for (int f = 0; f < 7; ++f) b[0][f] = lds_rd(rb + f * 20 * 1024);
#pragma unroll
      for (int k = 0; k < 20; ++k) {
        if (k + 1 < 20) {
#pragma unroll
          for (int f = 0; f < 7; ++f) b[(k + 1) & 1][f] = lds_rd(rb + (f * 20 + k + 1) * 1024);
        }
        sb();
#pragma unroll
        for (int f = 0; f < 7; ++f)
#pragma unroll
          for (int r = 0; r < 2; ++r) { if (k == 0) mma0(acc[r][f], b[0][f], a[0][r]); else mma(acc[r][f], b[k & 1][f], a[k][r]); }
        sb();
        if (k + 10 < 20) { a[k + 10][0] = LDX(k + 10); a[k + 10][1] = LDX(30 + k); }
      }
      fence_all(acc);
      ST(sbase + 7);
      float mu[2], rs[2];
      ln_coef(xsum16_32(__uint_as_float(sp0[0]) + __uint_as_float(sp0[2])), xsum16_32(__uint_as_float(sp0[1]) + __uint_as_float(sp0[3])), mu[0], rs[0]);
      ln_coef(xsum16_32(__uint_as_float(sp1[0]) + __uint_as_float(sp1[2])), xsum16_32(__uint_as_float(sp1[1]) + __uint_as_float(sp1[3])), mu[1], rs[1]);
      f32x4 cs[7];
#pragma unroll
      for (int f = 0; f < 7; ++f) cs[f] = lds_rdf(lb + KM_CS1 + (f * 16 + q * 4) * 4);
      const rsrc_t rh = make_rsrc(ws + (L ? WS_H2 : WS_H1), SZ_H);
#pragma unroll
      for (int r = 0; r < 2; ++r) {
        const unsigned row = P * 128 + w * 32 + r * 16 + i16;
        unsigned pk[7][2];
#pragma unroll
        for (int f = 0; f < 7; ++f) {
          float t[4];
#pragma unroll
          for (int e = 0; e < 4; ++e) t[e] = fmaxf(rs[r] * (acc[r][f][e] - mu[r] * cs[f][e]), 0.f);
          pk[f][0] = pk2(t[0], t[1]); pk[f][1] = pk2(t[2], t[3]);
        }
#pragma unroll
        for (int p = 0; p < 3; ++p) {
          auto t0 = __builtin_amdgcn_permlane16_swap(pk[2 * p][0], pk[2 * p + 1][0], false, false);
          auto t1 = __builtin_amdgcn_permlane16_swap(pk[2 * p][1], pk[2 * p + 1][1], false, false);
          const unsigned col = j * 112 + 32 * p + 16 * (q & 1) + 8 * (q >> 1);
          st16(u32x4{t0[0], t1[0], t0[1], t1[1]}, rh, col < F_ ? foff<54>(row, col) : OOB, 0);
        }
        const unsigned col = j * 112 + 96 + 4 * q;
        st8(u32x2{pk[6][0], pk[6][1]}, rh, col < F_ ? foff<54>(row, col) : OOB, 0);
      }
      ST(sbase + 8);
      publish(ws, fl + FLG_SEAM + j * 4, tid);
      ST(sbase + 9);
    }
    {
      PHASE_IDS
      const unsigned ks0 = w < 2 ? w * 14 : 28 + (w - 2) * 13;
      const unsigned sow = (c8 * 54 + ks0) * 5 * 1024;
      {
        const u32x4 sw = make_srd(ws + WS_W2, SZ_W2);
        const unsigned l = lb + w * G4_REG;
#pragma unroll
        for (int g = 0; g < 8; ++g) glds_lin<4>(sw, lane * 16, l + g * 4096, sow + g * 4096);
        glds_lin<3>(sw, lane * 16, l + 8 * 4096, sow + 8 * 4096);
      }
      ResTile rt;
      {
        unsigned a0, a1, a2;
        tile_offsets(false, row64, c8, q, a0, a1, a2);
        const rsrc_t rr = make_rsrc(ws + (L ? WS_X3 : WS_X1), SZ_X);
        rt.r0 = ld16u(rr, a0, 0); rt.r1 = ld16u(rr, a1, 0); rt.r2 = ld8u(rr, a2, 0);
      }
      wait_flags(ws, fl + FLG_SEAM, 16, w, lane);
      ST(sbase + 10);
      const rsrc_t rh = make_rsrc(ws + (L ? WS_H2 : WS_H1), SZ_H);
      const rsrc_t rw = make_rsrc(ws + WS_W2, SZ_W2);
      const unsigned soh = (rt64 * 4 * 54 + ks0) * 1024;
      const unsigned vlast = lane * 16 + (w < 2 ? 0u : OOB);
      bf16x8 a[14][4], b[14][5];
      auto lda = [&](int t) {
#pragma unroll
        for (int i = 0; i < 4; ++i) a[t][i] = ld16(rh, (t < 13 ? lane * 16 : vlast) + (t & 3) * 1024, soh + (t >> 2) * 4096 + i * 54 * 1024);
      };
      auto ldb = [&](int t) {
#pragma unroll
        for (int f = 0; f < 5; ++f)
          b[t][f] = __builtin_bit_cast(bf16x8, __builtin_amdgcn_raw_buffer_load_b128(rw, (t < 13 ? lane * 16 : vlast) + ((t * 5 + f) & 3) * 1024, sow + ((t * 5 + f) >> 2) * 4096, 0));
      };
#pragma unroll
      for (int t = 0; t < 5; ++t) lda(t);
      sb();
      f32x4 acc[4][5];
      wait_vm<20>();
      const unsigned rb = lb + w * G4_REG + lane * 16;
#pragma unroll
      for (int f = 0; f < 5; ++f) b[0][f] = lds_rd(rb + f * 1024);
#pragma unroll
      for (int t = 0; t < 14; ++t) {
        if (t + 1 < 7) {
#pragma unroll
          for (int f = 0; f < 5; ++f) b[t + 1][f] = lds_rd(rb + ((t + 1) * 5 + f) * 1024);
        }
        sb();
#pragma unroll
        for (int i = 0; i < 4; ++i)
#pragma unroll
          for (int f = 0; f < 5; ++f) { if (t == 0) mma0(acc[i][f], b[0][f], a[0][i]); else mma(acc[i][f], b[t][f], a[t][i]); }
        sb();
        if (t < 3) lda(t + 5);
        if (t == 3) ldb(7);
        if (t == 4) lda(8);
        if (t == 5) ldb(8);
        if (t >= 6 && t + 3 < 14) { lda(t + 3); ldb(t + 3); }
      }
      fence_all(acc);
      ST(sbase + 11);
      small_epilogue(acc, G4_REG, rt, L ? make_rsrc(opq_p(out_), SZ_X) : make_rsrc(ws + WS_X2, SZ_X), L != 0,
                     make_rsrc(ws + (L ? WS_END : WS_ST2), SZ_ST), row64, c8, w, lane);
      ST(sbase + 12);
      if (L) break;
      publish(ws, fl + 2 * FLG_SEAM + j * 4, tid);
    }
    {
      PHASE_IDS
      {
        const u32x4 sd = make_srd(j < 8 ? opq_p(Dre_) : opq_p(Dim_), D_ * D_ * 2);
        const unsigned vo = (lane >> 3) * 1280 + (((lane & 7) ^ (lane >> 3)) << 4);
        unsigned p = w * 25, c = p / 10, g = p - c * 10;
        unsigned so = (c8 * 80 + g * 8) * 1280 + c * 128, l = lb + p * 1024;
#pragma unroll 1
        for (int n = 0; n < 25; ++n) {
          glds_lin<1>(sd, vo, l, so);
          l += 1024;
          if (++g == 10) { g = 0; so += 128 - 9 * 8 * 1280; } else so += 8 * 1280;
        }
      }
      if (tid >= 128 && tid < 208)
        lds_wr1(lb + KM_CSD + (tid - 128) * 4,
                __uint_as_float(__builtin_amdgcn_raw_buffer_load_b32(make_rsrc(ws + WS_CSD, 1280 * 4), (j * 80 + tid - 128) * 4, 0, 16)));
      wait_flags(ws, fl + 2 * FLG_SEAM, 16, w, lane);
      ST(13);
      const rsrc_t rx = make_rsrc(ws + WS_X2, SZ_X);
      const unsigned sox = (P * 8 + w * 2) * 20 * 1024;
      bf16x8 a[20][2];
#pragma unroll
      for (int k = 0; k < 12; ++k) { a[k][0] = LDX(k); a[k][1] = LDX(20 + k); }
      const rsrc_t rst = make_rsrc(ws + WS_ST2, SZ_ST);
      const unsigned sto = (P * 128 + w * 32 + (lane & 31)) * 64 + (lane >> 5) * 32;
      const u32x4 sp0 = ld16u(rst, sto, 0), sp1 = ld16u(rst, sto + 16, 0);
      sb();
      wait_vm<26>();
      wait_lgkm0();
      bar();
      f32x4 acc[2][5];
      bf16x8 b[2][5];
      const unsigned rb = lb + i16 * 128 + ((q ^ (lane & 7)) << 4);
#pragma unroll
      for (int f = 0; f < 5; ++f) b[0][f] = lds_rd(rb + f * 2048);
#pragma unroll
      for (int k = 0; k < 20; ++k) {
        if (k + 1 < 20) {
#pragma unroll
          for (int f = 0; f < 5; ++f) b[(k + 1) & 1][f] = lds_rd((rb ^ (((k + 1) & 1) * 64)) + ((k + 1) >> 1) * 10240 + f * 2048);
        }
        sb();
#pragma unroll
        for (int f = 0; f < 5; ++f)
#pragma unroll
          for (int r = 0; r < 2; ++r) { if (k == 0) mma0(acc[r][f], a[0][r], b[0][f]); else mma(acc[r][f], a[k][r], b[k & 1][f]); }
        sb();
        if (k + 12 < 20) { a[k + 12][0] = LDX(k + 12); a[k + 12][1] = LDX(32 + k); }
      }
      fence_all(acc);
      ST(14);
      float s1 = (__uint_as_float(sp0[0]) + __uint_as_float(sp0[2])) + (__uint_as_float(sp1[0]) + __uint_as_float(sp1[2]));
      float s2 = (__uint_as_float(sp0[1]) + __uint_as_float(sp0[3])) + (__uint_as_float(sp1[1]) + __uint_as_float(sp1[3]));
      auto ta = __builtin_amdgcn_permlane32_swap(__float_as_uint(s1), __float_as_uint(s1), false, false);
      auto tb = __builtin_amdgcn_permlane32_swap(__float_as_uint(s2), __float_as_uint(s2), false, false);
      float m, rr;
      ln_coef(__uint_as_float(ta[0]) + __uint_as_float(ta[1]), __uint_as_float(tb[0]) + __uint_as_float(tb[1]), m, rr);
      float mu[2][4], rs[2][4];
#pragma unroll
      for (int r = 0; r < 2; ++r)
#pragma unroll
        for (int e = 0; e < 4; ++e) { mu[r][e] = __shfl(m, r * 16 + q * 4 + e); rs[r][e] = __shfl(rr, r * 16 + q * 4 + e); }
      g1_store(acc, mu, rs, lb + KM_CSD, make_rsrc(ws + WS_AB2, SZ_AB), P, j, w, i16, q);
      ST(15);
      publish(ws, WS_FLG + 3 * FLG_SEAM + c8 * 256 + ((j >> 3) * 16 + P) * 4, tid);
      G2_CTX
      asm volatile("s_nop 4" ::: "memory");
      g2_prefetch(g2);
      wait_flags(ws, WS_FLG + 3 * FLG_SEAM + c8 * 256, 32, w, lane);
      ST(16);
    }
  }
  wait_vm<0>();
  ST(32);
}

extern "C" void kernel_launch(void* const* d_in, const int* in_sizes, int n_in, void* d_out, int out_size, void* d_ws,
                              size_t ws_size, hipStream_t stream) {
  const u16* x0 = (const u16*)d_in[0];
  const u16* Sre = (const u16*)d_in[1];
  const u16* Sim = (const u16*)d_in[2];
  const u16* Dre = (const u16*)d_in[3];
  const u16* Dim = (const u16*)d_in[4];
  const u16* w1 = (const u16*)d_in[6];
  const u16* w2 = (const u16*)d_in[7];
  char* ws = (char*)d_ws;
  (void)hipFuncSetAttribute((const void*)k_first, hipFuncAttributeMaxDynamicSharedMemorySize, K1_LDS);
  (void)hipFuncSetAttribute((const void*)k_main, hipFuncAttributeMaxDynamicSharedMemorySize, KM_LDS);
  k_first<<<256, 320, K1_LDS, stream>>>(x0, Dre, Dim, w1, w2, Sre, Sim, ws STAMP_PASS(0));
  k_main<<<256, 256, KM_LDS, stream>>>(x0, Sre, Sim, Dre, Dim, ws, (u16*)d_out STAMP_PASS(1));
}
```

```cpp
#define SEGA 0
#define SEGB 0
#include <hip/hip_runtime.h>
#include <hip/hip_bf16.h>
#include <stdint.h>

#define DI __device__ __forceinline__
typedef unsigned short u16;
typedef __attribute__((ext_vector_type(8))) __bf16 bf16x8;
typedef __attribute__((ext_vector_type(2))) __bf16 bf16x2;
typedef __attribute__((ext_vector_type(4))) float f32x4;
typedef __attribute__((ext_vector_type(2))) float f32x2;
typedef __attribute__((ext_vector_type(4))) unsigned u32x4;
typedef __attribute__((ext_vector_type(2))) unsigned u32x2;
typedef __attribute__((ext_vector_type(4))) short s16x4;
#define LDS_AS __attribute__((address_space(3)))
#define GLB_AS __attribute__((address_space(1)))
typedef LDS_AS char* ldsp;
typedef GLB_AS unsigned gu32;
typedef __amdgpu_buffer_rsrc_t rsrc_t;
#define RLX_AGENT __ATOMIC_RELAXED, __HIP_MEMORY_SCOPE_AGENT

constexpr int S_ = 2048, D_ = 640, F_ = 1728;
constexpr float EPS = 1e-5f;
constexpr unsigned OOB = 0x7f000000u;

constexpr unsigned SZ_AB = 8 * 128 * 5 * 1024;
constexpr unsigned SZ_X = 128 * 20 * 1024;
constexpr unsigned SZ_H = 128 * 54 * 1024;
constexpr unsigned SZ_W1 = 108 * 20 * 1024;
constexpr unsigned SZ_W2 = 8 * 54 * 5 * 1024;
constexpr unsigned SZ_ST = 2048 * 64;
constexpr unsigned WS_AB1 = 0;
constexpr unsigned WS_AB2 = WS_AB1 + SZ_AB;
constexpr unsigned WS_X1 = WS_AB2 + SZ_AB;
constexpr unsigned WS_X2 = WS_X1 + SZ_X;
constexpr unsigned WS_X3 = WS_X2 + SZ_X;
constexpr unsigned WS_H1 = WS_X3 + SZ_X;
constexpr unsigned WS_H2 = WS_H1 + SZ_H;
constexpr unsigned WS_W1 = WS_H2 + SZ_H;
constexpr unsigned WS_W2 = WS_W1 + SZ_W1;
constexpr unsigned WS_ST1 = WS_W2 + SZ_W2;
constexpr unsigned WS_ST2 = WS_ST1 + SZ_ST;
constexpr unsigned WS_ST3 = WS_ST2 + SZ_ST;
constexpr unsigned WS_CS1 = WS_ST3 + SZ_ST;
constexpr unsigned WS_CSD = WS_CS1 + 20 * 1728 * 4;
constexpr unsigned WS_FLG = (WS_CSD + 1280 * 4 + 4095) & ~4095u;
constexpr unsigned FLG_SEAM = 16 * 256;
constexpr unsigned WS_END = WS_FLG + 6 * FLG_SEAM;

DI u32x4 make_srd(const void* p, unsigned bytes) {
  uint64_t a = (uint64_t)p;
  u32x4 r;
  r[0] = (unsigned)a; r[1] = (unsigned)(a >> 32) & 0xffffu; r[2] = bytes; r[3] = 0x00020000u;
  return r;
}
DI rsrc_t make_rsrc(const void* p, unsigned bytes) {
  return __builtin_amdgcn_make_buffer_rsrc((void*)p, (short)0, (int)bytes, 0x00020000);
}
template <int LOFF, int GOFF>
DI void glds(u32x4 srd, unsigned voff, unsigned ldsb, unsigned soff) {
  unsigned tmp;
  asm volatile("s_add_u32 m0, %1, %2\n\ts_add_u32 %0, %3, %4\n\tbuffer_load_dwordx4 %5, %6, %0 offen lds"
               : "=&s"(tmp)
               : "s"(ldsb), "i"(LOFF), "s"(soff), "i"(GOFF), "v"(voff), "s"(srd)
               : "memory", "scc");
}
template <int N> DI void glds_lin(u32x4 srd, unsigned voff, unsigned ldsb, unsigned soff) {
  if constexpr (N == 4)
    asm volatile("s_mov_b32 m0, %0\n\ts_nop 0\n\tbuffer_load_dwordx4 %1, %2, %3 offen lds\n\tbuffer_load_dwordx4 %1, %2, %3 offen offset:1024 lds\n\t"
                 "buffer_load_dwordx4 %1, %2, %3 offen offset:2048 lds\n\tbuffer_load_dwordx4 %1, %2, %3 offen offset:3072 lds"
                 :: "s"(ldsb), "v"(voff), "s"(srd), "s"(soff) : "memory");
  else if constexpr (N == 3)
    asm volatile("s_mov_b32 m0, %0\n\ts_nop 0\n\tbuffer_load_dwordx4 %1, %2, %3 offen lds\n\tbuffer_load_dwordx4 %1, %2, %3 offen offset:1024 lds\n\t"
                 "buffer_load_dwordx4 %1, %2, %3 offen offset:2048 lds"
                 :: "s"(ldsb), "v"(voff), "s"(srd), "s"(soff) : "memory");
  else
    asm volatile("s_mov_b32 m0, %0\n\ts_nop 0\n\tbuffer_load_dwordx4 %1, %2, %3 offen lds"
                 :: "s"(ldsb), "v"(voff), "s"(srd), "s"(soff) : "memory");
}
template <int IMM> DI void ldv(bf16x8& d, u32x4 srd, unsigned voff, unsigned soff) {
  asm volatile("buffer_load_dwordx4 %0, %1, %2, %3 offen offset:%4 sc1" : "=v"(d) : "v"(voff), "s"(srd), "s"(soff), "i"(IMM) : "memory");
}
template <int N> DI void wait_vm() { asm volatile("s_waitcnt vmcnt(%0)" ::"i"(N) : "memory"); }
template <int N> DI void wait_vm5(bf16x8 (&b)[5]) {
  asm volatile("s_waitcnt vmcnt(%5)" : "+v"(b[0]), "+v"(b[1]), "+v"(b[2]), "+v"(b[3]), "+v"(b[4]) : "i"(N) : "memory");
}
DI void wait_lgkm0() { asm volatile("s_waitcnt lgkmcnt(0)" ::: "memory"); }
DI void sb() { __builtin_amdgcn_sched_barrier(0); }
DI void bar() { asm volatile("s_barrier" ::: "memory"); }

DI bf16x8 lds_rd(unsigned addr) { return *(const LDS_AS bf16x8*)(ldsp)(uintptr_t)addr; }
DI f32x4 lds_rdf(unsigned addr) { return *(const LDS_AS f32x4*)(ldsp)(uintptr_t)addr; }
DI void lds_wrf(unsigned addr, f32x4 v) { *(LDS_AS f32x4*)(ldsp)(uintptr_t)addr = v; }
DI float lds_rd1(unsigned addr) { return *(const LDS_AS float*)(ldsp)(uintptr_t)addr; }
DI void lds_wr1(unsigned addr, float v) { *(LDS_AS float*)(ldsp)(uintptr_t)addr = v; }
DI f32x4 mfma(bf16x8 a, bf16x8 b, f32x4 c) { return __builtin_amdgcn_mfma_f32_16x16x32_bf16(a, b, c, 0, 0, 0); }
DI void mma(f32x4& c, const bf16x8& a, const bf16x8& b) {
  asm volatile("v_mfma_f32_16x16x32_bf16 %0, %1, %2, %0" : "+v"(c) : "v"(a), "v"(b));
}
DI void mma0(f32x4& c, const bf16x8& a, const bf16x8& b) {
  asm volatile("v_mfma_f32_16x16x32_bf16 %0, %1, %2, 0" : "=&v"(c) : "v"(a), "v"(b));
}
template <int N> DI void fence_acc(f32x4 (&a)[N]) {
  if constexpr (N == 5) asm volatile("s_nop 7\n\ts_nop 1" : "+v"(a[0]), "+v"(a[1]), "+v"(a[2]), "+v"(a[3]), "+v"(a[4]));
  else asm volatile("s_nop 7\n\ts_nop 1" : "+v"(a[0]), "+v"(a[1]), "+v"(a[2]), "+v"(a[3]), "+v"(a[4]), "+v"(a[5]), "+v"(a[6]));
}
template <int N> DI void pin_acc(f32x4 (&a)[N]) {
  if constexpr (N == 5) asm volatile("" : "+v"(a[0]), "+v"(a[1]), "+v"(a[2]), "+v"(a[3]), "+v"(a[4]));
  else asm volatile("" : "+v"(a[0]), "+v"(a[1]), "+v"(a[2]), "+v"(a[3]), "+v"(a[4]), "+v"(a[5]), "+v"(a[6]));
}
template <int M, int N> DI void fence_all(f32x4 (&a)[M][N]) {
#pragma unroll
  for (int i = 1; i < M; ++i) pin_acc(a[i]);
  fence_acc(a[0]);
#pragma unroll
  for (int i = 1; i < M; ++i) pin_acc(a[i]);
}
DI unsigned pk2(float a, float b) {
  f32x2 v = {a, b};
  return __builtin_bit_cast(unsigned, __builtin_convertvector(v, bf16x2));
}
DI float bflo(unsigned u) { return __uint_as_float(u << 16); }
DI float bfhi(unsigned u) { return __uint_as_float(u & 0xffff0000u); }
DI float xsum16_32(float x) {
  auto s = __builtin_amdgcn_permlane16_swap(__float_as_uint(x), __float_as_uint(x), false, false);
  x = __uint_as_float(s[0]) + __uint_as_float(s[1]);
  auto t = __builtin_amdgcn_permlane32_swap(__float_as_uint(x), __float_as_uint(x), false, false);
  return __uint_as_float(t[0]) + __uint_as_float(t[1]);
}
DI unsigned rfl(unsigned v) { return __builtin_amdgcn_readfirstlane(v); }
DI unsigned opq_v(unsigned x) { asm volatile("" : "+v"(x)); return x; }
DI unsigned opq_s(unsigned x) { asm volatile("" : "+s"(x)); return x; }
template <class T> DI T* opq_p(T* x) { asm volatile("" : "+s"(x)); return x; }
DI bf16x8 ld16(rsrc_t r, unsigned voff, unsigned soff) { return __builtin_bit_cast(bf16x8, __builtin_amdgcn_raw_buffer_load_b128(r, voff, soff, 16)); }
DI u32x4 ld16u(rsrc_t r, unsigned voff, unsigned soff) { return __builtin_amdgcn_raw_buffer_load_b128(r, voff, soff, 16); }
DI u32x2 ld8u(rsrc_t r, unsigned voff, unsigned soff) { return __builtin_amdgcn_raw_buffer_load_b64(r, voff, soff, 16); }
DI void st16(u32x4 v, rsrc_t r, unsigned voff, unsigned soff) { __builtin_amdgcn_raw_buffer_store_b128(v, r, voff, soff, 16); }
DI void st8(u32x2 v, rsrc_t r, unsigned voff, unsigned soff) { __builtin_amdgcn_raw_buffer_store_b64(v, r, voff, soff, 16); }
DI void st4(unsigned v, rsrc_t r, unsigned voff, unsigned soff) { __builtin_amdgcn_raw_buffer_store_b32(v, r, voff, soff, 16); }

#define TM(i) do { if (blockIdx.x == 0 && (threadIdx.x >> 6) == 0) { unsigned t = (unsigned)__builtin_amdgcn_s_memrealtime(); st4(t, make_rsrc(TMWS, WS_END + 4096), (threadIdx.x & 63) == 0 ? 0 : OOB, WS_END + (i) * 4); } } while (0)
#define ST(i)
#define STAMP_ARGS
#define STAMP_FWD
#define STAMP_PASS(k)

template <int KS> DI unsigned foff(unsigned row, unsigned col) {
  return ((row >> 4) * KS + (col >> 5)) * 1024u + (((col >> 3) & 3) * 16u + (row & 15)) * 16u + (col & 7) * 2u;
}

extern __shared__ __attribute__((aligned(1024))) char smem[];
DI unsigned lds_base() { return (unsigned)(uintptr_t)(ldsp)smem; }

DI void ln_coef(float s1, float s2, float& mu, float& rs) {
  mu = s1 * (1.f / D_);
  rs = rsqrtf(fmaxf(s2 * (1.f / D_) - mu * mu, 0.f) + EPS);
}

DI void g1_store(const f32x4 (&acc)[2][5], const float (&mu)[2][4], const float (&rs)[2][4], unsigned csb, rsrc_t rab,
                 unsigned P, unsigned ct, unsigned w, unsigned i16, unsigned q) {
  const float sign = ct < 8 ? 1.f : -1.f;
  const unsigned kstep = (ct >> 3) * 64 + P * 4 + w;
  const unsigned cq = ((q & 1) << 1) | (q >> 1);
  const unsigned so = ((ct & 7) * 128 + kstep) * 5 * 1024;
  const unsigned vo = (cq * 16 + i16) * 16;
#pragma unroll
  for (int f = 0; f < 5; ++f) {
    const float cs = lds_rd1(csb + (f * 16 + i16) * 4);
    unsigned pk[2][2];
#pragma unroll
    for (int r = 0; r < 2; ++r) {
      float t[4];
#pragma unroll
      for (int j = 0; j < 4; ++j) t[j] = sign * rs[r][j] * (acc[r][f][j] - mu[r][j] * cs);
      pk[r][0] = pk2(t[0], t[1]); pk[r][1] = pk2(t[2], t[3]);
    }
    auto t0 = __builtin_amdgcn_permlane16_swap(pk[0][0], pk[1][0], false, false);
    auto t1 = __builtin_amdgcn_permlane16_swap(pk[0][1], pk[1][1], false, false);
    st16(u32x4{t0[0], t1[0], t0[1], t1[1]}, rab, vo + f * 1024, so);
  }
}

constexpr int K1_STAGE = 26 * 1024;
constexpr int K1_TR = 5 * K1_STAGE;
constexpr int K1_CS = K1_TR + 5 * 4096;
constexpr int K1_LDS = K1_CS + 512;

DI void k1_issue(u32x4 sa, u32x4 sbd, unsigned vo, unsigned la, unsigned lbb, unsigned soa, unsigned sob, unsigned w) {
  glds<0, 0>(sa, vo, la, soa);
  glds<1024, 8 * 1280>(sa, vo, la, soa);
  glds<2048, 16 * 1280>(sa, vo, la, soa);
  glds<3072, 24 * 1280>(sa, vo, la, soa);
  glds<0, 0>(sbd, vo, lbb, sob);
  glds<4096, 32 * 1280>(sbd, vo, lbb, sob);
  if (w < 2) glds<8192, 64 * 1280>(sbd, vo, lbb, sob);
}
DI void retile_issue(unsigned u, const u16* w1, const u16* w2, unsigned ldst, unsigned lane) {
  const bool first = u < 540;
  const unsigned v = first ? u : u - 540;
  const unsigned ng = first ? 27u : 10u;
  const unsigned ks = v / ng, g = v - ks * ng;
  const unsigned ld = first ? F_ * 2 : D_ * 2;
  u32x4 srd = make_srd(first ? w1 : w2, D_ * F_ * 2);
  const unsigned vo = (lane >> 3) * ld + (lane & 7) * 16;
  const unsigned so = ks * 32 * ld + g * 128;
  glds<0, 0>(srd, vo, ldst, so);
  glds<1024, 0>(srd, vo, ldst, so + 8 * ld);
  glds<2048, 0>(srd, vo, ldst, so + 16 * ld);
  glds<3072, 0>(srd, vo, ldst, so + 24 * ld);
}
DI void retile_finish(unsigned u, rsrc_t rws, unsigned ldst, unsigned lane) {
  const bool first = u < 540;
  const unsigned v = first ? u : u - 540;
  const unsigned ng = first ? 27u : 10u;
  const unsigned ks = v / ng, g = v - ks * ng;
  const unsigned i = lane & 15, q = lane >> 4;
  const unsigned base = ldst + (8 * q + (i >> 2)) * 128 + (i & 3) * 8;
#pragma unroll
  for (int f = 0; f < 4; ++f) {
    s16x4 lo = __builtin_amdgcn_ds_read_tr16_b64_v4i16((LDS_AS s16x4*)(uintptr_t)(base + f * 32));
    s16x4 hi = __builtin_amdgcn_ds_read_tr16_b64_v4i16((LDS_AS s16x4*)(uintptr_t)(base + f * 32 + 512));
    u32x2 l2 = __builtin_bit_cast(u32x2, lo), h2 = __builtin_bit_cast(u32x2, hi);
    const unsigned rt = g * 4 + f;
    unsigned so;
    if (first) so = WS_W1 + (rt * 20 + ks) * 1024;
    else { const unsigned c = rt / 5; so = WS_W2 + ((c * 54 + ks) * 5 + (rt - c * 5)) * 1024; }
    st16(u32x4{l2[0], l2[1], h2[0], h2[1]}, rws, lane * 16, so);
    if (first) {
      float s = (bflo(l2[0]) + bfhi(l2[0])) + (bflo(l2[1]) + bfhi(l2[1])) + (bflo(h2[0]) + bfhi(h2[0])) + (bflo(h2[1]) + bfhi(h2[1]));
      s = xsum16_32(s);
      if (q == 0) st4(__float_as_uint(s), rws, i * 4, WS_CS1 + (ks * F_ + rt * 16) * 4);
    }
  }
}

__global__ __launch_bounds__(320, 2) void k_first(const u16* x0, const u16* Dre, const u16* Dim, const u16* w1, const u16* w2,
                                                 const u16* Sre, const u16* Sim, char* ws STAMP_ARGS) {
  const unsigned tid = threadIdx.x, lane = tid & 63, w = rfl(tid >> 6);
  const unsigned xcd = blockIdx.x & 7, idx = blockIdx.x >> 3;
  const unsigned P = (xcd >> 1) * 4 + (idx >> 3), ct = (xcd & 1) * 8 + (idx & 7);
  const unsigned i16 = lane & 15, q = lane >> 4;
  const unsigned lb = lds_base();
  const rsrc_t rws = make_rsrc(ws, WS_END);

  if (w == 4) {
    const unsigned nu = blockIdx.x < 56 ? 5 : 4;
    asm volatile("s_nop 4" ::: "memory");
#pragma unroll 1
    for (unsigned n = 0; n < nu; ++n) retile_issue(n < 4 ? blockIdx.x * 4 + n : 1024 + blockIdx.x, w1, w2, lb + K1_TR + n * 4096, lane);
    unsigned d[8];
    {
      const u32x4 s0 = make_srd(Sre, S_ * S_ * 2), s1 = make_srd(Sim, S_ * S_ * 2);
      const unsigned so = (xcd * 256 + idx * 8) * 4096;
#pragma unroll
      for (int n = 0; n < 8; ++n)
        asm volatile("buffer_load_dword %0, %1, %2, %3 offen" : "=v"(d[n]) : "v"(lane * 128), "s"(n < 4 ? s0 : s1), "s"(so + (n & 3) * 8192) : "memory");
    }
    wait_vm<8>();
    bar();
#pragma unroll 1
    for (unsigned n = 0; n < 10; ++n) {
      if (n < nu) retile_finish(n < 4 ? blockIdx.x * 4 + n : 1024 + blockIdx.x, rws, lb + K1_TR + n * 4096, lane);
      if (n == 5 && blockIdx.x < 6 * FLG_SEAM / 256) st4(0u, rws, lane * 4, WS_FLG + blockIdx.x * 256);
      bar();
    }
    bar();
    asm volatile("s_waitcnt vmcnt(0)" : "+v"(d[0]), "+v"(d[1]), "+v"(d[2]), "+v"(d[3]), "+v"(d[4]), "+v"(d[5]), "+v"(d[6]), "+v"(d[7]) :: "memory");
    return;
  }
#define TMWS ws
  TM(0);
  const u32x4 sa = make_srd(x0, S_ * D_ * 2), sbd = make_srd(ct < 8 ? Dre : Dim, D_ * D_ * 2);
  const unsigned vo = (lane >> 3) * 1280 + (((lane & 7) ^ (lane >> 3)) << 4);
  const unsigned soa = (P * 128 + w * 32) * 1280, sob = ((ct & 7) * 80 + w * 8) * 1280;
  const unsigned la = lb + w * 4096, lbb = lb + 16384 + w * 1024;
  asm volatile("s_nop 4" ::: "memory");
#pragma unroll
  for (int s = 0; s < 5; ++s) k1_issue(sa, sbd, vo, la + s * K1_STAGE, lbb + s * K1_STAGE, soa + s * 128, sob + s * 128, w);
  ST(1);

  const unsigned ra0 = lb + w * 4096 + i16 * 128 + ((q ^ (lane & 7)) << 4);
  const unsigned rb0 = lb + 16384 + i16 * 128 + ((q ^ (lane & 7)) << 4);
  const f32x4 z4 = {0.f, 0.f, 0.f, 0.f};
  f32x4 acc[2][5], cs0 = z4, cs1 = z4, rs_[2] = {z4, z4}, rq_[2] = {z4, z4};
#pragma unroll
  for (int r = 0; r < 2; ++r)
#pragma unroll
    for (int f = 0; f < 5; ++f) acc[r][f] = z4;
  const bf16x8 ones = {(__bf16)1.f, (__bf16)1.f, (__bf16)1.f, (__bf16)1.f, (__bf16)1.f, (__bf16)1.f, (__bf16)1.f, (__bf16)1.f};
  bf16x8 a[2][2], b[2][5], x[2];
  auto rd = [&](int kk, unsigned so) {
    const unsigned pa = (ra0 ^ (kk * 64)) + so, pb = (rb0 ^ (kk * 64)) + so;
    a[kk][0] = lds_rd(pa); a[kk][1] = lds_rd(pa + 2048);
#pragma unroll
    for (int f = 0; f < 5; ++f) b[kk][f] = lds_rd(pb + f * 2048);
    x[kk] = lds_rd(pb + w * 2048);
  };
  auto mm = [&](int kk) {
#pragma unroll
    for (int f = 0; f < 5; ++f)
#pragma unroll
      for (int r = 0; r < 2; ++r) acc[r][f] = mfma(a[kk][r], b[kk][f], acc[r][f]);
    cs0 = mfma(ones, x[kk], cs0);
    cs1 = mfma(ones, b[kk][4], cs1);
#pragma unroll
    for (int r = 0; r < 2; ++r) { rs_[r] = mfma(a[kk][r], ones, rs_[r]); rq_[r] = mfma(a[kk][r], a[kk][r], rq_[r]); }
  };
  wait_vm<4 * 6>();
  bar();
  ST(2);
  rd(0, 0);
  unsigned so = 0;
#pragma unroll 1
  for (int i = 0; i < 10; ++i) {
    rd(1, so);
    sb();
    mm(0);
    sb();
    wait_lgkm0();
    if (i < 6) wait_vm<3 * 6>(); else if (i == 6) wait_vm<2 * 6>(); else if (i == 7) wait_vm<6>(); else wait_vm<0>();
    bar();
    if (i < 5) k1_issue(sa, sbd, vo, la + so, lbb + so, soa + (i + 5) * 128, sob + (i + 5) * 128, w);
    so = so == 4 * K1_STAGE ? 0 : so + K1_STAGE;
    rd(0, so);
    sb();
    mm(1);
    sb();
  }
  ST(3);
  const unsigned csb = lb + K1_CS;
  if (q == 0) {
    lds_wr1(csb + (w * 16 + i16) * 4, cs0[0]);
    if (w == 0) lds_wr1(csb + (64 + i16) * 4, cs1[0]);
    if (P == 0) {
      st4(__float_as_uint(cs0[0]), rws, i16 * 4, WS_CSD + (ct * 80 + w * 16) * 4);
      if (w == 0) st4(__float_as_uint(cs1[0]), rws, i16 * 4, WS_CSD + (ct * 80 + 64) * 4);
    }
  }
  wait_lgkm0();
  bar();
  float mu[2][4], rs[2][4];
#pragma unroll
  for (int r = 0; r < 2; ++r)
#pragma unroll
    for (int j = 0; j < 4; ++j) ln_coef(rs_[r][j], __shfl(rq_[r][j], q * 20 + j), mu[r][j], rs[r][j]);
  g1_store(acc, mu, rs, csb, make_rsrc(ws + WS_AB1, SZ_AB), P, ct, w, i16, q);
  wait_vm<0>();
  TM(1);
}
#undef TMWS
#define TMWS ws_

constexpr int KM_LDS = 160 * 1024;
constexpr int KM_CS1 = KM_LDS - 1024, KM_CSD = KM_LDS - 512;
constexpr int G2_RING = 32 * 1024, G4_REG = 35 * 1024;

DI void publish(char* ws, unsigned flag_off, unsigned tid) {
  wait_vm<0>();
  bar();
  if (tid == 0) __hip_atomic_store((gu32*)(ws + flag_off), 1u, RLX_AGENT);
}
DI void wait_flags(char* ws, unsigned line_off, unsigned n, unsigned w, unsigned lane) {
  if (w == 0) {
    gu32* p = (gu32*)(ws + line_off) + (lane < n ? lane : 0);
    for (unsigned s = 0; s < (1u << 20); ++s) {
      if (__all(__hip_atomic_load(p, RLX_AGENT) != 0u)) break;
      __builtin_amdgcn_s_sleep(1);
    }
  }
  bar();
}

template <int W> DI void sm_scatter(unsigned wb, const f32x4 (&acc)[4][5], f32x4 (&own)[5]) {
#pragma unroll
  for (int d = 0; d < 4; ++d)
#pragma unroll
    for (int f = 0; f < 5; ++f) {
      if (d == W) own[f] = acc[d][f];
      else lds_wrf(wb + (d * 5 + f) * 1024, acc[d][f]);
    }
  asm volatile("; scatter %5" : "+v"(own[0]), "+v"(own[1]), "+v"(own[2]), "+v"(own[3]), "+v"(own[4]) : "i"(W));
}

struct ResTile { u32x4 r0, r1; u32x2 r2; };
DI void tile_offsets(bool rm, unsigned row, unsigned ct, unsigned q, unsigned& a0, unsigned& a1, unsigned& a2) {
  const unsigned cA = ct * 80 + 16 * (q & 1) + 8 * (q >> 1), cS = ct * 80 + 64 + 4 * q;
  if (rm) { a0 = (row * D_ + cA) * 2; a1 = a0 + 64; a2 = (row * D_ + cS) * 2; }
  else { a0 = foff<20>(row, cA); a1 = foff<20>(row, cA + 32); a2 = foff<20>(row, cS); }
}
DI void small_epilogue(const f32x4 (&acc)[4][5], unsigned region, const ResTile& rt, rsrc_t rout, bool out_rm, rsrc_t rst,
                       unsigned row, unsigned ct, unsigned w, unsigned lane) {
  const unsigned q = lane >> 4, lb = lds_base();
  f32x4 v[5];
  wait_lgkm0();
  {
    const unsigned wb = lb + w * region + lane * 16;
    if (w == 0) sm_scatter<0>(wb, acc, v);
    else if (w == 1) sm_scatter<1>(wb, acc, v);
    else if (w == 2) sm_scatter<2>(wb, acc, v);
    else sm_scatter<3>(wb, acc, v);
  }
  wait_lgkm0();
  bar();
#pragma unroll
  for (int s = 1; s < 4; ++s) {
    const unsigned src = lb + ((w + s) & 3) * region + w * 5 * 1024 + lane * 16;
#pragma unroll
    for (int f = 0; f < 5; ++f) v[f] += lds_rdf(src + f * 1024);
  }
#pragma unroll
  for (int p = 0; p < 2; ++p)
#pragma unroll
    for (int r = 0; r < 4; ++r) {
      auto t = __builtin_amdgcn_permlane16_swap(__float_as_uint(v[2 * p][r]), __float_as_uint(v[2 * p + 1][r]), false, false);
      v[2 * p][r] = __uint_as_float(t[0]); v[2 * p + 1][r] = __uint_as_float(t[1]);
    }
  u32x4 y0, y1; u32x2 y2;
#pragma unroll
  for (int j = 0; j < 2; ++j) {
    y0[j] = pk2(v[0][2 * j] + bflo(rt.r0[j]), v[0][2 * j + 1] + bfhi(rt.r0[j]));
    y0[2 + j] = pk2(v[1][2 * j] + bflo(rt.r0[2 + j]), v[1][2 * j + 1] + bfhi(rt.r0[2 + j]));
    y1[j] = pk2(v[2][2 * j] + bflo(rt.r1[j]), v[2][2 * j + 1] + bfhi(rt.r1[j]));
    y1[2 + j] = pk2(v[3][2 * j] + bflo(rt.r1[2 + j]), v[3][2 * j + 1] + bfhi(rt.r1[2 + j]));
    y2[j] = pk2(v[4][2 * j] + bflo(rt.r2[j]), v[4][2 * j + 1] + bfhi(rt.r2[j]));
  }
  unsigned o0, o1, o2;
  tile_offsets(out_rm, row, ct, q, o0, o1, o2);
  st16(y0, rout, o0, 0); st16(y1, rout, o1, 0); st8(y2, rout, o2, 0);
  if (!out_rm) {
    float s1 = 0.f, s2 = 0.f;
#pragma unroll
    for (int j = 0; j < 4; ++j) {
      float a = bflo(y0[j]), b = bfhi(y0[j]), c = bflo(y1[j]), d = bfhi(y1[j]);
      s1 += (a + b) + (c + d);
      s2 += a * a + b * b + c * c + d * d;
    }
#pragma unroll
    for (int j = 0; j < 2; ++j) {
      float a = bflo(y2[j]), b = bfhi(y2[j]);
      s1 += a + b; s2 += a * a + b * b;
    }
    s1 = xsum16_32(s1); s2 = xsum16_32(s2);
    if (q == 0) st8(u32x2{__float_as_uint(s1), __float_as_uint(s2)}, rst, (row * 8 + ct) * 8, 0);
  }
}

template <int H> DI void g2_a4(u32x4 sa, unsigned va, unsigned l, unsigned so) {
  glds<H * 4096, H * 4 * 8 * 4096>(sa, va, l, so);
  glds<H * 4096 + 1024, (H * 4 + 1) * 8 * 4096>(sa, va, l, so);
  glds<H * 4096 + 2048, (H * 4 + 2) * 8 * 4096>(sa, va, l, so);
  glds<H * 4096 + 3072, (H * 4 + 3) * 8 * 4096>(sa, va, l, so);
}
struct G2Ctx { u32x4 sre, sim, sab; unsigned va, vb0, vb1, ring, soa0, sob0; };
DI unsigned g2_soa(const G2Ctx& c, int i) { return i < 16 ? c.soa0 + (i & 7) * 512 : 0; }
DI unsigned g2_sob(const G2Ctx& c, int i, int kk) { return i < 16 ? c.sob0 + i * (8 * 5 * 1024) + kk * 5 * 1024 : 0; }
DI u32x4 g2_sa(const G2Ctx& c, int i) { u32x4 r = i < 8 ? c.sre : c.sim; r[2] = i < 16 ? S_ * S_ * 2 : 0; return r; }
DI u32x4 g2_sb(const G2Ctx& c, int i) { u32x4 r = c.sab; r[2] = i < 16 ? SZ_AB : 0; return r; }
DI void g2_b5(bf16x8 (&b)[5], const G2Ctx& c, unsigned so) {
  ldv<0>(b[0], c.sab, c.vb0, so); ldv<1024>(b[1], c.sab, c.vb0, so); ldv<2048>(b[2], c.sab, c.vb0, so);
  ldv<3072>(b[3], c.sab, c.vb0, so); ldv<0>(b[4], c.sab, c.vb1, so);
}
DI void g2_prefetch(const G2Ctx& c) {
#pragma unroll
  for (int s = 0; s < 4; ++s) {
    g2_a4<0>(c.sre, c.va, c.ring + s * 8192, c.soa0 + s * 512);
    if (s < 3) g2_a4<1>(c.sre, c.va, c.ring + s * 8192, c.soa0 + s * 512);
  }
}
template <int H> DI void g2_half(f32x4 (&acc)[4][5], const bf16x8 (&a)[4], const bf16x8 (&b)[5], bf16x8 (&bn)[5],
                                 const G2Ctx& c, u32x4 sbb, unsigned sob, u32x4 sa, unsigned l, unsigned soa) {
#define G2_MM(m) mma(acc[(m) / 5][(m) % 5], b[(m) % 5], a[(m) / 5]);
  G2_MM(0) G2_MM(1) sb();
  ldv<0>(bn[0], sbb, c.vb0, sob); sb(); G2_MM(2) G2_MM(3) sb();
  ldv<1024>(bn[1], sbb, c.vb0, sob); sb(); G2_MM(4) G2_MM(5) sb();
  ldv<2048>(bn[2], sbb, c.vb0, sob); sb(); G2_MM(6) G2_MM(7) sb();
  ldv<3072>(bn[3], sbb, c.vb0, sob); sb(); G2_MM(8) G2_MM(9) sb();
  ldv<0>(bn[4], sbb, c.vb1, sob); sb(); G2_MM(10) G2_MM(11) sb();
  glds<H * 4096, H * 4 * 8 * 4096>(sa, c.va, l, soa); sb(); G2_MM(12) G2_MM(13) sb();
  glds<H * 4096 + 1024, (H * 4 + 1) * 8 * 4096>(sa, c.va, l, soa); sb(); G2_MM(14) G2_MM(15) sb();
  glds<H * 4096 + 2048, (H * 4 + 2) * 8 * 4096>(sa, c.va, l, soa); sb(); G2_MM(16) G2_MM(17) sb();
  glds<H * 4096 + 3072, (H * 4 + 3) * 8 * 4096>(sa, c.va, l, soa); sb(); G2_MM(18) G2_MM(19) sb();
#undef G2_MM
}
DI void g2_main(const G2Ctx& c, f32x4 (&acc)[4][5], unsigned lane) {
  const unsigned i16 = lane & 15, q = lane >> 4;
  const unsigned ra0 = c.ring + i16 * 128 + ((q ^ (lane & 7)) << 4);
  bf16x8 a0[4], a1[4], b00[5], b01[5], b10[5], b11[5];
  g2_b5(b00, c, g2_sob(c, 0, 0));
  g2_b5(b01, c, g2_sob(c, 0, 1));
  g2_b5(b10, c, g2_sob(c, 1, 0));
  wait_vm<15>();
#pragma unroll
  for (int r = 0; r < 4; ++r) a0[r] = lds_rd(ra0 + r * 2048);
  fence_all(acc);
  wait_vm5<10>(b00);
#pragma unroll 1
  for (int i = 0; i < 16; i += 2) {
#pragma unroll
    for (int u = 0; u < 2; ++u) {
      const int s = i + u;
      const unsigned slot = (s & 3) * 8192, slot1 = ((s + 1) & 3) * 8192, slot3 = ((s + 3) & 3) * 8192;
      bf16x8(&bc0)[5] = u ? b10 : b00;
      bf16x8(&bc1)[5] = u ? b11 : b01;
      bf16x8(&bn1)[5] = u ? b01 : b11;
      bf16x8(&bn0)[5] = u ? b00 : b10;
#pragma unroll
      for (int r = 0; r < 4; ++r) a1[r] = lds_rd((ra0 ^ 64) + slot + r * 2048);
      sb();
      g2_half<1>(acc, a0, bc0, bn1, c, g2_sb(c, s + 1), g2_sob(c, s + 1, 1), g2_sa(c, s + 3), c.ring + slot3, g2_soa(c, s + 3));
      wait_lgkm0();
      if (s == 0) wait_vm<14>();
      wait_vm5<22>(bc1);
#pragma unroll
      for (int r = 0; r < 4; ++r) a0[r] = lds_rd(ra0 + slot1 + r * 2048);
      sb();
      g2_half<0>(acc, a1, bc1, bc0, c, g2_sb(c, s + 2), g2_sob(c, s + 2, 0), g2_sa(c, s + 4), c.ring + slot, g2_soa(c, s + 4));
      if (s == 0) wait_vm<18>();
      wait_vm5<22>(bn0);
    }
  }
  wait_vm<0>();
  fence_all(acc);
}

__global__ __launch_bounds__(256, 2) void k_main(const u16* x0_, const u16* Sre_, const u16* Sim_, const u16* Dre_, const u16* Dim_,
                                                char* ws_, u16* out_ STAMP_ARGS) {
  TM(2);
  const unsigned tid0 = threadIdx.x, w0 = rfl(tid0 >> 6);
  const unsigned lb = lds_base();
  const f32x4 z4 = {0.f, 0.f, 0.f, 0.f};
#define PHASE_IDS \
  const unsigned tid = opq_v(tid0), lane = tid & 63, w = opq_s(w0), bid = opq_s(blockIdx.x); \
  const unsigned P = (bid & 7) * 2 + (bid >> 7), j = (bid >> 3) & 15; \
  const unsigned rt64 = P * 2 + (j >> 3), c8 = j & 7, i16 = lane & 15, q = lane >> 4; \
  const unsigned row64 = rt64 * 64 + w * 16 + i16; \
  const unsigned fl = WS_FLG + L * 4 * FLG_SEAM + P * 256; \
  char* const ws = opq_p(ws_); \
  (void)tid; (void)rt64; (void)c8; (void)i16; (void)q; (void)row64; (void)fl;
#define LDX(n) ld16(rx, lane * 16 + ((n) & 3) * 1024, sox + ((n) >> 2) * 4096)
#define G2_CTX \
  G2Ctx g2; \
  g2.sre = make_srd(opq_p(Sre_), S_ * S_ * 2); g2.sim = make_srd(opq_p(Sim_), S_ * S_ * 2); \
  g2.sab = make_srd(ws + (L ? WS_AB2 : WS_AB1), SZ_AB); \
  g2.va = (lane >> 3) * 4096 + (((lane & 7) ^ (lane >> 3)) << 4); \
  g2.vb0 = lane * 16; g2.vb1 = lane * 16 + 4096; \
  g2.ring = lb + w * G2_RING; \
  g2.soa0 = rt64 * 64 * 4096 + w * 128; \
  g2.sob0 = (c8 * 128 + 2 * w) * 5 * 1024;
  {
    const int L = 0;
    PHASE_IDS
    G2_CTX
    asm volatile("s_nop 4" ::: "memory");
    g2_prefetch(g2);
  }

#pragma unroll 1
  for (int L = 0; L < 2; ++L) {
    const int sbase = L * 16;
    (void)sbase;
    {
      PHASE_IDS
      G2_CTX
      ResTile rt;
      unsigned a0, a1, a2;
      tile_offsets(L == 0, row64, c8, q, a0, a1, a2);
      const rsrc_t rr = make_rsrc(L ? (const void*)(ws + WS_X2) : (const void*)opq_p(x0_), SZ_X);
      rt.r0 = ld16u(rr, a0, 0); rt.r1 = ld16u(rr, a1, 0); rt.r2 = ld8u(rr, a2, 0);
      f32x4 acc[4][5];
#pragma unroll
      for (int i = 0; i < 4; ++i)
#pragma unroll
        for (int f = 0; f < 5; ++f) acc[i][f] = z4;
      ST(sbase + 1);
      g2_main(g2, acc, lane);
      if (L == 0) TM(3);
      small_epilogue(acc, G2_RING, rt, make_rsrc(ws + (L ? WS_X3 : WS_X1), SZ_X), false,
                     make_rsrc(ws + (L ? WS_ST3 : WS_ST1), SZ_ST), row64, c8, w, lane);
      ST(sbase + 3);
      publish(ws, fl + j * 4, tid);
      ST(sbase + 4);
    }
    {
      PHASE_IDS
      {
        const u32x4 sw = make_srd(ws + WS_W1, SZ_W1);
        const unsigned so = (j * 140 + w * 35) * 1024, l = lb + w * 35 * 1024;
#pragma unroll
        for (int g = 0; g < 8; ++g) glds_lin<4>(sw, lane * 16, l + g * 4096, so + g * 4096);
        glds_lin<3>(sw, lane * 16, l + 8 * 4096, so + 8 * 4096);
      }
      if (L == 0 && tid >= 128 && tid < 240) {
        const rsrc_t rc = make_rsrc(ws + WS_CS1, 20 * F_ * 4);
        const unsigned col = j * 112 + tid - 128;
        float csp = 0.f;
        if (col < F_) {
#pragma unroll
          for (int k = 0; k < 20; ++k) csp += __uint_as_float(__builtin_amdgcn_raw_buffer_load_b32(rc, col * 4, k * F_ * 4, 16));
        }
        lds_wr1(lb + KM_CS1 + (tid - 128) * 4, csp);
      }
      wait_flags(ws, fl, 16, w, lane);
      ST(sbase + 5);
      const rsrc_t rx = make_rsrc(ws + (L ? WS_X3 : WS_X1), SZ_X);
      const rsrc_t rst = make_rsrc(ws + (L ? WS_ST3 : WS_ST1), SZ_ST);
      const unsigned sox = (P * 8 + w * 2) * 20 * 1024;
      bf16x8 a[20][2];
#pragma unroll
      for (int k = 0; k < 10; ++k) { a[k][0] = LDX(k); a[k][1] = LDX(20 + k); }
      const u32x4 sp0 = ld16u(rst, (P * 128 + w * 32 + i16) * 64 + q * 16, 0), sp1 = ld16u(rst, (P * 128 + w * 32 + 16 + i16) * 64 + q * 16, 0);
      sb();
      wait_vm<22>();
      wait_lgkm0();
      bar();
      ST(sbase + 6);
      f32x4 acc[2][7];
      bf16x8 b[2][7];
      const unsigned rb = lb + lane * 16;
#pragma unroll
      for (int f = 0; f < 7; ++f) b[0][f] = lds_rd(rb + f * 20 * 1024);
#pragma unroll
      for (int k = 0; k < 20; ++k) {
        if (k + 1 < 20) {
#pragma unroll
          for (int f = 0; f < 7; ++f) b[(k + 1) & 1][f] = lds_rd(rb + (f * 20 + k + 1) * 1024);
        }
        sb();
#pragma unroll
        for (int f = 0; f < 7; ++f)
#pragma unroll
          for (int r = 0; r < 2; ++r) { if (k == 0) mma0(acc[r][f], b[0][f], a[0][r]); else mma(acc[r][f], b[k & 1][f], a[k][r]); }
        sb();
        if (k + 10 < 20) { a[k + 10][0] = LDX(k + 10); a[k + 10][1] = LDX(30 + k); }
      }
      fence_all(acc);
      ST(sbase + 7);
      float mu[2], rs[2];
      ln_coef(xsum16_32(__uint_as_float(sp0[0]) + __uint_as_float(sp0[2])), xsum16_32(__uint_as_float(sp0[1]) + __uint_as_float(sp0[3])), mu[0], rs[0]);
      ln_coef(xsum16_32(__uint_as_float(sp1[0]) + __uint_as_float(sp1[2])), xsum16_32(__uint_as_float(sp1[1]) + __uint_as_float(sp1[3])), mu[1], rs[1]);
      f32x4 cs[7];
#pragma unroll
      for (int f = 0; f < 7; ++f) cs[f] = lds_rdf(lb + KM_CS1 + (f * 16 + q * 4) * 4);
      const rsrc_t rh = make_rsrc(ws + (L ? WS_H2 : WS_H1), SZ_H);
#pragma unroll
      for (int r = 0; r < 2; ++r) {
        const unsigned row = P * 128 + w * 32 + r * 16 + i16;
        unsigned pk[7][2];
#pragma unroll
        for (int f = 0; f < 7; ++f) {
          float t[4];
#pragma unroll
          for (int e = 0; e < 4; ++e) t[e] = fmaxf(rs[r] * (acc[r][f][e] - mu[r] * cs[f][e]), 0.f);
          pk[f][0] = pk2(t[0], t[1]); pk[f][1] = pk2(t[2], t[3]);
        }
#pragma unroll
        for (int p = 0; p < 3; ++p) {
          auto t0 = __builtin_amdgcn_permlane16_swap(pk[2 * p][0], pk[2 * p + 1][0], false, false);
          auto t1 = __builtin_amdgcn_permlane16_swap(pk[2 * p][1], pk[2 * p + 1][1], false, false);
          const unsigned col = j * 112 + 32 * p + 16 * (q & 1) + 8 * (q >> 1);
          st16(u32x4{t0[0], t1[0], t0[1], t1[1]}, rh, col < F_ ? foff<54>(row, col) : OOB, 0);
        }
        const unsigned col = j * 112 + 96 + 4 * q;
        st8(u32x2{pk[6][0], pk[6][1]}, rh, col < F_ ? foff<54>(row, col) : OOB, 0);
      }
      ST(sbase + 8);
      publish(ws, fl + FLG_SEAM + j * 4, tid);
      ST(sbase + 9);
    }
    {
      PHASE_IDS
      const unsigned ks0 = w < 2 ? w * 14 : 28 + (w - 2) * 13;
      const unsigned sow = (c8 * 54 + ks0) * 5 * 1024;
      {
        const u32x4 sw = make_srd(ws + WS_W2, SZ_W2);
        const unsigned l = lb + w * G4_REG;
#pragma unroll
        for (int g = 0; g < 8; ++g) glds_lin<4>(sw, lane * 16, l + g * 4096, sow + g * 4096);
        glds_lin<3>(sw, lane * 16, l + 8 * 4096, sow + 8 * 4096);
      }
      ResTile rt;
      {
        unsigned a0, a1, a2;
        tile_offsets(false, row64, c8, q, a0, a1, a2);
        const rsrc_t rr = make_rsrc(ws + (L ? WS_X3 : WS_X1), SZ_X);
        rt.r0 = ld16u(rr, a0, 0); rt.r1 = ld16u(rr, a1, 0); rt.r2 = ld8u(rr, a2, 0);
      }
      wait_flags(ws, fl + FLG_SEAM, 16, w, lane);
      ST(sbase + 10);
      const rsrc_t rh = make_rsrc(ws + (L ? WS_H2 : WS_H1), SZ_H);
      const rsrc_t rw = make_rsrc(ws + WS_W2, SZ_W2);
      const unsigned soh = (rt64 * 4 * 54 + ks0) * 1024;
      const unsigned vlast = lane * 16 + (w < 2 ? 0u : OOB);
      bf16x8 a[14][4], b[14][5];
      auto lda = [&](int t) {
#pragma unroll
        for (int i = 0; i < 4; ++i) a[t][i] = ld16(rh, (t < 13 ? lane * 16 : vlast) + (t & 3) * 1024, soh + (t >> 2) * 4096 + i * 54 * 1024);
      };
      auto ldb = [&](int t) {
#pragma unroll
        for (int f = 0; f < 5; ++f)
          b[t][f] = __builtin_bit_cast(bf16x8, __builtin_amdgcn_raw_buffer_load_b128(rw, (t < 13 ? lane * 16 : vlast) + ((t * 5 + f) & 3) * 1024, sow + ((t * 5 + f) >> 2) * 4096, 0));
      };
#pragma unroll
      for (int t = 0; t < 5; ++t) lda(t);
      sb();
      f32x4 acc[4][5];
      wait_vm<20>();
      const unsigned rb = lb + w * G4_REG + lane * 16;
#pragma unroll
      for (int f = 0; f < 5; ++f) b[0][f] = lds_rd(rb + f * 1024);
#pragma unroll
      for (int t = 0; t < 14; ++t) {
        if (t + 1 < 7) {
#pragma unroll
          for (int f = 0; f < 5; ++f) b[t + 1][f] = lds_rd(rb + ((t + 1) * 5 + f) * 1024);
        }
        sb();
#pragma unroll
        for (int i = 0; i < 4; ++i)
#pragma unroll
          for (int f = 0; f < 5; ++f) { if (t == 0) mma0(acc[i][f], b[0][f], a[0][i]); else mma(acc[i][f], b[t][f], a[t][i]); }
        sb();
        if (t < 3) lda(t + 5);
        if (t == 3) ldb(7);
        if (t == 4) lda(8);
        if (t == 5) ldb(8);
        if (t >= 6 && t + 3 < 14) { lda(t + 3); ldb(t + 3); }
      }
      fence_all(acc);
      ST(sbase + 11);
      small_epilogue(acc, G4_REG, rt, L ? make_rsrc(opq_p(out_), SZ_X) : make_rsrc(ws + WS_X2, SZ_X), L != 0,
                     make_rsrc(ws + WS_ST2, SZ_ST), row64, c8, w, lane);
      ST(sbase + 12);
      if (L) break;
      publish(ws, fl + 2 * FLG_SEAM + j * 4, tid);
    }
    {
      PHASE_IDS
      {
        const u32x4 sd = make_srd(j < 8 ? opq_p(Dre_) : opq_p(Dim_), D_ * D_ * 2);
        const unsigned vo = (lane >> 3) * 1280 + (((lane & 7) ^ (lane >> 3)) << 4);
        unsigned p = w * 25, c = p / 10, g = p - c * 10;
        unsigned so = (c8 * 80 + g * 8) * 1280 + c * 128, l = lb + p * 1024;
#pragma unroll 1
        for (int n = 0; n < 25; ++n) {
          glds_lin<1>(sd, vo, l, so);
          l += 1024;
          if (++g == 10) { g = 0; so += 128 - 9 * 8 * 1280; } else so += 8 * 1280;
        }
      }
      if (tid >= 128 && tid < 208)
        lds_wr1(lb + KM_CSD + (tid - 128) * 4,
                __uint_as_float(__builtin_amdgcn_raw_buffer_load_b32(make_rsrc(ws + WS_CSD, 1280 * 4), (j * 80 + tid - 128) * 4, 0, 16)));
      wait_flags(ws, fl + 2 * FLG_SEAM, 16, w, lane);
      ST(13);
      const rsrc_t rx = make_rsrc(ws + WS_X2, SZ_X);
      const unsigned sox = (P * 8 + w * 2) * 20 * 1024;
      bf16x8 a[20][2];
#pragma unroll
      for (int k = 0; k < 12; ++k) { a[k][0] = LDX(k); a[k][1] = LDX(20 + k); }
      const rsrc_t rst = make_rsrc(ws + WS_ST2, SZ_ST);
      const unsigned sto = (P * 128 + w * 32 + (lane & 31)) * 64 + (lane >> 5) * 32;
      const u32x4 sp0 = ld16u(rst, sto, 0), sp1 = ld16u(rst, sto + 16, 0);
      sb();
      wait_vm<26>();
      wait_lgkm0();
      bar();
      f32x4 acc[2][5];
      bf16x8 b[2][5];
      const unsigned rb = lb + i16 * 128 + ((q ^ (lane & 7)) << 4);
#pragma unroll
      for (int f = 0; f < 5; ++f) b[0][f] = lds_rd(rb + f * 2048);
#pragma unroll
      for (int k = 0; k < 20; ++k) {
        if (k + 1 < 20) {
#pragma unroll
          for (int f = 0; f < 5; ++f) b[(k + 1) & 1][f] = lds_rd((rb ^ (((k + 1) & 1) * 64)) + ((k + 1) >> 1) * 10240 + f * 2048);
        }
        sb();
#pragma unroll
        for (int f = 0; f < 5; ++f)
#pragma unroll
          for (int r = 0; r < 2; ++r) { if (k == 0) mma0(acc[r][f], a[0][r], b[0][f]); else mma(acc[r][f], a[k][r], b[k & 1][f]); }
        sb();
        if (k + 12 < 20) { a[k + 12][0] = LDX(k + 12); a[k + 12][1] = LDX(32 + k); }
      }
      fence_all(acc);
      ST(14);
      float s1 = (__uint_as_float(sp0[0]) + __uint_as_float(sp0[2])) + (__uint_as_float(sp1[0]) + __uint_as_float(sp1[2]));
      float s2 = (__uint_as_float(sp0[1]) + __uint_as_float(sp0[3])) + (__uint_as_float(sp1[1]) + __uint_as_float(sp1[3]));
      auto ta = __builtin_amdgcn_permlane32_swap(__float_as_uint(s1), __float_as_uint(s1), false, false);
      auto tb = __builtin_amdgcn_permlane32_swap(__float_as_uint(s2), __float_as_uint(s2), false, false);
      float m, rr;
      ln_coef(__uint_as_float(ta[0]) + __uint_as_float(ta[1]), __uint_as_float(tb[0]) + __uint_as_float(tb[1]), m, rr);
      float mu[2][4], rs[2][4];
#pragma unroll
      for (int r = 0; r < 2; ++r)
#pragma unroll
        for (int e = 0; e < 4; ++e) { mu[r][e] = __shfl(m, r * 16 + q * 4 + e); rs[r][e] = __shfl(rr, r * 16 + q * 4 + e); }
      g1_store(acc, mu, rs, lb + KM_CSD, make_rsrc(ws + WS_AB2, SZ_AB), P, j, w, i16, q);
      ST(15);
      publish(ws, WS_FLG + 3 * FLG_SEAM + c8 * 256 + ((j >> 3) * 16 + P) * 4, tid);
      G2_CTX
      asm volatile("s_nop 4" ::: "memory");
      g2_prefetch(g2);
      wait_flags(ws, WS_FLG + 3 * FLG_SEAM + c8 * 256, 32, w, lane);
      TM(4);
    }
  }
  wait_vm<0>();
  if (blockIdx.x == 0 && w0 == 0 && SEGA != SEGB) {
    const rsrc_t r = make_rsrc(ws_ + WS_END, 4096);
    unsigned t5 = (unsigned)__builtin_amdgcn_s_memrealtime();
    unsigned ta = rfl(__builtin_amdgcn_raw_buffer_load_b32(r, 0, SEGA * 4, 16));
    unsigned tb = SEGB == 5 ? t5 : rfl(__builtin_amdgcn_raw_buffer_load_b32(r, 0, SEGB * 4, 16));
    unsigned d = (tb - ta) * 4;
    if (d > 100000) d = 100000;
    while ((unsigned)__builtin_amdgcn_s_memrealtime() - t5 < d) __builtin_amdgcn_s_sleep(1);
  }
}

extern "C" void kernel_launch(void* const* d_in, const int* in_sizes, int n_in, void* d_out, int out_size, void* d_ws,
                              size_t ws_size, hipStream_t stream) {
  const u16* x0 = (const u16*)d_in[0];
  const u16* Sre = (const u16*)d_in[1];
  const u16* Sim = (const u16*)d_in[2];
  const u16* Dre = (const u16*)d_in[3];
  const u16* Dim = (const u16*)d_in[4];
  const u16* w1 = (const u16*)d_in[6];
  const u16* w2 = (const u16*)d_in[7];
  char* ws = (char*)d_ws;
  (void)hipFuncSetAttribute((const void*)k_first, hipFuncAttributeMaxDynamicSharedMemorySize, K1_LDS);
  (void)hipFuncSetAttribute((const void*)k_main, hipFuncAttributeMaxDynamicSharedMemorySize, KM_LDS);
  k_first<<<256, 320, K1_LDS, stream>>>(x0, Dre, Dim, w1, w2, Sre, Sim, ws STAMP_PASS(0));
  k_main<<<256, 256, KM_LDS, stream>>>(x0, Sre, Sim, Dre, Dim, ws, (u16*)d_out STAMP_PASS(1));
}
```

```cpp
#define LEAD 0
#define SEGA 0
#define SEGB 0
#include <hip/hip_runtime.h>
#include <hip/hip_bf16.h>
#include <stdint.h>

#define DI __device__ __forceinline__
typedef unsigned short u16;
typedef __attribute__((ext_vector_type(8))) __bf16 bf16x8;
typedef __attribute__((ext_vector_type(2))) __bf16 bf16x2;
typedef __attribute__((ext_vector_type(4))) float f32x4;
typedef __attribute__((ext_vector_type(2))) float f32x2;
typedef __attribute__((ext_vector_type(4))) unsigned u32x4;
typedef __attribute__((ext_vector_type(2))) unsigned u32x2;
typedef __attribute__((ext_vector_type(4))) short s16x4;
#define LDS_AS __attribute__((address_space(3)))
#define GLB_AS __attribute__((address_space(1)))
typedef LDS_AS char* ldsp;
typedef GLB_AS unsigned gu32;
typedef __amdgpu_buffer_rsrc_t rsrc_t;
#define RLX_AGENT __ATOMIC_RELAXED, __HIP_MEMORY_SCOPE_AGENT

constexpr int S_ = 2048, D_ = 640, F_ = 1728;
constexpr float EPS = 1e-5f;
constexpr unsigned OOB = 0x7f000000u;

constexpr unsigned SZ_AB = 8 * 128 * 5 * 1024;
constexpr unsigned SZ_X = 128 * 20 * 1024;
constexpr unsigned SZ_H = 128 * 54 * 1024;
constexpr unsigned SZ_W1 = 108 * 20 * 1024;
constexpr unsigned SZ_W2 = 8 * 54 * 5 * 1024;
constexpr unsigned SZ_ST = 2048 * 64;
constexpr unsigned WS_AB1 = 0;
constexpr unsigned WS_AB2 = WS_AB1 + SZ_AB;
constexpr unsigned WS_X1 = WS_AB2 + SZ_AB;
constexpr unsigned WS_X2 = WS_X1 + SZ_X;
constexpr unsigned WS_X3 = WS_X2 + SZ_X;
constexpr unsigned WS_H1 = WS_X3 + SZ_X;
constexpr unsigned WS_H2 = WS_H1 + SZ_H;
constexpr unsigned WS_W1 = WS_H2 + SZ_H;
constexpr unsigned WS_W2 = WS_W1 + SZ_W1;
constexpr unsigned WS_ST1 = WS_W2 + SZ_W2;
constexpr unsigned WS_ST2 = WS_ST1 + SZ_ST;
constexpr unsigned WS_ST3 = WS_ST2 + SZ_ST;
constexpr unsigned WS_CS1 = WS_ST3 + SZ_ST;
constexpr unsigned WS_CSD = WS_CS1 + 20 * 1728 * 4;
constexpr unsigned WS_FLG = (WS_CSD + 1280 * 4 + 4095) & ~4095u;
constexpr unsigned FLG_SEAM = 16 * 256;
constexpr unsigned WS_END = WS_FLG + 6 * FLG_SEAM;

DI u32x4 make_srd(const void* p, unsigned bytes) {
  uint64_t a = (uint64_t)p;
  u32x4 r;
  r[0] = (unsigned)a; r[1] = (unsigned)(a >> 32) & 0xffffu; r[2] = bytes; r[3] = 0x00020000u;
  return r;
}
DI rsrc_t make_rsrc(const void* p, unsigned bytes) {
  return __builtin_amdgcn_make_buffer_rsrc((void*)p, (short)0, (int)bytes, 0x00020000);
}
template <int LOFF, int GOFF>
DI void glds(u32x4 srd, unsigned voff, unsigned ldsb, unsigned soff) {
  unsigned tmp;
  asm volatile("s_add_u32 m0, %1, %2\n\ts_add_u32 %0, %3, %4\n\tbuffer_load_dwordx4 %5, %6, %0 offen lds"
               : "=&s"(tmp)
               : "s"(ldsb), "i"(LOFF), "s"(soff), "i"(GOFF), "v"(voff), "s"(srd)
               : "memory", "scc");
}
template <int N> DI void glds_lin(u32x4 srd, unsigned voff, unsigned ldsb, unsigned soff) {
  if constexpr (N == 4)
    asm volatile("s_mov_b32 m0, %0\n\ts_nop 0\n\tbuffer_load_dwordx4 %1, %2, %3 offen lds\n\tbuffer_load_dwordx4 %1, %2, %3 offen offset:1024 lds\n\t"
                 "buffer_load_dwordx4 %1, %2, %3 offen offset:2048 lds\n\tbuffer_load_dwordx4 %1, %2, %3 offen offset:3072 lds"
                 :: "s"(ldsb), "v"(voff), "s"(srd), "s"(soff) : "memory");
  else if constexpr (N == 3)
    asm volatile("s_mov_b32 m0, %0\n\ts_nop 0\n\tbuffer_load_dwordx4 %1, %2, %3 offen lds\n\tbuffer_load_dwordx4 %1, %2, %3 offen offset:1024 lds\n\t"
                 "buffer_load_dwordx4 %1, %2, %3 offen offset:2048 lds"
                 :: "s"(ldsb), "v"(voff), "s"(srd), "s"(soff) : "memory");
  else
    asm volatile("s_mov_b32 m0, %0\n\ts_nop 0\n\tbuffer_load_dwordx4 %1, %2, %3 offen lds"
                 :: "s"(ldsb), "v"(voff), "s"(srd), "s"(soff) : "memory");
}
template <int IMM> DI void ldv(bf16x8& d, u32x4 srd, unsigned voff, unsigned soff) {
  asm volatile("buffer_load_dwordx4 %0, %1, %2, %3 offen offset:%4 sc1" : "=v"(d) : "v"(voff), "s"(srd), "s"(soff), "i"(IMM) : "memory");
}
template <int N> DI void wait_vm() { asm volatile("s_waitcnt vmcnt(%0)" ::"i"(N) : "memory"); }
template <int N> DI void wait_vm5(bf16x8 (&b)[5]) {
  asm volatile("s_waitcnt vmcnt(%5)" : "+v"(b[0]), "+v"(b[1]), "+v"(b[2]), "+v"(b[3]), "+v"(b[4]) : "i"(N) : "memory");
}
DI void wait_lgkm0() { asm volatile("s_waitcnt lgkmcnt(0)" ::: "memory"); }
DI void sb() { __builtin_amdgcn_sched_barrier(0); }
DI void bar() { asm volatile("s_barrier" ::: "memory"); }

DI bf16x8 lds_rd(unsigned addr) { return *(const LDS_AS bf16x8*)(ldsp)(uintptr_t)addr; }
DI f32x4 lds_rdf(unsigned addr) { return *(const LDS_AS f32x4*)(ldsp)(uintptr_t)addr; }
DI void lds_wrf(unsigned addr, f32x4 v) { *(LDS_AS f32x4*)(ldsp)(uintptr_t)addr = v; }
DI float lds_rd1(unsigned addr) { return *(const LDS_AS float*)(ldsp)(uintptr_t)addr; }
DI void lds_wr1(unsigned addr, float v) { *(LDS_AS float*)(ldsp)(uintptr_t)addr = v; }
DI f32x4 mfma(bf16x8 a, bf16x8 b, f32x4 c) { return __builtin_amdgcn_mfma_f32_16x16x32_bf16(a, b, c, 0, 0, 0); }
DI void mma(f32x4& c, const bf16x8& a, const bf16x8& b) {
  asm volatile("v_mfma_f32_16x16x32_bf16 %0, %1, %2, %0" : "+v"(c) : "v"(a), "v"(b));
}
DI void mma0(f32x4& c, const bf16x8& a, const bf16x8& b) {
  asm volatile("v_mfma_f32_16x16x32_bf16 %0, %1, %2, 0" : "=&v"(c) : "v"(a), "v"(b));
}
template <int N> DI void fence_acc(f32x4 (&a)[N]) {
  if constexpr (N == 5) asm volatile("s_nop 7\n\ts_nop 1" : "+v"(a[0]), "+v"(a[1]), "+v"(a[2]), "+v"(a[3]), "+v"(a[4]));
  else asm volatile("s_nop 7\n\ts_nop 1" : "+v"(a[0]), "+v"(a[1]), "+v"(a[2]), "+v"(a[3]), "+v"(a[4]), "+v"(a[5]), "+v"(a[6]));
}
template <int N> DI void pin_acc(f32x4 (&a)[N]) {
  if constexpr (N == 5) asm volatile("" : "+v"(a[0]), "+v"(a[1]), "+v"(a[2]), "+v"(a[3]), "+v"(a[4]));
  else asm volatile("" : "+v"(a[0]), "+v"(a[1]), "+v"(a[2]), "+v"(a[3]), "+v"(a[4]), "+v"(a[5]), "+v"(a[6]));
}
template <int M, int N> DI void fence_all(f32x4 (&a)[M][N]) {
#pragma unroll
  for (int i = 1; i < M; ++i) pin_acc(a[i]);
  fence_acc(a[0]);
#pragma unroll
  for (int i = 1; i < M; ++i) pin_acc(a[i]);
}
DI unsigned pk2(float a, float b) {
  f32x2 v = {a, b};
  return __builtin_bit_cast(unsigned, __builtin_convertvector(v, bf16x2));
}
DI float bflo(unsigned u) { return __uint_as_float(u << 16); }
DI float bfhi(unsigned u) { return __uint_as_float(u & 0xffff0000u); }
DI float xsum16_32(float x) {
  auto s = __builtin_amdgcn_permlane16_swap(__float_as_uint(x), __float_as_uint(x), false, false);
  x = __uint_as_float(s[0]) + __uint_as_float(s[1]);
  auto t = __builtin_amdgcn_permlane32_swap(__float_as_uint(x), __float_as_uint(x), false, false);
  return __uint_as_float(t[0]) + __uint_as_float(t[1]);
}
DI unsigned rfl(unsigned v) { return __builtin_amdgcn_readfirstlane(v); }
DI unsigned opq_v(unsigned x) { asm volatile("" : "+v"(x)); return x; }
DI unsigned opq_s(unsigned x) { asm volatile("" : "+s"(x)); return x; }
template <class T> DI T* opq_p(T* x) { asm volatile("" : "+s"(x)); return x; }
DI bf16x8 ld16(rsrc_t r, unsigned voff, unsigned soff) { return __builtin_bit_cast(bf16x8, __builtin_amdgcn_raw_buffer_load_b128(r, voff, soff, 16)); }
DI u32x4 ld16u(rsrc_t r, unsigned voff, unsigned soff) { return __builtin_amdgcn_raw_buffer_load_b128(r, voff, soff, 16); }
DI u32x2 ld8u(rsrc_t r, unsigned voff, unsigned soff) { return __builtin_amdgcn_raw_buffer_load_b64(r, voff, soff, 16); }
DI void st16(u32x4 v, rsrc_t r, unsigned voff, unsigned soff) { __builtin_amdgcn_raw_buffer_store_b128(v, r, voff, soff, 16); }
DI void st8(u32x2 v, rsrc_t r, unsigned voff, unsigned soff) { __builtin_amdgcn_raw_buffer_store_b64(v, r, voff, soff, 16); }
DI void st4(unsigned v, rsrc_t r, unsigned voff, unsigned soff) { __builtin_amdgcn_raw_buffer_store_b32(v, r, voff, soff, 16); }

#define TM(i) do { if (blockIdx.x == 0 && (threadIdx.x >> 6) == 0) { unsigned t = (unsigned)__builtin_amdgcn_s_memrealtime(); st4(t, make_rsrc(TMWS, WS_END + 4096), (threadIdx.x & 63) == 0 ? 0 : OOB, WS_END + (i) * 4); } } while (0)
#define ST(i)
#define STAMP_ARGS
#define STAMP_FWD
#define STAMP_PASS(k)

template <int KS> DI unsigned foff(unsigned row, unsigned col) {
  return ((row >> 4) * KS + (col >> 5)) * 1024u + (((col >> 3) & 3) * 16u + (row & 15)) * 16u + (col & 7) * 2u;
}

extern __shared__ __attribute__((aligned(1024))) char smem[];
DI unsigned lds_base() { return (unsigned)(uintptr_t)(ldsp)smem; }

DI void ln_coef(float s1, float s2, float& mu, float& rs) {
  mu = s1 * (1.f / D_);
  rs = rsqrtf(fmaxf(s2 * (1.f / D_) - mu * mu, 0.f) + EPS);
}

DI void g1_store(const f32x4 (&acc)[2][5], const float (&mu)[2][4], const float (&rs)[2][4], unsigned csb, rsrc_t rab,
                 unsigned P, unsigned ct, unsigned w, unsigned i16, unsigned q) {
  const float sign = ct < 8 ? 1.f : -1.f;
  const unsigned kstep = (ct >> 3) * 64 + P * 4 + w;
  const unsigned cq = ((q & 1) << 1) | (q >> 1);
  const unsigned so = ((ct & 7) * 128 + kstep) * 5 * 1024;
  const unsigned vo = (cq * 16 + i16) * 16;
#pragma unroll
  for (int f = 0; f < 5; ++f) {
    const float cs = lds_rd1(csb + (f * 16 + i16) * 4);
    unsigned pk[2][2];
#pragma unroll
    for (int r = 0; r < 2; ++r) {
      float t[4];
#pragma unroll
      for (int j = 0; j < 4; ++j) t[j] = sign * rs[r][j] * (acc[r][f][j] - mu[r][j] * cs);
      pk[r][0] = pk2(t[0], t[1]); pk[r][1] = pk2(t[2], t[3]);
    }
    auto t0 = __builtin_amdgcn_permlane16_swap(pk[0][0], pk[1][0], false, false);
    auto t1 = __builtin_amdgcn_permlane16_swap(pk[0][1], pk[1][1], false, false);
    st16(u32x4{t0[0], t1[0], t0[1], t1[1]}, rab, vo + f * 1024, so);
  }
}

constexpr int K1_STAGE = 26 * 1024;
constexpr int K1_TR = 5 * K1_STAGE;
constexpr int K1_CS = K1_TR + 5 * 4096;
constexpr int K1_LDS = K1_CS + 512;

DI void k1_issue(u32x4 sa, u32x4 sbd, unsigned vo, unsigned la, unsigned lbb, unsigned soa, unsigned sob, unsigned w) {
  glds<0, 0>(sa, vo, la, soa);
  glds<1024, 8 * 1280>(sa, vo, la, soa);
  glds<2048, 16 * 1280>(sa, vo, la, soa);
  glds<3072, 24 * 1280>(sa, vo, la, soa);
  glds<0, 0>(sbd, vo, lbb, sob);
  glds<4096, 32 * 1280>(sbd, vo, lbb, sob);
  if (w < 2) glds<8192, 64 * 1280>(sbd, vo, lbb, sob);
}
DI void retile_issue(unsigned u, const u16* w1, const u16* w2, unsigned ldst, unsigned lane) {
  const bool first = u < 540;
  const unsigned v = first ? u : u - 540;
  const unsigned ng = first ? 27u : 10u;
  const unsigned ks = v / ng, g = v - ks * ng;
  const unsigned ld = first ? F_ * 2 : D_ * 2;
  u32x4 srd = make_srd(first ? w1 : w2, D_ * F_ * 2);
  const unsigned vo = (lane >> 3) * ld + (lane & 7) * 16;
  const unsigned so = ks * 32 * ld + g * 128;
  glds<0, 0>(srd, vo, ldst, so);
  glds<1024, 0>(srd, vo, ldst, so + 8 * ld);
  glds<2048, 0>(srd, vo, ldst, so + 16 * ld);
  glds<3072, 0>(srd, vo, ldst, so + 24 * ld);
}
DI void retile_finish(unsigned u, rsrc_t rws, unsigned ldst, unsigned lane) {
  const bool first = u < 540;
  const unsigned v = first ? u : u - 540;
  const unsigned ng = first ? 27u : 10u;
  const unsigned ks = v / ng, g = v - ks * ng;
  const unsigned i = lane & 15, q = lane >> 4;
  const unsigned base = ldst + (8 * q + (i >> 2)) * 128 + (i & 3) * 8;
#pragma unroll
  for (int f = 0; f < 4; ++f) {
    s16x4 lo = __builtin_amdgcn_ds_read_tr16_b64_v4i16((LDS_AS s16x4*)(uintptr_t)(base + f * 32));
    s16x4 hi = __builtin_amdgcn_ds_read_tr16_b64_v4i16((LDS_AS s16x4*)(uintptr_t)(base + f * 32 + 512));
    u32x2 l2 = __builtin_bit_cast(u32x2, lo), h2 = __builtin_bit_cast(u32x2, hi);
    const unsigned rt = g * 4 + f;
    unsigned so;
    if (first) so = WS_W1 + (rt * 20 + ks) * 1024;
    else { const unsigned c = rt / 5; so = WS_W2 + ((c * 54 + ks) * 5 + (rt - c * 5)) * 1024; }
    st16(u32x4{l2[0], l2[1], h2[0], h2[1]}, rws, lane * 16, so);
    if (first) {
      float s = (bflo(l2[0]) + bfhi(l2[0])) + (bflo(l2[1]) + bfhi(l2[1])) + (bflo(h2[0]) + bfhi(h2[0])) + (bflo(h2[1]) + bfhi(h2[1]));
      s = xsum16_32(s);
      if (q == 0) st4(__float_as_uint(s), rws, i * 4, WS_CS1 + (ks * F_ + rt * 16) * 4);
    }
  }
}

__global__ __launch_bounds__(320, 2) void k_first(const u16* x0, const u16* Dre, const u16* Dim, const u16* w1, const u16* w2,
                                                 const u16* Sre, const u16* Sim, char* ws STAMP_ARGS) {
  const unsigned tid = threadIdx.x, lane = tid & 63, w = rfl(tid >> 6);
  const unsigned xcd = blockIdx.x & 7, idx = blockIdx.x >> 3;
  const unsigned P = (xcd >> 1) * 4 + (idx >> 3), ct = (xcd & 1) * 8 + (idx & 7);
  const unsigned i16 = lane & 15, q = lane >> 4;
  const unsigned lb = lds_base();
  const rsrc_t rws = make_rsrc(ws, WS_END);

  if (w == 4) {
    const unsigned nu = blockIdx.x < 56 ? 5 : 4;
    asm volatile("s_nop 4" ::: "memory");
#pragma unroll 1
    for (unsigned n = 0; n < nu; ++n) retile_issue(n < 4 ? blockIdx.x * 4 + n : 1024 + blockIdx.x, w1, w2, lb + K1_TR + n * 4096, lane);
    unsigned d[8];
    {
      const u32x4 s0 = make_srd(Sre, S_ * S_ * 2), s1 = make_srd(Sim, S_ * S_ * 2);
      const unsigned so = (xcd * 256 + idx * 8) * 4096;
#pragma unroll
      for (int n = 0; n < 8; ++n)
        asm volatile("buffer_load_dword %0, %1, %2, %3 offen" : "=v"(d[n]) : "v"(lane * 128), "s"(n < 4 ? s0 : s1), "s"(so + (n & 3) * 8192) : "memory");
    }
    wait_vm<8>();
    bar();
#pragma unroll 1
    for (unsigned n = 0; n < 10; ++n) {
      if (n < nu) retile_finish(n < 4 ? blockIdx.x * 4 + n : 1024 + blockIdx.x, rws, lb + K1_TR + n * 4096, lane);
      if (n == 5 && blockIdx.x < 6 * FLG_SEAM / 256) st4(0u, rws, lane * 4, WS_FLG + blockIdx.x * 256);
      bar();
    }
    bar();
    asm volatile("s_waitcnt vmcnt(0)" : "+v"(d[0]), "+v"(d[1]), "+v"(d[2]), "+v"(d[3]), "+v"(d[4]), "+v"(d[5]), "+v"(d[6]), "+v"(d[7]) :: "memory");
    return;
  }
#define TMWS ws
  TM(0);
  const u32x4 sa = make_srd(x0, S_ * D_ * 2), sbd = make_srd(ct < 8 ? Dre : Dim, D_ * D_ * 2);
  const unsigned vo = (lane >> 3) * 1280 + (((lane & 7) ^ (lane >> 3)) << 4);
  const unsigned soa = (P * 128 + w * 32) * 1280, sob = ((ct & 7) * 80 + w * 8) * 1280;
  const unsigned la = lb + w * 4096, lbb = lb + 16384 + w * 1024;
  asm volatile("s_nop 4" ::: "memory");
#pragma unroll
  for (int s = 0; s < 5; ++s) k1_issue(sa, sbd, vo, la + s * K1_STAGE, lbb + s * K1_STAGE, soa + s * 128, sob + s * 128, w);
  ST(1);

  const unsigned ra0 = lb + w * 4096 + i16 * 128 + ((q ^ (lane & 7)) << 4);
  const unsigned rb0 = lb + 16384 + i16 * 128 + ((q ^ (lane & 7)) << 4);
  const f32x4 z4 = {0.f, 0.f, 0.f, 0.f};
  f32x4 acc[2][5], cs0 = z4, cs1 = z4, rs_[2] = {z4, z4}, rq_[2] = {z4, z4};
#pragma unroll
  for (int r = 0; r < 2; ++r)
#pragma unroll
    for (int f = 0; f < 5; ++f) acc[r][f] = z4;
  const bf16x8 ones = {(__bf16)1.f, (__bf16)1.f, (__bf16)1.f, (__bf16)1.f, (__bf16)1.f, (__bf16)1.f, (__bf16)1.f, (__bf16)1.f};
  bf16x8 a[2][2], b[2][5], x[2];
  auto rd = [&](int kk, unsigned so) {
    const unsigned pa = (ra0 ^ (kk * 64)) + so, pb = (rb0 ^ (kk * 64)) + so;
    a[kk][0] = lds_rd(pa); a[kk][1] = lds_rd(pa + 2048);
#pragma unroll
    for (int f = 0; f < 5; ++f) b[kk][f] = lds_rd(pb + f * 2048);
    x[kk] = lds_rd(pb + w * 2048);
  };
  auto mm = [&](int kk) {
#pragma unroll
    for (int f = 0; f < 5; ++f)
#pragma unroll
      for (int r = 0; r < 2; ++r) acc[r][f] = mfma(a[kk][r], b[kk][f], acc[r][f]);
    cs0 = mfma(ones, x[kk], cs0);
    cs1 = mfma(ones, b[kk][4], cs1);
#pragma unroll
    for (int r = 0; r < 2; ++r) { rs_[r] = mfma(a[kk][r], ones, rs_[r]); rq_[r] = mfma(a[kk][r], a[kk][r], rq_[r]); }
  };
  wait_vm<4 * 6>();
  bar();
  ST(2);
  rd(0, 0);
  unsigned so = 0;
#pragma unroll 1
  for (int i = 0; i < 10; ++i) {
    rd(1, so);
    sb();
    mm(0);
    sb();
    wait_lgkm0();
    if (i < 6) wait_vm<3 * 6>(); else if (i == 6) wait_vm<2 * 6>(); else if (i == 7) wait_vm<6>(); else wait_vm<0>();
    bar();
    if (i < 5) k1_issue(sa, sbd, vo, la + so, lbb + so, soa + (i + 5) * 128, sob + (i + 5) * 128, w);
    so = so == 4 * K1_STAGE ? 0 : so + K1_STAGE;
    rd(0, so);
    sb();
    mm(1);
    sb();
  }
  ST(3);
  const unsigned csb = lb + K1_CS;
  if (q == 0) {
    lds_wr1(csb + (w * 16 + i16) * 4, cs0[0]);
    if (w == 0) lds_wr1(csb + (64 + i16) * 4, cs1[0]);
    if (P == 0) {
      st4(__float_as_uint(cs0[0]), rws, i16 * 4, WS_CSD + (ct * 80 + w * 16) * 4);
      if (w == 0) st4(__float_as_uint(cs1[0]), rws, i16 * 4, WS_CSD + (ct * 80 + 64) * 4);
    }
  }
  wait_lgkm0();
  bar();
  float mu[2][4], rs[2][4];
#pragma unroll
  for (int r = 0; r < 2; ++r)
#pragma unroll
    for (int j = 0; j < 4; ++j) ln_coef(rs_[r][j], __shfl(rq_[r][j], q * 20 + j), mu[r][j], rs[r][j]);
  g1_store(acc, mu, rs, csb, make_rsrc(ws + WS_AB1, SZ_AB), P, ct, w, i16, q);
  wait_vm<0>();
  TM(1);
}
#undef TMWS
#define TMWS ws_

constexpr int KM_LDS = 160 * 1024;
constexpr int KM_CS1 = KM_LDS - 1024, KM_CSD = KM_LDS - 512;
constexpr int G2_RING = 32 * 1024, G4_REG = 35 * 1024;

DI void publish(char* ws, unsigned flag_off, unsigned tid) {
  wait_vm<0>();
  bar();
  if (tid == 0) __hip_atomic_store((gu32*)(ws + flag_off), 1u, RLX_AGENT);
}
DI void wait_flags(char* ws, unsigned line_off, unsigned n, unsigned w, unsigned lane) {
  if (w == 0) {
    gu32* p = (gu32*)(ws + line_off) + (lane < n ? lane : 0);
    for (unsigned s = 0; s < (1u << 20); ++s) {
      if (__all(__hip_atomic_load(p, RLX_AGENT) != 0u)) break;
      __builtin_amdgcn_s_sleep(1);
    }
  }
  bar();
}

template <int W> DI void sm_scatter(unsigned wb, const f32x4 (&acc)[4][5], f32x4 (&own)[5]) {
#pragma unroll
  for (int d = 0; d < 4; ++d)
#pragma unroll
    for (int f = 0; f < 5; ++f) {
      if (d == W) own[f] = acc[d][f];
      else lds_wrf(wb + (d * 5 + f) * 1024, acc[d][f]);
    }
  asm volatile("; scatter %5" : "+v"(own[0]), "+v"(own[1]), "+v"(own[2]), "+v"(own[3]), "+v"(own[4]) : "i"(W));
}

struct ResTile { u32x4 r0, r1; u32x2 r2; };
DI void tile_offsets(bool rm, unsigned row, unsigned ct, unsigned q, unsigned& a0, unsigned& a1, unsigned& a2) {
  const unsigned cA = ct * 80 + 16 * (q & 1) + 8 * (q >> 1), cS = ct * 80 + 64 + 4 * q;
  if (rm) { a0 = (row * D_ + cA) * 2; a1 = a0 + 64; a2 = (row * D_ + cS) * 2; }
  else { a0 = foff<20>(row, cA); a1 = foff<20>(row, cA + 32); a2 = foff<20>(row, cS); }
}
DI void small_epilogue(const f32x4 (&acc)[4][5], unsigned region, const ResTile& rt, rsrc_t rout, bool out_rm, rsrc_t rst,
                       unsigned row, unsigned ct, unsigned w, unsigned lane) {
  const unsigned q = lane >> 4, lb = lds_base();
  f32x4 v[5];
  wait_lgkm0();
  {
    const unsigned wb = lb + w * region + lane * 16;
    if (w == 0) sm_scatter<0>(wb, acc, v);
    else if (w == 1) sm_scatter<1>(wb, acc, v);
    else if (w == 2) sm_scatter<2>(wb, acc, v);
    else sm_scatter<3>(wb, acc, v);
  }
  wait_lgkm0();
  bar();
#pragma unroll
  for (int s = 1; s < 4; ++s) {
    const unsigned src = lb + ((w + s) & 3) * region + w * 5 * 1024 + lane * 16;
#pragma unroll
    for (int f = 0; f < 5; ++f) v[f] += lds_rdf(src + f * 1024);
  }
#pragma unroll
  for (int p = 0; p < 2; ++p)
#pragma unroll
    for (int r = 0; r < 4; ++r) {
      auto t = __builtin_amdgcn_permlane16_swap(__float_as_uint(v[2 * p][r]), __float_as_uint(v[2 * p + 1][r]), false, false);
      v[2 * p][r] = __uint_as_float(t[0]); v[2 * p + 1][r] = __uint_as_float(t[1]);
    }
  u32x4 y0, y1; u32x2 y2;
#pragma unroll
  for (int j = 0; j < 2; ++j) {
    y0[j] = pk2(v[0][2 * j] + bflo(rt.r0[j]), v[0][2 * j + 1] + bfhi(rt.r0[j]));
    y0[2 + j] = pk2(v[1][2 * j] + bflo(rt.r0[2 + j]), v[1][2 * j + 1] + bfhi(rt.r0[2 + j]));
    y1[j] = pk2(v[2][2 * j] + bflo(rt.r1[j]), v[2][2 * j + 1] + bfhi(rt.r1[j]));
    y1[2 + j] = pk2(v[3][2 * j] + bflo(rt.r1[2 + j]), v[3][2 * j + 1] + bfhi(rt.r1[2 + j]));
    y2[j] = pk2(v[4][2 * j] + bflo(rt.r2[j]), v[4][2 * j + 1] + bfhi(rt.r2[j]));
  }
  unsigned o0, o1, o2;
  tile_offsets(out_rm, row, ct, q, o0, o1, o2);
  st16(y0, rout, o0, 0); st16(y1, rout, o1, 0); st8(y2, rout, o2, 0);
  if (!out_rm) {
    float s1 = 0.f, s2 = 0.f;
#pragma unroll
    for (int j = 0; j < 4; ++j) {
      float a = bflo(y0[j]), b = bfhi(y0[j]), c = bflo(y1[j]), d = bfhi(y1[j]);
      s1 += (a + b) + (c + d);
      s2 += a * a + b * b + c * c + d * d;
    }
#pragma unroll
    for (int j = 0; j < 2; ++j) {
      float a = bflo(y2[j]), b = bfhi(y2[j]);
      s1 += a + b; s2 += a * a + b * b;
    }
    s1 = xsum16_32(s1); s2 = xsum16_32(s2);
    if (q == 0) st8(u32x2{__float_as_uint(s1), __float_as_uint(s2)}, rst, (row * 8 + ct) * 8, 0);
  }
}

template <int H> DI void g2_a4(u32x4 sa, unsigned va, unsigned l, unsigned so) {
  glds<H * 4096, H * 4 * 8 * 4096>(sa, va, l, so);
  glds<H * 4096 + 1024, (H * 4 + 1) * 8 * 4096>(sa, va, l, so);
  glds<H * 4096 + 2048, (H * 4 + 2) * 8 * 4096>(sa, va, l, so);
  glds<H * 4096 + 3072, (H * 4 + 3) * 8 * 4096>(sa, va, l, so);
}
struct G2Ctx { u32x4 sre, sim, sab; unsigned va, vb0, vb1, ring, soa0, sob0; };
DI unsigned g2_soa(const G2Ctx& c, int i) { return i < 16 ? c.soa0 + (i & 7) * 512 : 0; }
DI unsigned g2_sob(const G2Ctx& c, int i, int kk) { return i < 16 ? c.sob0 + i * (8 * 5 * 1024) + kk * 5 * 1024 : 0; }
DI u32x4 g2_sa(const G2Ctx& c, int i) { u32x4 r = i < 8 ? c.sre : c.sim; r[2] = i < 16 ? S_ * S_ * 2 : 0; return r; }
DI u32x4 g2_sb(const G2Ctx& c, int i) { u32x4 r = c.sab; r[2] = i < 16 ? SZ_AB : 0; return r; }
DI void g2_b5(bf16x8 (&b)[5], const G2Ctx& c, unsigned so) {
  ldv<0>(b[0], c.sab, c.vb0, so); ldv<1024>(b[1], c.sab, c.vb0, so); ldv<2048>(b[2], c.sab, c.vb0, so);
  ldv<3072>(b[3], c.sab, c.vb0, so); ldv<0>(b[4], c.sab, c.vb1, so);
}
DI void g2_prefetch(const G2Ctx& c) {
#pragma unroll
  for (int s = 0; s < 4; ++s) {
    g2_a4<0>(c.sre, c.va, c.ring + s * 8192, c.soa0 + s * 512);
    if (s < 3) g2_a4<1>(c.sre, c.va, c.ring + s * 8192, c.soa0 + s * 512);
  }
}
template <int H> DI void g2_half(f32x4 (&acc)[4][5], const bf16x8 (&a)[4], const bf16x8 (&b)[5], bf16x8 (&bn)[5],
                                 const G2Ctx& c, u32x4 sbb, unsigned sob, u32x4 sa, unsigned l, unsigned soa) {
#define G2_MM(m) mma(acc[(m) / 5][(m) % 5], b[(m) % 5], a[(m) / 5]);
  G2_MM(0) G2_MM(1) sb();
  ldv<0>(bn[0], sbb, c.vb0, sob); sb(); G2_MM(2) G2_MM(3) sb();
  ldv<1024>(bn[1], sbb, c.vb0, sob); sb(); G2_MM(4) G2_MM(5) sb();
  ldv<2048>(bn[2], sbb, c.vb0, sob); sb(); G2_MM(6) G2_MM(7) sb();
  ldv<3072>(bn[3], sbb, c.vb0, sob); sb(); G2_MM(8) G2_MM(9) sb();
  ldv<0>(bn[4], sbb, c.vb1, sob); sb(); G2_MM(10) G2_MM(11) sb();
  glds<H * 4096, H * 4 * 8 * 4096>(sa, c.va, l, soa); sb(); G2_MM(12) G2_MM(13) sb();
  glds<H * 4096 + 1024, (H * 4 + 1) * 8 * 4096>(sa, c.va, l, soa); sb(); G2_MM(14) G2_MM(15) sb();
  glds<H * 4096 + 2048, (H * 4 + 2) * 8 * 4096>(sa, c.va, l, soa); sb(); G2_MM(16) G2_MM(17) sb();
  glds<H * 4096 + 3072, (H * 4 + 3) * 8 * 4096>(sa, c.va, l, soa); sb(); G2_MM(18) G2_MM(19) sb();
#undef G2_MM
}
DI void g2_main(const G2Ctx& c, f32x4 (&acc)[4][5], unsigned lane) {
  const unsigned i16 = lane & 15, q = lane >> 4;
  const unsigned ra0 = c.ring + i16 * 128 + ((q ^ (lane & 7)) << 4);
  bf16x8 a0[4], a1[4], b00[5], b01[5], b10[5], b11[5];
  g2_b5(b00, c, g2_sob(c, 0, 0));
  g2_b5(b01, c, g2_sob(c, 0, 1));
  g2_b5(b10, c, g2_sob(c, 1, 0));
  wait_vm<15>();
#pragma unroll
  for (int r = 0; r < 4; ++r) a0[r] = lds_rd(ra0 + r * 2048);
  fence_all(acc);
  wait_vm5<10>(b00);
#pragma unroll 1
  for (int i = 0; i < 16; i += 2) {
#pragma unroll
    for (int u = 0; u < 2; ++u) {
      const int s = i + u;
      const unsigned slot = (s & 3) * 8192, slot1 = ((s + 1) & 3) * 8192, slot3 = ((s + 3) & 3) * 8192;
      bf16x8(&bc0)[5] = u ? b10 : b00;
      bf16x8(&bc1)[5] = u ? b11 : b01;
      bf16x8(&bn1)[5] = u ? b01 : b11;
      bf16x8(&bn0)[5] = u ? b00 : b10;
#pragma unroll
      for (int r = 0; r < 4; ++r) a1[r] = lds_rd((ra0 ^ 64) + slot + r * 2048);
      sb();
      g2_half<1>(acc, a0, bc0, bn1, c, g2_sb(c, s + 1), g2_sob(c, s + 1, 1), g2_sa(c, s + 3), c.ring + slot3, g2_soa(c, s + 3));
      wait_lgkm0();
      if (s == 0) wait_vm<14>();
      wait_vm5<22>(bc1);
#pragma unroll
      for (int r = 0; r < 4; ++r) a0[r] = lds_rd(ra0 + slot1 + r * 2048);
      sb();
      g2_half<0>(acc, a1, bc1, bc0, c, g2_sb(c, s + 2), g2_sob(c, s + 2, 0), g2_sa(c, s + 4), c.ring + slot, g2_soa(c, s + 4));
      if (s == 0) wait_vm<18>();
      wait_vm5<22>(bn0);
    }
  }
  wait_vm<0>();
  fence_all(acc);
}

__global__ __launch_bounds__(256, 2) void k_main(const u16* x0_, const u16* Sre_, const u16* Sim_, const u16* Dre_, const u16* Dim_,
                                                char* ws_, u16* out_ STAMP_ARGS) {
  TM(2);
  const unsigned tid0 = threadIdx.x, w0 = rfl(tid0 >> 6);
  const unsigned lb = lds_base();
  const f32x4 z4 = {0.f, 0.f, 0.f, 0.f};
#define PHASE_IDS \
  const unsigned tid = opq_v(tid0), lane = tid & 63, w = opq_s(w0), bid = opq_s(blockIdx.x); \
  const unsigned P = (bid & 7) * 2 + (bid >> 7), j = (bid >> 3) & 15; \
  const unsigned rt64 = P * 2 + (j >> 3), c8 = j & 7, i16 = lane & 15, q = lane >> 4; \
  const unsigned row64 = rt64 * 64 + w * 16 + i16; \
  const unsigned fl = WS_FLG + L * 4 * FLG_SEAM + P * 256; \
  char* const ws = opq_p(ws_); \
  (void)tid; (void)rt64; (void)c8; (void)i16; (void)q; (void)row64; (void)fl;
#define LDX(n) ld16(rx, lane * 16 + ((n) & 3) * 1024, sox + ((n) >> 2) * 4096)
#define G2_CTX \
  G2Ctx g2; \
  g2.sre = make_srd(opq_p(Sre_), S_ * S_ * 2); g2.sim = make_srd(opq_p(Sim_), S_ * S_ * 2); \
  g2.sab = make_srd(ws + (L ? WS_AB2 : WS_AB1), SZ_AB); \
  g2.va = (lane >> 3) * 4096 + (((lane & 7) ^ (lane >> 3)) << 4); \
  g2.vb0 = lane * 16; g2.vb1 = lane * 16 + 4096; \
  g2.ring = lb + w * G2_RING; \
  g2.soa0 = rt64 * 64 * 4096 + w * 128; \
  g2.sob0 = (c8 * 128 + 2 * w) * 5 * 1024;
  {
    const int L = 0;
    PHASE_IDS
    G2_CTX
    asm volatile("s_nop 4" ::: "memory");
    g2_prefetch(g2);
  }

#pragma unroll 1
  for (int L = 0; L < 2; ++L) {
    const int sbase = L * 16;
    (void)sbase;
    {
      PHASE_IDS
      G2_CTX
      ResTile rt;
      unsigned a0, a1, a2;
      tile_offsets(L == 0, row64, c8, q, a0, a1, a2);
      const rsrc_t rr = make_rsrc(L ? (const void*)(ws + WS_X2) : (const void*)opq_p(x0_), SZ_X);
      rt.r0 = ld16u(rr, a0, 0); rt.r1 = ld16u(rr, a1, 0); rt.r2 = ld8u(rr, a2, 0);
      f32x4 acc[4][5];
#pragma unroll
      for (int i = 0; i < 4; ++i)
#pragma unroll
        for (int f = 0; f < 5; ++f) acc[i][f] = z4;
      ST(sbase + 1);
      g2_main(g2, acc, lane);
      if (L == 0) TM(3);
      small_epilogue(acc, G2_RING, rt, make_rsrc(ws + (L ? WS_X3 : WS_X1), SZ_X), false,
                     make_rsrc(ws + (L ? WS_ST3 : WS_ST1), SZ_ST), row64, c8, w, lane);
      ST(sbase + 3);
      publish(ws, fl + j * 4, tid);
      ST(sbase + 4);
    }
    {
      PHASE_IDS
      {
        const u32x4 sw = make_srd(ws + WS_W1, SZ_W1);
        const unsigned so = (j * 140 + w * 35) * 1024, l = lb + w * 35 * 1024;
#pragma unroll
        for (int g = 0; g < 8; ++g) glds_lin<4>(sw, lane * 16, l + g * 4096, so + g * 4096);
        glds_lin<3>(sw, lane * 16, l + 8 * 4096, so + 8 * 4096);
      }
      if (L == 0 && tid >= 128 && tid < 240) {
        const rsrc_t rc = make_rsrc(ws + WS_CS1, 20 * F_ * 4);
        const unsigned col = j * 112 + tid - 128;
        float csp = 0.f;
        if (col < F_) {
#pragma unroll
          for (int k = 0; k < 20; ++k) csp += __uint_as_float(__builtin_amdgcn_raw_buffer_load_b32(rc, col * 4, k * F_ * 4, 16));
        }
        lds_wr1(lb + KM_CS1 + (tid - 128) * 4, csp);
      }
      wait_flags(ws, fl, 16, w, lane);
      ST(sbase + 5);
      const rsrc_t rx = make_rsrc(ws + (L ? WS_X3 : WS_X1), SZ_X);
      const rsrc_t rst = make_rsrc(ws + (L ? WS_ST3 : WS_ST1), SZ_ST);
      const unsigned sox = (P * 8 + w * 2) * 20 * 1024;
      bf16x8 a[20][2];
#pragma unroll
      for (int k = 0; k < 10; ++k) { a[k][0] = LDX(k); a[k][1] = LDX(20 + k); }
      const u32x4 sp0 = ld16u(rst, (P * 128 + w * 32 + i16) * 64 + q * 16, 0), sp1 = ld16u(rst, (P * 128 + w * 32 + 16 + i16) * 64 + q * 16, 0);
      sb();
      wait_vm<22>();
      wait_lgkm0();
      bar();
      ST(sbase + 6);
      f32x4 acc[2][7];
      bf16x8 b[2][7];
      const unsigned rb = lb + lane * 16;
#pragma unroll
      for (int f = 0; f < 7; ++f) b[0][f] = lds_rd(rb + f * 20 * 1024);
#pragma unroll
      for (int k = 0; k < 20; ++k) {
        if (k + 1 < 20) {
#pragma unroll
          for (int f = 0; f < 7; ++f) b[(k + 1) & 1][f] = lds_rd(rb + (f * 20 + k + 1) * 1024);
        }
        sb();
#pragma unroll
        for (int f = 0; f < 7; ++f)
#pragma unroll
          for (int r = 0; r < 2; ++r) { if (k == 0) mma0(acc[r][f], b[0][f], a[0][r]); else mma(acc[r][f], b[k & 1][f], a[k][r]); }
        sb();
        if (k + 10 < 20) { a[k + 10][0] = LDX(k + 10); a[k + 10][1] = LDX(30 + k); }
      }
      fence_all(acc);
      ST(sbase + 7);
      float mu[2], rs[2];
      ln_coef(xsum16_32(__uint_as_float(sp0[0]) + __uint_as_float(sp0[2])), xsum16_32(__uint_as_float(sp0[1]) + __uint_as_float(sp0[3])), mu[0], rs[0]);
      ln_coef(xsum16_32(__uint_as_float(sp1[0]) + __uint_as_float(sp1[2])), xsum16_32(__uint_as_float(sp1[1]) + __uint_as_float(sp1[3])), mu[1], rs[1]);
      f32x4 cs[7];
#pragma unroll
      for (int f = 0; f < 7; ++f) cs[f] = lds_rdf(lb + KM_CS1 + (f * 16 + q * 4) * 4);
      const rsrc_t rh = make_rsrc(ws + (L ? WS_H2 : WS_H1), SZ_H);
#pragma unroll
      for (int r = 0; r < 2; ++r) {
        const unsigned row = P * 128 + w * 32 + r * 16 + i16;
        unsigned pk[7][2];
#pragma unroll
        for (int f = 0; f < 7; ++f) {
          float t[4];
#pragma unroll
          for (int e = 0; e < 4; ++e) t[e] = fmaxf(rs[r] * (acc[r][f][e] - mu[r] * cs[f][e]), 0.f);
          pk[f][0] = pk2(t[0], t[1]); pk[f][1] = pk2(t[2], t[3]);
        }
#pragma unroll
        for (int p = 0; p < 3; ++p) {
          auto t0 = __builtin_amdgcn_permlane16_swap(pk[2 * p][0], pk[2 * p + 1][0], false, false);
          auto t1 = __builtin_amdgcn_permlane16_swap(pk[2 * p][1], pk[2 * p + 1][1], false, false);
          const unsigned col = j * 112 + 32 * p + 16 * (q & 1) + 8 * (q >> 1);
          st16(u32x4{t0[0], t1[0], t0[1], t1[1]}, rh, col < F_ ? foff<54>(row, col) : OOB, 0);
        }
        const unsigned col = j * 112 + 96 + 4 * q;
        st8(u32x2{pk[6][0], pk[6][1]}, rh, col < F_ ? foff<54>(row, col) : OOB, 0);
      }
      ST(sbase + 8);
      publish(ws, fl + FLG_SEAM + j * 4, tid);
      ST(sbase + 9);
    }
    {
      PHASE_IDS
      const unsigned ks0 = w < 2 ? w * 14 : 28 + (w - 2) * 13;
      const unsigned sow = (c8 * 54 + ks0) * 5 * 1024;
      {
        const u32x4 sw = make_srd(ws + WS_W2, SZ_W2);
        const unsigned l = lb + w * G4_REG;
#pragma unroll
        for (int g = 0; g < 8; ++g) glds_lin<4>(sw, lane * 16, l + g * 4096, sow + g * 4096);
        glds_lin<3>(sw, lane * 16, l + 8 * 4096, sow + 8 * 4096);
      }
      ResTile rt;
      {
        unsigned a0, a1, a2;
        tile_offsets(false, row64, c8, q, a0, a1, a2);
        const rsrc_t rr = make_rsrc(ws + (L ? WS_X3 : WS_X1), SZ_X);
        rt.r0 = ld16u(rr, a0, 0); rt.r1 = ld16u(rr, a1, 0); rt.r2 = ld8u(rr, a2, 0);
      }
      wait_flags(ws, fl + FLG_SEAM, 16, w, lane);
      ST(sbase + 10);
      const rsrc_t rh = make_rsrc(ws + (L ? WS_H2 : WS_H1), SZ_H);
      const rsrc_t rw = make_rsrc(ws + WS_W2, SZ_W2);
      const unsigned soh = (rt64 * 4 * 54 + ks0) * 1024;
      const unsigned vlast = lane * 16 + (w < 2 ? 0u : OOB);
      bf16x8 a[14][4], b[14][5];
      auto lda = [&](int t) {
#pragma unroll
        for (int i = 0; i < 4; ++i) a[t][i] = ld16(rh, (t < 13 ? lane * 16 : vlast) + (t & 3) * 1024, soh + (t >> 2) * 4096 + i * 54 * 1024);
      };
      auto ldb = [&](int t) {
#pragma unroll
        for (int f = 0; f < 5; ++f)
          b[t][f] = __builtin_bit_cast(bf16x8, __builtin_amdgcn_raw_buffer_load_b128(rw, (t < 13 ? lane * 16 : vlast) + ((t * 5 + f) & 3) * 1024, sow + ((t * 5 + f) >> 2) * 4096, 0));
      };
#pragma unroll
      for (int t = 0; t < 5; ++t) lda(t);
      sb();
      f32x4 acc[4][5];
      wait_vm<20>();
      const unsigned rb = lb + w * G4_REG + lane * 16;
#pragma unroll
      for (int f = 0; f < 5; ++f) b[0][f] = lds_rd(rb + f * 1024);
#pragma unroll
      for (int t = 0; t < 14; ++t) {
        if (t + 1 < 7) {
#pragma unroll
          for (int f = 0; f < 5; ++f) b[t + 1][f] = lds_rd(rb + ((t + 1) * 5 + f) * 1024);
        }
        sb();
#pragma unroll
        for (int i = 0; i < 4; ++i)
#pragma unroll
          for (int f = 0; f < 5; ++f) { if (t == 0) mma0(acc[i][f], b[0][f], a[0][i]); else mma(acc[i][f], b[t][f], a[t][i]); }
        sb();
        if (t < 3) lda(t + 5);
        if (t == 3) ldb(7);
        if (t == 4) lda(8);
        if (t == 5) ldb(8);
        if (t >= 6 && t + 3 < 14) { lda(t + 3); ldb(t + 3); }
      }
      fence_all(acc);
      ST(sbase + 11);
      small_epilogue(acc, G4_REG, rt, L ? make_rsrc(opq_p(out_), SZ_X) : make_rsrc(ws + WS_X2, SZ_X), L != 0,
                     make_rsrc(ws + WS_ST2, SZ_ST), row64, c8, w, lane);
      ST(sbase + 12);
      if (L) break;
      publish(ws, fl + 2 * FLG_SEAM + j * 4, tid);
    }
    {
      PHASE_IDS
      {
        const u32x4 sd = make_srd(j < 8 ? opq_p(Dre_) : opq_p(Dim_), D_ * D_ * 2);
        const unsigned vo = (lane >> 3) * 1280 + (((lane & 7) ^ (lane >> 3)) << 4);
        unsigned p = w * 25, c = p / 10, g = p - c * 10;
        unsigned so = (c8 * 80 + g * 8) * 1280 + c * 128, l = lb + p * 1024;
#pragma unroll 1
        for (int n = 0; n < 25; ++n) {
          glds_lin<1>(sd, vo, l, so);
          l += 1024;
          if (++g == 10) { g = 0; so += 128 - 9 * 8 * 1280; } else so += 8 * 1280;
        }
      }
      if (tid >= 128 && tid < 208)
        lds_wr1(lb + KM_CSD + (tid - 128) * 4,
                __uint_as_float(__builtin_amdgcn_raw_buffer_load_b32(make_rsrc(ws + WS_CSD, 1280 * 4), (j * 80 + tid - 128) * 4, 0, 16)));
      wait_flags(ws, fl + 2 * FLG_SEAM, 16, w, lane);
      ST(13);
      const rsrc_t rx = make_rsrc(ws + WS_X2, SZ_X);
      const unsigned sox = (P * 8 + w * 2) * 20 * 1024;
      bf16x8 a[20][2];
#pragma unroll
      for (int k = 0; k < 12; ++k) { a[k][0] = LDX(k); a[k][1] = LDX(20 + k); }
      const rsrc_t rst = make_rsrc(ws + WS_ST2, SZ_ST);
      const unsigned sto = (P * 128 + w * 32 + (lane & 31)) * 64 + (lane >> 5) * 32;
      const u32x4 sp0 = ld16u(rst, sto, 0), sp1 = ld16u(rst, sto + 16, 0);
      sb();
      wait_vm<26>();
      wait_lgkm0();
      bar();
      f32x4 acc[2][5];
      bf16x8 b[2][5];
      const unsigned rb = lb + i16 * 128 + ((q ^ (lane & 7)) << 4);
#pragma unroll
      for (int f = 0; f < 5; ++f) b[0][f] = lds_rd(rb + f * 2048);
#pragma unroll
      for (int k = 0; k < 20; ++k) {
        if (k + 1 < 20) {
#pragma unroll
          for (int f = 0; f < 5; ++f) b[(k + 1) & 1][f] = lds_rd((rb ^ (((k + 1) & 1) * 64)) + ((k + 1) >> 1) * 10240 + f * 2048);
        }
        sb();
#pragma unroll
        for (int f = 0; f < 5; ++f)
#pragma unroll
          for (int r = 0; r < 2; ++r) { if (k == 0) mma0(acc[r][f], a[0][r], b[0][f]); else mma(acc[r][f], a[k][r], b[k & 1][f]); }
        sb();
        if (k + 12 < 20) { a[k + 12][0] = LDX(k + 12); a[k + 12][1] = LDX(32 + k); }
      }
      fence_all(acc);
      ST(14);
      float s1 = (__uint_as_float(sp0[0]) + __uint_as_float(sp0[2])) + (__uint_as_float(sp1[0]) + __uint_as_float(sp1[2]));
      float s2 = (__uint_as_float(sp0[1]) + __uint_as_float(sp0[3])) + (__uint_as_float(sp1[1]) + __uint_as_float(sp1[3]));
      auto ta = __builtin_amdgcn_permlane32_swap(__float_as_uint(s1), __float_as_uint(s1), false, false);
      auto tb = __builtin_amdgcn_permlane32_swap(__float_as_uint(s2), __float_as_uint(s2), false, false);
      float m, rr;
      ln_coef(__uint_as_float(ta[0]) + __uint_as_float(ta[1]), __uint_as_float(tb[0]) + __uint_as_float(tb[1]), m, rr);
      float mu[2][4], rs[2][4];
#pragma unroll
      for (int r = 0; r < 2; ++r)
#pragma unroll
        for (int e = 0; e < 4; ++e) { mu[r][e] = __shfl(m, r * 16 + q * 4 + e); rs[r][e] = __shfl(rr, r * 16 + q * 4 + e); }
      g1_store(acc, mu, rs, lb + KM_CSD, make_rsrc(ws + WS_AB2, SZ_AB), P, j, w, i16, q);
      ST(15);
      publish(ws, WS_FLG + 3 * FLG_SEAM + c8 * 256 + ((j >> 3) * 16 + P) * 4, tid);
      G2_CTX
      asm volatile("s_nop 4" ::: "memory");
      g2_prefetch(g2);
      wait_flags(ws, WS_FLG + 3 * FLG_SEAM + c8 * 256, 32, w, lane);
      TM(4);
    }
  }
  wait_vm<0>();
  TM(5);
}
__global__ void k_lead(char* ws_) { TM(6); }
__global__ void k_trail(char* ws_) {
  const rsrc_t r = make_rsrc(ws_ + WS_END, 4096);
  unsigned t7 = (unsigned)__builtin_amdgcn_s_memrealtime();
  if (SEGA != SEGB) {
    unsigned ta = rfl(__builtin_amdgcn_raw_buffer_load_b32(r, 0, SEGA * 4, 16));
    unsigned tb = SEGB == 7 ? t7 : rfl(__builtin_amdgcn_raw_buffer_load_b32(r, 0, SEGB * 4, 16));
    unsigned d = (tb - ta) * 4;
    if (d > 100000) d = 100000;
    while ((unsigned)__builtin_amdgcn_s_memrealtime() - t7 < d) __builtin_amdgcn_s_sleep(1);
  }
}

extern "C" void kernel_launch(void* const* d_in, const int* in_sizes, int n_in, void* d_out, int out_size, void* d_ws,
                              size_t ws_size, hipStream_t stream) {
  const u16* x0 = (const u16*)d_in[0];
  const u16* Sre = (const u16*)d_in[1];
  const u16* Sim = (const u16*)d_in[2];
  const u16* Dre = (const u16*)d_in[3];
  const u16* Dim = (const u16*)d_in[4];
  const u16* w1 = (const u16*)d_in[6];
  const u16* w2 = (const u16*)d_in[7];
  char* ws = (char*)d_ws;
  (void)hipFuncSetAttribute((const void*)k_first, hipFuncAttributeMaxDynamicSharedMemorySize, K1_LDS);
  (void)hipFuncSetAttribute((const void*)k_main, hipFuncAttributeMaxDynamicSharedMemorySize, KM_LDS);
  if (LEAD) k_lead<<<1, 64, 0, stream>>>(ws);
  k_first<<<256, 320, K1_LDS, stream>>>(x0, Dre, Dim, w1, w2, Sre, Sim, ws STAMP_PASS(0));
  k_main<<<256, 256, KM_LDS, stream>>>(x0, Sre, Sim, Dre, Dim, ws, (u16*)d_out STAMP_PASS(1));
  k_trail<<<1, 64, 0, stream>>>(ws);
}
```

```cpp
#include <hip/hip_runtime.h>
#include <hip/hip_bf16.h>
#include <stdint.h>

#define DI __device__ __forceinline__
typedef unsigned short u16;
typedef __attribute__((ext_vector_type(8))) __bf16 bf16x8;
typedef __attribute__((ext_vector_type(2))) __bf16 bf16x2;
typedef __attribute__((ext_vector_type(4))) float f32x4;
typedef __attribute__((ext_vector_type(2))) float f32x2;
typedef __attribute__((ext_vector_type(4))) unsigned u32x4;
typedef __attribute__((ext_vector_type(2))) unsigned u32x2;
typedef __attribute__((ext_vector_type(4))) short s16x4;
#define LDS_AS __attribute__((address_space(3)))
#define GLB_AS __attribute__((address_space(1)))
typedef LDS_AS char* ldsp;
typedef GLB_AS unsigned gu32;
typedef unsigned long long u64;
typedef GLB_AS u64 gu64;
typedef __amdgpu_buffer_rsrc_t rsrc_t;
#define RLX_AGENT __ATOMIC_RELAXED, __HIP_MEMORY_SCOPE_AGENT

constexpr int S_ = 2048, D_ = 640, F_ = 1728;
constexpr float EPS = 1e-5f;
constexpr unsigned OOB = 0x7f000000u;

constexpr unsigned SZ_AB = 8 * 128 * 5 * 1024;
constexpr unsigned SZ_X = 128 * 20 * 1024;
constexpr unsigned SZ_H = 128 * 54 * 1024;
constexpr unsigned SZ_W1 = 108 * 20 * 1024;
constexpr unsigned SZ_W2 = 8 * 54 * 5 * 1024;
constexpr unsigned SZ_ST = 2048 * 64;
constexpr unsigned WS_AB1 = 0;
constexpr unsigned WS_AB2 = WS_AB1 + SZ_AB;
constexpr unsigned WS_X1 = WS_AB2 + SZ_AB;
constexpr unsigned WS_X2 = WS_X1 + SZ_X;
constexpr unsigned WS_X3 = WS_X2 + SZ_X;
constexpr unsigned WS_H1 = WS_X3 + SZ_X;
constexpr unsigned WS_H2 = WS_H1 + SZ_H;
constexpr unsigned WS_W1 = WS_H2 + SZ_H;
constexpr unsigned WS_W2 = WS_W1 + SZ_W1;
constexpr unsigned WS_ST1 = WS_W2 + SZ_W2;
constexpr unsigned WS_ST2 = WS_ST1 + SZ_ST;
constexpr unsigned WS_ST3 = WS_ST2 + SZ_ST;
constexpr unsigned WS_CS1 = WS_ST3 + SZ_ST;
constexpr unsigned WS_CSD = WS_CS1 + 20 * 1728 * 4;
constexpr unsigned XSLOT = 2560;
constexpr unsigned SZ_XP = 256 * 12 * XSLOT;
constexpr unsigned WS_XP = (WS_CSD + 256 * 320 + 4095) & ~4095u;
constexpr unsigned WS_FLG = WS_XP + 2 * SZ_XP;
constexpr unsigned FL_X = 0;
constexpr unsigned FL_2 = FL_X + 2048;
constexpr unsigned FL_3 = FL_2 + 2048;
constexpr unsigned FL_4 = FL_3 + 2048;
constexpr unsigned FL_5 = FL_4 + 1024;
constexpr unsigned FL_W = FL_5 + 2048;
constexpr unsigned FL_E = FL_W + 1088;
constexpr unsigned WS_END = WS_FLG + (FL_E + 1) * 128;

DI u32x4 make_srd(const void* p, unsigned bytes) {
  uint64_t a = (uint64_t)p;
  u32x4 r;
  r[0] = (unsigned)a; r[1] = (unsigned)(a >> 32) & 0xffffu; r[2] = bytes; r[3] = 0x00020000u;
  return r;
}
DI rsrc_t make_rsrc(const void* p, unsigned bytes) {
  return __builtin_amdgcn_make_buffer_rsrc((void*)p, (short)0, (int)bytes, 0x00020000);
}
template <int LOFF, int GOFF>
DI void glds(u32x4 srd, unsigned voff, unsigned ldsb, unsigned soff) {
  unsigned tmp;
  asm volatile("s_add_u32 m0, %1, %2\n\ts_add_u32 %0, %3, %4\n\tbuffer_load_dwordx4 %5, %6, %0 offen lds"
               : "=&s"(tmp)
               : "s"(ldsb), "i"(LOFF), "s"(soff), "i"(GOFF), "v"(voff), "s"(srd)
               : "memory", "scc");
}
template <int N> DI void glds_lin(u32x4 srd, unsigned voff, unsigned ldsb, unsigned soff) {
  if constexpr (N == 4)
    asm volatile("s_mov_b32 m0, %0\n\ts_nop 0\n\tbuffer_load_dwordx4 %1, %2, %3 offen lds\n\tbuffer_load_dwordx4 %1, %2, %3 offen offset:1024 lds\n\t"
                 "buffer_load_dwordx4 %1, %2, %3 offen offset:2048 lds\n\tbuffer_load_dwordx4 %1, %2, %3 offen offset:3072 lds"
                 :: "s"(ldsb), "v"(voff), "s"(srd), "s"(soff) : "memory");
  else if constexpr (N == 3)
    asm volatile("s_mov_b32 m0, %0\n\ts_nop 0\n\tbuffer_load_dwordx4 %1, %2, %3 offen lds\n\tbuffer_load_dwordx4 %1, %2, %3 offen offset:1024 lds\n\t"
                 "buffer_load_dwordx4 %1, %2, %3 offen offset:2048 lds"
                 :: "s"(ldsb), "v"(voff), "s"(srd), "s"(soff) : "memory");
  else
    asm volatile("s_mov_b32 m0, %0\n\ts_nop 0\n\tbuffer_load_dwordx4 %1, %2, %3 offen lds"
                 :: "s"(ldsb), "v"(voff), "s"(srd), "s"(soff) : "memory");
}
template <int IMM> DI void ldv(bf16x8& d, u32x4 srd, unsigned voff, unsigned soff) {
  asm volatile("buffer_load_dwordx4 %0, %1, %2, %3 offen offset:%4 sc1" : "=v"(d) : "v"(voff), "s"(srd), "s"(soff), "i"(IMM) : "memory");
}
template <int N> DI void wait_vm() { asm volatile("s_waitcnt vmcnt(%0)" ::"i"(N) : "memory"); }
template <int N> DI void wait_vm5(bf16x8 (&b)[5]) {
  asm volatile("s_waitcnt vmcnt(%5)" : "+v"(b[0]), "+v"(b[1]), "+v"(b[2]), "+v"(b[3]), "+v"(b[4]) : "i"(N) : "memory");
}
DI void wait_lgkm0() { asm volatile("s_waitcnt lgkmcnt(0)" ::: "memory"); }
DI void sb() { __builtin_amdgcn_sched_barrier(0); }
DI void bar() { asm volatile("s_barrier" ::: "memory"); }

DI bf16x8 lds_rd(unsigned addr) { return *(const LDS_AS bf16x8*)(ldsp)(uintptr_t)addr; }
DI f32x4 lds_rdf(unsigned addr) { return *(const LDS_AS f32x4*)(ldsp)(uintptr_t)addr; }
DI void lds_wrf(unsigned addr, f32x4 v) { *(LDS_AS f32x4*)(ldsp)(uintptr_t)addr = v; }
DI float lds_rd1(unsigned addr) { return *(const LDS_AS float*)(ldsp)(uintptr_t)addr; }
DI void lds_wr1(unsigned addr, float v) { *(LDS_AS float*)(ldsp)(uintptr_t)addr = v; }
DI f32x4 mfma(bf16x8 a, bf16x8 b, f32x4 c) { return __builtin_amdgcn_mfma_f32_16x16x32_bf16(a, b, c, 0, 0, 0); }
DI void mma(f32x4& c, const bf16x8& a, const bf16x8& b) {
  asm volatile("v_mfma_f32_16x16x32_bf16 %0, %1, %2, %0" : "+v"(c) : "v"(a), "v"(b));
}
DI void mma0(f32x4& c, const bf16x8& a, const bf16x8& b) {
  asm volatile("v_mfma_f32_16x16x32_bf16 %0, %1, %2, 0" : "=&v"(c) : "v"(a), "v"(b));
}
template <int N> DI void fence_acc(f32x4 (&a)[N]) {
  if constexpr (N == 5) asm volatile("s_nop 7\n\ts_nop 1" : "+v"(a[0]), "+v"(a[1]), "+v"(a[2]), "+v"(a[3]), "+v"(a[4]));
  else asm volatile("s_nop 7\n\ts_nop 1" : "+v"(a[0]), "+v"(a[1]), "+v"(a[2]), "+v"(a[3]), "+v"(a[4]), "+v"(a[5]), "+v"(a[6]));
}
template <int N> DI void pin_acc(f32x4 (&a)[N]) {
  if constexpr (N == 5) asm volatile("" : "+v"(a[0]), "+v"(a[1]), "+v"(a[2]), "+v"(a[3]), "+v"(a[4]));
  else asm volatile("" : "+v"(a[0]), "+v"(a[1]), "+v"(a[2]), "+v"(a[3]), "+v"(a[4]), "+v"(a[5]), "+v"(a[6]));
}
template <int M, int N> DI void fence_all(f32x4 (&a)[M][N]) {
#pragma unroll
  for (int i = 1; i < M; ++i) pin_acc(a[i]);
  fence_acc(a[0]);
#pragma unroll
  for (int i = 1; i < M; ++i) pin_acc(a[i]);
}
DI unsigned pk2(float a, float b) {
  f32x2 v = {a, b};
  return __builtin_bit_cast(unsigned, __builtin_convertvector(v, bf16x2));
}
DI float bflo(unsigned u) { return __uint_as_float(u << 16); }
DI float bfhi(unsigned u) { return __uint_as_float(u & 0xffff0000u); }
DI float dot2(unsigned a, unsigned b, float c) {
  return __builtin_amdgcn_fdot2_f32_bf16(__builtin_bit_cast(bf16x2, a), __builtin_bit_cast(bf16x2, b), c, false);
}
DI float xsum16_32(float x) {
  auto s = __builtin_amdgcn_permlane16_swap(__float_as_uint(x), __float_as_uint(x), false, false);
  x = __uint_as_float(s[0]) + __uint_as_float(s[1]);
  auto t = __builtin_amdgcn_permlane32_swap(__float_as_uint(x), __float_as_uint(x), false, false);
  return __uint_as_float(t[0]) + __uint_as_float(t[1]);
}
template <int V> struct IC { static constexpr int v = V; };
template <int I, int N, class Fn> DI void sfor(Fn&& f) {
  if constexpr (I < N) { f(IC<I>{}); sfor<I + 1, N>(f); }
}
DI unsigned rfl(unsigned v) { return __builtin_amdgcn_readfirstlane(v); }
DI unsigned opq_v(unsigned x) { asm volatile("" : "+v"(x)); return x; }
DI unsigned opq_s(unsigned x) { asm volatile("" : "+s"(x)); return x; }
template <class T> DI T* opq_p(T* x) { asm volatile("" : "+s"(x)); return x; }
DI bf16x8 ld16(rsrc_t r, unsigned voff, unsigned soff) { return __builtin_bit_cast(bf16x8, __builtin_amdgcn_raw_buffer_load_b128(r, voff, soff, 16)); }
DI u32x4 ld16u(rsrc_t r, unsigned voff, unsigned soff) { return __builtin_amdgcn_raw_buffer_load_b128(r, voff, soff, 16); }
DI u32x2 ld8u(rsrc_t r, unsigned voff, unsigned soff) { return __builtin_amdgcn_raw_buffer_load_b64(r, voff, soff, 16); }
DI void st16(u32x4 v, rsrc_t r, unsigned voff, unsigned soff) {
  __builtin_amdgcn_raw_buffer_store_b128(v, r, voff, soff, 16);
  asm volatile("s_nop 1" :: "v"(v) : "memory");
}
DI void st8(u32x2 v, rsrc_t r, unsigned voff, unsigned soff) { __builtin_amdgcn_raw_buffer_store_b64(v, r, voff, soff, 16); }
DI void st4(unsigned v, rsrc_t r, unsigned voff, unsigned soff) { __builtin_amdgcn_raw_buffer_store_b32(v, r, voff, soff, 16); }

#define ST(i)
#define STAMP_ARGS
#define STAMP_FWD
#define STAMP_PASS(k)

template <int KS> DI unsigned foff(unsigned row, unsigned col) {
  return ((row >> 4) * KS + (col >> 5)) * 1024u + (((col >> 3) & 3) * 16u + (row & 15)) * 16u + (col & 7) * 2u;
}

extern __shared__ __attribute__((aligned(1024))) char smem[];
DI unsigned lds_base() { return (unsigned)(uintptr_t)(ldsp)smem; }

DI void ln_coef(float s1, float s2, float& mu, float& rs) {
  mu = s1 * (1.f / D_);
  rs = __builtin_amdgcn_rsqf(fmaxf(s2 * (1.f / D_) - mu * mu, 0.f) + EPS);
}

DI void g1_store(const f32x4 (&acc)[2][5], const float (&mu)[2][4], const float (&rs)[2][4], const float (&csv)[5], rsrc_t rab,
                 unsigned P, unsigned ct, unsigned w, unsigned i16, unsigned q) {
  const unsigned kstep = (ct >> 3) * 64 + P * 4 + w;
  const unsigned cq = ((q & 1) << 1) | (q >> 1);
  const unsigned so = ((ct & 7) * 128 + kstep) * 5 * 1024;
  const unsigned vo = (cq * 16 + i16) * 16;
#pragma unroll
  for (int f = 0; f < 5; ++f) {
    const float cs = csv[f];
    unsigned pk[2][2];
#pragma unroll
    for (int r = 0; r < 2; ++r) {
      float t[4];
#pragma unroll
      for (int j = 0; j < 4; ++j) t[j] = rs[r][j] * fmaf(mu[r][j], cs, acc[r][f][j]);
      pk[r][0] = pk2(t[0], t[1]); pk[r][1] = pk2(t[2], t[3]);
    }
    auto t0 = __builtin_amdgcn_permlane16_swap(pk[0][0], pk[1][0], false, false);
    auto t1 = __builtin_amdgcn_permlane16_swap(pk[0][1], pk[1][1], false, false);
    st16(u32x4{t0[0], t1[0], t0[1], t1[1]}, rab, vo + f * 1024, so);
  }
}

constexpr int K1_STAGE = 26 * 1024;
constexpr int K1_TR = 5 * K1_STAGE;
constexpr int K1_CS = K1_TR + 5 * 4096;

DI void k1_issue(u32x4 sa, u32x4 sbd, unsigned vo, unsigned la, unsigned lbb, unsigned soa, unsigned sob, unsigned w) {
  glds<0, 0>(sa, vo, la, soa);
  glds<1024, 8 * 1280>(sa, vo, la, soa);
  glds<2048, 16 * 1280>(sa, vo, la, soa);
  glds<3072, 24 * 1280>(sa, vo, la, soa);
  glds<0, 0>(sbd, vo, lbb, sob);
  glds<4096, 32 * 1280>(sbd, vo, lbb, sob);
  if (w < 2) glds<8192, 64 * 1280>(sbd, vo, lbb, sob);
}
struct RtUnit { bool first; unsigned ks, g; };
DI RtUnit rt_unit(unsigned u) {
  RtUnit r;
  r.first = u < 540;
  if (r.first) { r.ks = u / 27u; r.g = u - r.ks * 27u; }
  else { const unsigned v = u - 540; r.ks = v / 10u; r.g = v - r.ks * 10u; }
  return r;
}
DI void retile_issue(unsigned u, const u16* w1, const u16* w2, unsigned ldst, unsigned lane) {
  const RtUnit t = rt_unit(u);
  const unsigned ld = t.first ? F_ * 2 : D_ * 2;
  u32x4 srd = make_srd(t.first ? w1 : w2, D_ * F_ * 2);
  const unsigned vo = (lane >> 3) * ld + (lane & 7) * 16;
  const unsigned so = t.ks * 32 * ld + t.g * 128;
  glds<0, 0>(srd, vo, ldst, so);
  glds<1024, 0>(srd, vo, ldst, so + 8 * ld);
  glds<2048, 0>(srd, vo, ldst, so + 16 * ld);
  glds<3072, 0>(srd, vo, ldst, so + 24 * ld);
}
DI void retile_finish(unsigned u, unsigned f0, rsrc_t rws, unsigned ldst, unsigned lane) {
  const RtUnit t = rt_unit(u);
  const unsigned i = lane & 15, q = lane >> 4;
  const unsigned base = ldst + (8 * q + (i >> 2)) * 128 + (i & 3) * 8 + f0 * 32;
  u32x4 v[2];
#pragma unroll
  for (int f = 0; f < 2; ++f) {
    s16x4 lo = __builtin_amdgcn_ds_read_tr16_b64_v4i16((LDS_AS s16x4*)(uintptr_t)(base + f * 32));
    s16x4 hi = __builtin_amdgcn_ds_read_tr16_b64_v4i16((LDS_AS s16x4*)(uintptr_t)(base + f * 32 + 512));
    u32x2 l2 = __builtin_bit_cast(u32x2, lo), h2 = __builtin_bit_cast(u32x2, hi);
    v[f] = u32x4{l2[0], l2[1], h2[0], h2[1]};
  }
  const unsigned rt = t.g * 4 + f0;
  if (t.first) {
    const unsigned so = WS_W1 + (rt * 20 + t.ks) * 1024;
    st16(v[0], rws, lane * 16, so);
    st16(v[1], rws, lane * 16, so + 20 * 1024);
    float s[2];
#pragma unroll
    for (int f = 0; f < 2; ++f)
      s[f] = xsum16_32(((bflo(v[f][0]) + bfhi(v[f][0])) + (bflo(v[f][1]) + bfhi(v[f][1]))) + ((bflo(v[f][2]) + bfhi(v[f][2])) + (bflo(v[f][3]) + bfhi(v[f][3]))));
    st4(__float_as_uint(q == 0 ? s[0] : s[1]), rws, lane < 32 ? lane * 4 : OOB, WS_CS1 + (t.ks * F_ + rt * 16) * 4);
  } else {
    const unsigned c0 = rt / 5u, c1 = (rt + 1) / 5u;
    st16(v[0], rws, lane * 16, WS_W2 + ((c0 * 54 + t.ks) * 5 + (rt - c0 * 5)) * 1024);
    st16(v[1], rws, lane * 16, WS_W2 + ((c1 * 54 + t.ks) * 5 + (rt + 1 - c1 * 5)) * 1024);
  }
}

#define WSLEEP 30
constexpr int KM_LDS = 160 * 1024;
constexpr int KM_CS1 = KM_LDS - 1024;
constexpr int G2_SLOT = 36 * 1024, G4_REG = 35 * 1024;

DI gu64* flag_ptr(char* ws, unsigned f) { return (gu64*)(ws + WS_FLG + f * 128); }
DI void raise(char* ws, unsigned f, u64 epoch, unsigned lane) {
  if (lane == 0) __hip_atomic_store(flag_ptr(ws, f), epoch, RLX_AGENT);
}
DI void publish(char* ws, unsigned f, u64 epoch, unsigned lane) {
  wait_vm<0>();
  raise(ws, f, epoch, lane);
}
DI void poll(char* ws, unsigned f, u64 epoch) {
  gu64* p = flag_ptr(ws, f);
#pragma unroll 1
  for (unsigned s = 0; s < (1u << 16); ++s) {
    if (__all(__hip_atomic_load(p, RLX_AGENT) == epoch)) break;
    __builtin_amdgcn_s_sleep(1);
  }
  asm volatile("" ::: "memory");
}
DI void warm(unsigned& sink, u32x4 srd, unsigned voff, unsigned soff) {
  asm volatile("buffer_load_dword %0, %1, %2, %3 offen" : "+v"(sink) : "v"(voff), "s"(srd), "s"(soff) : "memory");
}
template <int N> DI void glds_run(u32x4 srd, unsigned voff, unsigned l, unsigned so) {
#pragma unroll
  for (int g = 0; g < N / 4; ++g) glds_lin<4>(srd, voff, l + g * 4096, so + g * 4096);
  if constexpr (N % 4 == 3) glds_lin<3>(srd, voff, l + (N / 4) * 4096, so + (N / 4) * 4096);
  else {
#pragma unroll
    for (int g = N / 4 * 4; g < N; ++g) glds_lin<1>(srd, voff, l + g * 1024, so + g * 1024);
  }
}
DI void glds4_sc1(u32x4 srd, unsigned voff, unsigned l, unsigned so) {
  asm volatile("s_mov_b32 m0, %0\n\ts_nop 0\n\tbuffer_load_dwordx4 %1, %2, %3 offen sc1 lds\n\tbuffer_load_dwordx4 %1, %2, %3 offen offset:1024 sc1 lds\n\t"
               "buffer_load_dwordx4 %1, %2, %3 offen offset:2048 sc1 lds\n\tbuffer_load_dwordx4 %1, %2, %3 offen offset:3072 sc1 lds"
               :: "s"(l), "v"(voff), "s"(srd), "s"(so) : "memory");
}
DI void glds1_sc1(u32x4 srd, unsigned voff, unsigned l, unsigned so) {
  asm volatile("s_mov_b32 m0, %0\n\ts_nop 0\n\tbuffer_load_dwordx4 %1, %2, %3 offen sc1 lds" :: "s"(l), "v"(voff), "s"(srd), "s"(so) : "memory");
}
template <int N> DI void glds_run_sc1(u32x4 srd, unsigned voff, unsigned l, unsigned so) {
#pragma unroll
  for (int g = 0; g < N / 4; ++g) glds4_sc1(srd, voff, l + g * 4096, so + g * 4096);
#pragma unroll
  for (int g = N / 4 * 4; g < N; ++g) glds1_sc1(srd, voff, l + g * 1024, so + g * 1024);
}

struct ResTile { u32x4 r0, r1; u32x2 r2; };
struct TileOff { unsigned a0, a1, a2; };
DI TileOff tile_offsets(bool rm, unsigned row, unsigned ct, unsigned q) {
  const unsigned cA = ct * 80 + 16 * (q & 1) + 8 * (q >> 1), cS = ct * 80 + 64 + 4 * q;
  TileOff t;
  if (rm) { t.a0 = (row * D_ + cA) * 2; t.a1 = t.a0 + 64; t.a2 = (row * D_ + cS) * 2; }
  else { t.a0 = foff<20>(row, cA); t.a1 = foff<20>(row, cA + 32); t.a2 = foff<20>(row, cS); }
  asm volatile("" : "+v"(t.a0), "+v"(t.a1), "+v"(t.a2));
  return t;
}
DI ResTile load_res(rsrc_t rr, const TileOff& t) {
  ResTile rt;
  rt.r0 = ld16u(rr, t.a0, 0); rt.r1 = ld16u(rr, t.a1, 0); rt.r2 = ld8u(rr, t.a2, 0);
  return rt;
}
DI void finish_tile(f32x4 (&v)[5], const ResTile& rt, rsrc_t rout, const TileOff& o, bool stats, rsrc_t rst, unsigned sto) {
#pragma unroll
  for (int p = 0; p < 2; ++p)
#pragma unroll
    for (int r = 0; r < 4; ++r) {
      auto t = __builtin_amdgcn_permlane16_swap(__float_as_uint(v[2 * p][r]), __float_as_uint(v[2 * p + 1][r]), false, false);
      v[2 * p][r] = __uint_as_float(t[0]); v[2 * p + 1][r] = __uint_as_float(t[1]);
    }
  u32x4 y0, y1; u32x2 y2;
#pragma unroll
  for (int j = 0; j < 2; ++j) {
    y0[j] = pk2(v[0][2 * j] + bflo(rt.r0[j]), v[0][2 * j + 1] + bfhi(rt.r0[j]));
    y0[2 + j] = pk2(v[1][2 * j] + bflo(rt.r0[2 + j]), v[1][2 * j + 1] + bfhi(rt.r0[2 + j]));
    y1[j] = pk2(v[2][2 * j] + bflo(rt.r1[j]), v[2][2 * j + 1] + bfhi(rt.r1[j]));
    y1[2 + j] = pk2(v[3][2 * j] + bflo(rt.r1[2 + j]), v[3][2 * j + 1] + bfhi(rt.r1[2 + j]));
    y2[j] = pk2(v[4][2 * j] + bflo(rt.r2[j]), v[4][2 * j + 1] + bfhi(rt.r2[j]));
  }
  st16(y0, rout, o.a0, 0); st16(y1, rout, o.a1, 0); st8(y2, rout, o.a2, 0);
  if (stats) {
    float s1a = 0.f, s1b = 0.f, s2a = 0.f, s2b = 0.f;
#pragma unroll
    for (int j = 0; j < 4; ++j) {
      s1a = dot2(y0[j], 0x3f803f80u, s1a); s2a = dot2(y0[j], y0[j], s2a);
      s1b = dot2(y1[j], 0x3f803f80u, s1b); s2b = dot2(y1[j], y1[j], s2b);
    }
    s1a = dot2(y2[0], 0x3f803f80u, s1a); s2a = dot2(y2[0], y2[0], s2a);
    s1b = dot2(y2[1], 0x3f803f80u, s1b); s2b = dot2(y2[1], y2[1], s2b);
    const float s1 = xsum16_32(s1a + s1b), s2 = xsum16_32(s2a + s2b);
    st8(u32x2{__float_as_uint(s1), __float_as_uint(s2)}, rst, sto, 0);
  }
}

template <int W> DI void sm_scatter(unsigned wb, const f32x4 (&acc)[4][5], f32x4 (&own)[5]) {
#pragma unroll
  for (int d = 0; d < 4; ++d)
#pragma unroll
    for (int f = 0; f < 5; ++f) {
      if (d == W) own[f] = acc[d][f];
      else lds_wrf(wb + (((d - W - 1) & 3) * 5 + f) * 1024, acc[d][f]);
    }
  asm volatile("; scatter %5" : "+v"(own[0]), "+v"(own[1]), "+v"(own[2]), "+v"(own[3]), "+v"(own[4]) : "i"(W));
}
template <int KQ> DI void xp_send(const f32x4 (&acc)[4][5], f32x4 (&own)[5], rsrc_t rx, unsigned so, unsigned lane) {
#pragma unroll
  for (int d = 0; d < 4; ++d) {
    if (d == KQ) {
#pragma unroll
      for (int f = 0; f < 5; ++f) own[f] = acc[d][f];
    } else {
      const unsigned o = so + (d * 3 + ((KQ - d - 1) & 3)) * 4 * XSLOT;
      unsigned p[10];
#pragma unroll
      for (int f = 0; f < 5; ++f) { p[2 * f] = pk2(acc[d][f][0], acc[d][f][1]); p[2 * f + 1] = pk2(acc[d][f][2], acc[d][f][3]); }
      st16(u32x4{p[0], p[1], p[2], p[3]}, rx, lane * 16, o);
      st16(u32x4{p[4], p[5], p[6], p[7]}, rx, lane * 16 + 1024, o);
      st8(u32x2{p[8], p[9]}, rx, lane * 8 + 2048, o);
    }
  }
  asm volatile("; send %5" : "+v"(own[0]), "+v"(own[1]), "+v"(own[2]), "+v"(own[3]), "+v"(own[4]) : "i"(KQ));
}

DI constexpr unsigned wd_pos(int c) { return c < 8 ? 35 * (c >> 1) + 15 + 10 * (c & 1) : 140 + 10 * (c - 8); }
DI constexpr int a_when(int k) { return k < 3 ? -1 : (k < 17 ? (k - 3) / 2 : k - 10); }

#define PHASE_IDS \
  const unsigned lane = opq_v(tid0) & 63, w = opq_s(w0), bid = opq_s(blockIdx.x); \
  const unsigned xcd = bid & 7, idx = bid >> 3, i16 = lane & 15, q = lane >> 4; \
  char* const ws = opq_p(ws_); \
  (void)xcd; (void)idx; (void)i16; (void)q;
#define IDS_G2 const unsigned kq = xcd & 3, P = (xcd >> 2) * 8 + (idx >> 2), c4 = idx & 3;
#define IDS_G3 const unsigned P = (xcd >> 1) * 4 + (idx >> 3), ct = (xcd & 1) * 8 + (idx & 7);
#define IDS_G4 const unsigned rt64 = xcd * 4 + (idx >> 3), c8 = idx & 7;
#define WARM_STRIP(rx, sox) \
  ({ const unsigned n_ = (idx & 7) * 40 + lane; \
     __builtin_amdgcn_raw_buffer_load_b32(rx, lane < 40 ? (((n_ >> 3) & 1) * 20 + (n_ >> 4)) * 1024 + (n_ & 7) * 128 : OOB, sox, 16); })
#define LDX(n) ld16(rx, lane * 16 + ((n) & 3) * 1024, sox + ((n) >> 2) * 4096)

__global__ __launch_bounds__(512, 2) void k_all(const u16* x0_, const u16* Sre_, const u16* Sim_, const u16* Dre_, const u16* Dim_,
                                               const u16* w1_, const u16* w2_, char* ws_, u16* out_ STAMP_ARGS) {
  ST(0);
  const unsigned tid0 = threadIdx.x, wall = rfl(tid0 >> 6), w0 = wall & 3;
  const unsigned lb = lds_base();
  u64 epoch;

  if (wall >= 4) {
    unsigned sink = 0;
    unsigned ep0, ep1;
    asm volatile("global_load_dword %0, %2, %3 sc1\n\tglobal_load_dword %1, %2, %3 offset:4 sc1"
                 : "=&v"(ep0), "=&v"(ep1) : "v"(WS_FLG + FL_E * 128), "s"(ws_) : "memory");
    {
      PHASE_IDS
      IDS_G3
      const u32x4 sa = make_srd(opq_p(x0_), S_ * D_ * 2), sbd = make_srd(ct < 8 ? opq_p(Dre_) : opq_p(Dim_), D_ * D_ * 2);
      const unsigned vo = (lane >> 3) * 1280 + (((lane & 7) ^ (lane >> 3)) << 4);
      const unsigned soa = (P * 128 + w * 32) * 1280, sob = ((ct & 7) * 80 + w * 8) * 1280;
      const unsigned la = lb + w * 4096, lbb = lb + 16384 + w * 1024;
      const unsigned u0 = bid * 4 + w, u1 = 1024 + bid;
      const bool extra = w == 3 && bid < 56;
      const rsrc_t rws = make_rsrc(ws, WS_END);
      asm volatile("s_nop 4" ::: "memory");
      {
        const unsigned r0 = lane / 10u, id = ((idx >> 3) * 4 + w) * 50 + lane, r1 = id / 10u;
        warm(sink, sa, lane < 40 ? r0 * 1280 + (lane - r0 * 10) * 128 : OOB, (P * 128 + (idx & 7) * 16 + w * 4) * 1280);
        warm(sink, sbd, lane < 50 ? r1 * 1280 + (id - r1 * 10) * 128 : OOB, (ct & 7) * 80 * 1280);
      }
#pragma unroll
      for (int s = 0; s < 5; ++s) k1_issue(sa, sbd, vo, la + s * K1_STAGE, lbb + s * K1_STAGE, soa + s * 128, sob + s * 128, w);
      retile_issue(u0, w1_, w2_, lb + K1_TR + w * 4096, lane);
      if (extra) retile_issue(u1, w1_, w2_, lb + K1_TR + 4 * 4096, lane);
      ST(8);
      wait_vm<4 * 6 + 4>();
      {
        unsigned lo, hi;
        asm volatile("v_readfirstlane_b32 %0, %2\n\tv_readfirstlane_b32 %1, %3\n\ts_nop 4" : "=s"(lo), "=s"(hi) : "v"(ep0), "v"(ep1));
        epoch = (u64)lo + ((u64)hi << 32) + 1;
      }
      bar();
      unsigned so = 0;
#pragma unroll 1
      for (int i = 0; i < 10; ++i) {
        if (i < 4) wait_vm<3 * 6 + 4>(); else if (i < 6) wait_vm<3 * 6>(); else if (i == 6) wait_vm<2 * 6 + 1>(); else if (i == 7) wait_vm<6 + 1>(); else wait_vm<1>();
        bar();
        if (i < 5) k1_issue(sa, sbd, vo, la + so, lbb + so, soa + (i + 5) * 128, sob + (i + 5) * 128, w);
        so = so == 4 * K1_STAGE ? 0 : so + K1_STAGE;
        if (i == 4) {
          retile_finish(u0, 0, rws, lb + K1_TR + w * 4096, lane);
          retile_finish(u0, 2, rws, lb + K1_TR + w * 4096, lane);
        }
        if (i == 5) {
          if (extra) {
            retile_finish(u1, 0, rws, lb + K1_TR + 4 * 4096, lane);
            retile_finish(u1, 2, rws, lb + K1_TR + 4 * 4096, lane);
          }
          warm(sink, make_srd((xcd & 2) ? Sim_ : Sre_, S_ * S_ * 2), (lane & 31) * 4096 + (lane >> 5) * 128,
               ((xcd >> 2) * 1024 + idx * 32) * 4096 + (xcd & 1) * 2048 + w * 256);
        }
        if (i == 9) {
          raise(ws, FL_W + u0, epoch, lane);
          if (extra) raise(ws, FL_W + u1, epoch, lane);
        }
      }
      ST(9);
      bar();
    }
#pragma unroll 1
    for (int L = 0; L < 2; ++L) {
      const int sbase = L * 20;
      (void)sbase;
      {
        PHASE_IDS
        IDS_G2
        const u32x4 sa = make_srd(kq < 2 ? opq_p(Sre_) : opq_p(Sim_), S_ * S_ * 2);
        const u32x4 sb_ = make_srd(ws + (L ? WS_AB2 : WS_AB1), SZ_AB);
        const unsigned va = (lane >> 3) * 4096 + (((lane & 7) ^ (lane >> 3)) << 4), vb = lane * 16;
        const unsigned soa = (P * 128 + w * 32) * 4096 + (kq & 1) * 2048;
        const unsigned sob = ((2 * c4 + (w >> 1)) * 128 + kq * 32 + (w & 1)) * 5 * 1024;
        const unsigned la = lb + w * 4096, lbb = lb + 16384 + w * 5120;
        const unsigned ct = (xcd & 1) * 8 + (idx & 7);
        asm volatile("s_nop 4" ::: "memory");
        auto issue_a = [&](unsigned l, unsigned so) {
          glds<0, 0>(sa, va, l, so); glds<1024, 8 * 4096>(sa, va, l, so);
          glds<2048, 16 * 4096>(sa, va, l, so); glds<3072, 24 * 4096>(sa, va, l, so);
        };
#pragma unroll
        for (int s = 0; s < 4; ++s) issue_a(la + s * G2_SLOT, soa + s * 128);
        ST(sbase + 1);
        poll(ws, FL_5 + ((L * 4 + kq) * 4 + c4) * 64 + (((i16 >> 1) * 2 + (w >> 1)) * 4 + (w & 1) + 2 * (i16 & 1)), epoch);
        ST(sbase + 2);
#pragma unroll
        for (int s = 0; s < 4; ++s) glds_run_sc1<5>(sb_, vb, lbb + s * G2_SLOT, sob + s * 10240);
        wait_vm<15>();
        bar();
        ST(sbase + 3);
        const u32x4 sw = make_srd(ws + WS_W1, SZ_W1);
        const unsigned sow = ct * 140 * 1024;
        unsigned fw, fw0 = 0, fw1 = 0;
        {
          const unsigned n = w * 42 + (lane < 42 ? lane : 0), k3 = n / 3u, g3 = (7 * ct >> 2) + (n - k3 * 3), n2 = n - 60;
          fw = FL_W + (n < 60 ? k3 * 27 + (g3 < 26 ? g3 : 26) : 540 + (n2 >> 1) * 10 + (5 * (idx & 7) >> 2) + (n2 & 1));
        }
#pragma unroll 1
        for (int s = 0; s < 16; ++s) {
          if (s == 0) wait_vm<10>();
          if (s == 1) wait_vm<14>();
          wait_vm<18>();
          if (s == 4 && L == 0) {
            asm volatile("" : "+v"(fw0), "+v"(fw1));
            if (!__all(fw0 == (unsigned)epoch && fw1 == (unsigned)(epoch >> 32))) poll(ws, fw, epoch);
          }
          bar();
          const unsigned slot = (s & 3) * G2_SLOT;
          if (s < 12) {
            issue_a(la + slot, soa + (s + 4) * 128);
            glds_run_sc1<5>(sb_, vb, lbb + slot, sob + (s + 4) * 10240);
            if (s == 0 && L == 0)
              warm(sink, sa, (lane & 31) * 4096 + (lane >> 5) * 128, ((xcd >> 2) * 1024 + idx * 32) * 4096 + (xcd & 1) * 2048 + 1024 + w * 256);
            if (s == 1 && L == 0)
              asm volatile("global_load_dword %0, %2, %3 sc1\n\tglobal_load_dword %1, %2, %3 offset:4 sc1"
                           : "+v"(fw0), "+v"(fw1) : "v"(WS_FLG + fw * 128), "s"(ws) : "memory");
            if (s == 5) warm(sink, sw, lane * 128, sow + ((idx >> 3) * 4 + w) * 70 * 128);
          } else if (s < 15) glds_run_sc1<9>(sw, vb, lb + slot + w * 9216, sow + slot + w * 9216);
          else glds_run_sc1<8>(sw, vb, lb + slot + w * 8192, sow + slot + w * 8192);
        }
        ST(sbase + 4);
      }
      {
        PHASE_IDS
        IDS_G3
        if (w >= 2) {
          const rsrc_t rc = make_rsrc(ws + WS_CS1, 20 * F_ * 4);
          const unsigned c = (w - 2) * 64 + lane, col = ct * 112 + c;
          const unsigned vo = c < 112 && col < F_ ? col * 4 : OOB;
          float csp = 0.f;
#pragma unroll
          for (int k = 0; k < 20; ++k) csp += __uint_as_float(__builtin_amdgcn_raw_buffer_load_b32(rc, vo, k * F_ * 4, 16));
          lds_wr1(lb + KM_CS1 + c * 4, csp);
        }
        wait_vm<0>();
        wait_lgkm0();
        ST(sbase + 5);
        bar();
        __builtin_amdgcn_s_sleep(WSLEEP);
        {
          const unsigned ks0 = w < 2 ? w * 14 : 28 + (w - 2) * 13;
          warm(sink, make_srd(ws + WS_W2, SZ_W2), lane < 50 ? lane * 128 : OOB, ((idx & 7) * 54 + ks0 + 9) * 5 * 1024 + (idx >> 3) * 50 * 128);
        }
        if (L == 0) {
          const unsigned id = ((idx >> 3) * 4 + w) * 50 + lane, row = id / 10u;
          warm(sink, make_srd(ct < 8 ? opq_p(Dre_) : opq_p(Dim_), D_ * D_ * 2), lane < 50 ? row * 1280 + (id - row * 10) * 128 : OOB, (ct & 7) * 80 * 1280);
        }
        bar();
      }
      {
        PHASE_IDS
        IDS_G4
        const unsigned ks0 = w < 2 ? w * 14 : 28 + (w - 2) * 13;
        glds_run_sc1<35>(make_srd(ws + WS_W2, SZ_W2), lane * 16, lb + w * G4_REG, (c8 * 54 + ks0) * 5 * 1024);
        wait_vm<0>();
        ST(sbase + 6);
        bar();
        bar();
      }
      if (L) break;
      {
        PHASE_IDS
        IDS_G3
        const u32x4 sd = make_srd(ct < 8 ? opq_p(Dre_) : opq_p(Dim_), D_ * D_ * 2);
        const unsigned vo = (lane >> 3) * 1280 + (((lane & 7) ^ (lane >> 3)) << 4);
        const unsigned so = (ct & 7) * 80 * 1280;
        sfor<0, 10>([&](auto G) { glds<G.v * 1024, G.v * 8 * 1280>(sd, vo, lb + (35 * w + 15) * 1024, so + 2 * w * 128); });
        sfor<0, 10>([&](auto G) { glds<G.v * 1024, G.v * 8 * 1280>(sd, vo, lb + (35 * w + 25) * 1024, so + (2 * w + 1) * 128); });
        sfor<0, 5>([&](auto G) { glds<G.v * 1024, G.v * 8 * 1280>(sd, vo, lb + (140 + w * 5) * 1024, so + (8 + (w >> 1)) * 128 + (w & 1) * 40 * 1280); });
        wait_vm<0>();
        ST(7);
        bar();
        __builtin_amdgcn_s_sleep(25);
        warm(sink, make_srd((xcd & 2) ? opq_p(Sim_) : opq_p(Sre_), S_ * S_ * 2), (lane & 31) * 4096 + (lane >> 5) * 128,
             ((xcd >> 2) * 1024 + idx * 32) * 4096 + (xcd & 1) * 2048 + w * 256);
        bar();
      }
    }
    asm volatile("" :: "v"(sink));
    return;
  }

  {
    const u64 e = __hip_atomic_load(flag_ptr(ws_, FL_E), RLX_AGENT);
    epoch = (u64)rfl((unsigned)e) + ((u64)rfl((unsigned)(e >> 32)) << 32) + 1;
  }
#pragma unroll 1
  for (int L = 0; L < 2; ++L) {
    const int sbase = L * 20;
    (void)sbase;
    {
      PHASE_IDS
      IDS_G3
      f32x4 acc[2][5];
      float mu[2][4], rs[2][4], csv[5];
      const float sign = ct < 8 ? 1.f : -1.f;
      if (L == 0) {
        const unsigned ra0 = lb + w * 4096 + i16 * 128 + ((q ^ (lane & 7)) << 4);
        const unsigned rb0 = lb + 16384 + i16 * 128 + ((q ^ (lane & 7)) << 4);
        const f32x4 z4 = {0.f, 0.f, 0.f, 0.f};
        f32x4 cs0 = z4, cs1 = z4, rs_[2] = {z4, z4}, rq_[2] = {z4, z4};
#pragma unroll
        for (int r = 0; r < 2; ++r)
#pragma unroll
          for (int f = 0; f < 5; ++f) acc[r][f] = z4;
        bf16x8 fr[2][8];
        bf16x8 one = __builtin_bit_cast(bf16x8, u32x4{0x3f803f80u, 0x3f803f80u, 0x3f803f80u, 0x3f803f80u});
        auto half = [&](auto KK, unsigned so) {
          constexpr int kk = KK.v;
          const unsigned pa = (ra0 ^ ((kk ^ 1) * 64)) + so, pb = (rb0 ^ ((kk ^ 1) * 64)) + so;
          sfor<0, 16>([&](auto M) {
            constexpr int m = M.v;
            if constexpr (m < 10) mma(acc[m & 1][m >> 1], fr[kk][m & 1], fr[kk][2 + (m >> 1)]);
            else if constexpr (m == 10) mma(cs0, one, fr[kk][7]);
            else if constexpr (m == 11) mma(cs1, one, fr[kk][6]);
            else if constexpr (m < 14) mma(rs_[m - 12], fr[kk][m - 12], one);
            else mma(rq_[m - 14], fr[kk][m - 14], fr[kk][m - 14]);
            constexpr int n = m >> 1;
            if constexpr ((m & 1) == 0) fr[kk ^ 1][n] = n < 2 ? lds_rd(pa + n * 2048) : (n < 7 ? lds_rd(pb + (n - 2) * 2048) : lds_rd(pb + w * 2048));
            sb();
          });
        };
        asm volatile("s_nop 1" : "+v"(cs0), "+v"(cs1), "+v"(rs_[0]), "+v"(rs_[1]), "+v"(rq_[0]), "+v"(rq_[1]), "+v"(one));
        fence_all(acc);
        bar();
        ST(38);
        fr[0][0] = lds_rd(ra0); fr[0][1] = lds_rd(ra0 + 2048);
#pragma unroll
        for (int f = 0; f < 5; ++f) fr[0][2 + f] = lds_rd(rb0 + f * 2048);
        fr[0][7] = lds_rd(rb0 + w * 2048);
        sb();
        unsigned so = 0;
#pragma unroll 1
        for (int i = 0; i < 10; ++i) {
          half(IC<0>{}, so);
          wait_lgkm0();
          bar();
          so = so == 4 * K1_STAGE ? 0 : so + K1_STAGE;
          half(IC<1>{}, so);
        }
        fence_all(acc);
        asm volatile("" : "+v"(cs0), "+v"(cs1), "+v"(rs_[0]), "+v"(rs_[1]), "+v"(rq_[0]), "+v"(rq_[1]));
        const unsigned csb = lb + K1_CS;
        if (q == 0) {
          lds_wr1(csb + (w * 16 + i16) * 4, cs0[0]);
          if (w == 0) lds_wr1(csb + (64 + i16) * 4, cs1[0]);
        }
        wait_lgkm0();
        bar();
        ST(18);
#pragma unroll
        for (int f = 0; f < 5; ++f) csv[f] = lds_rd1(csb + (f * 16 + i16) * 4);
#pragma unroll
        for (int r = 0; r < 2; ++r)
#pragma unroll
          for (int j = 0; j < 4; ++j) {
            float m_, r_;
            ln_coef(rs_[r][j], __shfl(rq_[r][j], q * 20 + j), m_, r_);
            mu[r][j] = -m_; rs[r][j] = sign * r_;
          }
        if (w == 0) {
          const rsrc_t rc = make_rsrc(ws + WS_CSD, 256 * 320);
          st4(__float_as_uint(q == 0 ? csv[0] : (q == 1 ? csv[1] : (q == 2 ? csv[2] : csv[3]))), rc, lane * 4, bid * 320);
          st4(__float_as_uint(csv[4]), rc, q == 0 ? 256 + lane * 4 : OOB, bid * 320);
        }
      } else {
        {
          const rsrc_t rc = make_rsrc(ws + WS_CSD, 256 * 320);
#pragma unroll
          for (int f = 0; f < 5; ++f) csv[f] = __uint_as_float(__builtin_amdgcn_raw_buffer_load_b32(rc, (f * 16 + i16) * 4, bid * 320, 16));
        }
        poll(ws, FL_4 + P * 64 + w * 16 + i16, epoch);
        ST(17);
        const rsrc_t rx = make_rsrc(ws + WS_X2, SZ_X);
        const unsigned sox = (P * 8 + w * 2) * 20 * 1024;
        bf16x8 a[20][2];
        unsigned wsink = WARM_STRIP(rx, sox);
#pragma unroll
        for (int k = 0; k < 3; ++k) { a[k][0] = LDX(k); a[k][1] = LDX(20 + k); }
        const rsrc_t rst = make_rsrc(ws + WS_ST2, SZ_ST);
        const unsigned sto = ((lane >> 5) * 4 * S_ + P * 128 + w * 32 + (lane & 31)) * 8;
        const u32x2 sp0 = ld8u(rst, sto, 0), sp1 = ld8u(rst, sto, S_ * 8), sp2 = ld8u(rst, sto, 2 * S_ * 8), sp3 = ld8u(rst, sto, 3 * S_ * 8);
        sb();
        bar();
        bf16x8 b[2][5];
        const unsigned rb = lb + i16 * 128 + ((q ^ (lane & 7)) << 4);
#pragma unroll
        for (int f = 0; f < 5; ++f) b[0][f] = lds_rd(rb + wd_pos(0) * 1024 + f * 2048);
        sb();
        sfor<0, 20>([&](auto K) {
          constexpr int k = K.v;
          sfor<0, 10>([&](auto M) {
            constexpr int f = M.v >> 1, r = M.v & 1;
            if constexpr (k == 0) mma0(acc[r][f], a[0][r], b[0][f]); else mma(acc[r][f], a[k][r], b[k & 1][f]);
            if constexpr (k + 1 < 20 && r == 0) b[(k + 1) & 1][f] = lds_rd((rb ^ (((k + 1) & 1) * 64)) + wd_pos((k + 1) >> 1) * 1024 + f * 2048);
            if constexpr (r == 1 && f < 4) {
              constexpr int kn = k < 7 ? 2 * k + 3 + (f >> 1) : (f < 2 ? k + 10 : 20);
              if constexpr (kn < 20) a[kn][f & 1] = LDX((f & 1) * 20 + kn);
            }
            sb();
          });
          if constexpr (k == 0) ST(40);
          if constexpr (k == 4) ST(41);
          if constexpr (k == 9) ST(42);
        });
        asm volatile("" :: "v"(wsink));
        fence_all(acc);
        bar();
        ST(39);
        float s1 = (__uint_as_float(sp0[0]) + __uint_as_float(sp1[0])) + (__uint_as_float(sp2[0]) + __uint_as_float(sp3[0]));
        float s2 = (__uint_as_float(sp0[1]) + __uint_as_float(sp1[1])) + (__uint_as_float(sp2[1]) + __uint_as_float(sp3[1]));
        auto ta = __builtin_amdgcn_permlane32_swap(__float_as_uint(s1), __float_as_uint(s1), false, false);
        auto tb = __builtin_amdgcn_permlane32_swap(__float_as_uint(s2), __float_as_uint(s2), false, false);
        float m, rr;
        ln_coef(__uint_as_float(ta[0]) + __uint_as_float(ta[1]), __uint_as_float(tb[0]) + __uint_as_float(tb[1]), m, rr);
        m = -m; rr *= sign;
#pragma unroll
        for (int r = 0; r < 2; ++r)
#pragma unroll
          for (int e = 0; e < 4; ++e) { mu[r][e] = __shfl(m, r * 16 + q * 4 + e); rs[r][e] = __shfl(rr, r * 16 + q * 4 + e); }
      }
      g1_store(acc, mu, rs, csv, make_rsrc(ws + (L ? WS_AB2 : WS_AB1), SZ_AB), P, ct, w, i16, q);
      ST(L + 19);
      publish(ws, FL_5 + ((L * 4 + (ct >> 3) * 2 + (P >> 3)) * 4 + ((ct & 7) >> 1)) * 64 + ((P & 7) * 2 + (ct & 1)) * 4 + w, epoch, lane);
    }
    {
      PHASE_IDS
      IDS_G2
      const unsigned m = w >> 1, n = w & 1;
      const unsigned ra = lb + (m * 64 + i16) * 128 + ((q ^ (lane & 7)) << 4);
      const unsigned rb = lb + 16384 + n * 10240 + lane * 16;
      const unsigned row = P * 128 + m * 64 + kq * 16 + i16, ct = 2 * c4 + n;
      const TileOff ro = tile_offsets(L == 0, row, ct, q), oo = tile_offsets(false, row, ct, q);
      f32x4 acc[4][5];
#pragma unroll
      for (int i = 0; i < 4; ++i)
#pragma unroll
        for (int f = 0; f < 5; ++f) acc[i][f] = f32x4{0.f, 0.f, 0.f, 0.f};
      bf16x8 fa[2][4], fb[2][5];
      auto half = [&](auto KK, unsigned so) {
        constexpr int kk = KK.v;
        const unsigned pa = (ra ^ ((kk ^ 1) * 64)) + so, pb = rb + (kk ^ 1) * 5120 + so;
        sfor<0, 20>([&](auto M) {
          constexpr int mm = M.v;
          mma(acc[mm / 5][mm % 5], fb[kk][mm % 5], fa[kk][mm / 5]);
          if constexpr ((mm & 1) == 0 && mm < 10) fb[kk ^ 1][mm >> 1] = lds_rd(pb + (mm >> 1) * 1024);
          if constexpr ((mm & 1) == 0 && mm >= 10 && mm < 18) fa[kk ^ 1][(mm - 10) >> 1] = lds_rd(pa + ((mm - 10) >> 1) * 2048);
          sb();
        });
      };
      if (L) poll(ws, FL_4 + P * 64 + (m * 4 + kq) * 8 + ct, epoch);
      ResTile rt = load_res(make_rsrc(L ? (const void*)(ws + WS_X2) : (const void*)opq_p(x0_), SZ_X), ro);
      fence_all(acc);
      bar();
      ST(sbase + 1);
#pragma unroll
      for (int f = 0; f < 5; ++f) fb[0][f] = lds_rd(rb + f * 1024);
#pragma unroll
      for (int i = 0; i < 4; ++i) fa[0][i] = lds_rd(ra + i * 2048);
      sb();
#pragma unroll 1
      for (int s = 0; s < 16; ++s) {
        half(IC<0>{}, (s & 3) * G2_SLOT);
        wait_lgkm0();
        bar();
        half(IC<1>{}, ((s + 1) & 3) * G2_SLOT);
      }
      fence_all(acc);
      ST(sbase + 2);
      const rsrc_t rxp = make_rsrc(ws + WS_XP + L * SZ_XP, SZ_XP);
      const unsigned xo = ((P * 4 + c4) * 48 + w) * XSLOT;
      f32x4 v[5];
      if (kq == 0) xp_send<0>(acc, v, rxp, xo, lane);
      else if (kq == 1) xp_send<1>(acc, v, rxp, xo, lane);
      else if (kq == 2) xp_send<2>(acc, v, rxp, xo, lane);
      else xp_send<3>(acc, v, rxp, xo, lane);
      const unsigned fx = FL_X + ((L * 64 + P * 4 + c4) * 4 + w) * 4;
      ST(sbase + 3);
      publish(ws, fx + kq, epoch, lane);
      ST(sbase + 4);
      poll(ws, fx + (lane & 3), epoch);
      ST(sbase + 5);
      const unsigned xi = xo + kq * 12 * XSLOT;
      u32x4 g0[3], g1[3]; u32x2 g2[3];
#pragma unroll
      for (int s = 0; s < 3; ++s) {
        g0[s] = ld16u(rxp, lane * 16, xi + s * 4 * XSLOT);
        g1[s] = ld16u(rxp, lane * 16 + 1024, xi + s * 4 * XSLOT);
        g2[s] = ld8u(rxp, lane * 8 + 2048, xi + s * 4 * XSLOT);
      }
      sb();
#pragma unroll
      for (int s = 0; s < 3; ++s) {
        v[0] += f32x4{bflo(g0[s][0]), bfhi(g0[s][0]), bflo(g0[s][1]), bfhi(g0[s][1])};
        v[1] += f32x4{bflo(g0[s][2]), bfhi(g0[s][2]), bflo(g0[s][3]), bfhi(g0[s][3])};
        v[2] += f32x4{bflo(g1[s][0]), bfhi(g1[s][0]), bflo(g1[s][1]), bfhi(g1[s][1])};
        v[3] += f32x4{bflo(g1[s][2]), bfhi(g1[s][2]), bflo(g1[s][3]), bfhi(g1[s][3])};
        v[4] += f32x4{bflo(g2[s][0]), bfhi(g2[s][0]), bflo(g2[s][1]), bfhi(g2[s][1])};
      }
      finish_tile(v, rt, make_rsrc(ws + (L ? WS_X3 : WS_X1), SZ_X), oo, true, make_rsrc(ws + (L ? WS_ST3 : WS_ST1), SZ_ST),
                  q == 0 ? (ct * S_ + row) * 8 : OOB);
      ST(sbase + 6);
      publish(ws, FL_2 + (L * 16 + P) * 64 + (m * 4 + kq) * 8 + ct, epoch, lane);
      ST(sbase + 7);
    }
    {
      PHASE_IDS
      IDS_G3
      unsigned ho[4];
      {
        const unsigned row = P * 128 + w * 32 + i16;
#pragma unroll
        for (int p = 0; p < 4; ++p) {
          const unsigned col = ct * 112 + 32 * p + (p < 3 ? 16 * (q & 1) + 8 * (q >> 1) : 4 * q);
          ho[p] = col < F_ ? foff<54>(row, col) : OOB;
        }
        asm volatile("" : "+v"(ho[0]), "+v"(ho[1]), "+v"(ho[2]), "+v"(ho[3]));
      }
      poll(ws, FL_2 + (L * 16 + P) * 64 + w * 16 + i16, epoch);
      ST(sbase + 8);
      const rsrc_t rx = make_rsrc(ws + (L ? WS_X3 : WS_X1), SZ_X);
      const rsrc_t rst = make_rsrc(ws + (L ? WS_ST3 : WS_ST1), SZ_ST);
      const unsigned sox = (P * 8 + w * 2) * 20 * 1024;
      bf16x8 a[20][2];
      unsigned wsink = WARM_STRIP(rx, sox);
#pragma unroll
      for (int k = 0; k < 3; ++k) { a[k][0] = LDX(k); a[k][1] = LDX(20 + k); }
      const unsigned sto = (2 * q * S_ + P * 128 + w * 32 + i16) * 8;
      const u32x2 sa0 = ld8u(rst, sto, 0), sa1 = ld8u(rst, sto, S_ * 8), sb0 = ld8u(rst, sto + 128, 0), sb1 = ld8u(rst, sto + 128, S_ * 8);
      sb();
      bar();
      f32x4 acc[2][7];
      bf16x8 b[2][7];
      const unsigned rb = lb + lane * 16;
#pragma unroll
      for (int f = 0; f < 7; ++f) b[0][f] = lds_rd(rb + f * 20 * 1024);
      sb();
      sfor<0, 20>([&](auto K) {
        constexpr int k = K.v;
        sfor<0, 14>([&](auto M) {
          constexpr int f = M.v >> 1, r = M.v & 1;
          if constexpr (k == 0) mma0(acc[r][f], b[0][f], a[0][r]); else mma(acc[r][f], b[k & 1][f], a[k][r]);
          if constexpr (k + 1 < 20 && r == 0) b[(k + 1) & 1][f] = lds_rd(rb + (f * 20 + k + 1) * 1024);
          if constexpr (r == 1 && f < 4) {
            constexpr int kn = k < 7 ? 2 * k + 3 + (f >> 1) : (f < 2 ? k + 10 : 20);
            if constexpr (kn < 20) a[kn][f & 1] = LDX((f & 1) * 20 + kn);
          }
          sb();
        });
        if constexpr (k == 0) ST(sbase + 9);
        if constexpr (k == 4) ST(43 + L * 4);
        if constexpr (k == 9) ST(44 + L * 4);
      });
      f32x4 cs[7];
#pragma unroll
      for (int f = 0; f < 7; ++f) cs[f] = lds_rdf(lb + KM_CS1 + (f * 16 + q * 4) * 4);
      float mu[2], rs[2];
      ln_coef(xsum16_32(__uint_as_float(sa0[0]) + __uint_as_float(sa1[0])), xsum16_32(__uint_as_float(sa0[1]) + __uint_as_float(sa1[1])), mu[0], rs[0]);
      ln_coef(xsum16_32(__uint_as_float(sb0[0]) + __uint_as_float(sb1[0])), xsum16_32(__uint_as_float(sb0[1]) + __uint_as_float(sb1[1])), mu[1], rs[1]);
      asm volatile("" :: "v"(wsink));
      fence_all(acc);
      bar();
      ST(sbase + 10);
      const rsrc_t rh = make_rsrc(ws + (L ? WS_H2 : WS_H1), SZ_H);
#pragma unroll
      for (int r = 0; r < 2; ++r) {
        typedef __attribute__((ext_vector_type(2))) short s16x2;
        const f32x2 nm = {-mu[r], -mu[r]}, r2 = {rs[r], rs[r]};
        unsigned pk[7][2];
#pragma unroll
        for (int f = 0; f < 7; ++f)
#pragma unroll
          for (int e = 0; e < 2; ++e) {
            const f32x2 z = __builtin_elementwise_fma(nm, f32x2{cs[f][2 * e], cs[f][2 * e + 1]}, f32x2{acc[r][f][2 * e], acc[r][f][2 * e + 1]}) * r2;
            const s16x2 hb = __builtin_bit_cast(s16x2, __builtin_convertvector(z, bf16x2));
            pk[f][e] = __builtin_bit_cast(unsigned, __builtin_elementwise_max(hb, s16x2{0, 0}));
          }
#pragma unroll
        for (int p = 0; p < 3; ++p) {
          auto t0 = __builtin_amdgcn_permlane16_swap(pk[2 * p][0], pk[2 * p + 1][0], false, false);
          auto t1 = __builtin_amdgcn_permlane16_swap(pk[2 * p][1], pk[2 * p + 1][1], false, false);
          st16(u32x4{t0[0], t1[0], t0[1], t1[1]}, rh, ho[p], r * 54 * 1024);
        }
        st8(u32x2{pk[6][0], pk[6][1]}, rh, ho[3], r * 54 * 1024);
      }
      ST(sbase + 11);
      publish(ws, FL_3 + (L * 16 + P) * 64 + w * 16 + ct, epoch, lane);
      ST(sbase + 12);
    }
    {
      PHASE_IDS
      IDS_G4
      const unsigned ks0 = w < 2 ? w * 14 : 28 + (w - 2) * 13;
      const unsigned sow = (c8 * 54 + ks0) * 5 * 1024;
      const unsigned row = rt64 * 64 + w * 16 + i16;
      const rsrc_t rw = make_rsrc(ws + WS_W2, SZ_W2);
      bf16x8 a[14][4], bl[2][5], bg[7][5];
#define LDW(t, f, vo) bg[(t) - 7][f] = ld16(rw, (vo) + (((t) * 5 + (f)) & 3) * 1024, sow + (((t) * 5 + (f)) >> 2) * 4096)
      sfor<0, 10>([&](auto N) { LDW(7 + N.v / 5, N.v % 5, lane * 16); });
      const TileOff ro = tile_offsets(false, row, c8, q), oo = tile_offsets(L != 0, row, c8, q);
      {
        const unsigned l = lane < (w < 3 ? 8u : 10u) ? lane : 0;
        poll(ws, FL_3 + (L * 16 + (rt64 >> 1)) * 64 + ((rt64 & 1) * 2 + (l & 1)) * 16 + (w < 3 ? 4 * w : 11) + (l >> 1), epoch);
      }
      ST(sbase + 13);
      const rsrc_t rh = make_rsrc(ws + (L ? WS_H2 : WS_H1), SZ_H);
      const unsigned soh = (rt64 * 4 * 54 + ks0) * 1024;
      const unsigned vlast = lane * 16 + (w < 2 ? 0u : OOB);
      unsigned wsink;
      {
        const unsigned n = c8 * 56 + lane;
        wsink = __builtin_amdgcn_raw_buffer_load_b32(rh, lane < 56 && n < (w < 2 ? 448u : 416u) ? (((n >> 3) & 3) * 54 + (n >> 5)) * 1024 + (n & 7) * 128 : OOB, soh, 16);
      }
#pragma unroll
      for (int i = 0; i < 4; ++i) a[0][i] = ld16(rh, lane * 16, soh + i * 54 * 1024);
      ResTile rt;
      const rsrc_t rr = make_rsrc(ws + (L ? WS_X3 : WS_X1), SZ_X);
      sb();
      bar();
      f32x4 acc[4][5];
      const unsigned rb = lb + w * G4_REG + lane * 16;
#pragma unroll
      for (int f = 0; f < 5; ++f) bl[0][f] = lds_rd(rb + f * 1024);
      sb();
      sfor<0, 14>([&](auto T) {
        constexpr int t = T.v;
        constexpr int ta = t >= 1 && t + 2 < 14 ? t + 2 : -1;
        constexpr int tb = t >= 6 && t + 3 < 14 ? t + 3 : -1;
        sfor<0, 20>([&](auto M) {
          constexpr int m = M.v, i = m / 5, f = m % 5;
          if constexpr (t == 0) mma0(acc[i][f], bl[0][f], a[0][i]);
          else if constexpr (t < 7) mma(acc[i][f], bl[t & 1][f], a[t][i]);
          else mma(acc[i][f], bg[t - 7][f], a[t][i]);
          if constexpr (t + 1 < 7 && (m & 1) == 1 && m < 10) bl[(t + 1) & 1][m >> 1] = lds_rd(rb + ((t + 1) * 5 + (m >> 1)) * 1024);
          if constexpr (ta >= 0 && (m & 1) == 0 && m < 8)
            a[ta][m >> 1] = ld16(rh, (ta < 13 ? lane * 16 : vlast) + (ta & 3) * 1024, soh + (ta >> 2) * 4096 + (m >> 1) * 54 * 1024);
          if constexpr (tb >= 0 && (m & 1) == 0 && m >= 8 && m < 18) LDW(tb, (m - 8) >> 1, tb < 13 ? lane * 16 : vlast);
          if constexpr (t == 0 && (m & 1) == 0 && m < 16) a[1 + (m >> 3)][(m >> 1) & 3] = ld16(rh, lane * 16 + (1 + (m >> 3)) * 1024, soh + ((m >> 1) & 3) * 54 * 1024);
          if constexpr (t == 2 && m == 10) rt = load_res(rr, ro);
          sb();
        });
        if constexpr (t == 0) ST(sbase + 14);
        if constexpr (t == 3) ST(45 + L * 4);
        if constexpr (t == 6) ST(46 + L * 4);
      });
      asm volatile("" :: "v"(wsink));
      fence_all(acc);
      ST(sbase + 15);
      f32x4 v[5];
      {
        const unsigned wb = lb + w * G4_REG + lane * 16;
        if (w == 0) sm_scatter<0>(wb, acc, v);
        else if (w == 1) sm_scatter<1>(wb, acc, v);
        else if (w == 2) sm_scatter<2>(wb, acc, v);
        else sm_scatter<3>(wb, acc, v);
      }
      wait_lgkm0();
      bar();
#pragma unroll
      for (int s = 1; s < 4; ++s) {
        const unsigned src = lb + ((w + s) & 3) * G4_REG + (3 - s) * 5 * 1024 + lane * 16;
#pragma unroll
        for (int f = 0; f < 5; ++f) v[f] += lds_rdf(src + f * 1024);
      }
      finish_tile(v, rt, L ? make_rsrc(opq_p(out_), SZ_X) : make_rsrc(ws + WS_X2, SZ_X), oo, L == 0, make_rsrc(ws + WS_ST2, SZ_ST),
                  q == 0 ? (c8 * S_ + row) * 8 : OOB);
      ST(sbase + 16);
      if (L == 0) publish(ws, FL_4 + (rt64 >> 1) * 64 + ((rt64 & 1) * 4 + w) * 8 + c8, epoch, lane);
    }
  }
  if (blockIdx.x == 0 && w0 == 0) raise(ws_, FL_E, epoch, tid0);
  ST(37);
}

extern "C" void kernel_launch(void* const* d_in, const int* in_sizes, int n_in, void* d_out, int out_size, void* d_ws,
                              size_t ws_size, hipStream_t stream) {
  char* ws = (char*)d_ws;
  (void)hipFuncSetAttribute((const void*)k_all, hipFuncAttributeMaxDynamicSharedMemorySize, KM_LDS);
  k_all<<<256, 512, KM_LDS, stream>>>((const u16*)d_in[0], (const u16*)d_in[1], (const u16*)d_in[2], (const u16*)d_in[3],
                                     (const u16*)d_in[4], (const u16*)d_in[6], (const u16*)d_in[7], ws, (u16*)d_out STAMP_PASS(0));
}
```

```cpp
#include <hip/hip_runtime.h>
#include <hip/hip_bf16.h>
#include <stdint.h>

#define DI __device__ __forceinline__
typedef unsigned short u16;
typedef __attribute__((ext_vector_type(8))) __bf16 bf16x8;
typedef __attribute__((ext_vector_type(2))) __bf16 bf16x2;
typedef __attribute__((ext_vector_type(4))) float f32x4;
typedef __attribute__((ext_vector_type(2))) float f32x2;
typedef __attribute__((ext_vector_type(4))) unsigned u32x4;
typedef __attribute__((ext_vector_type(2))) unsigned u32x2;
typedef __attribute__((ext_vector_type(4))) short s16x4;
#define LDS_AS __attribute__((address_space(3)))
#define GLB_AS __attribute__((address_space(1)))
typedef LDS_AS char* ldsp;
typedef GLB_AS unsigned gu32;
typedef unsigned long long u64;
typedef GLB_AS u64 gu64;
typedef __amdgpu_buffer_rsrc_t rsrc_t;
#define RLX_AGENT __ATOMIC_RELAXED, __HIP_MEMORY_SCOPE_AGENT

constexpr int S_ = 2048, D_ = 640, F_ = 1728;
constexpr float EPS = 1e-5f;
constexpr unsigned OOB = 0x7f000000u;

constexpr unsigned SZ_AB = 8 * 128 * 5 * 1024;
constexpr unsigned SZ_X = 128 * 20 * 1024;
constexpr unsigned SZ_H = 128 * 54 * 1024;
constexpr unsigned SZ_W1 = 16 * 140 * 1024;
constexpr unsigned SZ_W2 = 8 * 54 * 5 * 1024;
constexpr unsigned SZ_ST = 2048 * 64;
constexpr unsigned WS_AB1 = 0;
constexpr unsigned WS_AB2 = WS_AB1 + SZ_AB;
constexpr unsigned WS_X1 = WS_AB2 + SZ_AB;
constexpr unsigned WS_X2 = WS_X1 + SZ_X;
constexpr unsigned WS_X3 = WS_X2 + SZ_X;
constexpr unsigned WS_H1 = WS_X3 + SZ_X;
constexpr unsigned WS_H2 = WS_H1 + SZ_H;
constexpr unsigned WS_W1 = WS_H2 + SZ_H;
constexpr unsigned WS_W2 = WS_W1 + SZ_W1;
constexpr unsigned WS_ST1 = WS_W2 + SZ_W2;
constexpr unsigned WS_ST2 = WS_ST1 + SZ_ST;
constexpr unsigned WS_ST3 = WS_ST2 + SZ_ST;
constexpr unsigned WS_CS1 = WS_ST3 + SZ_ST;
constexpr unsigned WS_CSD = WS_CS1 + 20 * 1728 * 4;
constexpr unsigned XSLOT = 2560;
constexpr unsigned SZ_XP = 256 * 12 * XSLOT;
constexpr unsigned WS_XP = (WS_CSD + 256 * 320 + 4095) & ~4095u;
constexpr unsigned WS_FLG = WS_XP + 2 * SZ_XP;
constexpr unsigned FL_X = 0;
constexpr unsigned FL_2 = FL_X + 2048;
constexpr unsigned FL_3 = FL_2 + 2048;
constexpr unsigned FL_4 = FL_3 + 4096;
constexpr unsigned FL_5 = FL_4 + 1024;
constexpr unsigned FL_W = FL_5 + 2048;
constexpr unsigned FL_E = FL_W + 1088;
constexpr unsigned WS_END = WS_FLG + (FL_E + 1) * 128;

DI u32x4 make_srd(const void* p, unsigned bytes) {
  uint64_t a = (uint64_t)p;
  u32x4 r;
  r[0] = (unsigned)a; r[1] = (unsigned)(a >> 32) & 0xffffu; r[2] = bytes; r[3] = 0x00020000u;
  return r;
}
DI rsrc_t make_rsrc(const void* p, unsigned bytes) {
  return __builtin_amdgcn_make_buffer_rsrc((void*)p, (short)0, (int)bytes, 0x00020000);
}
template <int LOFF, int GOFF>
DI void glds(u32x4 srd, unsigned voff, unsigned ldsb, unsigned soff) {
  unsigned tmp;
  asm volatile("s_add_u32 m0, %1, %2\n\ts_add_u32 %0, %3, %4\n\tbuffer_load_dwordx4 %5, %6, %0 offen lds"
               : "=&s"(tmp)
               : "s"(ldsb), "i"(LOFF), "s"(soff), "i"(GOFF), "v"(voff), "s"(srd)
               : "memory", "scc");
}
template <int N> DI void glds_lin(u32x4 srd, unsigned voff, unsigned ldsb, unsigned soff) {
  if constexpr (N == 4)
    asm volatile("s_mov_b32 m0, %0\n\ts_nop 0\n\tbuffer_load_dwordx4 %1, %2, %3 offen lds\n\tbuffer_load_dwordx4 %1, %2, %3 offen offset:1024 lds\n\t"
                 "buffer_load_dwordx4 %1, %2, %3 offen offset:2048 lds\n\tbuffer_load_dwordx4 %1, %2, %3 offen offset:3072 lds"
                 :: "s"(ldsb), "v"(voff), "s"(srd), "s"(soff) : "memory");
  else if constexpr (N == 3)
    asm volatile("s_mov_b32 m0, %0\n\ts_nop 0\n\tbuffer_load_dwordx4 %1, %2, %3 offen lds\n\tbuffer_load_dwordx4 %1, %2, %3 offen offset:1024 lds\n\t"
                 "buffer_load_dwordx4 %1, %2, %3 offen offset:2048 lds"
                 :: "s"(ldsb), "v"(voff), "s"(srd), "s"(soff) : "memory");
  else
    asm volatile("s_mov_b32 m0, %0\n\ts_nop 0\n\tbuffer_load_dwordx4 %1, %2, %3 offen lds"
                 :: "s"(ldsb), "v"(voff), "s"(srd), "s"(soff) : "memory");
}
template <int IMM> DI void ldv(bf16x8& d, u32x4 srd, unsigned voff, unsigned soff) {
  asm volatile("buffer_load_dwordx4 %0, %1, %2, %3 offen offset:%4 sc1" : "=v"(d) : "v"(voff), "s"(srd), "s"(soff), "i"(IMM) : "memory");
}
template <int N> DI void wait_vm() { asm volatile("s_waitcnt vmcnt(%0)" ::"i"(N) : "memory"); }
template <int N> DI void wait_vm5(bf16x8 (&b)[5]) {
  asm volatile("s_waitcnt vmcnt(%5)" : "+v"(b[0]), "+v"(b[1]), "+v"(b[2]), "+v"(b[3]), "+v"(b[4]) : "i"(N) : "memory");
}
DI void wait_lgkm0() { asm volatile("s_waitcnt lgkmcnt(0)" ::: "memory"); }
DI void sb() { __builtin_amdgcn_sched_barrier(0); }
DI void bar() { asm volatile("s_barrier" ::: "memory"); }

DI bf16x8 lds_rd(unsigned addr) { return *(const LDS_AS bf16x8*)(ldsp)(uintptr_t)addr; }
DI f32x4 lds_rdf(unsigned addr) { return *(const LDS_AS f32x4*)(ldsp)(uintptr_t)addr; }
DI void lds_wrf(unsigned addr, f32x4 v) { *(LDS_AS f32x4*)(ldsp)(uintptr_t)addr = v; }
DI float lds_rd1(unsigned addr) { return *(const LDS_AS float*)(ldsp)(uintptr_t)addr; }
DI void lds_wr1(unsigned addr, float v) { *(LDS_AS float*)(ldsp)(uintptr_t)addr = v; }
DI f32x4 mfma(bf16x8 a, bf16x8 b, f32x4 c) { return __builtin_amdgcn_mfma_f32_16x16x32_bf16(a, b, c, 0, 0, 0); }
DI void mma(f32x4& c, const bf16x8& a, const bf16x8& b) {
  asm volatile("v_mfma_f32_16x16x32_bf16 %0, %1, %2, %0" : "+v"(c) : "v"(a), "v"(b));
}
DI void mma0(f32x4& c, const bf16x8& a, const bf16x8& b) {
  asm volatile("v_mfma_f32_16x16x32_bf16 %0, %1, %2, 0" : "=&v"(c) : "v"(a), "v"(b));
}
template <int N> DI void fence_acc(f32x4 (&a)[N]) {
  if constexpr (N == 5) asm volatile("s_nop 7\n\ts_nop 1" : "+v"(a[0]), "+v"(a[1]), "+v"(a[2]), "+v"(a[3]), "+v"(a[4]));
  else asm volatile("s_nop 7\n\ts_nop 1" : "+v"(a[0]), "+v"(a[1]), "+v"(a[2]), "+v"(a[3]), "+v"(a[4]), "+v"(a[5]), "+v"(a[6]));
}
template <int N> DI void pin_acc(f32x4 (&a)[N]) {
  if constexpr (N == 5) asm volatile("" : "+v"(a[0]), "+v"(a[1]), "+v"(a[2]), "+v"(a[3]), "+v"(a[4]));
  else asm volatile("" : "+v"(a[0]), "+v"(a[1]), "+v"(a[2]), "+v"(a[3]), "+v"(a[4]), "+v"(a[5]), "+v"(a[6]));
}
template <int M, int N> DI void fence_all(f32x4 (&a)[M][N]) {
#pragma unroll
  for (int i = 1; i < M; ++i) pin_acc(a[i]);
  fence_acc(a[0]);
#pragma unroll
  for (int i = 1; i < M; ++i) pin_acc(a[i]);
}
DI unsigned pk2(float a, float b) {
  f32x2 v = {a, b};
  return __builtin_bit_cast(unsigned, __builtin_convertvector(v, bf16x2));
}
DI float bflo(unsigned u) { return __uint_as_float(u << 16); }
DI float bfhi(unsigned u) { return __uint_as_float(u & 0xffff0000u); }
DI float dot2(unsigned a, unsigned b, float c) {
  return __builtin_amdgcn_fdot2_f32_bf16(__builtin_bit_cast(bf16x2, a), __builtin_bit_cast(bf16x2, b), c, false);
}
DI float xsum16_32(float x) {
  auto s = __builtin_amdgcn_permlane16_swap(__float_as_uint(x), __float_as_uint(x), false, false);
  x = __uint_as_float(s[0]) + __uint_as_float(s[1]);
  auto t = __builtin_amdgcn_permlane32_swap(__float_as_uint(x), __float_as_uint(x), false, false);
  return __uint_as_float(t[0]) + __uint_as_float(t[1]);
}
template <int V> struct IC { static constexpr int v = V; };
template <int I, int N, class Fn> DI void sfor(Fn&& f) {
  if constexpr (I < N) { f(IC<I>{}); sfor<I + 1, N>(f); }
}
DI unsigned rfl(unsigned v) { return __builtin_amdgcn_readfirstlane(v); }
DI unsigned opq_v(unsigned x) { asm volatile("" : "+v"(x)); return x; }
DI unsigned opq_s(unsigned x) { asm volatile("" : "+s"(x)); return x; }
template <class T> DI T* opq_p(T* x) { asm volatile("" : "+s"(x)); return x; }
DI bf16x8 ld16(rsrc_t r, unsigned voff, unsigned soff) { return __builtin_bit_cast(bf16x8, __builtin_amdgcn_raw_buffer_load_b128(r, voff, soff, 16)); }
DI u32x4 ld16u(rsrc_t r, unsigned voff, unsigned soff) { return __builtin_amdgcn_raw_buffer_load_b128(r, voff, soff, 16); }
DI u32x2 ld8u(rsrc_t r, unsigned voff, unsigned soff) { return __builtin_amdgcn_raw_buffer_load_b64(r, voff, soff, 16); }
DI void st16(u32x4 v, rsrc_t r, unsigned voff, unsigned soff) {
  __builtin_amdgcn_raw_buffer_store_b128(v, r, voff, soff, 16);
  asm volatile("s_nop 1" :: "v"(v) : "memory");
}
DI void st16_raw(u32x4 v, rsrc_t r, unsigned voff, unsigned soff) { __builtin_amdgcn_raw_buffer_store_b128(v, r, voff, soff, 16); }
template <class... T> DI void st_guard(const T&... v) {
  (..., [](const auto& x) { asm volatile("" :: "v"(x) : "memory"); }(v));
  asm volatile("s_nop 1" ::: "memory");
  (..., [](const auto& x) { asm volatile("" :: "v"(x)); }(v));
}
DI void st8(u32x2 v, rsrc_t r, unsigned voff, unsigned soff) { __builtin_amdgcn_raw_buffer_store_b64(v, r, voff, soff, 16); }
DI void st4(unsigned v, rsrc_t r, unsigned voff, unsigned soff) { __builtin_amdgcn_raw_buffer_store_b32(v, r, voff, soff, 16); }

#define ST(i)
#define STAMP_ARGS
#define STAMP_FWD
#define STAMP_PASS(k)

template <int KS> DI unsigned foff(unsigned row, unsigned col) {
  return ((row >> 4) * KS + (col >> 5)) * 1024u + (((col >> 3) & 3) * 16u + (row & 15)) * 16u + (col & 7) * 2u;
}

extern __shared__ __attribute__((aligned(1024))) char smem[];
DI unsigned lds_base() { return (unsigned)(uintptr_t)(ldsp)smem; }

DI void ln_coef(float s1, float s2, float& mu, float& rs) {
  mu = s1 * (1.f / D_);
  rs = __builtin_amdgcn_rsqf(fmaxf(s2 * (1.f / D_) - mu * mu, 0.f) + EPS);
}

DI void g1_store(const f32x4 (&acc)[2][5], const float (&mu)[2][4], const float (&rs)[2][4], const float (&csv)[5], rsrc_t rab,
                 unsigned P, unsigned ct, unsigned w, unsigned i16, unsigned q) {
  const unsigned kstep = (ct >> 3) * 64 + P * 4 + w;
  const unsigned cq = ((q & 1) << 1) | (q >> 1);
  const unsigned so = ((ct & 7) * 128 + kstep) * 5 * 1024;
  const unsigned vo = (cq * 16 + i16) * 16;
  u32x4 y[5];
#pragma unroll
  for (int f = 0; f < 5; ++f) {
    const f32x2 cs = {csv[f], csv[f]};
    unsigned pk[2][2];
#pragma unroll
    for (int r = 0; r < 2; ++r)
#pragma unroll
      for (int e = 0; e < 2; ++e) {
        const f32x2 t = __builtin_elementwise_fma(f32x2{mu[r][2 * e], mu[r][2 * e + 1]}, cs, f32x2{acc[r][f][2 * e], acc[r][f][2 * e + 1]}) *
                        f32x2{rs[r][2 * e], rs[r][2 * e + 1]};
        pk[r][e] = __builtin_bit_cast(unsigned, __builtin_convertvector(t, bf16x2));
      }
    auto t0 = __builtin_amdgcn_permlane16_swap(pk[0][0], pk[1][0], false, false);
    auto t1 = __builtin_amdgcn_permlane16_swap(pk[0][1], pk[1][1], false, false);
    y[f] = u32x4{t0[0], t1[0], t0[1], t1[1]};
    st16_raw(y[f], rab, vo + (f & 3) * 1024, so + (f >> 2) * 4096);
  }
  st_guard(y[0], y[1], y[2], y[3], y[4]);
}

constexpr int K1_STAGE = 26 * 1024;
constexpr int K1_TR = 5 * K1_STAGE;
constexpr int K1_CS = K1_TR + 5 * 4096;

DI void k1_issue(u32x4 sa, u32x4 sbd, unsigned vo, unsigned la, unsigned lbb, unsigned soa, unsigned sob, unsigned w) {
  glds<0, 0>(sa, vo, la, soa);
  glds<1024, 8 * 1280>(sa, vo, la, soa);
  glds<2048, 16 * 1280>(sa, vo, la, soa);
  glds<3072, 24 * 1280>(sa, vo, la, soa);
  glds<0, 0>(sbd, vo, lbb, sob);
  glds<4096, 32 * 1280>(sbd, vo, lbb, sob);
  if (w < 2) glds<8192, 64 * 1280>(sbd, vo, lbb, sob);
}
struct RtUnit { bool first; unsigned ks, g; };
DI RtUnit rt_unit(unsigned u) {
  RtUnit r;
  r.first = u < 540;
  if (r.first) { r.ks = u / 27u; r.g = u - r.ks * 27u; }
  else { const unsigned v = u - 540; r.ks = v / 10u; r.g = v - r.ks * 10u; }
  return r;
}
DI void retile_issue(unsigned u, const u16* w1, const u16* w2, unsigned ldst, unsigned lane) {
  const RtUnit t = rt_unit(u);
  const unsigned ld = t.first ? F_ * 2 : D_ * 2;
  u32x4 srd = make_srd(t.first ? w1 : w2, D_ * F_ * 2);
  const unsigned vo = (lane >> 3) * ld + (lane & 7) * 16;
  const unsigned so = t.ks * 32 * ld + t.g * 128;
  glds<0, 0>(srd, vo, ldst, so);
  glds<1024, 0>(srd, vo, ldst, so + 8 * ld);
  glds<2048, 0>(srd, vo, ldst, so + 16 * ld);
  glds<3072, 0>(srd, vo, ldst, so + 24 * ld);
}
DI unsigned w1_off(unsigned rt, unsigned ks) {
  const unsigned c = rt / 7u, f = rt - c * 7, g = (c & 1) ? (f == 0 ? 6 : f - 1) : f;
  return WS_W1 + (c * 140 + (g < 4 ? ks * 4 + g : 80 + ks * 3 + g - 4)) * 1024;
}
DI void retile_finish(unsigned u, unsigned f0, rsrc_t rws, unsigned ldst, unsigned lane) {
  const RtUnit t = rt_unit(u);
  const unsigned i = lane & 15, q = lane >> 4;
  const unsigned base = ldst + (8 * q + (i >> 2)) * 128 + (i & 3) * 8 + f0 * 32;
  u32x4 v[2];
#pragma unroll
  for (int f = 0; f < 2; ++f) {
    s16x4 lo = __builtin_amdgcn_ds_read_tr16_b64_v4i16((LDS_AS s16x4*)(uintptr_t)(base + f * 32));
    s16x4 hi = __builtin_amdgcn_ds_read_tr16_b64_v4i16((LDS_AS s16x4*)(uintptr_t)(base + f * 32 + 512));
    u32x2 l2 = __builtin_bit_cast(u32x2, lo), h2 = __builtin_bit_cast(u32x2, hi);
    v[f] = u32x4{l2[0], l2[1], h2[0], h2[1]};
  }
  const unsigned rt = t.g * 4 + f0;
  if (t.first) {
    st16(v[0], rws, lane * 16, w1_off(rt, t.ks));
    st16(v[1], rws, lane * 16, w1_off(rt + 1, t.ks));
    float s[2];
#pragma unroll
    for (int f = 0; f < 2; ++f)
      s[f] = xsum16_32(((bflo(v[f][0]) + bfhi(v[f][0])) + (bflo(v[f][1]) + bfhi(v[f][1]))) + ((bflo(v[f][2]) + bfhi(v[f][2])) + (bflo(v[f][3]) + bfhi(v[f][3]))));
    st4(__float_as_uint(q == 0 ? s[0] : s[1]), rws, lane < 32 ? lane * 4 : OOB, WS_CS1 + (t.ks * F_ + rt * 16) * 4);
  } else {
    const unsigned c0 = rt / 5u, c1 = (rt + 1) / 5u;
    st16(v[0], rws, lane * 16, WS_W2 + ((c0 * 54 + t.ks) * 5 + (rt - c0 * 5)) * 1024);
    st16(v[1], rws, lane * 16, WS_W2 + ((c1 * 54 + t.ks) * 5 + (rt + 1 - c1 * 5)) * 1024);
  }
}

#define ASLEEP 0
#define W2SLEEP 0
#define WDSLEEP 0
#define WARM_X 0
#define WARM_W 0
#define WARM_H 0
#define WARM_S 0
#define WARM_0 0
#define WS1 3
#define WS2 6
#define ROT1 1
#define ROT2 0
#define ROT3 0
#define ROT4 1
#define ROT5 0
#define PUB1 14
#define LSLEEP 20
#define PDELAY 6
#define WSLEEP 52
#define SSLEEP 46
constexpr int KM_LDS = 160 * 1024;
constexpr int KM_CS1 = KM_LDS - 1024;
constexpr int G2_SLOT = 36 * 1024, G4_LATE = 80 * 1024;

DI gu64* flag_ptr(char* ws, unsigned f) { return (gu64*)(ws + WS_FLG + f * 128); }
DI void raise(char* ws, unsigned f, u64 epoch, unsigned lane) {
  if (lane < 16) __hip_atomic_store(flag_ptr(ws, f) + lane, epoch, RLX_AGENT);
}
DI void publish(char* ws, unsigned f, u64 epoch, unsigned lane) {
  wait_vm<0>();
  raise(ws, f, epoch, lane);
}
DI void poll0(char* ws, unsigned f, u64 epoch) {
  gu64* p = flag_ptr(ws, f);
#pragma unroll 1
  for (unsigned s = 0; s < (1u << 16); ++s) {
    if (__all(__hip_atomic_load(p, RLX_AGENT) == epoch)) break;
    __builtin_amdgcn_s_sleep(1);
  }
  asm volatile("" ::: "memory");
}
DI void poll(char* ws, unsigned f, u64 epoch) {
  if (PDELAY) __builtin_amdgcn_s_sleep(PDELAY);
  poll0(ws, f, epoch);
}
DI void warm(unsigned& sink, u32x4 srd, unsigned voff, unsigned soff) {
  asm volatile("buffer_load_dword %0, %1, %2, %3 offen" : "+v"(sink) : "v"(voff), "s"(srd), "s"(soff) : "memory");
}
template <int N> DI void glds_run(u32x4 srd, unsigned voff, unsigned l, unsigned so) {
#pragma unroll
  for (int g = 0; g < N / 4; ++g) glds_lin<4>(srd, voff, l + g * 4096, so + g * 4096);
  if constexpr (N % 4 == 3) glds_lin<3>(srd, voff, l + (N / 4) * 4096, so + (N / 4) * 4096);
  else {
#pragma unroll
    for (int g = N / 4 * 4; g < N; ++g) glds_lin<1>(srd, voff, l + g * 1024, so + g * 1024);
  }
}
DI void glds4_sc1(u32x4 srd, unsigned voff, unsigned l, unsigned so) {
  asm volatile("s_mov_b32 m0, %0\n\ts_nop 0\n\tbuffer_load_dwordx4 %1, %2, %3 offen sc1 lds\n\tbuffer_load_dwordx4 %1, %2, %3 offen offset:1024 sc1 lds\n\t"
               "buffer_load_dwordx4 %1, %2, %3 offen offset:2048 sc1 lds\n\tbuffer_load_dwordx4 %1, %2, %3 offen offset:3072 sc1 lds"
               :: "s"(l), "v"(voff), "s"(srd), "s"(so) : "memory");
}
DI void glds1_sc1(u32x4 srd, unsigned voff, unsigned l, unsigned so) {
  asm volatile("s_mov_b32 m0, %0\n\ts_nop 0\n\tbuffer_load_dwordx4 %1, %2, %3 offen sc1 lds" :: "s"(l), "v"(voff), "s"(srd), "s"(so) : "memory");
}
template <int N> DI void glds_run_sc1(u32x4 srd, unsigned voff, unsigned l, unsigned so) {
#pragma unroll
  for (int g = 0; g < N / 4; ++g) glds4_sc1(srd, voff, l + g * 4096, so + g * 4096);
#pragma unroll
  for (int g = N / 4 * 4; g < N; ++g) glds1_sc1(srd, voff, l + g * 1024, so + g * 1024);
}

struct ResTile { u32x4 r0, r1; u32x2 r2; };
struct TileOff { unsigned a0, a1, a2; };
DI TileOff tile_offsets(bool rm, unsigned row, unsigned ct, unsigned q) {
  const unsigned cA = ct * 80 + 16 * (q & 1) + 8 * (q >> 1), cS = ct * 80 + 64 + 4 * q;
  TileOff t;
  if (rm) { t.a0 = (row * D_ + cA) * 2; t.a1 = t.a0 + 64; t.a2 = (row * D_ + cS) * 2; }
  else { t.a0 = foff<20>(row, cA); t.a1 = foff<20>(row, cA + 32); t.a2 = foff<20>(row, cS); }
  asm volatile("" : "+v"(t.a0), "+v"(t.a1), "+v"(t.a2));
  return t;
}
DI ResTile load_res(rsrc_t rr, const TileOff& t) {
  ResTile rt;
  rt.r0 = ld16u(rr, t.a0, 0); rt.r1 = ld16u(rr, t.a1, 0); rt.r2 = ld8u(rr, t.a2, 0);
  return rt;
}
DI void finish_tile(f32x4 (&v)[5], const ResTile& rt, rsrc_t rout, const TileOff& o, bool stats, rsrc_t rst, unsigned sto) {
#pragma unroll
  for (int p = 0; p < 2; ++p)
#pragma unroll
    for (int r = 0; r < 4; ++r) {
      auto t = __builtin_amdgcn_permlane16_swap(__float_as_uint(v[2 * p][r]), __float_as_uint(v[2 * p + 1][r]), false, false);
      v[2 * p][r] = __uint_as_float(t[0]); v[2 * p + 1][r] = __uint_as_float(t[1]);
    }
  u32x4 y0, y1; u32x2 y2;
#pragma unroll
  for (int j = 0; j < 2; ++j) {
    y0[j] = pk2(v[0][2 * j] + bflo(rt.r0[j]), v[0][2 * j + 1] + bfhi(rt.r0[j]));
    y0[2 + j] = pk2(v[1][2 * j] + bflo(rt.r0[2 + j]), v[1][2 * j + 1] + bfhi(rt.r0[2 + j]));
    y1[j] = pk2(v[2][2 * j] + bflo(rt.r1[j]), v[2][2 * j + 1] + bfhi(rt.r1[j]));
    y1[2 + j] = pk2(v[3][2 * j] + bflo(rt.r1[2 + j]), v[3][2 * j + 1] + bfhi(rt.r1[2 + j]));
    y2[j] = pk2(v[4][2 * j] + bflo(rt.r2[j]), v[4][2 * j + 1] + bfhi(rt.r2[j]));
  }
  st16_raw(y0, rout, o.a0, 0); st16_raw(y1, rout, o.a1, 0); st8(y2, rout, o.a2, 0);
  st_guard(y0, y1);
  if (stats) {
    float s1a = 0.f, s1b = 0.f, s2a = 0.f, s2b = 0.f;
#pragma unroll
    for (int j = 0; j < 4; ++j) {
      s1a = dot2(y0[j], 0x3f803f80u, s1a); s2a = dot2(y0[j], y0[j], s2a);
      s1b = dot2(y1[j], 0x3f803f80u, s1b); s2b = dot2(y1[j], y1[j], s2b);
    }
    s1a = dot2(y2[0], 0x3f803f80u, s1a); s2a = dot2(y2[0], y2[0], s2a);
    s1b = dot2(y2[1], 0x3f803f80u, s1b); s2b = dot2(y2[1], y2[1], s2b);
    const float s1 = xsum16_32(s1a + s1b), s2 = xsum16_32(s2a + s2b);
    st8(u32x2{__float_as_uint(s1), __float_as_uint(s2)}, rst, sto, 0);
  }
}

template <int W> DI void sm_scatter(unsigned wb, const f32x4 (&acc)[4][5], f32x4 (&own)[5]) {
#pragma unroll
  for (int d = 0; d < 4; ++d)
#pragma unroll
    for (int f = 0; f < 5; ++f) {
      if (d == W) own[f] = acc[d][f];
      else lds_wrf(wb + (((d - W - 1) & 3) * 5 + f) * 1024, acc[d][f]);
    }
  asm volatile("; scatter %5" : "+v"(own[0]), "+v"(own[1]), "+v"(own[2]), "+v"(own[3]), "+v"(own[4]) : "i"(W));
}
template <int KQ> DI void xp_send(const f32x4 (&acc)[4][5], f32x4 (&own)[5], rsrc_t rx, unsigned so, unsigned lane) {
  u32x4 y[3][2];
  sfor<0, 3>([&](auto S) {
    constexpr int d = (KQ + 1 + S.v) & 3;
    const unsigned o = so + (d * 3 + ((KQ - d - 1) & 3)) * 4 * XSLOT;
    unsigned p[10];
#pragma unroll
    for (int f = 0; f < 5; ++f) { p[2 * f] = pk2(acc[d][f][0], acc[d][f][1]); p[2 * f + 1] = pk2(acc[d][f][2], acc[d][f][3]); }
    y[S.v][0] = u32x4{p[0], p[1], p[2], p[3]}; y[S.v][1] = u32x4{p[4], p[5], p[6], p[7]};
    st16_raw(y[S.v][0], rx, lane * 16, o);
    st16_raw(y[S.v][1], rx, lane * 16 + 1024, o);
    st8(u32x2{p[8], p[9]}, rx, lane * 8 + 2048, o);
  });
  st_guard(y[0][0], y[0][1], y[1][0], y[1][1], y[2][0], y[2][1]);
#pragma unroll
  for (int f = 0; f < 5; ++f) own[f] = acc[KQ][f];
  asm volatile("; send %5" : "+v"(own[0]), "+v"(own[1]), "+v"(own[2]), "+v"(own[3]), "+v"(own[4]) : "i"(KQ));
}

DI constexpr unsigned wd_pos(int c) { return c < 8 ? 10 * c : 60 + 10 * c; }
DI constexpr int a_when(int k) { return k < 3 ? -1 : (k < 17 ? (k - 3) / 2 : k - 10); }

#define PHASE_IDS \
  const unsigned lane = opq_v(tid0) & 63, w = opq_s(w0), bid = opq_s(blockIdx.x); \
  const unsigned xcd = bid & 7, idx = bid >> 3, i16 = lane & 15, q = lane >> 4; \
  char* const ws = opq_p(ws_); \
  (void)xcd; (void)idx; (void)i16; (void)q;
#define IDS_G2 const unsigned kq = xcd & 3, P = (xcd >> 2) * 8 + (idx >> 2), c4 = idx & 3;
#define IDS_G3 const unsigned P = (xcd >> 1) * 4 + (idx >> 3), ct = (xcd & 1) * 8 + (idx & 7);
#define IDS_G4 const unsigned rt64 = xcd * 4 + (idx >> 3), c8 = idx & 7;
#define WARM_STRIP(rx, sox) \
  ({ const unsigned n_ = (idx & 7) * 40 + lane; \
     __builtin_amdgcn_raw_buffer_load_b32(rx, lane < 40 ? (((n_ >> 3) & 1) * 20 + (n_ >> 4)) * 1024 + (n_ & 7) * 128 : OOB, sox, 16); })
#define KROT(k) ((k) + rot >= 20 ? (k) + rot - 20 : (k) + rot)
#define LDX(r, k) ld16(rx, lane * 16, sox + (r) * 20480 + KROT(k) * 1024)

__global__ __launch_bounds__(512, 2) void k_all(const u16* x0_, const u16* Sre_, const u16* Sim_, const u16* Dre_, const u16* Dim_,
                                               const u16* w1_, const u16* w2_, char* ws_, u16* out_ STAMP_ARGS) {
  ST(0);
  const unsigned tid0 = threadIdx.x, wall = rfl(tid0 >> 6), w0 = wall & 3;
  const unsigned lb = lds_base();
  u64 epoch;

  if (wall >= 4) {
    unsigned sink = 0;
    unsigned ep0, ep1;
    asm volatile("global_load_dword %0, %2, %3 sc1\n\tglobal_load_dword %1, %2, %3 offset:4 sc1"
                 : "=&v"(ep0), "=&v"(ep1) : "v"(WS_FLG + FL_E * 128), "s"(ws_) : "memory");
    {
      PHASE_IDS
      IDS_G3
      const u32x4 sa = make_srd(opq_p(x0_), S_ * D_ * 2), sbd = make_srd(ct < 8 ? opq_p(Dre_) : opq_p(Dim_), D_ * D_ * 2);
      const unsigned vo = (lane >> 3) * 1280 + (((lane & 7) ^ (lane >> 3)) << 4);
      const unsigned soa = (P * 128 + w * 32) * 1280, sob = ((ct & 7) * 80 + w * 8) * 1280;
      const unsigned la = lb + w * 4096, lbb = lb + 16384 + w * 1024;
      const unsigned u0 = bid * 4 + w, u1 = 1024 + bid;
      const bool extra = w == 3 && bid < 56;
      const unsigned rot = ROT1 ? ((idx & 7) + 3 * (idx >> 3)) % 10u : 0;
      auto ko = [&](unsigned s) { const unsigned c = s + rot; return (c >= 10 ? c - 10 : c) * 128; };
      const rsrc_t rws = make_rsrc(ws, WS_END);
      asm volatile("s_nop 4" ::: "memory");
      if (WARM_0) {
        const unsigned r0 = lane / 10u, id = ((idx >> 3) * 4 + w) * 50 + lane, r1 = id / 10u;
        warm(sink, sa, lane < 40 ? r0 * 1280 + (lane - r0 * 10) * 128 : OOB, (P * 128 + (idx & 7) * 16 + w * 4) * 1280);
        warm(sink, sbd, lane < 50 ? r1 * 1280 + (id - r1 * 10) * 128 : OOB, (ct & 7) * 80 * 1280);
      }
      k1_issue(sa, sbd, vo, la, lbb, soa + ko(0), sob + ko(0), w);
      k1_issue(sa, sbd, vo, la + K1_STAGE, lbb + K1_STAGE, soa + ko(1), sob + ko(1), w);
      ST(8);
      wait_vm<6>();
      {
        unsigned lo, hi;
        asm volatile("v_readfirstlane_b32 %0, %2\n\tv_readfirstlane_b32 %1, %3\n\ts_nop 4" : "=s"(lo), "=s"(hi) : "v"(ep0), "v"(ep1));
        epoch = (u64)lo + ((u64)hi << 32) + 1;
      }
      bar();
      k1_issue(sa, sbd, vo, la + 2 * K1_STAGE, lbb + 2 * K1_STAGE, soa + ko(2), sob + ko(2), w);
      const u32x4 ss = make_srd((xcd & 2) ? Sim_ : Sre_, S_ * S_ * 2);
      const unsigned vs = (lane & 31) * 4096 + (lane >> 5) * 128, sos = ((xcd >> 2) * 1024 + idx * 32) * 4096 + (xcd & 1) * 2048 + w * 256;
      unsigned so = 0;
#pragma unroll 1
      for (int i = 0; i < 10; ++i) {
        if (i == 0) wait_vm<6>(); else if (i == 1) wait_vm<2 * 6>(); else if (i < 6) wait_vm<3 * 6>();
        else if (i == 6) wait_vm<2 * 6 + 4>(); else if (i == 7) wait_vm<6 + 4>(); else wait_vm<4 + (WS2 >= 4) + (WS1 >= 4)>();
        bar();
        if (i == 0) {
          k1_issue(sa, sbd, vo, la + 3 * K1_STAGE, lbb + 3 * K1_STAGE, soa + ko(3), sob + ko(3), w);
          k1_issue(sa, sbd, vo, la + 4 * K1_STAGE, lbb + 4 * K1_STAGE, soa + ko(4), sob + ko(4), w);
        } else if (i == 1) {
          k1_issue(sa, sbd, vo, la, lbb, soa + ko(5), sob + ko(5), w);
          k1_issue(sa, sbd, vo, la + K1_STAGE, lbb + K1_STAGE, soa + ko(6), sob + ko(6), w);
        } else if (i < 5) k1_issue(sa, sbd, vo, la + so, lbb + so, soa + ko(i + 5), sob + ko(i + 5), w);
        so = so == 4 * K1_STAGE ? 0 : so + K1_STAGE;
        if (i == WS1) warm(sink, ss, vs, sos);
        if (i == WS2) warm(sink, ss, vs, sos + 1024);
        if (i == 5) {
          retile_issue(u0, w1_, w2_, lb + K1_TR + w * 4096, lane);
          if (extra) retile_issue(u1, w1_, w2_, lb + K1_TR + 4 * 4096, lane);
        }
      }
      ST(9);
      bar();
    }
#pragma unroll 1
    for (int L = 0; L < 2; ++L) {
      const int sbase = L * 20;
      (void)sbase;
      {
        PHASE_IDS
        IDS_G2
        const u32x4 sa = make_srd(kq < 2 ? opq_p(Sre_) : opq_p(Sim_), S_ * S_ * 2);
        const u32x4 sb_ = make_srd(ws + (L ? WS_AB2 : WS_AB1), SZ_AB);
        const unsigned va = (lane >> 3) * 4096 + (((lane & 7) ^ (lane >> 3)) << 4), vb = lane * 16;
        const unsigned soa = (P * 128 + w * 32) * 4096 + (kq & 1) * 2048;
        const unsigned sob = ((2 * c4 + (w >> 1)) * 128 + kq * 32 + (w & 1)) * 5 * 1024;
        const unsigned la = lb + w * 4096, lbb = lb + 16384 + w * 5120;
        const unsigned ct = (xcd & 1) * 8 + (idx & 7);
        const unsigned rot = ROT2 ? (c4 * 4 + (idx >> 2) * 2 + (idx >> 4)) & 15 : 0;
        asm volatile("s_nop 4" ::: "memory");
        auto issue_a = [&](unsigned l, unsigned so) {
          glds<0, 0>(sa, va, l, so); glds<1024, 8 * 4096>(sa, va, l, so);
          glds<2048, 16 * 4096>(sa, va, l, so); glds<3072, 24 * 4096>(sa, va, l, so);
        };
        if (L == 0) {
          const unsigned u0 = bid * 4 + w, u1 = 1024 + bid;
          const rsrc_t rws = make_rsrc(ws, WS_END);
          wait_vm<0>();
          retile_finish(u0, 0, rws, lb + K1_TR + w * 4096, lane);
          retile_finish(u0, 2, rws, lb + K1_TR + w * 4096, lane);
          if (w == 3 && bid < 56) {
            retile_finish(u1, 0, rws, lb + K1_TR + 4 * 4096, lane);
            retile_finish(u1, 2, rws, lb + K1_TR + 4 * 4096, lane);
          }
        }
#pragma unroll
        for (int s = 0; s < 4; ++s) issue_a(la + s * G2_SLOT, soa + ((s + rot) & 15) * 128);
        if (L == 0) {
          wait_vm<16>();
          raise(ws, FL_W + bid * 4 + w, epoch, lane);
          if (w == 3 && bid < 56) raise(ws, FL_W + 1024 + bid, epoch, lane);
        }
        ST(sbase + 1);
        poll(ws, FL_5 + ((L * 4 + kq) * 4 + c4) * 64 + (((i16 >> 1) * 2 + (w >> 1)) * 4 + (w & 1) + 2 * (i16 & 1)), epoch);
        ST(sbase + 2);
        glds_run_sc1<5>(sb_, vb, lbb, sob + rot * 10240);
        glds_run_sc1<5>(sb_, vb, lbb + G2_SLOT, sob + ((1 + rot) & 15) * 10240);
        wait_vm<5>();
        bar();
        glds_run_sc1<5>(sb_, vb, lbb + 2 * G2_SLOT, sob + ((2 + rot) & 15) * 10240);
        glds_run_sc1<5>(sb_, vb, lbb + 3 * G2_SLOT, sob + ((3 + rot) & 15) * 10240);
        ST(sbase + 3);
        const u32x4 sw = make_srd(ws + WS_W1, SZ_W1);
        const unsigned sow = ct * 140 * 1024;
        unsigned fw, fw0 = 0, fw1 = 0;
        {
          const unsigned n = w * 42 + (lane < 42 ? lane : 0), k3 = n / 3u, g3 = (7 * ct >> 2) + (n - k3 * 3), n2 = n - 60;
          fw = FL_W + (n < 60 ? k3 * 27 + (g3 < 26 ? g3 : 26) : 540 + (n2 >> 1) * 10 + (5 * (idx & 7) >> 2) + (n2 & 1));
        }
#pragma unroll 1
        for (int s = 0; s < 16; ++s) {
          if (s == 0) wait_vm<10>();
          if (s == 1) wait_vm<14>();
          wait_vm<18>();
          if (s == 9 && L == 0) {
            asm volatile("" : "+v"(fw0), "+v"(fw1));
            if (!__all(fw0 == (unsigned)epoch && fw1 == (unsigned)(epoch >> 32))) poll(ws, fw, epoch);
          }
          bar();
          const unsigned slot = (s & 3) * G2_SLOT;
          if (s < 12) {
            issue_a(la + slot, soa + ((s + 4 + rot) & 15) * 128);
            glds_run_sc1<5>(sb_, vb, lbb + slot, sob + ((s + 4 + rot) & 15) * 10240);
            if (s == 6 && L == 0)
              asm volatile("global_load_dword %0, %2, %3 sc1\n\tglobal_load_dword %1, %2, %3 offset:4 sc1"
                           : "+v"(fw0), "+v"(fw1) : "v"(WS_FLG + fw * 128), "s"(ws) : "memory");
          } else if (s < 15) glds_run_sc1<9>(sw, vb, lb + slot + w * 9216, sow + slot + w * 9216);
          else glds_run_sc1<8>(sw, vb, lb + slot + w * 8192, sow + slot + w * 8192);
        }
        ST(sbase + 4);
      }
      {
        PHASE_IDS
        IDS_G3
        if (w >= 2) {
          const rsrc_t rc = make_rsrc(ws + WS_CS1, 20 * F_ * 4);
          const unsigned c = (w - 2) * 64 + lane, col = ct * 112 + c;
          const unsigned vo = c < 112 && col < F_ ? col * 4 : OOB;
          float csp = 0.f;
#pragma unroll
          for (int k = 0; k < 20; ++k) csp += __uint_as_float(__builtin_amdgcn_raw_buffer_load_b32(rc, vo, k * F_ * 4, 16));
          lds_wr1(lb + KM_CS1 + c * 4, csp);
        }
        wait_vm<0>();
        wait_lgkm0();
        ST(sbase + 5);
        bar();
        bar();
      }
      {
        PHASE_IDS
        IDS_G4
        const u32x4 s2 = make_srd(ws + WS_W2, SZ_W2);
        const unsigned so = (c8 * 54 + w * 14) * 5 * 1024;
        glds_run_sc1<10>(s2, lane * 16, lb + w * 20480, so + 7 * 5120);
        glds_run_sc1<5>(s2, lane * 16, lb + w * 20480 + 10240, so + 11 * 5120);
        glds_run_sc1<5>(s2, lane * 16 + (w < 3 ? 0u : OOB), lb + w * 20480 + 15360, so + 12 * 5120);
        wait_vm<0>();
        ST(sbase + 6);
        bar();
        if (LSLEEP) __builtin_amdgcn_s_sleep(LSLEEP);
        glds_run_sc1<10>(s2, lane * 16, lb + G4_LATE + w * 15360, so + 2 * 5120);
        glds_run_sc1<5>(s2, lane * 16, lb + G4_LATE + w * 15360 + 10240, so + 6 * 5120);
        wait_vm<0>();
        bar();
        bar();
      }
      if (L) break;
      {
        PHASE_IDS
        IDS_G3
        const u32x4 sd = make_srd(ct < 8 ? opq_p(Dre_) : opq_p(Dim_), D_ * D_ * 2);
        const unsigned vo = (lane >> 3) * 1280 + (((lane & 7) ^ (lane >> 3)) << 4);
        const unsigned so = (ct & 7) * 80 * 1280;
        sfor<0, 10>([&](auto G) { glds<G.v * 1024, G.v * 8 * 1280>(sd, vo, lb + 20 * w * 1024, so + 2 * w * 128); });
        sfor<0, 10>([&](auto G) { glds<G.v * 1024, G.v * 8 * 1280>(sd, vo, lb + (20 * w + 10) * 1024, so + (2 * w + 1) * 128); });
        sfor<0, 5>([&](auto G) { glds<G.v * 1024, G.v * 8 * 1280>(sd, vo, lb + (140 + w * 5) * 1024, so + (8 + (w >> 1)) * 128 + (w & 1) * 40 * 1280); });
        wait_vm<0>();
        ST(7);
        bar();
        if (WARM_S) {
        __builtin_amdgcn_s_sleep(SSLEEP);
        warm(sink, make_srd((xcd & 2) ? opq_p(Sim_) : opq_p(Sre_), S_ * S_ * 2), (lane & 31) * 4096 + (lane >> 5) * 128,
             ((xcd >> 2) * 1024 + idx * 32) * 4096 + (xcd & 1) * 2048 + w * 256);
        }
        bar();
      }
    }
    asm volatile("" :: "v"(sink));
    return;
  }

  {
    const u64 e = __hip_atomic_load(flag_ptr(ws_, FL_E), RLX_AGENT);
    epoch = (u64)rfl((unsigned)e) + ((u64)rfl((unsigned)(e >> 32)) << 32) + 1;
  }
#pragma unroll 1
  for (int L = 0; L < 2; ++L) {
    const int sbase = L * 20;
    (void)sbase;
    {
      PHASE_IDS
      IDS_G3
      f32x4 acc[2][5];
      float mu[2][4], rs[2][4], csv[5];
      const float sign = ct < 8 ? 1.f : -1.f;
      if (L == 0) {
        const unsigned ra0 = lb + w * 4096 + i16 * 128 + ((q ^ (lane & 7)) << 4);
        const unsigned rb0 = lb + 16384 + i16 * 128 + ((q ^ (lane & 7)) << 4);
        const f32x4 z4 = {0.f, 0.f, 0.f, 0.f};
        f32x4 cs0 = z4, cs1 = z4, rs_[2] = {z4, z4}, rq_[2] = {z4, z4};
#pragma unroll
        for (int r = 0; r < 2; ++r)
#pragma unroll
          for (int f = 0; f < 5; ++f) acc[r][f] = z4;
        bf16x8 fr[2][8];
        bf16x8 one = __builtin_bit_cast(bf16x8, u32x4{0x3f803f80u, 0x3f803f80u, 0x3f803f80u, 0x3f803f80u});
        auto half = [&](auto KK, unsigned so) {
          constexpr int kk = KK.v;
          const unsigned pa = (ra0 ^ ((kk ^ 1) * 64)) + so, pb = (rb0 ^ ((kk ^ 1) * 64)) + so;
          sfor<0, 16>([&](auto M) {
            constexpr int m = M.v;
            if constexpr (m < 10) mma(acc[m & 1][m >> 1], fr[kk][m & 1], fr[kk][2 + (m >> 1)]);
            else if constexpr (m == 10) mma(cs0, one, fr[kk][7]);
            else if constexpr (m == 11) mma(cs1, one, fr[kk][6]);
            else if constexpr (m < 14) mma(rs_[m - 12], fr[kk][m - 12], one);
            else mma(rq_[m - 14], fr[kk][m - 14], fr[kk][m - 14]);
            constexpr int n = m >> 1;
            if constexpr ((m & 1) == 0) fr[kk ^ 1][n] = n < 2 ? lds_rd(pa + n * 2048) : (n < 7 ? lds_rd(pb + (n - 2) * 2048) : lds_rd(pb + w * 2048));
            sb();
          });
        };
        asm volatile("s_nop 1" : "+v"(cs0), "+v"(cs1), "+v"(rs_[0]), "+v"(rs_[1]), "+v"(rq_[0]), "+v"(rq_[1]), "+v"(one));
        fence_all(acc);
        bar();
        ST(38);
        fr[0][0] = lds_rd(ra0); fr[0][1] = lds_rd(ra0 + 2048);
#pragma unroll
        for (int f = 0; f < 5; ++f) fr[0][2 + f] = lds_rd(rb0 + f * 2048);
        fr[0][7] = lds_rd(rb0 + w * 2048);
        sb();
        unsigned so = 0;
#pragma unroll 1
        for (int i = 0; i < 10; ++i) {
          half(IC<0>{}, so);
          wait_lgkm0();
          bar();
          so = so == 4 * K1_STAGE ? 0 : so + K1_STAGE;
          half(IC<1>{}, so);
        }
        fence_all(acc);
        asm volatile("" : "+v"(cs0), "+v"(cs1), "+v"(rs_[0]), "+v"(rs_[1]), "+v"(rq_[0]), "+v"(rq_[1]));
        const unsigned csb = lb + K1_CS;
        if (q == 0) {
          lds_wr1(csb + (w * 16 + i16) * 4, cs0[0]);
          if (w == 0) lds_wr1(csb + (64 + i16) * 4, cs1[0]);
        }
        wait_lgkm0();
        bar();
        ST(18);
#pragma unroll
        for (int f = 0; f < 5; ++f) csv[f] = lds_rd1(csb + (f * 16 + i16) * 4);
#pragma unroll
        for (int r = 0; r < 2; ++r)
#pragma unroll
          for (int j = 0; j < 4; ++j) {
            float m_, r_;
            ln_coef(rs_[r][j], __shfl(rq_[r][j], q * 20 + j), m_, r_);
            mu[r][j] = -m_; rs[r][j] = sign * r_;
          }
        if (w == 0) {
          const rsrc_t rc = make_rsrc(ws + WS_CSD, 256 * 320);
          st4(__float_as_uint(q == 0 ? csv[0] : (q == 1 ? csv[1] : (q == 2 ? csv[2] : csv[3]))), rc, lane * 4, bid * 320);
          st4(__float_as_uint(csv[4]), rc, q == 0 ? 256 + lane * 4 : OOB, bid * 320);
        }
      } else {
        {
          const rsrc_t rc = make_rsrc(ws + WS_CSD, 256 * 320);
#pragma unroll
          for (int f = 0; f < 5; ++f) csv[f] = __uint_as_float(__builtin_amdgcn_raw_buffer_load_b32(rc, (f * 16 + i16) * 4, bid * 320, 16));
        }
        poll(ws, FL_4 + P * 64 + w * 16 + i16, epoch);
        ST(17);
        const rsrc_t rx = make_rsrc(ws + WS_X2, SZ_X);
        const unsigned sox = (P * 8 + w * 2) * 20 * 1024;
        const unsigned rot = ROT5 ? 2 * ((idx & 7) + ((idx & 7) >> 2)) : 0;
        auto wdp = [&](int c) { const unsigned cc = c + (rot >> 1) >= 10 ? c + (rot >> 1) - 10 : c + (rot >> 1);
                                return (cc < 8 ? 10 * cc : 60 + 10 * cc) * 1024; };
        bf16x8 a[20][2];
        unsigned wsink = WARM_X ? WARM_STRIP(rx, sox) : 0u;
#pragma unroll
        for (int k = 0; k < 3; ++k) { a[k][0] = LDX(0, k); a[k][1] = LDX(1, k); }
        const rsrc_t rst = make_rsrc(ws + WS_ST2, SZ_ST);
        const unsigned sto = ((lane >> 5) * 4 * S_ + P * 128 + w * 32 + (lane & 31)) * 8;
        const u32x2 sp0 = ld8u(rst, sto, 0), sp1 = ld8u(rst, sto, S_ * 8), sp2 = ld8u(rst, sto, 2 * S_ * 8), sp3 = ld8u(rst, sto, 3 * S_ * 8);
        sb();
        bar();
        bf16x8 b[2][5];
        const unsigned rb = lb + i16 * 128 + ((q ^ (lane & 7)) << 4);
#pragma unroll
        for (int f = 0; f < 5; ++f) b[0][f] = lds_rd(rb + wdp(0) + f * 2048);
        sb();
        sfor<0, 20>([&](auto K) {
          constexpr int k = K.v;
          const unsigned pb = (rb ^ (((k + 1) & 1) * 64)) + wdp(((k + 1) >> 1) % 10);
          sfor<0, 10>([&](auto M) {
            constexpr int f = M.v >> 1, r = M.v & 1;
            if constexpr (k == 0) mma0(acc[r][f], a[0][r], b[0][f]); else mma(acc[r][f], a[k][r], b[k & 1][f]);
            if constexpr (k + 1 < 20 && r == 0) b[(k + 1) & 1][f] = lds_rd(pb + f * 2048);
            if constexpr (r == 1 && f < 4) {
              constexpr int kn = k < 7 ? 2 * k + 3 + (f >> 1) : (f < 2 ? k + 10 : 20);
              if constexpr (kn < 20) a[kn][f & 1] = LDX(f & 1, kn);
            }
            sb();
          });
          if constexpr (k == 0) ST(40);
          if constexpr (k == 4) ST(41);
          if constexpr (k == 9) ST(42);
        });
        asm volatile("" :: "v"(wsink));
        fence_all(acc);
        bar();
        ST(39);
        float s1 = (__uint_as_float(sp0[0]) + __uint_as_float(sp1[0])) + (__uint_as_float(sp2[0]) + __uint_as_float(sp3[0]));
        float s2 = (__uint_as_float(sp0[1]) + __uint_as_float(sp1[1])) + (__uint_as_float(sp2[1]) + __uint_as_float(sp3[1]));
        auto ta = __builtin_amdgcn_permlane32_swap(__float_as_uint(s1), __float_as_uint(s1), false, false);
        auto tb = __builtin_amdgcn_permlane32_swap(__float_as_uint(s2), __float_as_uint(s2), false, false);
        float m, rr;
        ln_coef(__uint_as_float(ta[0]) + __uint_as_float(ta[1]), __uint_as_float(tb[0]) + __uint_as_float(tb[1]), m, rr);
        m = -m; rr *= sign;
#pragma unroll
        for (int r = 0; r < 2; ++r)
#pragma unroll
          for (int e = 0; e < 4; ++e) { mu[r][e] = __shfl(m, r * 16 + q * 4 + e); rs[r][e] = __shfl(rr, r * 16 + q * 4 + e); }
      }
      g1_store(acc, mu, rs, csv, make_rsrc(ws + (L ? WS_AB2 : WS_AB1), SZ_AB), P, ct, w, i16, q);
      ST(L + 19);
      publish(ws, FL_5 + ((L * 4 + (ct >> 3) * 2 + (P >> 3)) * 4 + ((ct & 7) >> 1)) * 64 + ((P & 7) * 2 + (ct & 1)) * 4 + w, epoch, lane);
    }
    {
      PHASE_IDS
      IDS_G2
      const unsigned m = w >> 1, n = w & 1;
      const unsigned ra = lb + (m * 64 + i16) * 128 + ((q ^ (lane & 7)) << 4);
      const unsigned rb = lb + 16384 + n * 10240 + lane * 16;
      const unsigned row = P * 128 + m * 64 + kq * 16 + i16, ct = 2 * c4 + n;
      const TileOff ro = tile_offsets(L == 0, row, ct, q), oo = tile_offsets(false, row, ct, q);
      f32x4 acc[4][5];
#pragma unroll
      for (int i = 0; i < 4; ++i)
#pragma unroll
        for (int f = 0; f < 5; ++f) acc[i][f] = f32x4{0.f, 0.f, 0.f, 0.f};
      bf16x8 fa[2][4], fb[2][5];
      auto half = [&](auto KK, unsigned so) {
        constexpr int kk = KK.v;
        const unsigned pa = (ra ^ ((kk ^ 1) * 64)) + so, pb = rb + (kk ^ 1) * 5120 + so;
        sfor<0, 20>([&](auto M) {
          constexpr int mm = M.v;
          mma(acc[mm / 5][mm % 5], fb[kk][mm % 5], fa[kk][mm / 5]);
          if constexpr ((mm & 1) == 0 && mm < 10) fb[kk ^ 1][mm >> 1] = lds_rd(pb + (mm >> 1) * 1024);
          if constexpr ((mm & 1) == 0 && mm >= 10 && mm < 18) fa[kk ^ 1][(mm - 10) >> 1] = lds_rd(pa + ((mm - 10) >> 1) * 2048);
          sb();
        });
      };
      if (L) poll(ws, FL_4 + P * 64 + (m * 4 + kq) * 8 + ct, epoch);
      ResTile rt = load_res(make_rsrc(L ? (const void*)(ws + WS_X2) : (const void*)opq_p(x0_), SZ_X), ro);
      fence_all(acc);
      bar();
      ST(sbase + 1);
#pragma unroll
      for (int f = 0; f < 5; ++f) fb[0][f] = lds_rd(rb + f * 1024);
#pragma unroll
      for (int i = 0; i < 4; ++i) fa[0][i] = lds_rd(ra + i * 2048);
      sb();
#pragma unroll 1
      for (int s = 0; s < 16; ++s) {
        half(IC<0>{}, (s & 3) * G2_SLOT);
        wait_lgkm0();
        bar();
        half(IC<1>{}, ((s + 1) & 3) * G2_SLOT);
      }
      fence_all(acc);
      ST(sbase + 2);
      const rsrc_t rxp = make_rsrc(ws + WS_XP + L * SZ_XP, SZ_XP);
      const unsigned xo = ((P * 4 + c4) * 48 + w) * XSLOT;
      f32x4 v[5];
      if (kq == 0) xp_send<0>(acc, v, rxp, xo, lane);
      else if (kq == 1) xp_send<1>(acc, v, rxp, xo, lane);
      else if (kq == 2) xp_send<2>(acc, v, rxp, xo, lane);
      else xp_send<3>(acc, v, rxp, xo, lane);
      const unsigned fx = FL_X + ((L * 64 + P * 4 + c4) * 4 + w) * 4;
      ST(sbase + 3);
      publish(ws, fx + kq, epoch, lane);
      ST(sbase + 4);
      poll(ws, fx + (lane & 3), epoch);
      ST(sbase + 5);
      const unsigned xi = xo + kq * 12 * XSLOT;
      u32x4 g0[3], g1[3]; u32x2 g2[3];
#pragma unroll
      for (int s = 0; s < 3; ++s) {
        g0[s] = ld16u(rxp, lane * 16, xi + s * 4 * XSLOT);
        g1[s] = ld16u(rxp, lane * 16 + 1024, xi + s * 4 * XSLOT);
        g2[s] = ld8u(rxp, lane * 8 + 2048, xi + s * 4 * XSLOT);
      }
      sb();
#pragma unroll
      for (int s = 0; s < 3; ++s) {
        v[0] += f32x4{bflo(g0[s][0]), bfhi(g0[s][0]), bflo(g0[s][1]), bfhi(g0[s][1])};
        v[1] += f32x4{bflo(g0[s][2]), bfhi(g0[s][2]), bflo(g0[s][3]), bfhi(g0[s][3])};
        v[2] += f32x4{bflo(g1[s][0]), bfhi(g1[s][0]), bflo(g1[s][1]), bfhi(g1[s][1])};
        v[3] += f32x4{bflo(g1[s][2]), bfhi(g1[s][2]), bflo(g1[s][3]), bfhi(g1[s][3])};
        v[4] += f32x4{bflo(g2[s][0]), bfhi(g2[s][0]), bflo(g2[s][1]), bfhi(g2[s][1])};
      }
      finish_tile(v, rt, make_rsrc(ws + (L ? WS_X3 : WS_X1), SZ_X), oo, true, make_rsrc(ws + (L ? WS_ST3 : WS_ST1), SZ_ST),
                  q == 0 ? (ct * S_ + row) * 8 : OOB);
      ST(sbase + 6);
      publish(ws, FL_2 + (L * 16 + P) * 64 + (m * 4 + kq) * 8 + ct, epoch, lane);
      ST(sbase + 7);
    }
    u32x4 y2[2]; u32x2 y1[2];
    bf16x8 bp[4][5];
    {
      PHASE_IDS
      IDS_G3
      typedef __attribute__((ext_vector_type(2))) short s16x2;
      poll(ws, FL_2 + (L * 16 + P) * 64 + w * 16 + i16, epoch);
      ST(sbase + 8);
      const rsrc_t rx = make_rsrc(ws + (L ? WS_X3 : WS_X1), SZ_X);
      const rsrc_t rst = make_rsrc(ws + (L ? WS_ST3 : WS_ST1), SZ_ST);
      const unsigned sox = (P * 8 + w * 2) * 20 * 1024;
      const unsigned rot = ROT3 ? (5 * (idx & 7)) >> 1 : 0;
      const unsigned par = ct & 1;
      bf16x8 a[20][2];
#pragma unroll
      for (int k = 0; k < 3; ++k) { a[k][0] = LDX(0, k); a[k][1] = LDX(1, k); }
      const unsigned sto = (2 * q * S_ + P * 128 + w * 32 + i16) * 8;
      const u32x2 sa0 = ld8u(rst, sto, 0), sa1 = ld8u(rst, sto, S_ * 8), sb0 = ld8u(rst, sto + 128, 0), sb1 = ld8u(rst, sto + 128, S_ * 8);
      sb();
      bar();
      f32x4 acc[2][4];
      bf16x8 b[2][4];
      const unsigned rb = lb + lane * 16;
#pragma unroll
      for (int f = 0; f < 4; ++f) b[0][f] = lds_rd(rb + rot * 4096 + f * 1024);
      sb();
      sfor<0, 20>([&](auto K) {
        constexpr int k = K.v;
        const unsigned pb = k + 1 < 20 ? rb + KROT((k + 1) % 20) * 4096 : rb + 80 * 1024 + rot * 3072;
        sfor<0, 8>([&](auto M) {
          constexpr int f = M.v >> 1, r = M.v & 1;
          if constexpr (k == 0) mma0(acc[r][f], b[0][f], a[0][r]); else mma(acc[r][f], b[k & 1][f], a[k][r]);
          if constexpr (r == 0 && (k + 1 < 20 || f < 3)) b[(k + 1) & 1][f] = lds_rd(pb + f * 1024);
          if constexpr (r == 1 && 2 * k + 3 + (f >> 1) < 20) a[2 * k + 3 + (f >> 1)][f & 1] = LDX(f & 1, 2 * k + 3 + (f >> 1));
          sb();
        });
        if constexpr (k == 0) ST(sbase + 9);
        if constexpr (k == 3) ST(51 + L * 5);
        if constexpr (k == 7) ST(52 + L * 5);
        if constexpr (k == 11) ST(53 + L * 5);
        if constexpr (k == 15) ST(54 + L * 5);
        if constexpr (k == 19) ST(55 + L * 5);
      });
      float mu[2], rs[2];
      ln_coef(xsum16_32(__uint_as_float(sa0[0]) + __uint_as_float(sa1[0])), xsum16_32(__uint_as_float(sa0[1]) + __uint_as_float(sa1[1])), mu[0], rs[0]);
      ln_coef(xsum16_32(__uint_as_float(sb0[0]) + __uint_as_float(sb1[0])), xsum16_32(__uint_as_float(sb0[1]) + __uint_as_float(sb1[1])), mu[1], rs[1]);
      auto act = [&](const f32x4& c, int r, const f32x4& cs, unsigned (&pk)[2]) {
        const f32x2 nm = {-mu[r], -mu[r]}, r2 = {rs[r], rs[r]};
#pragma unroll
        for (int e = 0; e < 2; ++e) {
          const f32x2 z = __builtin_elementwise_fma(nm, f32x2{cs[2 * e], cs[2 * e + 1]}, f32x2{c[2 * e], c[2 * e + 1]}) * r2;
          const s16x2 hb = __builtin_bit_cast(s16x2, __builtin_convertvector(z, bf16x2));
          pk[e] = __builtin_bit_cast(unsigned, __builtin_elementwise_max(hb, s16x2{0, 0}));
        }
      };
      auto pair = [&](const unsigned (&u)[2], const unsigned (&v)[2]) {
        auto t0 = __builtin_amdgcn_permlane16_swap(u[0], v[0], false, false);
        auto t1 = __builtin_amdgcn_permlane16_swap(u[1], v[1], false, false);
        return u32x4{t0[0], t1[0], t0[1], t1[1]};
      };
      const unsigned csb = lb + KM_CS1 + q * 16;
      asm volatile("s_nop 7\n\ts_nop 1" : "+v"(acc[0][0]), "+v"(acc[0][1]), "+v"(acc[0][2]), "+v"(acc[0][3]),
                                          "+v"(acc[1][0]), "+v"(acc[1][1]), "+v"(acc[1][2]), "+v"(acc[1][3]));
      wait_lgkm0();
      bar();
      ST(43 + L * 4);
      {
        const rsrc_t rh = make_rsrc(ws + (L ? WS_H2 : WS_H1), SZ_H);
        const unsigned vo = ((((q & 1) << 1) | (q >> 1)) * 16 + i16) * 16;
        const unsigned so = ((P * 8 + w * 2) * 54 + 7 * (ct >> 1) + 4 * par) * 1024;
        u32x4 y[2][2];
#pragma unroll
        for (int p = 0; p < 2; ++p) {
          const f32x4 c0 = lds_rdf(csb + (2 * p + par) * 64), c1 = lds_rdf(csb + (2 * p + 1 + par) * 64);
#pragma unroll
          for (int r = 0; r < 2; ++r) {
            unsigned u[2], v[2];
            act(acc[r][2 * p], r, c0, u); act(acc[r][2 * p + 1], r, c1, v);
            y[p][r] = pair(u, v);
            st16_raw(y[p][r], rh, p == 1 && ct == 15 ? OOB : vo, so + (r * 54 + p) * 1024);
          }
        }
        st_guard(y[0][0], y[0][1], y[1][0], y[1][1]);
      }
      ST(44 + L * 4);
      const rsrc_t rw = make_rsrc(ws + WS_W2, SZ_W2);
      const unsigned sow = ((idx & 7) * 54 + w * 14) * 5 * 1024;
      sb();
      sfor<0, 20>([&](auto K) {
        constexpr int k = K.v;
        const unsigned pb = rb + 80 * 1024 + KROT((k + 1) % 20) * 3072;
        sfor<0, 6>([&](auto M) {
          constexpr int f = M.v >> 1, r = M.v & 1;
          if constexpr (k == 0) mma0(acc[r][f], b[0][f], a[0][r]); else mma(acc[r][f], b[k & 1][f], a[k][r]);
          if constexpr (r == 0 && k + 1 < 20) b[(k + 1) & 1][f] = lds_rd(pb + f * 1024);
          if constexpr (r == 1 && f < 2 && k >= 2 && k < 12) {
            constexpr int n = (k - 2) * 2 + f, st = (n / 5 < 2 ? n / 5 : n / 5 + 2) * 5 + n % 5;
            bp[n / 5][n % 5] = ld16(rw, lane * 16 + (st & 3) * 1024, sow + (st >> 2) * 4096);
          }
          sb();
        });
        if constexpr (k == PUB1) {
          wait_vm<(k >= 11 ? 20 : (k < 2 ? 0 : 2 * (k - 1)))>();
          raise(ws, FL_3 + (L * 32 + P) * 64 + w * 16 + ct, epoch, lane);
        }
      });
      const f32x4 c0 = lds_rdf(csb + (4 + par) * 64), c1 = lds_rdf(csb + (5 + par) * 64), c2 = lds_rdf(csb + (par ? 0 : 6) * 64);
      asm volatile("s_nop 7\n\ts_nop 1" : "+v"(acc[0][0]), "+v"(acc[0][1]), "+v"(acc[0][2]), "+v"(acc[1][0]), "+v"(acc[1][1]), "+v"(acc[1][2]));
      ST(sbase + 10);
#pragma unroll
      for (int r = 0; r < 2; ++r) {
        unsigned u[2], v[2], s[2];
        act(acc[r][0], r, c0, u); act(acc[r][1], r, c1, v); act(acc[r][2], r, c2, s);
        y2[r] = pair(u, v);
        y1[r] = u32x2{s[0], s[1]};
      }
      asm volatile("" : "+v"(y2[0]), "+v"(y2[1]), "+v"(y1[0]), "+v"(y1[1]));
      ST(sbase + 11);
    }
    {
      PHASE_IDS
      IDS_G4
      constexpr int ORD[14] = {0, 1, 4, 5, 7, 8, 11, 12, 2, 3, 6, 9, 10, 13};
      const unsigned sow = (c8 * 54 + w * 14) * 5 * 1024;
      const unsigned row = rt64 * 64 + w * 16 + i16;
      const rsrc_t rw = make_rsrc(ws + WS_W2, SZ_W2);
      const rsrc_t rh = make_rsrc(ws + (L ? WS_H2 : WS_H1), SZ_H);
      const unsigned soh = (rt64 * 4 * 54 + w * 14) * 1024;
      const unsigned vl = lane * 16, vz = lane * 16 + (w < 3 ? 0u : OOB);
      bf16x8 a[14][4], b[14][5];
#define LDW(t, f) b[t][f] = ld16(rw, (ORD[t] < 12 ? vl : vz) + ((ORD[t] * 5 + (f)) & 3) * 1024, sow + ((ORD[t] * 5 + (f)) >> 2) * 4096)
#define LDH(t, i) a[t][i] = ld16(rh, (ORD[t] < 12 ? vl : vz) + (ORD[t] & 3) * 1024, soh + (ORD[t] >> 2) * 4096 + (i) * 54 * 1024)
      const TileOff ro = tile_offsets(false, row, c8, q), oo = tile_offsets(L != 0, row, c8, q);
      const unsigned f3 = FL_3 + (L * 32 + (rt64 >> 1)) * 64 + ((rt64 & 1) * 2 + (lane & 1)) * 16 + 4 * w + ((lane >> 1) & 3);
      poll0(ws, f3, epoch);
      ST(sbase + 13);
      sfor<0, 12>([&](auto N) { LDH(N.v / 4, N.v % 4); });
      sb();
      {
        const unsigned P = (xcd >> 1) * 4 + (idx >> 3), ct = (xcd & 1) * 8 + (idx & 7), par = ct & 1;
        const unsigned vo = ((((q & 1) << 1) | (q >> 1)) * 16 + i16) * 16;
        const unsigned v1 = ((2 * par + (q >> 1)) * 16 + i16) * 16 + (q & 1) * 8;
        const unsigned so = ((P * 8 + w * 2) * 54 + 7 * (ct >> 1)) * 1024;
#pragma unroll
        for (int r = 0; r < 2; ++r) {
          st16_raw(y2[r], rh, ct == 15 ? OOB : vo, so + (r * 54 + 2 + 4 * par) * 1024);
          st8(y1[r], rh, v1, so + (r * 54 + 3) * 1024);
        }
        st_guard(y2[0], y2[1]);
      }
      ResTile rt;
      u64 fl2;
      const rsrc_t rr = make_rsrc(ws + (L ? WS_X3 : WS_X1), SZ_X);
      sb();
      bar();
      f32x4 acc[4][5];
      const unsigned rbe = lb + w * 20480 + lane * 16, rbl = lb + G4_LATE + w * 15360 + lane * 16;
      sfor<0, 14>([&](auto T) {
        constexpr int t = T.v;
        constexpr int ta = t >= 1 && t + 2 < 14 ? t + 2 : -1;
        constexpr int tb = t == 6 ? 11 : (t == 8 ? 12 : (t == 10 ? 13 : -1));
        if constexpr (t == 6) { if (!__all(fl2 == epoch)) poll0(ws, f3 + 16 * 64, epoch); }
        if constexpr (t == 7) bar();
        sfor<0, 20>([&](auto M) {
          constexpr int m = M.v, i = m / 5, f = m % 5;
          if constexpr (t == 0) mma0(acc[i][f], bp[0][f], a[0][i]);
          else if constexpr (t < 4) mma(acc[i][f], bp[t][f], a[t][i]);
          else mma(acc[i][f], b[t][f], a[t][i]);
          if constexpr (t >= 3 && t < 7 && (m & 1) == 1 && m < 10) b[t + 1][m >> 1] = lds_rd(rbe + ((t - 3) * 5 + (m >> 1)) * 1024);
          if constexpr (t >= 7 && t < 10 && (m & 1) == 1 && m < 10) b[t + 1][m >> 1] = lds_rd(rbl + ((t - 7) * 5 + (m >> 1)) * 1024);
          if constexpr (ta >= 0 && (m & 1) == 0 && m < 8) LDH(ta, m >> 1);
          if constexpr (tb >= 0 && (m & 1) == 0 && m >= 8 && m < 18) LDW(tb, (m - 8) >> 1);
          if constexpr (t == 2 && m == 10) rt = load_res(rr, ro);
          if constexpr (t == 4 && m == 18) fl2 = __hip_atomic_load(flag_ptr(ws, f3 + 16 * 64), RLX_AGENT);
          sb();
        });
        if constexpr (t == 2) {
          wait_vm<11>();
          raise(ws, FL_3 + (L * 32 + 16 + (xcd >> 1) * 4 + (idx >> 3)) * 64 + w * 16 + (xcd & 1) * 8 + (idx & 7), epoch, lane);
          ST(sbase + 12);
        }
        if constexpr (t == 0) ST(sbase + 14);
        if constexpr (t == 3) ST(45 + L * 4);
        if constexpr (t == 6) ST(46 + L * 4);
      });
      fence_all(acc);
      ST(sbase + 15);
      f32x4 v[5];
      if (w == 0) sm_scatter<0>(rbl, acc, v);
      else if (w == 1) sm_scatter<1>(rbl, acc, v);
      else if (w == 2) sm_scatter<2>(rbl, acc, v);
      else sm_scatter<3>(rbl, acc, v);
      wait_lgkm0();
      bar();
#pragma unroll
      for (int s = 1; s < 4; ++s) {
        const unsigned src = lb + G4_LATE + ((w + s) & 3) * 15360 + (3 - s) * 5 * 1024 + lane * 16;
#pragma unroll
        for (int f = 0; f < 5; ++f) v[f] += lds_rdf(src + f * 1024);
      }
      finish_tile(v, rt, L ? make_rsrc(opq_p(out_), SZ_X) : make_rsrc(ws + WS_X2, SZ_X), oo, L == 0, make_rsrc(ws + WS_ST2, SZ_ST),
                  q == 0 ? (c8 * S_ + row) * 8 : OOB);
      ST(sbase + 16);
      if (L == 0) publish(ws, FL_4 + (rt64 >> 1) * 64 + ((rt64 & 1) * 4 + w) * 8 + c8, epoch, lane);
    }
  }
  if (blockIdx.x == 0 && w0 == 0) raise(ws_, FL_E, epoch, tid0);
  ST(37);
}

extern "C" void kernel_launch(void* const* d_in, const int* in_sizes, int n_in, void* d_out, int out_size, void* d_ws,
                              size_t ws_size, hipStream_t stream) {
  char* ws = (char*)d_ws;
  (void)hipFuncSetAttribute((const void*)k_all, hipFuncAttributeMaxDynamicSharedMemorySize, KM_LDS);
  k_all<<<256, 512, KM_LDS, stream>>>((const u16*)d_in[0], (const u16*)d_in[1], (const u16*)d_in[2], (const u16*)d_in[3],
                                     (const u16*)d_in[4], (const u16*)d_in[6], (const u16*)d_in[7], ws, (u16*)d_out STAMP_PASS(0));
}
```

```cpp
#include <hip/hip_runtime.h>
#include <hip/hip_bf16.h>
#include <stdint.h>

#define DI __device__ __forceinline__
typedef unsigned short u16;
typedef __attribute__((ext_vector_type(8))) __bf16 bf16x8;
typedef __attribute__((ext_vector_type(2))) __bf16 bf16x2;
typedef __attribute__((ext_vector_type(4))) float f32x4;
typedef __attribute__((ext_vector_type(2))) float f32x2;
typedef __attribute__((ext_vector_type(4))) unsigned u32x4;
typedef __attribute__((ext_vector_type(2))) unsigned u32x2;
typedef __attribute__((ext_vector_type(4))) short s16x4;
#define LDS_AS __attribute__((address_space(3)))
#define GLB_AS __attribute__((address_space(1)))
typedef LDS_AS char* ldsp;
typedef GLB_AS unsigned gu32;
typedef unsigned long long u64;
typedef GLB_AS u64 gu64;
typedef __amdgpu_buffer_rsrc_t rsrc_t;
#define RLX_AGENT __ATOMIC_RELAXED, __HIP_MEMORY_SCOPE_AGENT

constexpr int S_ = 2048, D_ = 640, F_ = 1728;
constexpr float EPS = 1e-5f;
constexpr unsigned OOB = 0x7f000000u;

constexpr unsigned SZ_AB = 8 * 128 * 5 * 1024;
constexpr unsigned SZ_X = 128 * 20 * 1024;
constexpr unsigned SZ_H = 128 * 54 * 1024;
constexpr unsigned SZ_W1 = 16 * 140 * 1024;
constexpr unsigned SZ_W2 = 8 * 54 * 5 * 1024;
constexpr unsigned SZ_ST = 2048 * 64;
constexpr unsigned WS_AB1 = 0;
constexpr unsigned WS_AB2 = WS_AB1 + SZ_AB;
constexpr unsigned WS_X1 = WS_AB2 + SZ_AB;
constexpr unsigned WS_X2 = WS_X1 + SZ_X;
constexpr unsigned WS_X3 = WS_X2 + SZ_X;
constexpr unsigned WS_H1 = WS_X3 + SZ_X;
constexpr unsigned WS_H2 = WS_H1 + SZ_H;
constexpr unsigned WS_W1 = WS_H2 + SZ_H;
constexpr unsigned WS_W2 = WS_W1 + SZ_W1;
constexpr unsigned WS_ST1 = WS_W2 + SZ_W2;
constexpr unsigned WS_ST2 = WS_ST1 + SZ_ST;
constexpr unsigned WS_ST3 = WS_ST2 + SZ_ST;
constexpr unsigned WS_CS1 = WS_ST3 + SZ_ST;
constexpr unsigned WS_CSD = WS_CS1 + 20 * 1728 * 4;
constexpr unsigned XSLOT = 2560;
constexpr unsigned SZ_XP = 256 * 12 * XSLOT;
constexpr unsigned WS_XP = (WS_CSD + 256 * 320 + 4095) & ~4095u;
constexpr unsigned WS_FLG = WS_XP + 2 * SZ_XP;
constexpr unsigned FL_X = 0;
constexpr unsigned FL_2 = FL_X + 2048;
constexpr unsigned FL_3 = FL_2 + 2048;
constexpr unsigned FL_4 = FL_3 + 4096;
constexpr unsigned FL_5 = FL_4 + 1024;
constexpr unsigned FL_W = FL_5 + 2048;
constexpr unsigned FL_E = FL_W + 1088;
constexpr unsigned WS_END = WS_FLG + (FL_E + 1) * 128;

DI u32x4 make_srd(const void* p, unsigned bytes) {
  uint64_t a = (uint64_t)p;
  u32x4 r;
  r[0] = (unsigned)a; r[1] = (unsigned)(a >> 32) & 0xffffu; r[2] = bytes; r[3] = 0x00020000u;
  return r;
}
DI rsrc_t make_rsrc(const void* p, unsigned bytes) {
  return __builtin_amdgcn_make_buffer_rsrc((void*)p, (short)0, (int)bytes, 0x00020000);
}
template <int LOFF, int GOFF>
DI void glds(u32x4 srd, unsigned voff, unsigned ldsb, unsigned soff) {
  unsigned tmp;
  asm volatile("s_add_u32 m0, %1, %2\n\ts_add_u32 %0, %3, %4\n\tbuffer_load_dwordx4 %5, %6, %0 offen lds"
               : "=&s"(tmp)
               : "s"(ldsb), "i"(LOFF), "s"(soff), "i"(GOFF), "v"(voff), "s"(srd)
               : "memory", "scc");
}
template <int N> DI void glds_lin(u32x4 srd, unsigned voff, unsigned ldsb, unsigned soff) {
  if constexpr (N == 4)
    asm volatile("s_mov_b32 m0, %0\n\ts_nop 0\n\tbuffer_load_dwordx4 %1, %2, %3 offen lds\n\tbuffer_load_dwordx4 %1, %2, %3 offen offset:1024 lds\n\t"
                 "buffer_load_dwordx4 %1, %2, %3 offen offset:2048 lds\n\tbuffer_load_dwordx4 %1, %2, %3 offen offset:3072 lds"
                 :: "s"(ldsb), "v"(voff), "s"(srd), "s"(soff) : "memory");
  else if constexpr (N == 3)
    asm volatile("s_mov_b32 m0, %0\n\ts_nop 0\n\tbuffer_load_dwordx4 %1, %2, %3 offen lds\n\tbuffer_load_dwordx4 %1, %2, %3 offen offset:1024 lds\n\t"
                 "buffer_load_dwordx4 %1, %2, %3 offen offset:2048 lds"
                 :: "s"(ldsb), "v"(voff), "s"(srd), "s"(soff) : "memory");
  else
    asm volatile("s_mov_b32 m0, %0\n\ts_nop 0\n\tbuffer_load_dwordx4 %1, %2, %3 offen lds"
                 :: "s"(ldsb), "v"(voff), "s"(srd), "s"(soff) : "memory");
}
template <int IMM> DI void ldv(bf16x8& d, u32x4 srd, unsigned voff, unsigned soff) {
  asm volatile("buffer_load_dwordx4 %0, %1, %2, %3 offen offset:%4 sc1" : "=v"(d) : "v"(voff), "s"(srd), "s"(soff), "i"(IMM) : "memory");
}
template <int N> DI void wait_vm() { asm volatile("s_waitcnt vmcnt(%0)" ::"i"(N) : "memory"); }
template <int N> DI void wait_vm5(bf16x8 (&b)[5]) {
  asm volatile("s_waitcnt vmcnt(%5)" : "+v"(b[0]), "+v"(b[1]), "+v"(b[2]), "+v"(b[3]), "+v"(b[4]) : "i"(N) : "memory");
}
DI void wait_lgkm0() { asm volatile("s_waitcnt lgkmcnt(0)" ::: "memory"); }
DI void sb() { __builtin_amdgcn_sched_barrier(0); }
DI void bar() { asm volatile("s_barrier" ::: "memory"); }

DI bf16x8 lds_rd(unsigned addr) { return *(const LDS_AS bf16x8*)(ldsp)(uintptr_t)addr; }
DI f32x4 lds_rdf(unsigned addr) { return *(const LDS_AS f32x4*)(ldsp)(uintptr_t)addr; }
DI void lds_wrf(unsigned addr, f32x4 v) { *(LDS_AS f32x4*)(ldsp)(uintptr_t)addr = v; }
DI float lds_rd1(unsigned addr) { return *(const LDS_AS float*)(ldsp)(uintptr_t)addr; }
DI void lds_wr1(unsigned addr, float v) { *(LDS_AS float*)(ldsp)(uintptr_t)addr = v; }
DI f32x4 mfma(bf16x8 a, bf16x8 b, f32x4 c) { return __builtin_amdgcn_mfma_f32_16x16x32_bf16(a, b, c, 0, 0, 0); }
DI void mma(f32x4& c, const bf16x8& a, const bf16x8& b) {
  asm volatile("v_mfma_f32_16x16x32_bf16 %0, %1, %2, %0" : "+v"(c) : "v"(a), "v"(b));
}
DI void mma0(f32x4& c, const bf16x8& a, const bf16x8& b) {
  asm volatile("v_mfma_f32_16x16x32_bf16 %0, %1, %2, 0" : "=&v"(c) : "v"(a), "v"(b));
}
template <int N> DI void fence_acc(f32x4 (&a)[N]) {
  if constexpr (N == 5) asm volatile("s_nop 7\n\ts_nop 1" : "+v"(a[0]), "+v"(a[1]), "+v"(a[2]), "+v"(a[3]), "+v"(a[4]));
  else asm volatile("s_nop 7\n\ts_nop 1" : "+v"(a[0]), "+v"(a[1]), "+v"(a[2]), "+v"(a[3]), "+v"(a[4]), "+v"(a[5]), "+v"(a[6]));
}
template <int N> DI void pin_acc(f32x4 (&a)[N]) {
  if constexpr (N == 5) asm volatile("" : "+v"(a[0]), "+v"(a[1]), "+v"(a[2]), "+v"(a[3]), "+v"(a[4]));
  else asm volatile("" : "+v"(a[0]), "+v"(a[1]), "+v"(a[2]), "+v"(a[3]), "+v"(a[4]), "+v"(a[5]), "+v"(a[6]));
}
template <int M, int N> DI void fence_all(f32x4 (&a)[M][N]) {
#pragma unroll
  for (int i = 1; i < M; ++i) pin_acc(a[i]);
  fence_acc(a[0]);
#pragma unroll
  for (int i = 1; i < M; ++i) pin_acc(a[i]);
}
DI unsigned pk2(float a, float b) {
  f32x2 v = {a, b};
  return __builtin_bit_cast(unsigned, __builtin_convertvector(v, bf16x2));
}
DI float bflo(unsigned u) { return __uint_as_float(u << 16); }
DI float bfhi(unsigned u) { return __uint_as_float(u & 0xffff0000u); }
DI float dot2(unsigned a, unsigned b, float c) {
  return __builtin_amdgcn_fdot2_f32_bf16(__builtin_bit_cast(bf16x2, a), __builtin_bit_cast(bf16x2, b), c, false);
}
DI float xsum16_32(float x) {
  auto s = __builtin_amdgcn_permlane16_swap(__float_as_uint(x), __float_as_uint(x), false, false);
  x = __uint_as_float(s[0]) + __uint_as_float(s[1]);
  auto t = __builtin_amdgcn_permlane32_swap(__float_as_uint(x), __float_as_uint(x), false, false);
  return __uint_as_float(t[0]) + __uint_as_float(t[1]);
}
template <int V> struct IC { static constexpr int v = V; };
template <int I, int N, class Fn> DI void sfor(Fn&& f) {
  if constexpr (I < N) { f(IC<I>{}); sfor<I + 1, N>(f); }
}
DI unsigned rfl(unsigned v) { return __builtin_amdgcn_readfirstlane(v); }
DI unsigned opq_v(unsigned x) { asm volatile("" : "+v"(x)); return x; }
DI unsigned opq_s(unsigned x) { asm volatile("" : "+s"(x)); return x; }
template <class T> DI T* opq_p(T* x) { asm volatile("" : "+s"(x)); return x; }
DI bf16x8 ld16(rsrc_t r, unsigned voff, unsigned soff) { return __builtin_bit_cast(bf16x8, __builtin_amdgcn_raw_buffer_load_b128(r, voff, soff, 16)); }
DI u32x4 ld16u(rsrc_t r, unsigned voff, unsigned soff) { return __builtin_amdgcn_raw_buffer_load_b128(r, voff, soff, 16); }
DI u32x2 ld8u(rsrc_t r, unsigned voff, unsigned soff) { return __builtin_amdgcn_raw_buffer_load_b64(r, voff, soff, 16); }
DI void st16(u32x4 v, rsrc_t r, unsigned voff, unsigned soff) {
  __builtin_amdgcn_raw_buffer_store_b128(v, r, voff, soff, 16);
  asm volatile("s_nop 1" :: "v"(v) : "memory");
}
DI void st16_raw(u32x4 v, rsrc_t r, unsigned voff, unsigned soff) { __builtin_amdgcn_raw_buffer_store_b128(v, r, voff, soff, 16); }
template <class... T> DI void st_guard(const T&... v) {
  (..., [](const auto& x) { asm volatile("" :: "v"(x) : "memory"); }(v));
  asm volatile("s_nop 1" ::: "memory");
  (..., [](const auto& x) { asm volatile("" :: "v"(x)); }(v));
}
DI void st8(u32x2 v, rsrc_t r, unsigned voff, unsigned soff) { __builtin_amdgcn_raw_buffer_store_b64(v, r, voff, soff, 16); }
DI void st4(unsigned v, rsrc_t r, unsigned voff, unsigned soff) { __builtin_amdgcn_raw_buffer_store_b32(v, r, voff, soff, 16); }

#define ST(i)
#define STAMP_ARGS
#define STAMP_FWD
#define STAMP_PASS(k)

template <int KS> DI unsigned foff(unsigned row, unsigned col) {
  return ((row >> 4) * KS + (col >> 5)) * 1024u + (((col >> 3) & 3) * 16u + (row & 15)) * 16u + (col & 7) * 2u;
}

extern __shared__ __attribute__((aligned(1024))) char smem[];
DI unsigned lds_base() { return (unsigned)(uintptr_t)(ldsp)smem; }

DI void ln_coef(float s1, float s2, float& mu, float& rs) {
  mu = s1 * (1.f / D_);
  rs = __builtin_amdgcn_rsqf(fmaxf(s2 * (1.f / D_) - mu * mu, 0.f) + EPS);
}

DI void g1_store(const f32x4 (&acc)[2][5], const float (&mu)[2][4], const float (&rs)[2][4], const float (&csv)[5], rsrc_t rab,
                 unsigned P, unsigned ct, unsigned w, unsigned i16, unsigned q) {
  const unsigned kstep = (ct >> 3) * 64 + P * 4 + w;
  const unsigned cq = ((q & 1) << 1) | (q >> 1);
  const unsigned so = ((ct & 7) * 128 + kstep) * 5 * 1024;
  const unsigned vo = (cq * 16 + i16) * 16;
  u32x4 y[5];
#pragma unroll
  for (int f = 0; f < 5; ++f) {
    const f32x2 cs = {csv[f], csv[f]};
    unsigned pk[2][2];
#pragma unroll
    for (int r = 0; r < 2; ++r)
#pragma unroll
      for (int e = 0; e < 2; ++e) {
        const f32x2 t = __builtin_elementwise_fma(f32x2{mu[r][2 * e], mu[r][2 * e + 1]}, cs, f32x2{acc[r][f][2 * e], acc[r][f][2 * e + 1]}) *
                        f32x2{rs[r][2 * e], rs[r][2 * e + 1]};
        pk[r][e] = __builtin_bit_cast(unsigned, __builtin_convertvector(t, bf16x2));
      }
    auto t0 = __builtin_amdgcn_permlane16_swap(pk[0][0], pk[1][0], false, false);
    auto t1 = __builtin_amdgcn_permlane16_swap(pk[0][1], pk[1][1], false, false);
    y[f] = u32x4{t0[0], t1[0], t0[1], t1[1]};
    st16_raw(y[f], rab, vo + (f & 3) * 1024, so + (f >> 2) * 4096);
  }
  st_guard(y[0], y[1], y[2], y[3], y[4]);
}

constexpr int K1_STAGE = 26 * 1024;
constexpr int K1_TR = 5 * K1_STAGE;
constexpr int K1_CS = K1_TR + 5 * 4096;

DI void k1_issue(u32x4 sa, u32x4 sbd, unsigned vo, unsigned la, unsigned lbb, unsigned soa, unsigned sob, unsigned w) {
  glds<0, 0>(sa, vo, la, soa);
  glds<1024, 8 * 1280>(sa, vo, la, soa);
  glds<2048, 16 * 1280>(sa, vo, la, soa);
  glds<3072, 24 * 1280>(sa, vo, la, soa);
  glds<0, 0>(sbd, vo, lbb, sob);
  glds<4096, 32 * 1280>(sbd, vo, lbb, sob);
  if (w < 2) glds<8192, 64 * 1280>(sbd, vo, lbb, sob);
}
struct RtUnit { bool first; unsigned ks, g; };
DI RtUnit rt_unit(unsigned u) {
  RtUnit r;
  r.first = u < 540;
  if (r.first) { r.ks = u / 27u; r.g = u - r.ks * 27u; }
  else { const unsigned v = u - 540; r.ks = v / 10u; r.g = v - r.ks * 10u; }
  return r;
}
DI void retile_issue(unsigned u, const u16* w1, const u16* w2, unsigned ldst, unsigned lane) {
  const RtUnit t = rt_unit(u);
  const unsigned ld = t.first ? F_ * 2 : D_ * 2;
  u32x4 srd = make_srd(t.first ? w1 : w2, D_ * F_ * 2);
  const unsigned vo = (lane >> 3) * ld + (lane & 7) * 16;
  const unsigned so = t.ks * 32 * ld + t.g * 128;
  glds<0, 0>(srd, vo, ldst, so);
  glds<1024, 0>(srd, vo, ldst, so + 8 * ld);
  glds<2048, 0>(srd, vo, ldst, so + 16 * ld);
  glds<3072, 0>(srd, vo, ldst, so + 24 * ld);
}
DI unsigned w1_off(unsigned rt, unsigned ks) {
  const unsigned c = rt / 7u, f = rt - c * 7, g = (c & 1) ? (f == 0 ? 6 : f - 1) : f;
  return WS_W1 + (c * 140 + (g < 4 ? ks * 4 + g : 80 + ks * 3 + g - 4)) * 1024;
}
DI void retile_finish(unsigned u, unsigned f0, rsrc_t rws, unsigned ldst, unsigned lane) {
  const RtUnit t = rt_unit(u);
  const unsigned i = lane & 15, q = lane >> 4;
  const unsigned base = ldst + (8 * q + (i >> 2)) * 128 + (i & 3) * 8 + f0 * 32;
  u32x4 v[2];
#pragma unroll
  for (int f = 0; f < 2; ++f) {
    s16x4 lo = __builtin_amdgcn_ds_read_tr16_b64_v4i16((LDS_AS s16x4*)(uintptr_t)(base + f * 32));
    s16x4 hi = __builtin_amdgcn_ds_read_tr16_b64_v4i16((LDS_AS s16x4*)(uintptr_t)(base + f * 32 + 512));
    u32x2 l2 = __builtin_bit_cast(u32x2, lo), h2 = __builtin_bit_cast(u32x2, hi);
    v[f] = u32x4{l2[0], l2[1], h2[0], h2[1]};
  }
  const unsigned rt = t.g * 4 + f0;
  if (t.first) {
    st16(v[0], rws, lane * 16, w1_off(rt, t.ks));
    st16(v[1], rws, lane * 16, w1_off(rt + 1, t.ks));
    float s[2];
#pragma unroll
    for (int f = 0; f < 2; ++f)
      s[f] = xsum16_32(((bflo(v[f][0]) + bfhi(v[f][0])) + (bflo(v[f][1]) + bfhi(v[f][1]))) + ((bflo(v[f][2]) + bfhi(v[f][2])) + (bflo(v[f][3]) + bfhi(v[f][3]))));
    st4(__float_as_uint(q == 0 ? s[0] : s[1]), rws, lane < 32 ? lane * 4 : OOB, WS_CS1 + (t.ks * F_ + rt * 16) * 4);
  } else {
    const unsigned c0 = rt / 5u, c1 = (rt + 1) / 5u;
    st16(v[0], rws, lane * 16, WS_W2 + ((c0 * 54 + t.ks) * 5 + (rt - c0 * 5)) * 1024);
    st16(v[1], rws, lane * 16, WS_W2 + ((c1 * 54 + t.ks) * 5 + (rt + 1 - c1 * 5)) * 1024);
  }
}

#define ASLEEP 0
#define W2SLEEP 0
#define WDSLEEP 0
#define WARM_X 0
#define WARM_W 0
#define WARM_H 0
#define WARM_S 0
#define WARM_0 0
#define WS1 3
#define WS2 6
#define ROT1 1
#define ROT2 0
#define ROT3 0
#define ROT4 1
#define ROT5 0
#define PUB1 12
#define LSLEEP 20
#define RGAP 10
#define RSLEEP 0
#define ASLEEP2 40
#define RDPAT 1
#define LAZY5 0
#define LAZY2 0
#define WDPRE 6
#define CODEWARM 1
#define PDELAY 6
#define WSLEEP 52
#define SSLEEP 46
constexpr int KM_LDS = 160 * 1024;
constexpr int KM_CS1 = KM_LDS - 1024;
constexpr int G2_A = 80 * 1024, G2_AS = 16 * 1024, G2_BS = 20 * 1024;
constexpr int G4_LATE = 80 * 1024;

DI gu64* flag_ptr(char* ws, unsigned f) { return (gu64*)(ws + WS_FLG + f * 128); }
DI void raise(char* ws, unsigned f, u64 epoch, unsigned lane) {
  if (lane < 16) __hip_atomic_store(flag_ptr(ws, f) + lane, epoch, RLX_AGENT);
}
DI void publish(char* ws, unsigned f, u64 epoch, unsigned lane) {
  wait_vm<0>();
  raise(ws, f, epoch, lane);
}
DI void poll0(char* ws, unsigned f, u64 epoch) {
  gu64* p = flag_ptr(ws, f);
#pragma unroll 1
  for (unsigned s = 0; s < (1u << 16); ++s) {
    if (__all(__hip_atomic_load(p, RLX_AGENT) == epoch)) break;
    __builtin_amdgcn_s_sleep(1);
  }
  asm volatile("" ::: "memory");
}
DI u64 poll_mask(char* ws, unsigned f, u64 epoch, u64 need) {
  gu64* p = flag_ptr(ws, f);
  u64 got = 0;
#pragma unroll 1
  for (unsigned s = 0; s < (1u << 16); ++s) {
    got = __ballot(__hip_atomic_load(p, RLX_AGENT) == epoch);
    if ((got & need) == need) break;
    __builtin_amdgcn_s_sleep(1);
  }
  asm volatile("" ::: "memory");
  return got;
}
DI void poll(char* ws, unsigned f, u64 epoch) {
  if (PDELAY) __builtin_amdgcn_s_sleep(PDELAY);
  poll0(ws, f, epoch);
}
DI void warm(unsigned& sink, u32x4 srd, unsigned voff, unsigned soff) {
  asm volatile("buffer_load_dword %0, %1, %2, %3 offen" : "+v"(sink) : "v"(voff), "s"(srd), "s"(soff) : "memory");
}
template <int N> DI void glds_run(u32x4 srd, unsigned voff, unsigned l, unsigned so) {
#pragma unroll
  for (int g = 0; g < N / 4; ++g) glds_lin<4>(srd, voff, l + g * 4096, so + g * 4096);
  if constexpr (N % 4 == 3) glds_lin<3>(srd, voff, l + (N / 4) * 4096, so + (N / 4) * 4096);
  else {
#pragma unroll
    for (int g = N / 4 * 4; g < N; ++g) glds_lin<1>(srd, voff, l + g * 1024, so + g * 1024);
  }
}
DI void glds4_sc1(u32x4 srd, unsigned voff, unsigned l, unsigned so) {
  asm volatile("s_mov_b32 m0, %0\n\ts_nop 0\n\tbuffer_load_dwordx4 %1, %2, %3 offen sc1 lds\n\tbuffer_load_dwordx4 %1, %2, %3 offen offset:1024 sc1 lds\n\t"
               "buffer_load_dwordx4 %1, %2, %3 offen offset:2048 sc1 lds\n\tbuffer_load_dwordx4 %1, %2, %3 offen offset:3072 sc1 lds"
               :: "s"(l), "v"(voff), "s"(srd), "s"(so) : "memory");
}
DI void glds1_sc1(u32x4 srd, unsigned voff, unsigned l, unsigned so) {
  asm volatile("s_mov_b32 m0, %0\n\ts_nop 0\n\tbuffer_load_dwordx4 %1, %2, %3 offen sc1 lds" :: "s"(l), "v"(voff), "s"(srd), "s"(so) : "memory");
}
template <int N> DI void glds_run_sc1(u32x4 srd, unsigned voff, unsigned l, unsigned so) {
#pragma unroll
  for (int g = 0; g < N / 4; ++g) glds4_sc1(srd, voff, l + g * 4096, so + g * 4096);
#pragma unroll
  for (int g = N / 4 * 4; g < N; ++g) glds1_sc1(srd, voff, l + g * 1024, so + g * 1024);
}

struct ResTile { u32x4 r0, r1; u32x2 r2; };
struct TileOff { unsigned a0, a1, a2; };
DI TileOff tile_offsets(bool rm, unsigned row, unsigned ct, unsigned q) {
  const unsigned cA = ct * 80 + 16 * (q & 1) + 8 * (q >> 1), cS = ct * 80 + 64 + 4 * q;
  TileOff t;
  if (rm) { t.a0 = (row * D_ + cA) * 2; t.a1 = t.a0 + 64; t.a2 = (row * D_ + cS) * 2; }
  else { t.a0 = foff<20>(row, cA); t.a1 = foff<20>(row, cA + 32); t.a2 = foff<20>(row, cS); }
  asm volatile("" : "+v"(t.a0), "+v"(t.a1), "+v"(t.a2));
  return t;
}
DI ResTile load_res(rsrc_t rr, const TileOff& t) {
  ResTile rt;
  rt.r0 = ld16u(rr, t.a0, 0); rt.r1 = ld16u(rr, t.a1, 0); rt.r2 = ld8u(rr, t.a2, 0);
  return rt;
}
DI void finish_tile(f32x4 (&v)[5], const ResTile& rt, rsrc_t rout, const TileOff& o, bool stats, rsrc_t rst, unsigned sto) {
#pragma unroll
  for (int p = 0; p < 2; ++p)
#pragma unroll
    for (int r = 0; r < 4; ++r) {
      auto t = __builtin_amdgcn_permlane16_swap(__float_as_uint(v[2 * p][r]), __float_as_uint(v[2 * p + 1][r]), false, false);
      v[2 * p][r] = __uint_as_float(t[0]); v[2 * p + 1][r] = __uint_as_float(t[1]);
    }
  u32x4 y0, y1; u32x2 y2;
#pragma unroll
  for (int j = 0; j < 2; ++j) {
    y0[j] = pk2(v[0][2 * j] + bflo(rt.r0[j]), v[0][2 * j + 1] + bfhi(rt.r0[j]));
    y0[2 + j] = pk2(v[1][2 * j] + bflo(rt.r0[2 + j]), v[1][2 * j + 1] + bfhi(rt.r0[2 + j]));
    y1[j] = pk2(v[2][2 * j] + bflo(rt.r1[j]), v[2][2 * j + 1] + bfhi(rt.r1[j]));
    y1[2 + j] = pk2(v[3][2 * j] + bflo(rt.r1[2 + j]), v[3][2 * j + 1] + bfhi(rt.r1[2 + j]));
    y2[j] = pk2(v[4][2 * j] + bflo(rt.r2[j]), v[4][2 * j + 1] + bfhi(rt.r2[j]));
  }
  st16_raw(y0, rout, o.a0, 0); st16_raw(y1, rout, o.a1, 0); st8(y2, rout, o.a2, 0);
  st_guard(y0, y1);
  if (stats) {
    float s1a = 0.f, s1b = 0.f, s2a = 0.f, s2b = 0.f;
#pragma unroll
    for (int j = 0; j < 4; ++j) {
      s1a = dot2(y0[j], 0x3f803f80u, s1a); s2a = dot2(y0[j], y0[j], s2a);
      s1b = dot2(y1[j], 0x3f803f80u, s1b); s2b = dot2(y1[j], y1[j], s2b);
    }
    s1a = dot2(y2[0], 0x3f803f80u, s1a); s2a = dot2(y2[0], y2[0], s2a);
    s1b = dot2(y2[1], 0x3f803f80u, s1b); s2b = dot2(y2[1], y2[1], s2b);
    const float s1 = xsum16_32(s1a + s1b), s2 = xsum16_32(s2a + s2b);
    st8(u32x2{__float_as_uint(s1), __float_as_uint(s2)}, rst, sto, 0);
  }
}

template <int W> DI void sm_scatter(unsigned wb, const f32x4 (&acc)[4][5], f32x4 (&own)[5]) {
#pragma unroll
  for (int d = 0; d < 4; ++d)
#pragma unroll
    for (int f = 0; f < 5; ++f) {
      if (d == W) own[f] = acc[d][f];
      else lds_wrf(wb + (((d - W - 1) & 3) * 5 + f) * 1024, acc[d][f]);
    }
  asm volatile("; scatter %5" : "+v"(own[0]), "+v"(own[1]), "+v"(own[2]), "+v"(own[3]), "+v"(own[4]) : "i"(W));
}
template <int KQ> DI void xp_send(const f32x4 (&acc)[4][5], f32x4 (&own)[5], rsrc_t rx, unsigned so, unsigned lane) {
  u32x4 y[3][2];
  sfor<0, 3>([&](auto S) {
    constexpr int d = (KQ + 1 + S.v) & 3;
    const unsigned o = so + (d * 3 + ((KQ - d - 1) & 3)) * 4 * XSLOT;
    unsigned p[10];
#pragma unroll
    for (int f = 0; f < 5; ++f) { p[2 * f] = pk2(acc[d][f][0], acc[d][f][1]); p[2 * f + 1] = pk2(acc[d][f][2], acc[d][f][3]); }
    y[S.v][0] = u32x4{p[0], p[1], p[2], p[3]}; y[S.v][1] = u32x4{p[4], p[5], p[6], p[7]};
    st16_raw(y[S.v][0], rx, lane * 16, o);
    st16_raw(y[S.v][1], rx, lane * 16 + 1024, o);
    st8(u32x2{p[8], p[9]}, rx, lane * 8 + 2048, o);
  });
  st_guard(y[0][0], y[0][1], y[1][0], y[1][1], y[2][0], y[2][1]);
#pragma unroll
  for (int f = 0; f < 5; ++f) own[f] = acc[KQ][f];
  asm volatile("; send %5" : "+v"(own[0]), "+v"(own[1]), "+v"(own[2]), "+v"(own[3]), "+v"(own[4]) : "i"(KQ));
}

DI constexpr unsigned wd_pos(int c) { return c < 8 ? 10 * c : 60 + 10 * c; }
DI constexpr int a_when(int k) { return k < 3 ? -1 : (k < 17 ? (k - 3) / 2 : k - 10); }

constexpr int ORD[14] = {0, 1, 4, 5, 7, 8, 11, 12, 2, 3, 6, 9, 10, 13};
DI constexpr int g4_late(int t) { return (t >= 4 && t < 7) || t >= 11; }
DI constexpr int g4_slot(int t) { return (t < 4 ? t : (t < 7 ? t - 4 : (t < 11 ? t - 7 : t - 11))) * 5120; }

#define PHASE_IDS \
  const unsigned lane = opq_v(tid0) & 63, w = opq_s(w0), bid = opq_s(blockIdx.x); \
  const unsigned xcd = bid & 7, idx = bid >> 3, i16 = lane & 15, q = lane >> 4; \
  char* const ws = opq_p(ws_); \
  (void)xcd; (void)idx; (void)i16; (void)q;
#define IDS_G2 const unsigned kq = xcd & 3, P = (xcd >> 2) * 8 + (idx >> 2), c4 = idx & 3;
#define G2_ROT (LAZY5 && (xcd >> 1) == 2 * (kq & 1) ? 8u : 0u)
#define IDS_G3 const unsigned P = (xcd >> 1) * 4 + (idx >> 3), ct = (xcd & 1) * 8 + (idx & 7);
#define G4PAIR 1
#define C8_OF (G4PAIR ? (xcd & 1) * 4 + (idx & 3) : idx & 7)
#define IDS_G4 const unsigned rt64 = G4PAIR ? (xcd >> 1) * 8 + (idx >> 2) : xcd * 4 + (idx >> 3), c8 = C8_OF;
#define WARM_STRIP(rx, sox) \
  ({ const unsigned n_ = (idx & 7) * 40 + lane; \
     __builtin_amdgcn_raw_buffer_load_b32(rx, lane < 40 ? (((n_ >> 3) & 1) * 20 + (n_ >> 4)) * 1024 + (n_ & 7) * 128 : OOB, sox, 16); })
#define KROT(k) ((k) + rot >= 20 ? (k) + rot - 20 : (k) + rot)
#define LDX(r, k) ld16(rx, lane * 16, sox + (r) * 20480 + KROT(k) * 1024)

__global__ __launch_bounds__(512, 2) void k_all(const u16* x0_, const u16* Sre_, const u16* Sim_, const u16* Dre_, const u16* Dim_,
                                               const u16* w1_, const u16* w2_, char* ws_, u16* out_ STAMP_ARGS) {
  ST(0);
  const unsigned tid0 = threadIdx.x, wall = rfl(tid0 >> 6), w0 = wall & 3;
  const unsigned lb = lds_base();
  const char* code; unsigned code_len;
  asm volatile("s_getpc_b64 %0\n.Lcw_begin:\n\ts_mov_b32 %1, .Lcw_end-.Lcw_begin" : "=s"(code), "=s"(code_len));
  u64 epoch;

  if (wall >= 4) {
    unsigned sink = 0;
    unsigned ep0, ep1;
    asm volatile("global_load_dword %0, %2, %3 sc1\n\tglobal_load_dword %1, %2, %3 offset:4 sc1"
                 : "=&v"(ep0), "=&v"(ep1) : "v"(WS_FLG + FL_E * 128), "s"(ws_) : "memory");
    {
      PHASE_IDS
      IDS_G3
      const u32x4 sa = make_srd(opq_p(x0_), S_ * D_ * 2), sbd = make_srd(ct < 8 ? opq_p(Dre_) : opq_p(Dim_), D_ * D_ * 2);
      const unsigned vo = (lane >> 3) * 1280 + (((lane & 7) ^ (lane >> 3)) << 4);
      const unsigned soa = (P * 128 + w * 32) * 1280, sob = ((ct & 7) * 80 + w * 8) * 1280;
      const unsigned la = lb + w * 4096, lbb = lb + 16384 + w * 1024;
      const unsigned u0 = bid * 4 + w, u1 = 1024 + bid;
      const bool extra = w == 3 && bid < 56;
      const unsigned rot = ROT1 ? ((idx & 7) + 3 * (idx >> 3)) % 10u : 0;
      auto ko = [&](unsigned s) { const unsigned c = s + rot; return (c >= 10 ? c - 10 : c) * 128; };
      const rsrc_t rws = make_rsrc(ws, WS_END);
      asm volatile("s_nop 4" ::: "memory");
      if (WARM_0) {
        const unsigned r0 = lane / 10u, id = ((idx >> 3) * 4 + w) * 50 + lane, r1 = id / 10u;
        warm(sink, sa, lane < 40 ? r0 * 1280 + (lane - r0 * 10) * 128 : OOB, (P * 128 + (idx & 7) * 16 + w * 4) * 1280);
        warm(sink, sbd, lane < 50 ? r1 * 1280 + (id - r1 * 10) * 128 : OOB, (ct & 7) * 80 * 1280);
      }
      k1_issue(sa, sbd, vo, la, lbb, soa + ko(0), sob + ko(0), w);
      k1_issue(sa, sbd, vo, la + K1_STAGE, lbb + K1_STAGE, soa + ko(1), sob + ko(1), w);
      if (CODEWARM && w == 3) warm(sink, make_srd(code, code_len), lane < 16 ? idx * 2048 + lane * 128 : OOB, 0);
      ST(8);
      wait_vm<6>();
      {
        unsigned lo, hi;
        asm volatile("v_readfirstlane_b32 %0, %2\n\tv_readfirstlane_b32 %1, %3\n\ts_nop 4" : "=s"(lo), "=s"(hi) : "v"(ep0), "v"(ep1));
        epoch = (u64)lo + ((u64)hi << 32) + 1;
      }
      bar();
      k1_issue(sa, sbd, vo, la + 2 * K1_STAGE, lbb + 2 * K1_STAGE, soa + ko(2), sob + ko(2), w);
      const u32x4 ss = make_srd((xcd & 2) ? Sim_ : Sre_, S_ * S_ * 2);
      const unsigned vs = (lane & 31) * 4096 + (lane >> 5) * 128, sos = ((xcd >> 2) * 1024 + idx * 32) * 4096 + (xcd & 1) * 2048 + w * 256;
      unsigned so = 0;
#pragma unroll 1
      for (int i = 0; i < 10; ++i) {
        if (i == 0) wait_vm<6>(); else if (i == 1) wait_vm<2 * 6>(); else if (i < 6) wait_vm<3 * 6>();
        else if (i == 6) wait_vm<2 * 6 + 4>(); else if (i == 7) wait_vm<6 + 4>(); else wait_vm<4 + (WS2 >= 4) + (WS1 >= 4)>();
        bar();
        if (i == 0) {
          k1_issue(sa, sbd, vo, la + 3 * K1_STAGE, lbb + 3 * K1_STAGE, soa + ko(3), sob + ko(3), w);
          k1_issue(sa, sbd, vo, la + 4 * K1_STAGE, lbb + 4 * K1_STAGE, soa + ko(4), sob + ko(4), w);
        } else if (i == 1) {
          k1_issue(sa, sbd, vo, la, lbb, soa + ko(5), sob + ko(5), w);
          k1_issue(sa, sbd, vo, la + K1_STAGE, lbb + K1_STAGE, soa + ko(6), sob + ko(6), w);
        } else if (i < 5) k1_issue(sa, sbd, vo, la + so, lbb + so, soa + ko(i + 5), sob + ko(i + 5), w);
        so = so == 4 * K1_STAGE ? 0 : so + K1_STAGE;
        if (i == WS1) warm(sink, ss, vs, sos);
        if (i == WS2) warm(sink, ss, vs, sos + 1024);
        if (i == 5) {
          retile_issue(u0, w1_, w2_, lb + K1_TR + w * 4096, lane);
          if (extra) retile_issue(u1, w1_, w2_, lb + K1_TR + 4 * 4096, lane);
        }
      }
      ST(9);
      bar();
    }
    bf16x8 wr[10][5];
#pragma unroll
    for (int t = 0; t < 10; ++t)
#pragma unroll
      for (int f = 0; f < 5; ++f) wr[t][f] = bf16x8{};
#pragma unroll 1
    for (int L = 0; L < 2; ++L) {
      const int sbase = L * 20;
      (void)sbase;
      {
        PHASE_IDS
        IDS_G2
        const u32x4 sa = make_srd(kq < 2 ? opq_p(Sre_) : opq_p(Sim_), S_ * S_ * 2);
        const u32x4 sb_ = make_srd(ws + (L ? WS_AB2 : WS_AB1), SZ_AB);
        const unsigned va = (lane >> 3) * 4096 + (((lane & 7) ^ (lane >> 3)) << 4), vb = lane * 16;
        const unsigned soa = (P * 128 + w * 32) * 4096 + (kq & 1) * 2048;
        const unsigned sob = ((2 * c4 + (w >> 1)) * 128 + kq * 32 + (w & 1)) * 5 * 1024;
        const unsigned la = lb + G2_A + w * 4096, lbb = lb + w * 5120;
        const unsigned ct = (xcd & 1) * 8 + (idx & 7);
        const unsigned rot = G2_ROT;
        asm volatile("s_nop 4" ::: "memory");
        auto issue_a = [&](unsigned l, unsigned so) {
          glds<0, 0>(sa, va, l, so); glds<1024, 8 * 4096>(sa, va, l, so);
          glds<2048, 16 * 4096>(sa, va, l, so); glds<3072, 24 * 4096>(sa, va, l, so);
        };
        if (L == 0) {
          const unsigned u0 = bid * 4 + w, u1 = 1024 + bid;
          const rsrc_t rws = make_rsrc(ws, WS_END);
          wait_vm<0>();
          retile_finish(u0, 0, rws, lb + K1_TR + w * 4096, lane);
          retile_finish(u0, 2, rws, lb + K1_TR + w * 4096, lane);
          if (w == 3 && bid < 56) {
            retile_finish(u1, 0, rws, lb + K1_TR + 4 * 4096, lane);
            retile_finish(u1, 2, rws, lb + K1_TR + 4 * 4096, lane);
          }
        }
        if (L == 0) {
#pragma unroll
          for (int s = 0; s < 3; ++s) issue_a(la + s * G2_AS, soa + ((s + rot) & 15) * 128);
        } else {
          issue_a(la + 3 * G2_AS, soa + ((3 + rot) & 15) * 128);
        }
        if (L == 0) {
          wait_vm<12>();
          raise(ws, FL_W + bid * 4 + w, epoch, lane);
          if (w == 3 && bid < 56) raise(ws, FL_W + 1024 + bid, epoch, lane);
        }
        ST(sbase + 1);
        const unsigned f5 = FL_5 + ((L * 4 + kq) * 4 + c4) * 64 + (((((i16 + rot) & 15) >> 1) * 2 + (w >> 1)) * 4 + (w & 1) + 2 * (i16 & 1));
        __builtin_amdgcn_s_sleep(PDELAY);
        u64 got5 = poll_mask(ws, f5, epoch, LAZY5 ? 0xfull : 0xffffull);
        ST(sbase + 2);
        glds_run_sc1<5>(sb_, vb, lbb, sob + rot * 10240);
        glds_run_sc1<5>(sb_, vb, lbb + G2_BS, sob + ((1 + rot) & 15) * 10240);
        wait_vm<5>();
        bar();
        glds_run_sc1<5>(sb_, vb, lbb + 2 * G2_BS, sob + ((2 + rot) & 15) * 10240);
        if (L == 0) issue_a(la + 3 * G2_AS, soa + ((3 + rot) & 15) * 128);
        glds_run_sc1<5>(sb_, vb, lbb + 3 * G2_BS, sob + ((3 + rot) & 15) * 10240);
        ST(sbase + 3);
        const u32x4 sw = make_srd(ws + WS_W1, SZ_W1);
        const unsigned sow = ct * 140 * 1024;
        unsigned fw, fw0 = 0, fw1 = 0;
        {
          const unsigned n = w * 42 + (lane < 42 ? lane : 0), k3 = n / 3u, g3 = (7 * ct >> 2) + (n - k3 * 3), n2 = n - 60;
          fw = FL_W + (n < 60 ? k3 * 27 + (g3 < 26 ? g3 : 26) : 540 + (n2 >> 1) * 10 + (5 * C8_OF >> 2) + (n2 & 1));
        }
#pragma unroll 1
        for (int s = 0; s < 16; ++s) {
          if (s == 0) wait_vm<10>();
          if (s == 1) wait_vm<14>();
          wait_vm<18>();
          if (s == 9 && L == 0) {
            asm volatile("" : "+v"(fw0), "+v"(fw1));
            if (!__all(fw0 == (unsigned)epoch && fw1 == (unsigned)(epoch >> 32))) poll(ws, fw, epoch);
          }
          bar();
          const unsigned sl = s & 3;
          if (s < 12) {
            const u64 need = 3ull << ((s + 4) & ~1);
            if ((got5 & need) != need) got5 = poll_mask(ws, f5, epoch, need);
            issue_a(la + sl * G2_AS, soa + ((s + 4 + rot) & 15) * 128);
            glds_run_sc1<5>(sb_, vb, lbb + sl * G2_BS, sob + ((s + 4 + rot) & 15) * 10240);
            if (s == 6 && L == 0)
              asm volatile("global_load_dword %0, %2, %3 sc1\n\tglobal_load_dword %1, %2, %3 offset:4 sc1"
                           : "+v"(fw0), "+v"(fw1) : "v"(WS_FLG + fw * 128), "s"(ws) : "memory");
          } else {
            glds_run_sc1<5>(sw, vb, lbb + sl * G2_BS, sow + w * 5120 + sl * G2_BS);
            if (s < 15) glds_run_sc1<4>(sw, vb, la + sl * G2_AS, sow + G2_A + w * 4096 + sl * G2_AS);
            else glds_run_sc1<3>(sw, vb, lb + G2_A + 3 * G2_AS + w * 3072, sow + G2_A + 3 * G2_AS + w * 3072);
          }
        }
        ST(sbase + 4);
      }
      {
        PHASE_IDS
        IDS_G4
        const unsigned ct = (xcd & 1) * 8 + (idx & 7);
        if (w >= 2) {
          const rsrc_t rc = make_rsrc(ws + WS_CS1, 20 * F_ * 4);
          const unsigned c = (w - 2) * 64 + lane, col = ct * 112 + c;
          const unsigned vo = c < 112 && col < F_ ? col * 4 : OOB;
          float csp = 0.f;
#pragma unroll
          for (int k = 0; k < 20; ++k) csp += __uint_as_float(__builtin_amdgcn_raw_buffer_load_b32(rc, vo, k * F_ * 4, 16));
          lds_wr1(lb + KM_CS1 + c * 4, csp);
        }
        const unsigned so = (c8 * 54 + w * 14) * 5 * 1024;
        wait_vm<0>();
        if (L == 0) {
          if (RSLEEP) __builtin_amdgcn_s_sleep(RSLEEP);
          const rsrc_t rw = make_rsrc(ws + WS_W2, SZ_W2);
          const unsigned vl = lane * 16, vz = lane * 16 + (w < 3 ? 0u : OOB);
          sfor<0, 50>([&](auto N) {
            constexpr int t = 4 + N.v / 5, n = ORD[t] * 5 + N.v % 5;
            wr[t - 4][N.v % 5] = ld16(rw, (ORD[t] < 12 ? vl : vz) + (n & 3) * 1024, so + (n >> 2) * 4096);
            if constexpr (RGAP && N.v % 5 == 4 && N.v < 49) { sb(); __builtin_amdgcn_s_sleep(RGAP); sb(); }
          });
          sb();
        }
        wait_lgkm0();
        ST(sbase + 5);
        bar();
        bar();
        const u32x4 s2 = make_srd(ws + WS_W2, SZ_W2);
        const unsigned sg = rfl(so);
        asm volatile("s_nop 4" ::: "memory");
        glds_run_sc1<10>(s2, lane * 16, lb + w * 20480, sg);
        glds_run_sc1<10>(s2, lane * 16, lb + w * 20480 + 10240, sg + 4 * 5120);
        wait_vm<0>();
        ST(sbase + 6);
        const unsigned pe = lb + w * 20480 + lane * 16, pl = lb + G4_LATE + w * 15360 + lane * 16;
        auto put = [&](auto T0, auto T1) {
          sfor<T0.v * 5, T1.v * 5>([&](auto N) {
            constexpr int t = N.v / 5, f = N.v % 5;
            *(LDS_AS bf16x8*)(ldsp)(uintptr_t)((g4_late(t) ? pl : pe) + g4_slot(t) + f * 1024) = wr[t - 4][f];
          });
          wait_lgkm0();
        };
        bar();
        put(IC<4>{}, IC<7>{});
        bar();
        put(IC<7>{}, IC<11>{});
        bar();
        put(IC<11>{}, IC<14>{});
        bar();
      }
      if (L) { bar(); break; }
      {
        PHASE_IDS
        IDS_G3
        const u32x4 sd = make_srd(ct < 8 ? opq_p(Dre_) : opq_p(Dim_), D_ * D_ * 2);
        const unsigned vo = (lane >> 3) * 1280 + (((lane & 7) ^ (lane >> 3)) << 4);
        const unsigned so = (ct & 7) * 80 * 1280;
        sfor<0, 10>([&](auto G) { glds<G.v * 1024, G.v * 8 * 1280>(sd, vo, lb + 20 * w * 1024, so + 2 * w * 128); });
        sfor<0, WDPRE>([&](auto G) { glds<G.v * 1024, G.v * 8 * 1280>(sd, vo, lb + (20 * w + 10) * 1024, so + (2 * w + 1) * 128); });
        bar();
        sfor<WDPRE, 10>([&](auto G) { glds<G.v * 1024, G.v * 8 * 1280>(sd, vo, lb + (20 * w + 10) * 1024, so + (2 * w + 1) * 128); });
        sfor<0, 5>([&](auto G) { glds<G.v * 1024, G.v * 8 * 1280>(sd, vo, lb + (140 + w * 5) * 1024, so + (8 + (w >> 1)) * 128 + (w & 1) * 40 * 1280); });
        wait_vm<0>();
        ST(7);
        bar();
      }
      {
        PHASE_IDS
        IDS_G2
        const u32x4 sa = make_srd(kq < 2 ? opq_p(Sre_) : opq_p(Sim_), S_ * S_ * 2);
        const unsigned va = (lane >> 3) * 4096 + (((lane & 7) ^ (lane >> 3)) << 4);
        const unsigned soa = (P * 128 + w * 32) * 4096 + (kq & 1) * 2048;
        const unsigned rot = G2_ROT;
        __builtin_amdgcn_s_sleep(ASLEEP2);
#pragma unroll
        for (int s = 0; s < 3; ++s) {
          const unsigned l = lb + G2_A + w * 4096 + s * G2_AS, so = soa + ((s + rot) & 15) * 128;
          glds<0, 0>(sa, va, l, so); glds<1024, 8 * 4096>(sa, va, l, so);
          glds<2048, 16 * 4096>(sa, va, l, so); glds<3072, 24 * 4096>(sa, va, l, so);
        }
        bar();
      }
    }
    asm volatile(".Lcw_end:" :: "v"(sink));
    return;
  }

  {
    const u64 e = __hip_atomic_load(flag_ptr(ws_, FL_E), RLX_AGENT);
    epoch = (u64)rfl((unsigned)e) + ((u64)rfl((unsigned)(e >> 32)) << 32) + 1;
  }
#pragma unroll 1
  for (int L = 0; L < 2; ++L) {
    const int sbase = L * 20;
    (void)sbase;
    {
      PHASE_IDS
      IDS_G3
      f32x4 acc[2][5];
      float mu[2][4], rs[2][4], csv[5];
      const float sign = ct < 8 ? 1.f : -1.f;
      if (L == 0) {
        const unsigned ra0 = lb + w * 4096 + i16 * 128 + ((q ^ (lane & 7)) << 4);
        const unsigned rb0 = lb + 16384 + i16 * 128 + ((q ^ (lane & 7)) << 4);
        const f32x4 z4 = {0.f, 0.f, 0.f, 0.f};
        f32x4 cs0 = z4, cs1 = z4, rs_[2] = {z4, z4}, rq_[2] = {z4, z4};
#pragma unroll
        for (int r = 0; r < 2; ++r)
#pragma unroll
          for (int f = 0; f < 5; ++f) acc[r][f] = z4;
        bf16x8 fr[2][8];
        bf16x8 one = __builtin_bit_cast(bf16x8, u32x4{0x3f803f80u, 0x3f803f80u, 0x3f803f80u, 0x3f803f80u});
        auto half = [&](auto KK, unsigned so) {
          constexpr int kk = KK.v;
          const unsigned pa = (ra0 ^ ((kk ^ 1) * 64)) + so, pb = (rb0 ^ ((kk ^ 1) * 64)) + so;
          sfor<0, 16>([&](auto M) {
            constexpr int m = M.v;
            if constexpr (m < 10) mma(acc[m & 1][m >> 1], fr[kk][m & 1], fr[kk][2 + (m >> 1)]);
            else if constexpr (m == 10) mma(cs0, one, fr[kk][7]);
            else if constexpr (m == 11) mma(cs1, one, fr[kk][6]);
            else if constexpr (m < 14) mma(rs_[m - 12], fr[kk][m - 12], one);
            else mma(rq_[m - 14], fr[kk][m - 14], fr[kk][m - 14]);
            constexpr int n = m >> 1;
            if constexpr ((m & 1) == 0) fr[kk ^ 1][n] = n < 2 ? lds_rd(pa + n * 2048) : (n < 7 ? lds_rd(pb + (n - 2) * 2048) : lds_rd(pb + w * 2048));
            sb();
          });
        };
        asm volatile("s_nop 1" : "+v"(cs0), "+v"(cs1), "+v"(rs_[0]), "+v"(rs_[1]), "+v"(rq_[0]), "+v"(rq_[1]), "+v"(one));
        fence_all(acc);
        bar();
        ST(38);
        fr[0][0] = lds_rd(ra0); fr[0][1] = lds_rd(ra0 + 2048);
#pragma unroll
        for (int f = 0; f < 5; ++f) fr[0][2 + f] = lds_rd(rb0 + f * 2048);
        fr[0][7] = lds_rd(rb0 + w * 2048);
        sb();
        unsigned so = 0;
#pragma unroll 1
        for (int i = 0; i < 10; ++i) {
          half(IC<0>{}, so);
          wait_lgkm0();
          bar();
          so = so == 4 * K1_STAGE ? 0 : so + K1_STAGE;
          half(IC<1>{}, so);
        }
        fence_all(acc);
        asm volatile("" : "+v"(cs0), "+v"(cs1), "+v"(rs_[0]), "+v"(rs_[1]), "+v"(rq_[0]), "+v"(rq_[1]));
        const unsigned csb = lb + K1_CS;
        if (q == 0) {
          lds_wr1(csb + (w * 16 + i16) * 4, cs0[0]);
          if (w == 0) lds_wr1(csb + (64 + i16) * 4, cs1[0]);
        }
        wait_lgkm0();
        bar();
        ST(18);
#pragma unroll
        for (int f = 0; f < 5; ++f) csv[f] = lds_rd1(csb + (f * 16 + i16) * 4);
#pragma unroll
        for (int r = 0; r < 2; ++r)
#pragma unroll
          for (int j = 0; j < 4; ++j) {
            float m_, r_;
            ln_coef(rs_[r][j], __shfl(rq_[r][j], q * 20 + j), m_, r_);
            mu[r][j] = -m_; rs[r][j] = sign * r_;
          }
        if (w == 0) {
          const rsrc_t rc = make_rsrc(ws + WS_CSD, 256 * 320);
          st4(__float_as_uint(q == 0 ? csv[0] : (q == 1 ? csv[1] : (q == 2 ? csv[2] : csv[3]))), rc, lane * 4, bid * 320);
          st4(__float_as_uint(csv[4]), rc, q == 0 ? 256 + lane * 4 : OOB, bid * 320);
        }
      } else {
        {
          const rsrc_t rc = make_rsrc(ws + WS_CSD, 256 * 320);
#pragma unroll
          for (int f = 0; f < 5; ++f) csv[f] = __uint_as_float(__builtin_amdgcn_raw_buffer_load_b32(rc, (f * 16 + i16) * 4, bid * 320, 16));
        }
        poll(ws, FL_4 + P * 64 + w * 16 + i16, epoch);
        ST(17);
        const rsrc_t rx = make_rsrc(ws + WS_X2, SZ_X);
        const unsigned sox = (P * 8 + w * 2) * 20 * 1024;
        const unsigned rot = ROT5 ? 2 * ((idx & 7) + ((idx & 7) >> 2)) : 0;
        auto wdp = [&](int c) { const unsigned cc = c + (rot >> 1) >= 10 ? c + (rot >> 1) - 10 : c + (rot >> 1);
                                return (cc < 8 ? 10 * cc : 60 + 10 * cc) * 1024; };
        bf16x8 a[20][2];
        unsigned wsink = WARM_X ? WARM_STRIP(rx, sox) : 0u;
#pragma unroll
        for (int k = 0; k < 3; ++k) { a[k][0] = LDX(0, k); a[k][1] = LDX(1, k); }
        const rsrc_t rst = make_rsrc(ws + WS_ST2, SZ_ST);
        const unsigned sto = ((lane >> 5) * 4 * S_ + P * 128 + w * 32 + (lane & 31)) * 8;
        const u32x2 sp0 = ld8u(rst, sto, 0), sp1 = ld8u(rst, sto, S_ * 8), sp2 = ld8u(rst, sto, 2 * S_ * 8), sp3 = ld8u(rst, sto, 3 * S_ * 8);
        sb();
        bar();
        bf16x8 b[2][5];
        const unsigned rb = lb + i16 * 128 + ((q ^ (lane & 7)) << 4);
#pragma unroll
        for (int f = 0; f < 5; ++f) b[0][f] = lds_rd(rb + wdp(0) + f * 2048);
        sb();
        sfor<0, 20>([&](auto K) {
          constexpr int k = K.v;
          const unsigned pb = (rb ^ (((k + 1) & 1) * 64)) + wdp(((k + 1) >> 1) % 10);
          sfor<0, 10>([&](auto M) {
            constexpr int f = M.v >> 1, r = M.v & 1;
            if constexpr (k == 0) mma0(acc[r][f], a[0][r], b[0][f]); else mma(acc[r][f], a[k][r], b[k & 1][f]);
            if constexpr (k + 1 < 20 && r == 0) b[(k + 1) & 1][f] = lds_rd(pb + f * 2048);
            if constexpr (r == 1 && f < 4) {
              constexpr int kn = k < 7 ? 2 * k + 3 + (f >> 1) : (f < 2 ? k + 10 : 20);
              if constexpr (kn < 20) a[kn][f & 1] = LDX(f & 1, kn);
            }
            sb();
          });
          if constexpr (k == 0) ST(40);
          if constexpr (k == 4) ST(41);
          if constexpr (k == 9) ST(42);
        });
        asm volatile("" :: "v"(wsink));
        fence_all(acc);
        bar();
        ST(39);
        float s1 = (__uint_as_float(sp0[0]) + __uint_as_float(sp1[0])) + (__uint_as_float(sp2[0]) + __uint_as_float(sp3[0]));
        float s2 = (__uint_as_float(sp0[1]) + __uint_as_float(sp1[1])) + (__uint_as_float(sp2[1]) + __uint_as_float(sp3[1]));
        auto ta = __builtin_amdgcn_permlane32_swap(__float_as_uint(s1), __float_as_uint(s1), false, false);
        auto tb = __builtin_amdgcn_permlane32_swap(__float_as_uint(s2), __float_as_uint(s2), false, false);
        float m, rr;
        ln_coef(__uint_as_float(ta[0]) + __uint_as_float(ta[1]), __uint_as_float(tb[0]) + __uint_as_float(tb[1]), m, rr);
        m = -m; rr *= sign;
#pragma unroll
        for (int r = 0; r < 2; ++r)
#pragma unroll
          for (int e = 0; e < 4; ++e) { mu[r][e] = __shfl(m, r * 16 + q * 4 + e); rs[r][e] = __shfl(rr, r * 16 + q * 4 + e); }
      }
      g1_store(acc, mu, rs, csv, make_rsrc(ws + (L ? WS_AB2 : WS_AB1), SZ_AB), P, ct, w, i16, q);
      ST(L + 19);
      publish(ws, FL_5 + ((L * 4 + (ct >> 3) * 2 + (P >> 3)) * 4 + ((ct & 7) >> 1)) * 64 + ((P & 7) * 2 + (ct & 1)) * 4 + w, epoch, lane);
    }
    {
      PHASE_IDS
      IDS_G2
      const unsigned m = w >> 1, n = w & 1;
      const unsigned ra = lb + G2_A + (m * 64 + i16) * 128 + ((q ^ (lane & 7)) << 4);
      const unsigned rb = lb + n * 10240 + lane * 16;
      const unsigned row = P * 128 + m * 64 + kq * 16 + i16, ct = 2 * c4 + n;
      const TileOff ro = tile_offsets(L == 0, row, ct, q), oo = tile_offsets(false, row, ct, q);
      f32x4 acc[4][5];
#pragma unroll
      for (int i = 0; i < 4; ++i)
#pragma unroll
        for (int f = 0; f < 5; ++f) acc[i][f] = f32x4{0.f, 0.f, 0.f, 0.f};
      bf16x8 fa[2][4], fb[2][5];
      auto half = [&](auto KK, unsigned sl) {
        constexpr int kk = KK.v;
        const unsigned pa = (ra ^ ((kk ^ 1) * 64)) + sl * G2_AS, pb = rb + (kk ^ 1) * 5120 + sl * G2_BS;
        sfor<0, 20>([&](auto M) {
          constexpr int mm = M.v;
          mma(acc[mm / 5][mm % 5], fb[kk][mm % 5], fa[kk][mm / 5]);
          constexpr int rn = RDPAT == 0 ? ((mm & 1) == 0 && mm < 18 ? mm >> 1 : -1) : (RDPAT == 1 ? (mm < 9 ? mm : -1) : (mm % 3 != 2 && mm - mm / 3 < 9 ? mm - mm / 3 : -1));
          if constexpr (rn >= 0 && rn < 5) fb[kk ^ 1][rn] = lds_rd(pb + rn * 1024);
          if constexpr (rn >= 5) fa[kk ^ 1][rn - 5] = lds_rd(pa + (rn - 5) * 2048);
          sb();
        });
      };
      if (L) poll(ws, FL_4 + P * 64 + (m * 4 + kq) * 8 + ct, epoch);
      ResTile rt = load_res(make_rsrc(L ? (const void*)(ws + WS_X2) : (const void*)opq_p(x0_), SZ_X), ro);
      fence_all(acc);
      bar();
      ST(sbase + 1);
#pragma unroll
      for (int f = 0; f < 5; ++f) fb[0][f] = lds_rd(rb + f * 1024);
#pragma unroll
      for (int i = 0; i < 4; ++i) fa[0][i] = lds_rd(ra + i * 2048);
      sb();
#pragma unroll 1
      for (int s = 0; s < 16; ++s) {
        half(IC<0>{}, s & 3);
        wait_lgkm0();
        bar();
        half(IC<1>{}, (s + 1) & 3);
      }
      fence_all(acc);
      ST(sbase + 2);
      const rsrc_t rxp = make_rsrc(ws + WS_XP + L * SZ_XP, SZ_XP);
      const unsigned xo = ((P * 4 + c4) * 48 + w) * XSLOT;
      f32x4 v[5];
      if (kq == 0) xp_send<0>(acc, v, rxp, xo, lane);
      else if (kq == 1) xp_send<1>(acc, v, rxp, xo, lane);
      else if (kq == 2) xp_send<2>(acc, v, rxp, xo, lane);
      else xp_send<3>(acc, v, rxp, xo, lane);
      const unsigned fx = FL_X + ((L * 64 + P * 4 + c4) * 4 + w) * 4;
      ST(sbase + 3);
      publish(ws, fx + kq, epoch, lane);
      ST(sbase + 4);
      poll(ws, fx + (lane & 3), epoch);
      ST(sbase + 5);
      const unsigned xi = xo + kq * 12 * XSLOT;
      u32x4 g0[3], g1[3]; u32x2 g2[3];
#pragma unroll
      for (int s = 0; s < 3; ++s) {
        g0[s] = ld16u(rxp, lane * 16, xi + s * 4 * XSLOT);
        g1[s] = ld16u(rxp, lane * 16 + 1024, xi + s * 4 * XSLOT);
        g2[s] = ld8u(rxp, lane * 8 + 2048, xi + s * 4 * XSLOT);
      }
      sb();
#pragma unroll
      for (int s = 0; s < 3; ++s) {
        v[0] += f32x4{bflo(g0[s][0]), bfhi(g0[s][0]), bflo(g0[s][1]), bfhi(g0[s][1])};
        v[1] += f32x4{bflo(g0[s][2]), bfhi(g0[s][2]), bflo(g0[s][3]), bfhi(g0[s][3])};
        v[2] += f32x4{bflo(g1[s][0]), bfhi(g1[s][0]), bflo(g1[s][1]), bfhi(g1[s][1])};
        v[3] += f32x4{bflo(g1[s][2]), bfhi(g1[s][2]), bflo(g1[s][3]), bfhi(g1[s][3])};
        v[4] += f32x4{bflo(g2[s][0]), bfhi(g2[s][0]), bflo(g2[s][1]), bfhi(g2[s][1])};
      }
      finish_tile(v, rt, make_rsrc(ws + (L ? WS_X3 : WS_X1), SZ_X), oo, true, make_rsrc(ws + (L ? WS_ST3 : WS_ST1), SZ_ST),
                  q == 0 ? (ct * S_ + row) * 8 : OOB);
      ST(sbase + 6);
      publish(ws, FL_2 + (L * 16 + P) * 64 + (m * 4 + kq) * 8 + ct, epoch, lane);
      ST(sbase + 7);
    }
    u32x4 y2[2]; u32x2 y1[2];
    {
      PHASE_IDS
      IDS_G3
      typedef __attribute__((ext_vector_type(2))) short s16x2;
      const unsigned f2 = FL_2 + (L * 16 + P) * 64 + w * 16 + i16;
      __builtin_amdgcn_s_sleep(PDELAY);
      const u64 got2 = poll_mask(ws, f2, epoch, LAZY2 ? 0x0f0full : 0xffffull);
      ST(sbase + 8);
      const rsrc_t rx = make_rsrc(ws + (L ? WS_X3 : WS_X1), SZ_X);
      const rsrc_t rst = make_rsrc(ws + (L ? WS_ST3 : WS_ST1), SZ_ST);
      const unsigned sox = (P * 8 + w * 2) * 20 * 1024;
      const unsigned rot = ROT3 ? (5 * (idx & 7)) >> 1 : 0;
      const unsigned par = ct & 1;
      bf16x8 a[20][2];
#pragma unroll
      for (int k = 0; k < 3; ++k) { a[k][0] = LDX(0, k); a[k][1] = LDX(1, k); }
      const unsigned sto = (2 * q * S_ + P * 128 + w * 32 + i16) * 8;
      u32x2 sa0, sa1, sb0, sb1;
      sb();
      bar();
      f32x4 acc[2][4];
      bf16x8 b[2][4];
      const unsigned rb = lb + lane * 16;
#pragma unroll
      for (int f = 0; f < 4; ++f) b[0][f] = lds_rd(rb + rot * 4096 + f * 1024);
      sb();
      sfor<0, 20>([&](auto K) {
        constexpr int k = K.v;
        const unsigned pb = k + 1 < 20 ? rb + KROT((k + 1) % 20) * 4096 : rb + 80 * 1024 + rot * 3072;
        if constexpr (k == 3) { if ((got2 & 0xffffull) != 0xffffull) poll0(ws, f2, epoch); }
        if constexpr (k == 9) { sa0 = ld8u(rst, sto, 0); sa1 = ld8u(rst, sto, S_ * 8); sb0 = ld8u(rst, sto + 128, 0); sb1 = ld8u(rst, sto + 128, S_ * 8); }
        sfor<0, 8>([&](auto M) {
          constexpr int f = M.v >> 1, r = M.v & 1;
          if constexpr (k == 0) mma0(acc[r][f], b[0][f], a[0][r]); else mma(acc[r][f], b[k & 1][f], a[k][r]);
          if constexpr (r == 0 && (k + 1 < 20 || f < 3)) b[(k + 1) & 1][f] = lds_rd(pb + f * 1024);
          if constexpr (r == 1 && 2 * k + 3 + (f >> 1) < 20) a[2 * k + 3 + (f >> 1)][f & 1] = LDX(f & 1, 2 * k + 3 + (f >> 1));
          sb();
        });
        if constexpr (k == 0) ST(sbase + 9);
        if constexpr (k == 3) ST(51 + L * 5);
        if constexpr (k == 7) ST(52 + L * 5);
        if constexpr (k == 11) ST(53 + L * 5);
        if constexpr (k == 15) ST(54 + L * 5);
        if constexpr (k == 19) ST(55 + L * 5);
      });
      float mu[2], rs[2];
      ln_coef(xsum16_32(__uint_as_float(sa0[0]) + __uint_as_float(sa1[0])), xsum16_32(__uint_as_float(sa0[1]) + __uint_as_float(sa1[1])), mu[0], rs[0]);
      ln_coef(xsum16_32(__uint_as_float(sb0[0]) + __uint_as_float(sb1[0])), xsum16_32(__uint_as_float(sb0[1]) + __uint_as_float(sb1[1])), mu[1], rs[1]);
      auto act = [&](const f32x4& c, int r, const f32x4& cs, unsigned (&pk)[2]) {
        const f32x2 nm = {-mu[r], -mu[r]}, r2 = {rs[r], rs[r]};
#pragma unroll
        for (int e = 0; e < 2; ++e) {
          const f32x2 z = __builtin_elementwise_fma(nm, f32x2{cs[2 * e], cs[2 * e + 1]}, f32x2{c[2 * e], c[2 * e + 1]}) * r2;
          const s16x2 hb = __builtin_bit_cast(s16x2, __builtin_convertvector(z, bf16x2));
          pk[e] = __builtin_bit_cast(unsigned, __builtin_elementwise_max(hb, s16x2{0, 0}));
        }
      };
      auto pair = [&](const unsigned (&u)[2], const unsigned (&v)[2]) {
        auto t0 = __builtin_amdgcn_permlane16_swap(u[0], v[0], false, false);
        auto t1 = __builtin_amdgcn_permlane16_swap(u[1], v[1], false, false);
        return u32x4{t0[0], t1[0], t0[1], t1[1]};
      };
      const unsigned csb = lb + KM_CS1 + q * 16;
      asm volatile("s_nop 7\n\ts_nop 1" : "+v"(acc[0][0]), "+v"(acc[0][1]), "+v"(acc[0][2]), "+v"(acc[0][3]),
                                          "+v"(acc[1][0]), "+v"(acc[1][1]), "+v"(acc[1][2]), "+v"(acc[1][3]));
      wait_lgkm0();
      bar();
      ST(43 + L * 4);
      {
        const rsrc_t rh = make_rsrc(ws + (L ? WS_H2 : WS_H1), SZ_H);
        const unsigned vo = ((((q & 1) << 1) | (q >> 1)) * 16 + i16) * 16;
        const unsigned so = ((P * 8 + w * 2) * 54 + 7 * (ct >> 1) + 4 * par) * 1024;
        u32x4 y[2][2];
#pragma unroll
        for (int p = 0; p < 2; ++p) {
          const f32x4 c0 = lds_rdf(csb + (2 * p + par) * 64), c1 = lds_rdf(csb + (2 * p + 1 + par) * 64);
#pragma unroll
          for (int r = 0; r < 2; ++r) {
            unsigned u[2], v[2];
            act(acc[r][2 * p], r, c0, u); act(acc[r][2 * p + 1], r, c1, v);
            y[p][r] = pair(u, v);
            st16_raw(y[p][r], rh, p == 1 && ct == 15 ? OOB : vo, so + (r * 54 + p) * 1024);
          }
        }
        st_guard(y[0][0], y[0][1], y[1][0], y[1][1]);
      }
      ST(44 + L * 4);
      sb();
      sfor<0, 20>([&](auto K) {
        constexpr int k = K.v;
        const unsigned pb = rb + 80 * 1024 + KROT((k + 1) % 20) * 3072;
        sfor<0, 6>([&](auto M) {
          constexpr int f = M.v >> 1, r = M.v & 1;
          if constexpr (k == 0) mma0(acc[r][f], b[0][f], a[0][r]); else mma(acc[r][f], b[k & 1][f], a[k][r]);
          if constexpr (r == 0 && k + 1 < 20) b[(k + 1) & 1][f] = lds_rd(pb + f * 1024);
          sb();
        });
        if constexpr (k == PUB1) publish(ws, FL_3 + (L * 32 + P) * 64 + w * 16 + ct, epoch, lane);
      });
      const f32x4 c0 = lds_rdf(csb + (4 + par) * 64), c1 = lds_rdf(csb + (5 + par) * 64), c2 = lds_rdf(csb + (par ? 0 : 6) * 64);
      asm volatile("s_nop 7\n\ts_nop 1" : "+v"(acc[0][0]), "+v"(acc[0][1]), "+v"(acc[0][2]), "+v"(acc[1][0]), "+v"(acc[1][1]), "+v"(acc[1][2]));
      ST(sbase + 10);
#pragma unroll
      for (int r = 0; r < 2; ++r) {
        unsigned u[2], v[2], s[2];
        act(acc[r][0], r, c0, u); act(acc[r][1], r, c1, v); act(acc[r][2], r, c2, s);
        y2[r] = pair(u, v);
        y1[r] = u32x2{s[0], s[1]};
      }
      asm volatile("" : "+v"(y2[0]), "+v"(y2[1]), "+v"(y1[0]), "+v"(y1[1]));
      ST(sbase + 11);
    }
    {
      PHASE_IDS
      IDS_G4
      const unsigned row = rt64 * 64 + w * 16 + i16;
      const rsrc_t rh = make_rsrc(ws + (L ? WS_H2 : WS_H1), SZ_H);
      const unsigned soh = (rt64 * 4 * 54 + w * 14) * 1024;
      const unsigned vl = lane * 16, vz = lane * 16 + (w < 3 ? 0u : OOB);
      bf16x8 a[14][4], b[2][5];
#define LDH(t, i) a[t][i] = ld16(rh, (ORD[t] < 12 ? vl : vz) + (ORD[t] & 3) * 1024, soh + (ORD[t] >> 2) * 4096 + (i) * 54 * 1024)
      const TileOff ro = tile_offsets(false, row, c8, q), oo = tile_offsets(L != 0, row, c8, q);
      const unsigned f3 = FL_3 + (L * 32 + (rt64 >> 1)) * 64 + ((rt64 & 1) * 2 + (lane & 1)) * 16 + 4 * w + ((lane >> 1) & 3);
      poll0(ws, f3, epoch);
      ST(sbase + 13);
      sfor<0, 12>([&](auto N) { LDH(N.v / 4, N.v % 4); });
      sb();
      {
        const unsigned P = (xcd >> 1) * 4 + (idx >> 3), ct = (xcd & 1) * 8 + (idx & 7), par = ct & 1;
        const unsigned vo = ((((q & 1) << 1) | (q >> 1)) * 16 + i16) * 16;
        const unsigned v1 = ((2 * par + (q >> 1)) * 16 + i16) * 16 + (q & 1) * 8;
        const unsigned so = ((P * 8 + w * 2) * 54 + 7 * (ct >> 1)) * 1024;
#pragma unroll
        for (int r = 0; r < 2; ++r) {
          st16_raw(y2[r], rh, ct == 15 ? OOB : vo, so + (r * 54 + 2 + 4 * par) * 1024);
          st8(y1[r], rh, v1, so + (r * 54 + 3) * 1024);
        }
        st_guard(y2[0], y2[1]);
      }
      ResTile rt;
      u64 fl2;
      const rsrc_t rr = make_rsrc(ws + (L ? WS_X3 : WS_X1), SZ_X);
      sb();
      bar();
      f32x4 acc[4][5];
      const unsigned rbe = lb + w * 20480 + lane * 16, rbl = lb + G4_LATE + w * 15360 + lane * 16;
#pragma unroll
      for (int f = 0; f < 5; ++f) b[0][f] = lds_rd(rbe + f * 1024);
      sb();
      sfor<0, 14>([&](auto T) {
        constexpr int t = T.v;
        constexpr int ta = t < 5 ? t + 3 : (t == 5 ? -1 : (t == 6 ? 8 : (t + 3 < 14 ? t + 3 : -1)));
        if constexpr (t == 3 || t == 6 || t == 10) { wait_lgkm0(); bar(); }
        if constexpr (t == 6) { if (!__all(fl2 == epoch)) poll0(ws, f3 + 16 * 64, epoch); }
        sfor<0, 20>([&](auto M) {
          constexpr int m = M.v, i = m / 5, f = m % 5;
          if constexpr (t == 0) mma0(acc[i][f], b[0][f], a[0][i]); else mma(acc[i][f], b[t & 1][f], a[t][i]);
          if constexpr (t + 1 < 14 && (m & 1) == 1 && m < 10)
            b[(t + 1) & 1][m >> 1] = lds_rd((g4_late(t + 1) ? rbl : rbe) + g4_slot(t + 1) + (m >> 1) * 1024);
          if constexpr (ta >= 0 && (m & 1) == 0 && m < 8) LDH(ta, m >> 1);
          if constexpr (t == 6 && (m & 1) == 0 && m >= 8 && m < 16) LDH(9, (m - 8) >> 1);
          if constexpr (t == 2 && m == 10) rt = load_res(rr, ro);
          if constexpr (t == 4 && m == 18) fl2 = __hip_atomic_load(flag_ptr(ws, f3 + 16 * 64), RLX_AGENT);
          sb();
        });
        if constexpr (t == 2) {
          wait_vm<15>();
          raise(ws, FL_3 + (L * 32 + 16 + (xcd >> 1) * 4 + (idx >> 3)) * 64 + w * 16 + (xcd & 1) * 8 + (idx & 7), epoch, lane);
          ST(sbase + 12);
        }
        if constexpr (t == 0) ST(sbase + 14);
        if constexpr (t == 3) ST(45 + L * 4);
        if constexpr (t == 6) ST(46 + L * 4);
      });
      fence_all(acc);
      ST(sbase + 15);
      f32x4 v[5];
      if (w == 0) sm_scatter<0>(rbl, acc, v);
      else if (w == 1) sm_scatter<1>(rbl, acc, v);
      else if (w == 2) sm_scatter<2>(rbl, acc, v);
      else sm_scatter<3>(rbl, acc, v);
      wait_lgkm0();
      bar();
#pragma unroll
      for (int s = 1; s < 4; ++s) {
        const unsigned src = lb + G4_LATE + ((w + s) & 3) * 15360 + (3 - s) * 5 * 1024 + lane * 16;
#pragma unroll
        for (int f = 0; f < 5; ++f) v[f] += lds_rdf(src + f * 1024);
      }
      finish_tile(v, rt, L ? make_rsrc(opq_p(out_), SZ_X) : make_rsrc(ws + WS_X2, SZ_X), oo, L == 0, make_rsrc(ws + WS_ST2, SZ_ST),
                  q == 0 ? (c8 * S_ + row) * 8 : OOB);
      ST(sbase + 16);
      if (L == 0) publish(ws, FL_4 + (rt64 >> 1) * 64 + ((rt64 & 1) * 4 + w) * 8 + c8, epoch, lane);
    }
  }
  if (blockIdx.x == 0 && w0 == 0) raise(ws_, FL_E, epoch, tid0);
  ST(37);
}

extern "C" void kernel_launch(void* const* d_in, const int* in_sizes, int n_in, void* d_out, int out_size, void* d_ws,
                              size_t ws_size, hipStream_t stream) {
  char* ws = (char*)d_ws;
  (void)hipFuncSetAttribute((const void*)k_all, hipFuncAttributeMaxDynamicSharedMemorySize, KM_LDS);
  k_all<<<256, 512, KM_LDS, stream>>>((const u16*)d_in[0], (const u16*)d_in[1], (const u16*)d_in[2], (const u16*)d_in[3],
                                     (const u16*)d_in[4], (const u16*)d_in[6], (const u16*)d_in[7], ws, (u16*)d_out STAMP_PASS(0));
}
```

```cpp
#include <hip/hip_runtime.h>
#include <hip/hip_bf16.h>
#include <stdint.h>

#define DI __device__ __forceinline__
typedef unsigned short u16;
typedef __attribute__((ext_vector_type(8))) __bf16 bf16x8;
typedef __attribute__((ext_vector_type(2))) __bf16 bf16x2;
typedef __attribute__((ext_vector_type(4))) float f32x4;
typedef __attribute__((ext_vector_type(2))) float f32x2;
typedef __attribute__((ext_vector_type(4))) unsigned u32x4;
typedef __attribute__((ext_vector_type(2))) unsigned u32x2;
typedef __attribute__((ext_vector_type(4))) short s16x4;
#define LDS_AS __attribute__((address_space(3)))
#define GLB_AS __attribute__((address_space(1)))
typedef LDS_AS char* ldsp;
typedef GLB_AS unsigned gu32;
typedef unsigned long long u64;
typedef GLB_AS u64 gu64;
typedef __amdgpu_buffer_rsrc_t rsrc_t;
#define RLX_AGENT __ATOMIC_RELAXED, __HIP_MEMORY_SCOPE_AGENT

constexpr int S_ = 2048, D_ = 640, F_ = 1728;
constexpr float EPS = 1e-5f;
constexpr unsigned OOB = 0x7f000000u;

constexpr unsigned SZ_AB = 8 * 128 * 5 * 1024;
constexpr unsigned SZ_X = 128 * 20 * 1024;
constexpr unsigned SZ_H = 128 * 54 * 1024;
constexpr unsigned SZ_W1 = 16 * 140 * 1024;
constexpr unsigned SZ_W2 = 8 * 54 * 5 * 1024;
constexpr unsigned SZ_ST = 2048 * 64;
constexpr unsigned WS_AB1 = 0;
constexpr unsigned WS_AB2 = WS_AB1 + SZ_AB;
constexpr unsigned WS_X1 = WS_AB2 + SZ_AB;
constexpr unsigned WS_X2 = WS_X1 + SZ_X;
constexpr unsigned WS_X3 = WS_X2 + SZ_X;
constexpr unsigned WS_H1 = WS_X3 + SZ_X;
constexpr unsigned WS_H2 = WS_H1 + SZ_H;
constexpr unsigned WS_W1 = WS_H2 + SZ_H;
constexpr unsigned WS_W2 = WS_W1 + SZ_W1;
constexpr unsigned WS_ST1 = WS_W2 + SZ_W2;
constexpr unsigned WS_ST2 = WS_ST1 + SZ_ST;
constexpr unsigned WS_ST3 = WS_ST2 + SZ_ST;
constexpr unsigned WS_CS1 = WS_ST3 + SZ_ST;
constexpr unsigned WS_CSD = WS_CS1 + 20 * 1728 * 4;
constexpr unsigned XSLOT = 2560;
constexpr unsigned SZ_XP = 256 * 12 * XSLOT;
constexpr unsigned WS_XP = (WS_CSD + 256 * 320 + 4095) & ~4095u;
constexpr unsigned WS_FLG = WS_XP + 2 * SZ_XP;
constexpr unsigned FL_X = 0;
constexpr unsigned FL_2 = FL_X + 2048;
constexpr unsigned FL_3 = FL_2 + 2048;
constexpr unsigned FL_4 = FL_3 + 4096;
constexpr unsigned FL_5 = FL_4 + 1024;
constexpr unsigned FL_W = FL_5 + 2048;
constexpr unsigned FL_E = FL_W + 1088;
constexpr unsigned WS_END = WS_FLG + (FL_E + 1) * 128;

DI u32x4 make_srd(const void* p, unsigned bytes) {
  uint64_t a = (uint64_t)p;
  u32x4 r;
  r[0] = (unsigned)a; r[1] = (unsigned)(a >> 32) & 0xffffu; r[2] = bytes; r[3] = 0x00020000u;
  return r;
}
DI rsrc_t make_rsrc(const void* p, unsigned bytes) {
  return __builtin_amdgcn_make_buffer_rsrc((void*)p, (short)0, (int)bytes, 0x00020000);
}
template <int LOFF, int GOFF>
DI void glds(u32x4 srd, unsigned voff, unsigned ldsb, unsigned soff) {
  unsigned tmp;
  asm volatile("s_add_u32 m0, %1, %2\n\ts_add_u32 %0, %3, %4\n\tbuffer_load_dwordx4 %5, %6, %0 offen lds"
               : "=&s"(tmp)
               : "s"(ldsb), "i"(LOFF), "s"(soff), "i"(GOFF), "v"(voff), "s"(srd)
               : "memory", "scc");
}
template <int N> DI void glds_lin(u32x4 srd, unsigned voff, unsigned ldsb, unsigned soff) {
  if constexpr (N == 4)
    asm volatile("s_mov_b32 m0, %0\n\ts_nop 0\n\tbuffer_load_dwordx4 %1, %2, %3 offen lds\n\tbuffer_load_dwordx4 %1, %2, %3 offen offset:1024 lds\n\t"
                 "buffer_load_dwordx4 %1, %2, %3 offen offset:2048 lds\n\tbuffer_load_dwordx4 %1, %2, %3 offen offset:3072 lds"
                 :: "s"(ldsb), "v"(voff), "s"(srd), "s"(soff) : "memory");
  else if constexpr (N == 3)
    asm volatile("s_mov_b32 m0, %0\n\ts_nop 0\n\tbuffer_load_dwordx4 %1, %2, %3 offen lds\n\tbuffer_load_dwordx4 %1, %2, %3 offen offset:1024 lds\n\t"
                 "buffer_load_dwordx4 %1, %2, %3 offen offset:2048 lds"
                 :: "s"(ldsb), "v"(voff), "s"(srd), "s"(soff) : "memory");
  else
    asm volatile("s_mov_b32 m0, %0\n\ts_nop 0\n\tbuffer_load_dwordx4 %1, %2, %3 offen lds"
                 :: "s"(ldsb), "v"(voff), "s"(srd), "s"(soff) : "memory");
}
template <int IMM> DI void ldv(bf16x8& d, u32x4 srd, unsigned voff, unsigned soff) {
  asm volatile("buffer_load_dwordx4 %0, %1, %2, %3 offen offset:%4 sc1" : "=v"(d) : "v"(voff), "s"(srd), "s"(soff), "i"(IMM) : "memory");
}
template <int N> DI void wait_vm() { asm volatile("s_waitcnt vmcnt(%0)" ::"i"(N) : "memory"); }
template <int N> DI void wait_vm5(bf16x8 (&b)[5]) {
  asm volatile("s_waitcnt vmcnt(%5)" : "+v"(b[0]), "+v"(b[1]), "+v"(b[2]), "+v"(b[3]), "+v"(b[4]) : "i"(N) : "memory");
}
DI void wait_lgkm0() { asm volatile("s_waitcnt lgkmcnt(0)" ::: "memory"); }
DI void sb() { __builtin_amdgcn_sched_barrier(0); }
DI void bar() { asm volatile("s_barrier" ::: "memory"); }

DI bf16x8 lds_rd(unsigned addr) { return *(const LDS_AS bf16x8*)(ldsp)(uintptr_t)addr; }
DI f32x4 lds_rdf(unsigned addr) { return *(const LDS_AS f32x4*)(ldsp)(uintptr_t)addr; }
DI void lds_wrf(unsigned addr, f32x4 v) { *(LDS_AS f32x4*)(ldsp)(uintptr_t)addr = v; }
DI float lds_rd1(unsigned addr) { return *(const LDS_AS float*)(ldsp)(uintptr_t)addr; }
DI void lds_wr1(unsigned addr, float v) { *(LDS_AS float*)(ldsp)(uintptr_t)addr = v; }
DI f32x4 mfma(bf16x8 a, bf16x8 b, f32x4 c) { return __builtin_amdgcn_mfma_f32_16x16x32_bf16(a, b, c, 0, 0, 0); }
DI void mma(f32x4& c, const bf16x8& a, const bf16x8& b) {
  asm volatile("v_mfma_f32_16x16x32_bf16 %0, %1, %2, %0" : "+v"(c) : "v"(a), "v"(b));
}
DI void mma0(f32x4& c, const bf16x8& a, const bf16x8& b) {
  asm volatile("v_mfma_f32_16x16x32_bf16 %0, %1, %2, 0" : "=&v"(c) : "v"(a), "v"(b));
}
template <int N> DI void fence_acc(f32x4 (&a)[N]) {
  if constexpr (N == 5) asm volatile("s_nop 7\n\ts_nop 1" : "+v"(a[0]), "+v"(a[1]), "+v"(a[2]), "+v"(a[3]), "+v"(a[4]));
  else asm volatile("s_nop 7\n\ts_nop 1" : "+v"(a[0]), "+v"(a[1]), "+v"(a[2]), "+v"(a[3]), "+v"(a[4]), "+v"(a[5]), "+v"(a[6]));
}
template <int N> DI void pin_acc(f32x4 (&a)[N]) {
  if constexpr (N == 5) asm volatile("" : "+v"(a[0]), "+v"(a[1]), "+v"(a[2]), "+v"(a[3]), "+v"(a[4]));
  else asm volatile("" : "+v"(a[0]), "+v"(a[1]), "+v"(a[2]), "+v"(a[3]), "+v"(a[4]), "+v"(a[5]), "+v"(a[6]));
}
template <int M, int N> DI void fence_all(f32x4 (&a)[M][N]) {
#pragma unroll
  for (int i = 1; i < M; ++i) pin_acc(a[i]);
  fence_acc(a[0]);
#pragma unroll
  for (int i = 1; i < M; ++i) pin_acc(a[i]);
}
DI unsigned pk2(float a, float b) {
  f32x2 v = {a, b};
  return __builtin_bit_cast(unsigned, __builtin_convertvector(v, bf16x2));
}
DI float bflo(unsigned u) { return __uint_as_float(u << 16); }
DI float bfhi(unsigned u) { return __uint_as_float(u & 0xffff0000u); }
DI float dot2(unsigned a, unsigned b, float c) {
  return __builtin_amdgcn_fdot2_f32_bf16(__builtin_bit_cast(bf16x2, a), __builtin_bit_cast(bf16x2, b), c, false);
}
DI float xsum16_32(float x) {
  auto s = __builtin_amdgcn_permlane16_swap(__float_as_uint(x), __float_as_uint(x), false, false);
  x = __uint_as_float(s[0]) + __uint_as_float(s[1]);
  auto t = __builtin_amdgcn_permlane32_swap(__float_as_uint(x), __float_as_uint(x), false, false);
  return __uint_as_float(t[0]) + __uint_as_float(t[1]);
}
template <int V> struct IC { static constexpr int v = V; };
template <int I, int N, class Fn> DI void sfor(Fn&& f) {
  if constexpr (I < N) { f(IC<I>{}); sfor<I + 1, N>(f); }
}
DI unsigned rfl(unsigned v) { return __builtin_amdgcn_readfirstlane(v); }
DI unsigned opq_v(unsigned x) { asm volatile("" : "+v"(x)); return x; }
DI unsigned opq_s(unsigned x) { asm volatile("" : "+s"(x)); return x; }
template <class T> DI T* opq_p(T* x) { asm volatile("" : "+s"(x)); return x; }
DI bf16x8 ld16(rsrc_t r, unsigned voff, unsigned soff) { return __builtin_bit_cast(bf16x8, __builtin_amdgcn_raw_buffer_load_b128(r, voff, soff, 16)); }
DI u32x4 ld16u(rsrc_t r, unsigned voff, unsigned soff) { return __builtin_amdgcn_raw_buffer_load_b128(r, voff, soff, 16); }
DI u32x2 ld8u(rsrc_t r, unsigned voff, unsigned soff) { return __builtin_amdgcn_raw_buffer_load_b64(r, voff, soff, 16); }
DI void st16(u32x4 v, rsrc_t r, unsigned voff, unsigned soff) {
  __builtin_amdgcn_raw_buffer_store_b128(v, r, voff, soff, 16);
  asm volatile("s_nop 1" :: "v"(v) : "memory");
}
DI void st16_raw(u32x4 v, rsrc_t r, unsigned voff, unsigned soff) { __builtin_amdgcn_raw_buffer_store_b128(v, r, voff, soff, 16); }
template <class... T> DI void st_guard(const T&... v) {
  (..., [](const auto& x) { asm volatile("" :: "v"(x) : "memory"); }(v));
  asm volatile("s_nop 1" ::: "memory");
  (..., [](const auto& x) { asm volatile("" :: "v"(x)); }(v));
}
DI void st8(u32x2 v, rsrc_t r, unsigned voff, unsigned soff) { __builtin_amdgcn_raw_buffer_store_b64(v, r, voff, soff, 16); }
DI void st4(unsigned v, rsrc_t r, unsigned voff, unsigned soff) { __builtin_amdgcn_raw_buffer_store_b32(v, r, voff, soff, 16); }

#define ST(i)
#define STAMP_ARGS
#define STAMP_FWD
#define STAMP_PASS(k)

template <int KS> DI unsigned foff(unsigned row, unsigned col) {
  return ((row >> 4) * KS + (col >> 5)) * 1024u + (((col >> 3) & 3) * 16u + (row & 15)) * 16u + (col & 7) * 2u;
}

extern __shared__ __attribute__((aligned(1024))) char smem[];
DI unsigned lds_base() { return (unsigned)(uintptr_t)(ldsp)smem; }

DI void ln_coef(float s1, float s2, float& mu, float& rs) {
  mu = s1 * (1.f / D_);
  rs = __builtin_amdgcn_rsqf(fmaxf(s2 * (1.f / D_) - mu * mu, 0.f) + EPS);
}

DI void g1_store(const f32x4 (&acc)[2][5], const float (&mu)[2][4], const float (&rs)[2][4], const float (&csv)[5], rsrc_t rab,
                 unsigned P, unsigned ct, unsigned w, unsigned i16, unsigned q) {
  const unsigned kstep = (ct >> 3) * 64 + P * 4 + w;
  const unsigned cq = ((q & 1) << 1) | (q >> 1);
  const unsigned so = ((ct & 7) * 128 + kstep) * 5 * 1024;
  const unsigned vo = (cq * 16 + i16) * 16;
  u32x4 y[5];
#pragma unroll
  for (int f = 0; f < 5; ++f) {
    const f32x2 cs = {csv[f], csv[f]};
    unsigned pk[2][2];
#pragma unroll
    for (int r = 0; r < 2; ++r)
#pragma unroll
      for (int e = 0; e < 2; ++e) {
        const f32x2 t = __builtin_elementwise_fma(f32x2{mu[r][2 * e], mu[r][2 * e + 1]}, cs, f32x2{acc[r][f][2 * e], acc[r][f][2 * e + 1]}) *
                        f32x2{rs[r][2 * e], rs[r][2 * e + 1]};
        pk[r][e] = __builtin_bit_cast(unsigned, __builtin_convertvector(t, bf16x2));
      }
    auto t0 = __builtin_amdgcn_permlane16_swap(pk[0][0], pk[1][0], false, false);
    auto t1 = __builtin_amdgcn_permlane16_swap(pk[0][1], pk[1][1], false, false);
    y[f] = u32x4{t0[0], t1[0], t0[1], t1[1]};
    st16_raw(y[f], rab, vo + (f & 3) * 1024, so + (f >> 2) * 4096);
  }
  st_guard(y[0], y[1], y[2], y[3], y[4]);
}

constexpr int K1_STAGE = 26 * 1024;
constexpr int K1_TR = 5 * K1_STAGE;
constexpr int K1_CS = K1_TR + 5 * 4096;

DI void k1_issue(u32x4 sa, u32x4 sbd, unsigned vo, unsigned la, unsigned lbb, unsigned soa, unsigned sob, unsigned w) {
  glds<0, 0>(sa, vo, la, soa);
  glds<1024, 8 * 1280>(sa, vo, la, soa);
  glds<2048, 16 * 1280>(sa, vo, la, soa);
  glds<3072, 24 * 1280>(sa, vo, la, soa);
  glds<0, 0>(sbd, vo, lbb, sob);
  glds<4096, 32 * 1280>(sbd, vo, lbb, sob);
  if (w < 2) glds<8192, 64 * 1280>(sbd, vo, lbb, sob);
}
struct RtUnit { bool first; unsigned ks, g; };
DI RtUnit rt_unit(unsigned u) {
  RtUnit r;
  r.first = u < 540;
  if (r.first) { r.ks = u / 27u; r.g = u - r.ks * 27u; }
  else { const unsigned v = u - 540; r.ks = v / 10u; r.g = v - r.ks * 10u; }
  return r;
}
DI void retile_issue(unsigned u, const u16* w1, const u16* w2, unsigned ldst, unsigned lane) {
  const RtUnit t = rt_unit(u);
  const unsigned ld = t.first ? F_ * 2 : D_ * 2;
  u32x4 srd = make_srd(t.first ? w1 : w2, D_ * F_ * 2);
  const unsigned vo = (lane >> 3) * ld + (lane & 7) * 16;
  const unsigned so = t.ks * 32 * ld + t.g * 128;
  glds<0, 0>(srd, vo, ldst, so);
  glds<1024, 0>(srd, vo, ldst, so + 8 * ld);
  glds<2048, 0>(srd, vo, ldst, so + 16 * ld);
  glds<3072, 0>(srd, vo, ldst, so + 24 * ld);
}
DI unsigned w1_off(unsigned rt, unsigned ks) {
  const unsigned c = rt / 7u, f = rt - c * 7, g = (c & 1) ? (f == 0 ? 6 : f - 1) : f;
  return WS_W1 + (c * 140 + (g < 4 ? ks * 4 + g : 80 + ks * 3 + g - 4)) * 1024;
}
DI void retile_finish(unsigned u, unsigned f0, rsrc_t rws, unsigned ldst, unsigned lane) {
  const RtUnit t = rt_unit(u);
  const unsigned i = lane & 15, q = lane >> 4;
  const unsigned base = ldst + (8 * q + (i >> 2)) * 128 + (i & 3) * 8 + f0 * 32;
  u32x4 v[2];
#pragma unroll
  for (int f = 0; f < 2; ++f) {
    s16x4 lo = __builtin_amdgcn_ds_read_tr16_b64_v4i16((LDS_AS s16x4*)(uintptr_t)(base + f * 32));
    s16x4 hi = __builtin_amdgcn_ds_read_tr16_b64_v4i16((LDS_AS s16x4*)(uintptr_t)(base + f * 32 + 512));
    u32x2 l2 = __builtin_bit_cast(u32x2, lo), h2 = __builtin_bit_cast(u32x2, hi);
    v[f] = u32x4{l2[0], l2[1], h2[0], h2[1]};
  }
  const unsigned rt = t.g * 4 + f0;
  if (t.first) {
    st16(v[0], rws, lane * 16, w1_off(rt, t.ks));
    st16(v[1], rws, lane * 16, w1_off(rt + 1, t.ks));
    float s[2];
#pragma unroll
    for (int f = 0; f < 2; ++f)
      s[f] = xsum16_32(((bflo(v[f][0]) + bfhi(v[f][0])) + (bflo(v[f][1]) + bfhi(v[f][1]))) + ((bflo(v[f][2]) + bfhi(v[f][2])) + (bflo(v[f][3]) + bfhi(v[f][3]))));
    st4(__float_as_uint(q == 0 ? s[0] : s[1]), rws, lane < 32 ? lane * 4 : OOB, WS_CS1 + (t.ks * F_ + rt * 16) * 4);
  } else {
    const unsigned c0 = rt / 5u, c1 = (rt + 1) / 5u;
    st16(v[0], rws, lane * 16, WS_W2 + ((c0 * 54 + t.ks) * 5 + (rt - c0 * 5)) * 1024);
    st16(v[1], rws, lane * 16, WS_W2 + ((c1 * 54 + t.ks) * 5 + (rt + 1 - c1 * 5)) * 1024);
  }
}

#define ASLEEP 0
#define W2SLEEP 0
#define WDSLEEP 0
#define WARM_X 0
#define WARM_W 0
#define WARM_H 0
#define WARM_S 0
#define WARM_0 0
#define WS1 3
#define WS2 6
#define ROT1 1
#define ROT2 0
#define ROT3 0
#define ROT4 1
#define ROT5 0
#define PUB1 12
#define LSLEEP 20
#define RGAP 10
#define RSLEEP 0
#define ASLEEP2 40
#define RDPAT 1
#define LAZY5 0
#define LAZY2 0
#define WDPRE 6
#define CODEWARM 1
#define PDELAY 18
#define WSLEEP 52
#define SSLEEP 46
constexpr int KM_LDS = 160 * 1024;
constexpr int KM_CS1 = KM_LDS - 1024;
constexpr int G2_A = 80 * 1024, G2_AS = 16 * 1024, G2_BS = 20 * 1024;
constexpr int G4_LATE = 80 * 1024;

DI gu64* flag_ptr(char* ws, unsigned f) { return (gu64*)(ws + WS_FLG + f * 128); }
DI void raise(char* ws, unsigned f, u64 epoch, unsigned lane) {
  if (lane < 16) __hip_atomic_store(flag_ptr(ws, f) + lane, epoch, RLX_AGENT);
}
DI void publish(char* ws, unsigned f, u64 epoch, unsigned lane) {
  wait_vm<0>();
  raise(ws, f, epoch, lane);
}
#define P3DELAY 0
#define P5DELAY0 10
#define P5DELAY 50
#define POLLATOM 0
#define PSL 1
DI void poll0(char* ws, unsigned f, unsigned n, u64 epoch, unsigned lane) {
  gu64* p = flag_ptr(ws, f);
  bool ok = lane >= n;
#pragma unroll 1
  for (unsigned s = 0; s < (1u << 16); ++s) {
    if (!ok) ok = (POLLATOM ? __hip_atomic_fetch_or(p, 0ull, RLX_AGENT) : __hip_atomic_load(p, RLX_AGENT)) == epoch;
    if (__all(ok)) break;
    __builtin_amdgcn_s_sleep(PSL);
  }
  asm volatile("" ::: "memory");
}
DI void poll(char* ws, unsigned f, unsigned n, u64 epoch, unsigned lane) {
  __builtin_amdgcn_s_sleep(PDELAY);
  poll0(ws, f, n, epoch, lane);
}
DI void warm(unsigned ldump, u32x4 srd, unsigned voff, unsigned soff) {
  asm volatile("s_mov_b32 m0, %0\n\ts_nop 0\n\tbuffer_load_dword %1, %2, %3 offen lds" :: "s"(ldump), "v"(voff), "s"(srd), "s"(soff) : "memory");
}
template <int N> DI void glds_run(u32x4 srd, unsigned voff, unsigned l, unsigned so) {
#pragma unroll
  for (int g = 0; g < N / 4; ++g) glds_lin<4>(srd, voff, l + g * 4096, so + g * 4096);
  if constexpr (N % 4 == 3) glds_lin<3>(srd, voff, l + (N / 4) * 4096, so + (N / 4) * 4096);
  else {
#pragma unroll
    for (int g = N / 4 * 4; g < N; ++g) glds_lin<1>(srd, voff, l + g * 1024, so + g * 1024);
  }
}
DI void glds4_sc1(u32x4 srd, unsigned voff, unsigned l, unsigned so) {
  asm volatile("s_mov_b32 m0, %0\n\ts_nop 0\n\tbuffer_load_dwordx4 %1, %2, %3 offen sc1 lds\n\tbuffer_load_dwordx4 %1, %2, %3 offen offset:1024 sc1 lds\n\t"
               "buffer_load_dwordx4 %1, %2, %3 offen offset:2048 sc1 lds\n\tbuffer_load_dwordx4 %1, %2, %3 offen offset:3072 sc1 lds"
               :: "s"(l), "v"(voff), "s"(srd), "s"(so) : "memory");
}
DI void glds1_sc1(u32x4 srd, unsigned voff, unsigned l, unsigned so) {
  asm volatile("s_mov_b32 m0, %0\n\ts_nop 0\n\tbuffer_load_dwordx4 %1, %2, %3 offen sc1 lds" :: "s"(l), "v"(voff), "s"(srd), "s"(so) : "memory");
}
template <int N> DI void glds_run_sc1(u32x4 srd, unsigned voff, unsigned l, unsigned so) {
#pragma unroll
  for (int g = 0; g < N / 4; ++g) glds4_sc1(srd, voff, l + g * 4096, so + g * 4096);
#pragma unroll
  for (int g = N / 4 * 4; g < N; ++g) glds1_sc1(srd, voff, l + g * 1024, so + g * 1024);
}

struct ResTile { u32x4 r0, r1; u32x2 r2; };
struct TileOff { unsigned a0, a1, a2; };
DI TileOff tile_offsets(bool rm, unsigned row, unsigned ct, unsigned q) {
  const unsigned cA = ct * 80 + 16 * (q & 1) + 8 * (q >> 1), cS = ct * 80 + 64 + 4 * q;
  TileOff t;
  if (rm) { t.a0 = (row * D_ + cA) * 2; t.a1 = t.a0 + 64; t.a2 = (row * D_ + cS) * 2; }
  else { t.a0 = foff<20>(row, cA); t.a1 = foff<20>(row, cA + 32); t.a2 = foff<20>(row, cS); }
  asm volatile("" : "+v"(t.a0), "+v"(t.a1), "+v"(t.a2));
  return t;
}
DI ResTile load_res(rsrc_t rr, const TileOff& t) {
  ResTile rt;
  rt.r0 = ld16u(rr, t.a0, 0); rt.r1 = ld16u(rr, t.a1, 0); rt.r2 = ld8u(rr, t.a2, 0);
  return rt;
}
DI void finish_tile(f32x4 (&v)[5], const ResTile& rt, rsrc_t rout, const TileOff& o, bool stats, rsrc_t rst, unsigned sto) {
#pragma unroll
  for (int p = 0; p < 2; ++p)
#pragma unroll
    for (int r = 0; r < 4; ++r) {
      auto t = __builtin_amdgcn_permlane16_swap(__float_as_uint(v[2 * p][r]), __float_as_uint(v[2 * p + 1][r]), false, false);
      v[2 * p][r] = __uint_as_float(t[0]); v[2 * p + 1][r] = __uint_as_float(t[1]);
    }
  u32x4 y0, y1; u32x2 y2;
#pragma unroll
  for (int j = 0; j < 2; ++j) {
    y0[j] = pk2(v[0][2 * j] + bflo(rt.r0[j]), v[0][2 * j + 1] + bfhi(rt.r0[j]));
    y0[2 + j] = pk2(v[1][2 * j] + bflo(rt.r0[2 + j]), v[1][2 * j + 1] + bfhi(rt.r0[2 + j]));
    y1[j] = pk2(v[2][2 * j] + bflo(rt.r1[j]), v[2][2 * j + 1] + bfhi(rt.r1[j]));
    y1[2 + j] = pk2(v[3][2 * j] + bflo(rt.r1[2 + j]), v[3][2 * j + 1] + bfhi(rt.r1[2 + j]));
    y2[j] = pk2(v[4][2 * j] + bflo(rt.r2[j]), v[4][2 * j + 1] + bfhi(rt.r2[j]));
  }
  st16_raw(y0, rout, o.a0, 0); st16_raw(y1, rout, o.a1, 0); st8(y2, rout, o.a2, 0);
  st_guard(y0, y1);
  if (stats) {
    float s1a = 0.f, s1b = 0.f, s2a = 0.f, s2b = 0.f;
#pragma unroll
    for (int j = 0; j < 4; ++j) {
      s1a = dot2(y0[j], 0x3f803f80u, s1a); s2a = dot2(y0[j], y0[j], s2a);
      s1b = dot2(y1[j], 0x3f803f80u, s1b); s2b = dot2(y1[j], y1[j], s2b);
    }
    s1a = dot2(y2[0], 0x3f803f80u, s1a); s2a = dot2(y2[0], y2[0], s2a);
    s1b = dot2(y2[1], 0x3f803f80u, s1b); s2b = dot2(y2[1], y2[1], s2b);
    const float s1 = xsum16_32(s1a + s1b), s2 = xsum16_32(s2a + s2b);
    st8(u32x2{__float_as_uint(s1), __float_as_uint(s2)}, rst, sto, 0);
  }
}

template <int W> DI void sm_scatter(unsigned wb, const f32x4 (&acc)[4][5], f32x4 (&own)[5]) {
#pragma unroll
  for (int d = 0; d < 4; ++d)
#pragma unroll
    for (int f = 0; f < 5; ++f) {
      if (d == W) own[f] = acc[d][f];
      else lds_wrf(wb + (((d - W - 1) & 3) * 5 + f) * 1024, acc[d][f]);
    }
  asm volatile("; scatter %5" : "+v"(own[0]), "+v"(own[1]), "+v"(own[2]), "+v"(own[3]), "+v"(own[4]) : "i"(W));
}
template <int KQ> DI void xp_send(const f32x4 (&acc)[4][5], f32x4 (&own)[5], rsrc_t rx, unsigned so, unsigned lane) {
  u32x4 y[3][2];
  sfor<0, 3>([&](auto S) {
    constexpr int d = (KQ + 1 + S.v) & 3;
    const unsigned o = so + (d * 3 + ((KQ - d - 1) & 3)) * 4 * XSLOT;
    unsigned p[10];
#pragma unroll
    for (int f = 0; f < 5; ++f) { p[2 * f] = pk2(acc[d][f][0], acc[d][f][1]); p[2 * f + 1] = pk2(acc[d][f][2], acc[d][f][3]); }
    y[S.v][0] = u32x4{p[0], p[1], p[2], p[3]}; y[S.v][1] = u32x4{p[4], p[5], p[6], p[7]};
    st16_raw(y[S.v][0], rx, lane * 16, o);
    st16_raw(y[S.v][1], rx, lane * 16 + 1024, o);
    st8(u32x2{p[8], p[9]}, rx, lane * 8 + 2048, o);
  });
  st_guard(y[0][0], y[0][1], y[1][0], y[1][1], y[2][0], y[2][1]);
#pragma unroll
  for (int f = 0; f < 5; ++f) own[f] = acc[KQ][f];
  asm volatile("; send %5" : "+v"(own[0]), "+v"(own[1]), "+v"(own[2]), "+v"(own[3]), "+v"(own[4]) : "i"(KQ));
}

DI constexpr unsigned wd_pos(int c) { return c < 8 ? 10 * c : 60 + 10 * c; }
DI constexpr int a_when(int k) { return k < 3 ? -1 : (k < 17 ? (k - 3) / 2 : k - 10); }

constexpr int ORD[14] = {0, 1, 4, 5, 7, 8, 11, 12, 2, 3, 6, 9, 10, 13};
DI constexpr int g4_late(int t) { return (t >= 4 && t < 7) || t >= 11; }
DI constexpr int g4_slot(int t) { return (t < 4 ? t : (t < 7 ? t - 4 : (t < 11 ? t - 7 : t - 11))) * 5120; }

#define PHASE_IDS \
  const unsigned lane = opq_v(tid0) & 63, w = opq_s(w0), bid = opq_s(blockIdx.x); \
  const unsigned xcd = bid & 7, idx = bid >> 3, i16 = lane & 15, q = lane >> 4; \
  char* const ws = opq_p(ws_); \
  (void)xcd; (void)idx; (void)i16; (void)q;
#define IDS_G2 const unsigned kq = xcd & 3, P = (xcd >> 2) * 8 + (idx >> 2), c4 = idx & 3;
#define G2_ROT 0u
#define IDS_G3 const unsigned P = (xcd >> 1) * 4 + (idx >> 3), ct = (xcd & 1) * 8 + (idx & 7);
#define G4PAIR 1
#define C8_OF (G4PAIR ? (xcd & 1) * 4 + (idx & 3) : idx & 7)
#define IDS_G4 const unsigned rt64 = G4PAIR ? (xcd >> 1) * 8 + (idx >> 2) : xcd * 4 + (idx >> 3), c8 = C8_OF;
#define WARM_STRIP(rx, sox) \
  ({ const unsigned n_ = (idx & 7) * 40 + lane; \
     __builtin_amdgcn_raw_buffer_load_b32(rx, lane < 40 ? (((n_ >> 3) & 1) * 20 + (n_ >> 4)) * 1024 + (n_ & 7) * 128 : OOB, sox, 16); })
#define KROT(k) ((k) + rot >= 20 ? (k) + rot - 20 : (k) + rot)
#define LDX(r, k) ld16(rx, lane * 16, sox + (r) * 20480 + KROT(k) * 1024)

__global__ __launch_bounds__(512, 2) void k_all(const u16* x0_, const u16* Sre_, const u16* Sim_, const u16* Dre_, const u16* Dim_,
                                               const u16* w1_, const u16* w2_, char* ws_, u16* out_ STAMP_ARGS) {
  ST(0);
  const unsigned tid0 = threadIdx.x, wall = rfl(tid0 >> 6), w0 = wall & 3;
  const unsigned lb = lds_base();
  const char* code; unsigned code_len;
  asm volatile("s_getpc_b64 %0\n.Lcw_begin:\n\ts_mov_b32 %1, .Lcw_end-.Lcw_begin" : "=s"(code), "=s"(code_len));
  u64 epoch;

  if (wall >= 4) {
    const unsigned sink = lb + K1_CS + 1024;
    unsigned ep0, ep1;
    asm volatile("global_load_dword %0, %2, %3 sc1\n\tglobal_load_dword %1, %2, %3 offset:4 sc1"
                 : "=&v"(ep0), "=&v"(ep1) : "v"(WS_FLG + FL_E * 128), "s"(ws_) : "memory");
    {
      PHASE_IDS
      IDS_G3
      const u32x4 sa = make_srd(opq_p(x0_), S_ * D_ * 2), sbd = make_srd(ct < 8 ? opq_p(Dre_) : opq_p(Dim_), D_ * D_ * 2);
      const unsigned vo = (lane >> 3) * 1280 + (((lane & 7) ^ (lane >> 3)) << 4);
      const unsigned soa = (P * 128 + w * 32) * 1280, sob = ((ct & 7) * 80 + w * 8) * 1280;
      const unsigned la = lb + w * 4096, lbb = lb + 16384 + w * 1024;
      const unsigned u0 = bid * 4 + w, u1 = 1024 + bid;
      const bool extra = w == 3 && bid < 56;
      const unsigned rot = ROT1 ? ((idx & 7) + 3 * (idx >> 3)) % 10u : 0;
      auto ko = [&](unsigned s) { const unsigned c = s + rot; return (c >= 10 ? c - 10 : c) * 128; };
      const rsrc_t rws = make_rsrc(ws, WS_END);
      asm volatile("s_nop 4" ::: "memory");
      if (WARM_0) {
        const unsigned r0 = lane / 10u, id = ((idx >> 3) * 4 + w) * 50 + lane, r1 = id / 10u;
        warm(sink, sa, lane < 40 ? r0 * 1280 + (lane - r0 * 10) * 128 : OOB, (P * 128 + (idx & 7) * 16 + w * 4) * 1280);
        warm(sink, sbd, lane < 50 ? r1 * 1280 + (id - r1 * 10) * 128 : OOB, (ct & 7) * 80 * 1280);
      }
      k1_issue(sa, sbd, vo, la, lbb, soa + ko(0), sob + ko(0), w);
      k1_issue(sa, sbd, vo, la + K1_STAGE, lbb + K1_STAGE, soa + ko(1), sob + ko(1), w);
      if (CODEWARM && w == 3) warm(sink, make_srd(code, code_len), lane < 16 ? idx * 2048 + lane * 128 : OOB, 0);
      ST(8);
      wait_vm<6>();
      {
        unsigned lo, hi;
        asm volatile("v_readfirstlane_b32 %0, %2\n\tv_readfirstlane_b32 %1, %3\n\ts_nop 4" : "=s"(lo), "=s"(hi) : "v"(ep0), "v"(ep1));
        epoch = (u64)lo + ((u64)hi << 32) + 1;
      }
      bar();
      k1_issue(sa, sbd, vo, la + 2 * K1_STAGE, lbb + 2 * K1_STAGE, soa + ko(2), sob + ko(2), w);
      const u32x4 ss = make_srd((xcd & 2) ? Sim_ : Sre_, S_ * S_ * 2);
#define WPAT 0
      const unsigned vs = WPAT ? (lane >> 4) * 4096 + (lane & 15) * 128 : (lane & 31) * 4096 + (lane >> 5) * 128;
      const unsigned sos = ((xcd >> 2) * 1024 + idx * 32) * 4096 + (xcd & 1) * 2048 + (WPAT ? w * 8 * 4096 : w * 256);
      unsigned so = 0;
#pragma unroll 1
      for (int i = 0; i < 10; ++i) {
        if (i == 0) wait_vm<6>(); else if (i == 1) wait_vm<2 * 6>(); else if (i < 6) wait_vm<3 * 6>();
        else if (i == 6) wait_vm<2 * 6 + 4>(); else if (i == 7) wait_vm<6 + 4>(); else wait_vm<4 + (WS2 >= 4) + (WS1 >= 4)>();
        bar();
        if (i == 0) {
          k1_issue(sa, sbd, vo, la + 3 * K1_STAGE, lbb + 3 * K1_STAGE, soa + ko(3), sob + ko(3), w);
          k1_issue(sa, sbd, vo, la + 4 * K1_STAGE, lbb + 4 * K1_STAGE, soa + ko(4), sob + ko(4), w);
        } else if (i == 1) {
          k1_issue(sa, sbd, vo, la, lbb, soa + ko(5), sob + ko(5), w);
          k1_issue(sa, sbd, vo, la + K1_STAGE, lbb + K1_STAGE, soa + ko(6), sob + ko(6), w);
        } else if (i < 5) k1_issue(sa, sbd, vo, la + so, lbb + so, soa + ko(i + 5), sob + ko(i + 5), w);
        so = so == 4 * K1_STAGE ? 0 : so + K1_STAGE;
        if (i == WS1) warm(sink, ss, vs, sos);
        if (i == WS2) warm(sink, ss, vs, sos + (WPAT ? 4 * 4096 : 1024));
        if (i == 5) {
          retile_issue(u0, w1_, w2_, lb + K1_TR + w * 4096, lane);
          if (extra) retile_issue(u1, w1_, w2_, lb + K1_TR + 4 * 4096, lane);
        }
      }
      ST(9);
      bar();
    }
    bf16x8 wr[10][5];
#pragma unroll
    for (int t = 0; t < 10; ++t)
#pragma unroll
      for (int f = 0; f < 5; ++f) wr[t][f] = bf16x8{};
#pragma unroll 1
    for (int L = 0; L < 2; ++L) {
      const int sbase = L * 20;
      (void)sbase;
      {
        PHASE_IDS
        IDS_G2
        const u32x4 sa = make_srd(kq < 2 ? opq_p(Sre_) : opq_p(Sim_), S_ * S_ * 2);
        const u32x4 sb_ = make_srd(ws + (L ? WS_AB2 : WS_AB1), SZ_AB);
        const unsigned va = (lane >> 3) * 4096 + (((lane & 7) ^ (lane >> 3)) << 4), vb = lane * 16;
        const unsigned soa = (P * 128 + w * 32) * 4096 + (kq & 1) * 2048;
        const unsigned sob = ((2 * c4 + (w >> 1)) * 128 + kq * 32 + (w & 1)) * 5 * 1024;
        const unsigned la = lb + G2_A + w * 4096, lbb = lb + w * 5120;
        const unsigned ct = (xcd & 1) * 8 + (idx & 7);
        const unsigned rot = G2_ROT;
        asm volatile("s_nop 4" ::: "memory");
        auto issue_a = [&](unsigned l, unsigned so) {
          glds<0, 0>(sa, va, l, so); glds<1024, 8 * 4096>(sa, va, l, so);
          glds<2048, 16 * 4096>(sa, va, l, so); glds<3072, 24 * 4096>(sa, va, l, so);
        };
        if (L == 0) {
          const unsigned u0 = bid * 4 + w, u1 = 1024 + bid;
          const rsrc_t rws = make_rsrc(ws, WS_END);
          wait_vm<0>();
          retile_finish(u0, 0, rws, lb + K1_TR + w * 4096, lane);
          retile_finish(u0, 2, rws, lb + K1_TR + w * 4096, lane);
          if (w == 3 && bid < 56) {
            retile_finish(u1, 0, rws, lb + K1_TR + 4 * 4096, lane);
            retile_finish(u1, 2, rws, lb + K1_TR + 4 * 4096, lane);
          }
        }
        if (L == 0) {
#pragma unroll
          for (int s = 0; s < 3; ++s) issue_a(la + s * G2_AS, soa + ((s + rot) & 15) * 128);
        } else {
          issue_a(la + 3 * G2_AS, soa + ((3 + rot) & 15) * 128);
        }
        if (L == 0) {
          wait_vm<12>();
          raise(ws, FL_W + bid * 4 + w, epoch, lane);
          if (w == 3 && bid < 56) raise(ws, FL_W + 1024 + bid, epoch, lane);
        }
        ST(sbase + 1);
        if (L) __builtin_amdgcn_s_sleep(P5DELAY); else __builtin_amdgcn_s_sleep(P5DELAY0);
        poll0(ws, FL_5 + ((L * 4 + kq) * 4 + c4) * 64 + ((i16 >> 1) * 2 + (w >> 1)) * 4 + (w & 1) + 2 * (i16 & 1), 16, epoch, lane);
        ST(sbase + 2);
        glds_run_sc1<5>(sb_, vb, lbb, sob + rot * 10240);
        glds_run_sc1<5>(sb_, vb, lbb + G2_BS, sob + ((1 + rot) & 15) * 10240);
        wait_vm<5>();
        bar();
        glds_run_sc1<5>(sb_, vb, lbb + 2 * G2_BS, sob + ((2 + rot) & 15) * 10240);
        if (L == 0) issue_a(la + 3 * G2_AS, soa + ((3 + rot) & 15) * 128);
        glds_run_sc1<5>(sb_, vb, lbb + 3 * G2_BS, sob + ((3 + rot) & 15) * 10240);
        ST(sbase + 3);
        const u32x4 sw = make_srd(ws + WS_W1, SZ_W1);
        const unsigned sow = ct * 140 * 1024;
        unsigned fw, fw0 = 0, fw1 = 0;
        {
          const unsigned n = w * 42 + (lane < 42 ? lane : 0), k3 = n / 3u, g3 = (7 * ct >> 2) + (n - k3 * 3), n2 = n - 60;
          fw = FL_W + (n < 60 ? k3 * 27 + (g3 < 26 ? g3 : 26) : 540 + (n2 >> 1) * 10 + (5 * C8_OF >> 2) + (n2 & 1));
        }
#pragma unroll 1
        for (int s = 0; s < 16; ++s) {
          if (s == 0) wait_vm<10>();
          if (s == 1) wait_vm<14>();
          wait_vm<18>();
          if (s == 9 && L == 0) {
            asm volatile("" : "+v"(fw0), "+v"(fw1));
            if (!__all(fw0 == (unsigned)epoch && fw1 == (unsigned)(epoch >> 32))) poll0(ws, fw, 42, epoch, lane);
          }
          bar();
          const unsigned sl = s & 3;
          if (s < 12) {
            issue_a(la + sl * G2_AS, soa + ((s + 4 + rot) & 15) * 128);
            glds_run_sc1<5>(sb_, vb, lbb + sl * G2_BS, sob + ((s + 4 + rot) & 15) * 10240);
            if (s == 6 && L == 0)
              asm volatile("global_load_dword %0, %2, %3 sc1\n\tglobal_load_dword %1, %2, %3 offset:4 sc1"
                           : "+v"(fw0), "+v"(fw1) : "v"(WS_FLG + fw * 128), "s"(ws) : "memory");
          } else {
            glds_run_sc1<5>(sw, vb, lbb + sl * G2_BS, sow + w * 5120 + sl * G2_BS);
            if (s < 15) glds_run_sc1<4>(sw, vb, la + sl * G2_AS, sow + G2_A + w * 4096 + sl * G2_AS);
            else glds_run_sc1<3>(sw, vb, lb + G2_A + 3 * G2_AS + w * 3072, sow + G2_A + 3 * G2_AS + w * 3072);
          }
        }
        ST(sbase + 4);
      }
      {
        PHASE_IDS
        IDS_G4
        const unsigned ct = (xcd & 1) * 8 + (idx & 7);
        if (w >= 2) {
          const rsrc_t rc = make_rsrc(ws + WS_CS1, 20 * F_ * 4);
          const unsigned c = (w - 2) * 64 + lane, col = ct * 112 + c;
          const unsigned vo = c < 112 && col < F_ ? col * 4 : OOB;
          float csp = 0.f;
#pragma unroll
          for (int k = 0; k < 20; ++k) csp += __uint_as_float(__builtin_amdgcn_raw_buffer_load_b32(rc, vo, k * F_ * 4, 16));
          lds_wr1(lb + KM_CS1 + c * 4, csp);
        }
        const unsigned so = (c8 * 54 + w * 14) * 5 * 1024;
        wait_vm<0>();
        if (L == 0) {
          if (RSLEEP) __builtin_amdgcn_s_sleep(RSLEEP);
          const rsrc_t rw = make_rsrc(ws + WS_W2, SZ_W2);
          const unsigned vl = lane * 16, vz = lane * 16 + (w < 3 ? 0u : OOB);
          sfor<0, 50>([&](auto N) {
            constexpr int t = 4 + N.v / 5, n = ORD[t] * 5 + N.v % 5;
            wr[t - 4][N.v % 5] = ld16(rw, (ORD[t] < 12 ? vl : vz) + (n & 3) * 1024, so + (n >> 2) * 4096);
            if constexpr (RGAP && N.v % 5 == 4 && N.v < 49) { sb(); __builtin_amdgcn_s_sleep(RGAP); sb(); }
          });
          sb();
        }
        wait_lgkm0();
        ST(sbase + 5);
        bar();
        bar();
        const u32x4 s2 = make_srd(ws + WS_W2, SZ_W2);
        const unsigned sg = rfl(so);
        asm volatile("s_nop 4" ::: "memory");
        glds_run_sc1<10>(s2, lane * 16, lb + w * 20480, sg);
        glds_run_sc1<10>(s2, lane * 16, lb + w * 20480 + 10240, sg + 4 * 5120);
        wait_vm<0>();
        ST(sbase + 6);
        const unsigned pe = lb + w * 20480 + lane * 16, pl = lb + G4_LATE + w * 15360 + lane * 16;
        auto put = [&](auto T0, auto T1) {
          sfor<T0.v * 5, T1.v * 5>([&](auto N) {
            constexpr int t = N.v / 5, f = N.v % 5;
            *(LDS_AS bf16x8*)(ldsp)(uintptr_t)((g4_late(t) ? pl : pe) + g4_slot(t) + f * 1024) = wr[t - 4][f];
          });
          wait_lgkm0();
        };
        bar();
        put(IC<4>{}, IC<7>{});
        bar();
        put(IC<7>{}, IC<11>{});
        bar();
        put(IC<11>{}, IC<14>{});
        bar();
      }
      if (L) { bar(); break; }
      {
        PHASE_IDS
        IDS_G3
        const u32x4 sd = make_srd(ct < 8 ? opq_p(Dre_) : opq_p(Dim_), D_ * D_ * 2);
        const unsigned vo = (lane >> 3) * 1280 + (((lane & 7) ^ (lane >> 3)) << 4);
        const unsigned so = (ct & 7) * 80 * 1280;
        sfor<0, 10>([&](auto G) { glds<G.v * 1024, G.v * 8 * 1280>(sd, vo, lb + 20 * w * 1024, so + 2 * w * 128); });
        sfor<0, WDPRE>([&](auto G) { glds<G.v * 1024, G.v * 8 * 1280>(sd, vo, lb + (20 * w + 10) * 1024, so + (2 * w + 1) * 128); });
        bar();
        sfor<WDPRE, 10>([&](auto G) { glds<G.v * 1024, G.v * 8 * 1280>(sd, vo, lb + (20 * w + 10) * 1024, so + (2 * w + 1) * 128); });
        sfor<0, 5>([&](auto G) { glds<G.v * 1024, G.v * 8 * 1280>(sd, vo, lb + (140 + w * 5) * 1024, so + (8 + (w >> 1)) * 128 + (w & 1) * 40 * 1280); });
        wait_vm<0>();
        ST(7);
        bar();
      }
      {
        PHASE_IDS
        IDS_G2
        const u32x4 sa = make_srd(kq < 2 ? opq_p(Sre_) : opq_p(Sim_), S_ * S_ * 2);
        const unsigned va = (lane >> 3) * 4096 + (((lane & 7) ^ (lane >> 3)) << 4);
        const unsigned soa = (P * 128 + w * 32) * 4096 + (kq & 1) * 2048;
        const unsigned rot = G2_ROT;
        __builtin_amdgcn_s_sleep(ASLEEP2);
#pragma unroll
        for (int s = 0; s < 3; ++s) {
          const unsigned l = lb + G2_A + w * 4096 + s * G2_AS, so = soa + ((s + rot) & 15) * 128;
          glds<0, 0>(sa, va, l, so); glds<1024, 8 * 4096>(sa, va, l, so);
          glds<2048, 16 * 4096>(sa, va, l, so); glds<3072, 24 * 4096>(sa, va, l, so);
        }
        bar();
      }
    }
    asm volatile(".Lcw_end:");
    return;
  }

  {
    const u64 e = __hip_atomic_load(flag_ptr(ws_, FL_E), RLX_AGENT);
    epoch = (u64)rfl((unsigned)e) + ((u64)rfl((unsigned)(e >> 32)) << 32) + 1;
  }
#pragma unroll 1
  for (int L = 0; L < 2; ++L) {
    const int sbase = L * 20;
    (void)sbase;
    {
      PHASE_IDS
      IDS_G3
      f32x4 acc[2][5];
      float mu[2][4], rs[2][4], csv[5];
      const float sign = ct < 8 ? 1.f : -1.f;
      if (L == 0) {
        const unsigned ra0 = lb + w * 4096 + i16 * 128 + ((q ^ (lane & 7)) << 4);
        const unsigned rb0 = lb + 16384 + i16 * 128 + ((q ^ (lane & 7)) << 4);
        const f32x4 z4 = {0.f, 0.f, 0.f, 0.f};
        f32x4 cs0 = z4, cs1 = z4, rs_[2] = {z4, z4}, rq_[2] = {z4, z4};
#pragma unroll
        for (int r = 0; r < 2; ++r)
#pragma unroll
          for (int f = 0; f < 5; ++f) acc[r][f] = z4;
        bf16x8 fr[2][8];
        bf16x8 one = __builtin_bit_cast(bf16x8, u32x4{0x3f803f80u, 0x3f803f80u, 0x3f803f80u, 0x3f803f80u});
        auto half = [&](auto KK, unsigned so) {
          constexpr int kk = KK.v;
          const unsigned pa = (ra0 ^ ((kk ^ 1) * 64)) + so, pb = (rb0 ^ ((kk ^ 1) * 64)) + so;
          sfor<0, 16>([&](auto M) {
            constexpr int m = M.v;
            if constexpr (m < 10) mma(acc[m & 1][m >> 1], fr[kk][m & 1], fr[kk][2 + (m >> 1)]);
            else if constexpr (m == 10) mma(cs0, one, fr[kk][7]);
            else if constexpr (m == 11) mma(cs1, one, fr[kk][6]);
            else if constexpr (m < 14) mma(rs_[m - 12], fr[kk][m - 12], one);
            else mma(rq_[m - 14], fr[kk][m - 14], fr[kk][m - 14]);
            constexpr int n = m >> 1;
            if constexpr ((m & 1) == 0) fr[kk ^ 1][n] = n < 2 ? lds_rd(pa + n * 2048) : (n < 7 ? lds_rd(pb + (n - 2) * 2048) : lds_rd(pb + w * 2048));
            sb();
          });
        };
        asm volatile("s_nop 1" : "+v"(cs0), "+v"(cs1), "+v"(rs_[0]), "+v"(rs_[1]), "+v"(rq_[0]), "+v"(rq_[1]), "+v"(one));
        fence_all(acc);
        bar();
        ST(38);
        fr[0][0] = lds_rd(ra0); fr[0][1] = lds_rd(ra0 + 2048);
#pragma unroll
        for (int f = 0; f < 5; ++f) fr[0][2 + f] = lds_rd(rb0 + f * 2048);
        fr[0][7] = lds_rd(rb0 + w * 2048);
        sb();
        unsigned so = 0;
#pragma unroll 1
        for (int i = 0; i < 10; ++i) {
          half(IC<0>{}, so);
          wait_lgkm0();
          bar();
          so = so == 4 * K1_STAGE ? 0 : so + K1_STAGE;
          half(IC<1>{}, so);
        }
        fence_all(acc);
        asm volatile("" : "+v"(cs0), "+v"(cs1), "+v"(rs_[0]), "+v"(rs_[1]), "+v"(rq_[0]), "+v"(rq_[1]));
        const unsigned csb = lb + K1_CS;
        if (q == 0) {
          lds_wr1(csb + (w * 16 + i16) * 4, cs0[0]);
          if (w == 0) lds_wr1(csb + (64 + i16) * 4, cs1[0]);
        }
        wait_lgkm0();
        bar();
        ST(18);
#pragma unroll
        for (int f = 0; f < 5; ++f) csv[f] = lds_rd1(csb + (f * 16 + i16) * 4);
#pragma unroll
        for (int r = 0; r < 2; ++r)
#pragma unroll
          for (int j = 0; j < 4; ++j) {
            float m_, r_;
            ln_coef(rs_[r][j], __shfl(rq_[r][j], q * 20 + j), m_, r_);
            mu[r][j] = -m_; rs[r][j] = sign * r_;
          }
        if (w == 0) {
          const rsrc_t rc = make_rsrc(ws + WS_CSD, 256 * 320);
          st4(__float_as_uint(q == 0 ? csv[0] : (q == 1 ? csv[1] : (q == 2 ? csv[2] : csv[3]))), rc, lane * 4, bid * 320);
          st4(__float_as_uint(csv[4]), rc, q == 0 ? 256 + lane * 4 : OOB, bid * 320);
        }
      } else {
        {
          const rsrc_t rc = make_rsrc(ws + WS_CSD, 256 * 320);
#pragma unroll
          for (int f = 0; f < 5; ++f) csv[f] = __uint_as_float(__builtin_amdgcn_raw_buffer_load_b32(rc, (f * 16 + i16) * 4, bid * 320, 16));
        }
        poll(ws, FL_4 + P * 64 + w * 16 + i16, 16, epoch, lane);
        ST(17);
        const rsrc_t rx = make_rsrc(ws + WS_X2, SZ_X);
        const unsigned sox = (P * 8 + w * 2) * 20 * 1024;
        const unsigned rot = ROT5 ? 2 * ((idx & 7) + ((idx & 7) >> 2)) : 0;
        auto wdp = [&](int c) { const unsigned cc = c + (rot >> 1) >= 10 ? c + (rot >> 1) - 10 : c + (rot >> 1);
                                return (cc < 8 ? 10 * cc : 60 + 10 * cc) * 1024; };
        bf16x8 a[20][2];
        unsigned wsink = WARM_X ? WARM_STRIP(rx, sox) : 0u;
#pragma unroll
        for (int k = 0; k < 3; ++k) { a[k][0] = LDX(0, k); a[k][1] = LDX(1, k); }
        const rsrc_t rst = make_rsrc(ws + WS_ST2, SZ_ST);
        const unsigned sto = ((lane >> 5) * 4 * S_ + P * 128 + w * 32 + (lane & 31)) * 8;
        const u32x2 sp0 = ld8u(rst, sto, 0), sp1 = ld8u(rst, sto, S_ * 8), sp2 = ld8u(rst, sto, 2 * S_ * 8), sp3 = ld8u(rst, sto, 3 * S_ * 8);
        sb();
        bar();
        bf16x8 b[2][5];
        const unsigned rb = lb + i16 * 128 + ((q ^ (lane & 7)) << 4);
#pragma unroll
        for (int f = 0; f < 5; ++f) b[0][f] = lds_rd(rb + wdp(0) + f * 2048);
        sb();
        sfor<0, 20>([&](auto K) {
          constexpr int k = K.v;
          const unsigned pb = (rb ^ (((k + 1) & 1) * 64)) + wdp(((k + 1) >> 1) % 10);
          sfor<0, 10>([&](auto M) {
            constexpr int f = M.v >> 1, r = M.v & 1;
            if constexpr (k == 0) mma0(acc[r][f], a[0][r], b[0][f]); else mma(acc[r][f], a[k][r], b[k & 1][f]);
            if constexpr (k + 1 < 20 && r == 0) b[(k + 1) & 1][f] = lds_rd(pb + f * 2048);
            if constexpr (r == 1 && f < 4) {
              constexpr int kn = k < 7 ? 2 * k + 3 + (f >> 1) : (f < 2 ? k + 10 : 20);
              if constexpr (kn < 20) a[kn][f & 1] = LDX(f & 1, kn);
            }
            sb();
          });
          if constexpr (k == 0) ST(40);
          if constexpr (k == 4) ST(41);
          if constexpr (k == 9) ST(42);
        });
        asm volatile("" :: "v"(wsink));
        fence_all(acc);
        bar();
        ST(39);
        float s1 = (__uint_as_float(sp0[0]) + __uint_as_float(sp1[0])) + (__uint_as_float(sp2[0]) + __uint_as_float(sp3[0]));
        float s2 = (__uint_as_float(sp0[1]) + __uint_as_float(sp1[1])) + (__uint_as_float(sp2[1]) + __uint_as_float(sp3[1]));
        auto ta = __builtin_amdgcn_permlane32_swap(__float_as_uint(s1), __float_as_uint(s1), false, false);
        auto tb = __builtin_amdgcn_permlane32_swap(__float_as_uint(s2), __float_as_uint(s2), false, false);
        float m, rr;
        ln_coef(__uint_as_float(ta[0]) + __uint_as_float(ta[1]), __uint_as_float(tb[0]) + __uint_as_float(tb[1]), m, rr);
        m = -m; rr *= sign;
#pragma unroll
        for (int r = 0; r < 2; ++r)
#pragma unroll
          for (int e = 0; e < 4; ++e) { mu[r][e] = __shfl(m, r * 16 + q * 4 + e); rs[r][e] = __shfl(rr, r * 16 + q * 4 + e); }
      }
      g1_store(acc, mu, rs, csv, make_rsrc(ws + (L ? WS_AB2 : WS_AB1), SZ_AB), P, ct, w, i16, q);
      ST(L + 19);
      publish(ws, FL_5 + ((L * 4 + (ct >> 3) * 2 + (P >> 3)) * 4 + ((ct & 7) >> 1)) * 64 + ((P & 7) * 2 + (ct & 1)) * 4 + w, epoch, lane);
    }
    {
      PHASE_IDS
      IDS_G2
      const unsigned m = w >> 1, n = w & 1;
      const unsigned ra = lb + G2_A + (m * 64 + i16) * 128 + ((q ^ (lane & 7)) << 4);
      const unsigned rb = lb + n * 10240 + lane * 16;
      const unsigned row = P * 128 + m * 64 + kq * 16 + i16, ct = 2 * c4 + n;
      const TileOff ro = tile_offsets(L == 0, row, ct, q), oo = tile_offsets(false, row, ct, q);
      f32x4 acc[4][5];
#pragma unroll
      for (int i = 0; i < 4; ++i)
#pragma unroll
        for (int f = 0; f < 5; ++f) acc[i][f] = f32x4{0.f, 0.f, 0.f, 0.f};
      bf16x8 fa[2][4], fb[2][5];
      auto half = [&](auto KK, unsigned sl) {
        constexpr int kk = KK.v;
        const unsigned pa = (ra ^ ((kk ^ 1) * 64)) + sl * G2_AS, pb = rb + (kk ^ 1) * 5120 + sl * G2_BS;
        sfor<0, 20>([&](auto M) {
          constexpr int mm = M.v;
          mma(acc[mm / 5][mm % 5], fb[kk][mm % 5], fa[kk][mm / 5]);
          constexpr int rn = RDPAT == 0 ? ((mm & 1) == 0 && mm < 18 ? mm >> 1 : -1) : (RDPAT == 1 ? (mm < 9 ? mm : -1) : (mm % 3 != 2 && mm - mm / 3 < 9 ? mm - mm / 3 : -1));
          if constexpr (rn >= 0 && rn < 5) fb[kk ^ 1][rn] = lds_rd(pb + rn * 1024);
          if constexpr (rn >= 5) fa[kk ^ 1][rn - 5] = lds_rd(pa + (rn - 5) * 2048);
          sb();
        });
      };
      if (L) poll0(ws, FL_4 + P * 64 + (m * 4 + kq) * 8 + ct, 1, epoch, lane);
      ResTile rt = load_res(make_rsrc(L ? (const void*)(ws + WS_X2) : (const void*)opq_p(x0_), SZ_X), ro);
      fence_all(acc);
      bar();
      ST(sbase + 1);
#pragma unroll
      for (int f = 0; f < 5; ++f) fb[0][f] = lds_rd(rb + f * 1024);
#pragma unroll
      for (int i = 0; i < 4; ++i) fa[0][i] = lds_rd(ra + i * 2048);
      sb();
#pragma unroll 1
      for (int s = 0; s < 16; ++s) {
        half(IC<0>{}, s & 3);
        wait_lgkm0();
        bar();
        half(IC<1>{}, (s + 1) & 3);
      }
      fence_all(acc);
      ST(sbase + 2);
      const rsrc_t rxp = make_rsrc(ws + WS_XP + L * SZ_XP, SZ_XP);
      const unsigned xo = ((P * 4 + c4) * 48 + w) * XSLOT;
      f32x4 v[5];
      if (kq == 0) xp_send<0>(acc, v, rxp, xo, lane);
      else if (kq == 1) xp_send<1>(acc, v, rxp, xo, lane);
      else if (kq == 2) xp_send<2>(acc, v, rxp, xo, lane);
      else xp_send<3>(acc, v, rxp, xo, lane);
      const unsigned fx = FL_X + ((L * 64 + P * 4 + c4) * 4 + w) * 4;
      ST(sbase + 3);
      publish(ws, fx + kq, epoch, lane);
      ST(sbase + 4);
      poll(ws, fx + ((kq + 1 + lane) & 3), 3, epoch, lane);
      ST(sbase + 5);
      const unsigned xi = xo + kq * 12 * XSLOT;
      u32x4 g0[3], g1[3]; u32x2 g2[3];
#pragma unroll
      for (int s = 0; s < 3; ++s) {
        g0[s] = ld16u(rxp, lane * 16, xi + s * 4 * XSLOT);
        g1[s] = ld16u(rxp, lane * 16 + 1024, xi + s * 4 * XSLOT);
        g2[s] = ld8u(rxp, lane * 8 + 2048, xi + s * 4 * XSLOT);
      }
      sb();
#pragma unroll
      for (int s = 0; s < 3; ++s) {
        v[0] += f32x4{bflo(g0[s][0]), bfhi(g0[s][0]), bflo(g0[s][1]), bfhi(g0[s][1])};
        v[1] += f32x4{bflo(g0[s][2]), bfhi(g0[s][2]), bflo(g0[s][3]), bfhi(g0[s][3])};
        v[2] += f32x4{bflo(g1[s][0]), bfhi(g1[s][0]), bflo(g1[s][1]), bfhi(g1[s][1])};
        v[3] += f32x4{bflo(g1[s][2]), bfhi(g1[s][2]), bflo(g1[s][3]), bfhi(g1[s][3])};
        v[4] += f32x4{bflo(g2[s][0]), bfhi(g2[s][0]), bflo(g2[s][1]), bfhi(g2[s][1])};
      }
      finish_tile(v, rt, make_rsrc(ws + (L ? WS_X3 : WS_X1), SZ_X), oo, true, make_rsrc(ws + (L ? WS_ST3 : WS_ST1), SZ_ST),
                  q == 0 ? (ct * S_ + row) * 8 : OOB);
      ST(sbase + 6);
      publish(ws, FL_2 + (L * 16 + P) * 64 + (m * 4 + kq) * 8 + ct, epoch, lane);
      ST(sbase + 7);
    }
    u32x4 y2[2]; u32x2 y1[2];
    {
      PHASE_IDS
      IDS_G3
      typedef __attribute__((ext_vector_type(2))) short s16x2;
      poll(ws, FL_2 + (L * 16 + P) * 64 + w * 16 + i16, 16, epoch, lane);
      ST(sbase + 8);
      const rsrc_t rx = make_rsrc(ws + (L ? WS_X3 : WS_X1), SZ_X);
      const rsrc_t rst = make_rsrc(ws + (L ? WS_ST3 : WS_ST1), SZ_ST);
      const unsigned sox = (P * 8 + w * 2) * 20 * 1024;
      const unsigned rot = ROT3 ? (5 * (idx & 7)) >> 1 : 0;
      const unsigned par = ct & 1;
      bf16x8 a[20][2];
#pragma unroll
      for (int k = 0; k < 3; ++k) { a[k][0] = LDX(0, k); a[k][1] = LDX(1, k); }
      const unsigned sto = (2 * q * S_ + P * 128 + w * 32 + i16) * 8;
      u32x2 sa0, sa1, sb0, sb1;
      sb();
      bar();
      f32x4 acc[2][4];
      bf16x8 b[2][4];
      const unsigned rb = lb + lane * 16;
#pragma unroll
      for (int f = 0; f < 4; ++f) b[0][f] = lds_rd(rb + rot * 4096 + f * 1024);
      sb();
      sfor<0, 20>([&](auto K) {
        constexpr int k = K.v;
        const unsigned pb = k + 1 < 20 ? rb + KROT((k + 1) % 20) * 4096 : rb + 80 * 1024 + rot * 3072;
        if constexpr (k == 9) { sa0 = ld8u(rst, sto, 0); sa1 = ld8u(rst, sto, S_ * 8); sb0 = ld8u(rst, sto + 128, 0); sb1 = ld8u(rst, sto + 128, S_ * 8); }
        sfor<0, 8>([&](auto M) {
          constexpr int f = M.v >> 1, r = M.v & 1;
          if constexpr (k == 0) mma0(acc[r][f], b[0][f], a[0][r]); else mma(acc[r][f], b[k & 1][f], a[k][r]);
          if constexpr (r == 0 && (k + 1 < 20 || f < 3)) b[(k + 1) & 1][f] = lds_rd(pb + f * 1024);
          if constexpr (r == 1 && 2 * k + 3 + (f >> 1) < 20) a[2 * k + 3 + (f >> 1)][f & 1] = LDX(f & 1, 2 * k + 3 + (f >> 1));
          sb();
        });
        if constexpr (k == 0) ST(sbase + 9);
        if constexpr (k == 3) ST(51 + L * 5);
        if constexpr (k == 7) ST(52 + L * 5);
        if constexpr (k == 11) ST(53 + L * 5);
        if constexpr (k == 15) ST(54 + L * 5);
        if constexpr (k == 19) ST(55 + L * 5);
      });
      float mu[2], rs[2];
      ln_coef(xsum16_32(__uint_as_float(sa0[0]) + __uint_as_float(sa1[0])), xsum16_32(__uint_as_float(sa0[1]) + __uint_as_float(sa1[1])), mu[0], rs[0]);
      ln_coef(xsum16_32(__uint_as_float(sb0[0]) + __uint_as_float(sb1[0])), xsum16_32(__uint_as_float(sb0[1]) + __uint_as_float(sb1[1])), mu[1], rs[1]);
      auto act = [&](const f32x4& c, int r, const f32x4& cs, unsigned (&pk)[2]) {
        const f32x2 nm = {-mu[r], -mu[r]}, r2 = {rs[r], rs[r]};
#pragma unroll
        for (int e = 0; e < 2; ++e) {
          const f32x2 z = __builtin_elementwise_fma(nm, f32x2{cs[2 * e], cs[2 * e + 1]}, f32x2{c[2 * e], c[2 * e + 1]}) * r2;
          const s16x2 hb = __builtin_bit_cast(s16x2, __builtin_convertvector(z, bf16x2));
          pk[e] = __builtin_bit_cast(unsigned, __builtin_elementwise_max(hb, s16x2{0, 0}));
        }
      };
      auto pair = [&](const unsigned (&u)[2], const unsigned (&v)[2]) {
        auto t0 = __builtin_amdgcn_permlane16_swap(u[0], v[0], false, false);
        auto t1 = __builtin_amdgcn_permlane16_swap(u[1], v[1], false, false);
        return u32x4{t0[0], t1[0], t0[1], t1[1]};
      };
      const unsigned csb = lb + KM_CS1 + q * 16;
      asm volatile("s_nop 7\n\ts_nop 1" : "+v"(acc[0][0]), "+v"(acc[0][1]), "+v"(acc[0][2]), "+v"(acc[0][3]),
                                          "+v"(acc[1][0]), "+v"(acc[1][1]), "+v"(acc[1][2]), "+v"(acc[1][3]));
      wait_lgkm0();
      bar();
      ST(43 + L * 4);
      {
        const rsrc_t rh = make_rsrc(ws + (L ? WS_H2 : WS_H1), SZ_H);
        const unsigned vo = ((((q & 1) << 1) | (q >> 1)) * 16 + i16) * 16;
        const unsigned so = ((P * 8 + w * 2) * 54 + 7 * (ct >> 1) + 4 * par) * 1024;
        u32x4 y[2][2];
#pragma unroll
        for (int p = 0; p < 2; ++p) {
          const f32x4 c0 = lds_rdf(csb + (2 * p + par) * 64), c1 = lds_rdf(csb + (2 * p + 1 + par) * 64);
#pragma unroll
          for (int r = 0; r < 2; ++r) {
            unsigned u[2], v[2];
            act(acc[r][2 * p], r, c0, u); act(acc[r][2 * p + 1], r, c1, v);
            y[p][r] = pair(u, v);
            st16_raw(y[p][r], rh, p == 1 && ct == 15 ? OOB : vo, so + (r * 54 + p) * 1024);
          }
        }
        st_guard(y[0][0], y[0][1], y[1][0], y[1][1]);
      }
      ST(44 + L * 4);
      sb();
      sfor<0, 20>([&](auto K) {
        constexpr int k = K.v;
        const unsigned pb = rb + 80 * 1024 + KROT((k + 1) % 20) * 3072;
        sfor<0, 6>([&](auto M) {
          constexpr int f = M.v >> 1, r = M.v & 1;
          if constexpr (k == 0) mma0(acc[r][f], b[0][f], a[0][r]); else mma(acc[r][f], b[k & 1][f], a[k][r]);
          if constexpr (r == 0 && k + 1 < 20) b[(k + 1) & 1][f] = lds_rd(pb + f * 1024);
          sb();
        });
        if constexpr (k == PUB1) publish(ws, FL_3 + (L * 32 + P) * 64 + w * 16 + ct, epoch, lane);
      });
      const f32x4 c0 = lds_rdf(csb + (4 + par) * 64), c1 = lds_rdf(csb + (5 + par) * 64), c2 = lds_rdf(csb + (par ? 0 : 6) * 64);
      asm volatile("s_nop 7\n\ts_nop 1" : "+v"(acc[0][0]), "+v"(acc[0][1]), "+v"(acc[0][2]), "+v"(acc[1][0]), "+v"(acc[1][1]), "+v"(acc[1][2]));
      ST(sbase + 10);
#pragma unroll
      for (int r = 0; r < 2; ++r) {
        unsigned u[2], v[2], s[2];
        act(acc[r][0], r, c0, u); act(acc[r][1], r, c1, v); act(acc[r][2], r, c2, s);
        y2[r] = pair(u, v);
        y1[r] = u32x2{s[0], s[1]};
      }
      asm volatile("" : "+v"(y2[0]), "+v"(y2[1]), "+v"(y1[0]), "+v"(y1[1]));
      ST(sbase + 11);
    }
    {
      PHASE_IDS
      IDS_G4
      const unsigned row = rt64 * 64 + w * 16 + i16;
      const rsrc_t rh = make_rsrc(ws + (L ? WS_H2 : WS_H1), SZ_H);
      const unsigned soh = (rt64 * 4 * 54 + w * 14) * 1024;
      const unsigned vl = lane * 16, vz = lane * 16 + (w < 3 ? 0u : OOB);
      bf16x8 a[14][4], b[2][5];
#define LDH(t, i) a[t][i] = ld16(rh, (ORD[t] < 12 ? vl : vz) + (ORD[t] & 3) * 1024, soh + (ORD[t] >> 2) * 4096 + (i) * 54 * 1024)
      const TileOff ro = tile_offsets(false, row, c8, q), oo = tile_offsets(L != 0, row, c8, q);
      const unsigned f3 = FL_3 + (L * 32 + (rt64 >> 1)) * 64 + ((rt64 & 1) * 2 + (lane & 1)) * 16 + 4 * w + ((lane >> 1) & 3);
      if (P3DELAY) __builtin_amdgcn_s_sleep(P3DELAY);
      poll0(ws, f3, 8, epoch, lane);
      ST(sbase + 13);
      sfor<0, 12>([&](auto N) { LDH(N.v / 4, N.v % 4); });
      sb();
      {
        const unsigned P = (xcd >> 1) * 4 + (idx >> 3), ct = (xcd & 1) * 8 + (idx & 7), par = ct & 1;
        const unsigned vo = ((((q & 1) << 1) | (q >> 1)) * 16 + i16) * 16;
        const unsigned v1 = ((2 * par + (q >> 1)) * 16 + i16) * 16 + (q & 1) * 8;
        const unsigned so = ((P * 8 + w * 2) * 54 + 7 * (ct >> 1)) * 1024;
#pragma unroll
        for (int r = 0; r < 2; ++r) {
          st16_raw(y2[r], rh, ct == 15 ? OOB : vo, so + (r * 54 + 2 + 4 * par) * 1024);
          st8(y1[r], rh, v1, so + (r * 54 + 3) * 1024);
        }
        st_guard(y2[0], y2[1]);
      }
      ResTile rt;
      u64 fl2;
      const rsrc_t rr = make_rsrc(ws + (L ? WS_X3 : WS_X1), SZ_X);
      sb();
      bar();
      f32x4 acc[4][5];
      const unsigned rbe = lb + w * 20480 + lane * 16, rbl = lb + G4_LATE + w * 15360 + lane * 16;
#pragma unroll
      for (int f = 0; f < 5; ++f) b[0][f] = lds_rd(rbe + f * 1024);
      sb();
      sfor<0, 14>([&](auto T) {
        constexpr int t = T.v;
        constexpr int ta = t < 5 ? t + 3 : (t == 5 ? -1 : (t == 6 ? 8 : (t + 3 < 14 ? t + 3 : -1)));
        if constexpr (t == 3 || t == 6 || t == 10) { wait_lgkm0(); bar(); }
        if constexpr (t == 6) { if (!__all(fl2 == epoch)) poll0(ws, f3 + 16 * 64, 8, epoch, lane); }
        sfor<0, 20>([&](auto M) {
          constexpr int m = M.v, i = m / 5, f = m % 5;
          if constexpr (t == 0) mma0(acc[i][f], b[0][f], a[0][i]); else mma(acc[i][f], b[t & 1][f], a[t][i]);
          if constexpr (t + 1 < 14 && (m & 1) == 1 && m < 10)
            b[(t + 1) & 1][m >> 1] = lds_rd((g4_late(t + 1) ? rbl : rbe) + g4_slot(t + 1) + (m >> 1) * 1024);
          if constexpr (ta >= 0 && (m & 1) == 0 && m < 8) LDH(ta, m >> 1);
          if constexpr (t == 6 && (m & 1) == 0 && m >= 8 && m < 16) LDH(9, (m - 8) >> 1);
          if constexpr (t == 2 && m == 10) rt = load_res(rr, ro);
          if constexpr (t == 4 && m == 18) fl2 = __hip_atomic_load(flag_ptr(ws, f3 + 16 * 64), RLX_AGENT);
          sb();
        });
        if constexpr (t == 2) {
          wait_vm<15>();
          raise(ws, FL_3 + (L * 32 + 16 + (xcd >> 1) * 4 + (idx >> 3)) * 64 + w * 16 + (xcd & 1) * 8 + (idx & 7), epoch, lane);
          ST(sbase + 12);
        }
        if constexpr (t == 0) ST(sbase + 14);
        if constexpr (t == 3) ST(45 + L * 4);
        if constexpr (t == 6) ST(46 + L * 4);
      });
      fence_all(acc);
      ST(sbase + 15);
      f32x4 v[5];
      if (w == 0) sm_scatter<0>(rbl, acc, v);
      else if (w == 1) sm_scatter<1>(rbl, acc, v);
      else if (w == 2) sm_scatter<2>(rbl, acc, v);
      else sm_scatter<3>(rbl, acc, v);
      wait_lgkm0();
      bar();
#pragma unroll
      for (int s = 1; s < 4; ++s) {
        const unsigned src = lb + G4_LATE + ((w + s) & 3) * 15360 + (3 - s) * 5 * 1024 + lane * 16;
#pragma unroll
        for (int f = 0; f < 5; ++f) v[f] += lds_rdf(src + f * 1024);
      }
      finish_tile(v, rt, L ? make_rsrc(opq_p(out_), SZ_X) : make_rsrc(ws + WS_X2, SZ_X), oo, L == 0, make_rsrc(ws + WS_ST2, SZ_ST),
                  q == 0 ? (c8 * S_ + row) * 8 : OOB);
      ST(sbase + 16);
      if (L == 0) publish(ws, FL_4 + (rt64 >> 1) * 64 + ((rt64 & 1) * 4 + w) * 8 + c8, epoch, lane);
    }
  }
  if (blockIdx.x == 0 && w0 == 0) raise(ws_, FL_E, epoch, tid0);
  ST(37);
}

extern "C" void kernel_launch(void* const* d_in, const int* in_sizes, int n_in, void* d_out, int out_size, void* d_ws,
                              size_t ws_size, hipStream_t stream) {
  char* ws = (char*)d_ws;
  (void)hipFuncSetAttribute((const void*)k_all, hipFuncAttributeMaxDynamicSharedMemorySize, KM_LDS);
  k_all<<<256, 512, KM_LDS, stream>>>((const u16*)d_in[0], (const u16*)d_in[1], (const u16*)d_in[2], (const u16*)d_in[3],
                                     (const u16*)d_in[4], (const u16*)d_in[6], (const u16*)d_in[7], ws, (u16*)d_out STAMP_PASS(0));
}
```

```cpp
#include <hip/hip_runtime.h>
#include <hip/hip_bf16.h>
#include <stdint.h>

#define DI __device__ __forceinline__
typedef unsigned short u16;
typedef __attribute__((ext_vector_type(8))) __bf16 bf16x8;
typedef __attribute__((ext_vector_type(2))) __bf16 bf16x2;
typedef __attribute__((ext_vector_type(4))) float f32x4;
typedef __attribute__((ext_vector_type(2))) float f32x2;
typedef __attribute__((ext_vector_type(4))) unsigned u32x4;
typedef __attribute__((ext_vector_type(2))) unsigned u32x2;
typedef __attribute__((ext_vector_type(4))) short s16x4;
#define LDS_AS __attribute__((address_space(3)))
#define GLB_AS __attribute__((address_space(1)))
typedef LDS_AS char* ldsp;
typedef GLB_AS unsigned gu32;
typedef unsigned long long u64;
typedef GLB_AS u64 gu64;
typedef __amdgpu_buffer_rsrc_t rsrc_t;
#define RLX_AGENT __ATOMIC_RELAXED, __HIP_MEMORY_SCOPE_AGENT

constexpr int S_ = 2048, D_ = 640, F_ = 1728;
constexpr float EPS = 1e-5f;
constexpr unsigned OOB = 0x7f000000u;

constexpr unsigned SZ_AB = 8 * 128 * 5 * 1024;
constexpr unsigned SZ_X = 128 * 20 * 1024;
constexpr unsigned SZ_H = 128 * 54 * 1024;
constexpr unsigned SZ_W1 = 16 * 140 * 1024;
constexpr unsigned SZ_W2 = 8 * 54 * 5 * 1024;
constexpr unsigned SZ_ST = 2048 * 64;
constexpr unsigned WS_AB1 = 0;
constexpr unsigned WS_AB2 = WS_AB1 + SZ_AB;
constexpr unsigned WS_X1 = WS_AB2 + SZ_AB;
constexpr unsigned WS_X2 = WS_X1 + SZ_X;
constexpr unsigned WS_X3 = WS_X2 + SZ_X;
constexpr unsigned WS_H1 = WS_X3 + SZ_X;
constexpr unsigned WS_H2 = WS_H1 + SZ_H;
constexpr unsigned WS_W1 = WS_H2 + SZ_H;
constexpr unsigned WS_W2 = WS_W1 + SZ_W1;
constexpr unsigned WS_ST1 = WS_W2 + SZ_W2;
constexpr unsigned WS_ST2 = WS_ST1 + SZ_ST;
constexpr unsigned WS_ST3 = WS_ST2 + SZ_ST;
constexpr unsigned WS_CS1 = WS_ST3 + SZ_ST;
constexpr unsigned WS_CSD = WS_CS1 + 20 * 1728 * 4;
constexpr unsigned XSLOT = 2560;
constexpr unsigned SZ_XP = 256 * 12 * XSLOT;
constexpr unsigned WS_XP = (WS_CSD + 256 * 320 + 4095) & ~4095u;
constexpr unsigned WS_FLG = WS_XP + 2 * SZ_XP;
constexpr unsigned FL_X = 0;
constexpr unsigned FL_2 = FL_X + 2048;
constexpr unsigned FL_3 = FL_2 + 2048;
constexpr unsigned FL_4 = FL_3 + 4096;
constexpr unsigned FL_5 = FL_4 + 1024;
constexpr unsigned FL_W = FL_5 + 2048;
constexpr unsigned FL_E = FL_W + 1088;
constexpr unsigned WS_END = WS_FLG + (FL_E + 1) * 128;

DI u32x4 make_srd(const void* p, unsigned bytes) {
  uint64_t a = (uint64_t)p;
  u32x4 r;
  r[0] = (unsigned)a; r[1] = (unsigned)(a >> 32) & 0xffffu; r[2] = bytes; r[3] = 0x00020000u;
  return r;
}
DI rsrc_t make_rsrc(const void* p, unsigned bytes) {
  return __builtin_amdgcn_make_buffer_rsrc((void*)p, (short)0, (int)bytes, 0x00020000);
}
template <int LOFF, int GOFF>
DI void glds(u32x4 srd, unsigned voff, unsigned ldsb, unsigned soff) {
  unsigned tmp;
  asm volatile("s_add_u32 m0, %1, %2\n\ts_add_u32 %0, %3, %4\n\tbuffer_load_dwordx4 %5, %6, %0 offen lds"
               : "=&s"(tmp)
               : "s"(ldsb), "i"(LOFF), "s"(soff), "i"(GOFF), "v"(voff), "s"(srd)
               : "memory", "scc");
}
template <int N> DI void glds_lin(u32x4 srd, unsigned voff, unsigned ldsb, unsigned soff) {
  if constexpr (N == 4)
    asm volatile("s_mov_b32 m0, %0\n\ts_nop 0\n\tbuffer_load_dwordx4 %1, %2, %3 offen lds\n\tbuffer_load_dwordx4 %1, %2, %3 offen offset:1024 lds\n\t"
                 "buffer_load_dwordx4 %1, %2, %3 offen offset:2048 lds\n\tbuffer_load_dwordx4 %1, %2, %3 offen offset:3072 lds"
                 :: "s"(ldsb), "v"(voff), "s"(srd), "s"(soff) : "memory");
  else if constexpr (N == 3)
    asm volatile("s_mov_b32 m0, %0\n\ts_nop 0\n\tbuffer_load_dwordx4 %1, %2, %3 offen lds\n\tbuffer_load_dwordx4 %1, %2, %3 offen offset:1024 lds\n\t"
                 "buffer_load_dwordx4 %1, %2, %3 offen offset:2048 lds"
                 :: "s"(ldsb), "v"(voff), "s"(srd), "s"(soff) : "memory");
  else
    asm volatile("s_mov_b32 m0, %0\n\ts_nop 0\n\tbuffer_load_dwordx4 %1, %2, %3 offen lds"
                 :: "s"(ldsb), "v"(voff), "s"(srd), "s"(soff) : "memory");
}
template <int IMM> DI void ldv(bf16x8& d, u32x4 srd, unsigned voff, unsigned soff) {
  asm volatile("buffer_load_dwordx4 %0, %1, %2, %3 offen offset:%4 sc1" : "=v"(d) : "v"(voff), "s"(srd), "s"(soff), "i"(IMM) : "memory");
}
template <int N> DI void wait_vm() { asm volatile("s_waitcnt vmcnt(%0)" ::"i"(N) : "memory"); }
template <int N> DI void wait_vm5(bf16x8 (&b)[5]) {
  asm volatile("s_waitcnt vmcnt(%5)" : "+v"(b[0]), "+v"(b[1]), "+v"(b[2]), "+v"(b[3]), "+v"(b[4]) : "i"(N) : "memory");
}
DI void wait_lgkm0() { asm volatile("s_waitcnt lgkmcnt(0)" ::: "memory"); }
DI void sb() { __builtin_amdgcn_sched_barrier(0); }
DI void bar() { asm volatile("s_barrier" ::: "memory"); }

DI bf16x8 lds_rd(unsigned addr) { return *(const LDS_AS bf16x8*)(ldsp)(uintptr_t)addr; }
DI f32x4 lds_rdf(unsigned addr) { return *(const LDS_AS f32x4*)(ldsp)(uintptr_t)addr; }
DI void lds_wrf(unsigned addr, f32x4 v) { *(LDS_AS f32x4*)(ldsp)(uintptr_t)addr = v; }
DI float lds_rd1(unsigned addr) { return *(const LDS_AS float*)(ldsp)(uintptr_t)addr; }
DI void lds_wr1(unsigned addr, float v) { *(LDS_AS float*)(ldsp)(uintptr_t)addr = v; }
DI f32x4 mfma(bf16x8 a, bf16x8 b, f32x4 c) { return __builtin_amdgcn_mfma_f32_16x16x32_bf16(a, b, c, 0, 0, 0); }
DI void mma(f32x4& c, const bf16x8& a, const bf16x8& b) {
  asm volatile("v_mfma_f32_16x16x32_bf16 %0, %1, %2, %0" : "+v"(c) : "v"(a), "v"(b));
}
DI void mma0(f32x4& c, const bf16x8& a, const bf16x8& b) {
  asm volatile("v_mfma_f32_16x16x32_bf16 %0, %1, %2, 0" : "=&v"(c) : "v"(a), "v"(b));
}
template <int N> DI void fence_acc(f32x4 (&a)[N]) {
  if constexpr (N == 5) asm volatile("s_nop 7\n\ts_nop 1" : "+v"(a[0]), "+v"(a[1]), "+v"(a[2]), "+v"(a[3]), "+v"(a[4]));
  else asm volatile("s_nop 7\n\ts_nop 1" : "+v"(a[0]), "+v"(a[1]), "+v"(a[2]), "+v"(a[3]), "+v"(a[4]), "+v"(a[5]), "+v"(a[6]));
}
template <int N> DI void pin_acc(f32x4 (&a)[N]) {
  if constexpr (N == 5) asm volatile("" : "+v"(a[0]), "+v"(a[1]), "+v"(a[2]), "+v"(a[3]), "+v"(a[4]));
  else asm volatile("" : "+v"(a[0]), "+v"(a[1]), "+v"(a[2]), "+v"(a[3]), "+v"(a[4]), "+v"(a[5]), "+v"(a[6]));
}
template <int M, int N> DI void fence_all(f32x4 (&a)[M][N]) {
#pragma unroll
  for (int i = 1; i < M; ++i) pin_acc(a[i]);
  fence_acc(a[0]);
#pragma unroll
  for (int i = 1; i < M; ++i) pin_acc(a[i]);
}
DI unsigned pk2(float a, float b) {
  f32x2 v = {a, b};
  return __builtin_bit_cast(unsigned, __builtin_convertvector(v, bf16x2));
}
DI float bflo(unsigned u) { return __uint_as_float(u << 16); }
DI float bfhi(unsigned u) { return __uint_as_float(u & 0xffff0000u); }
DI float dot2(unsigned a, unsigned b, float c) {
  return __builtin_amdgcn_fdot2_f32_bf16(__builtin_bit_cast(bf16x2, a), __builtin_bit_cast(bf16x2, b), c, false);
}
DI float xsum16_32(float x) {
  auto s = __builtin_amdgcn_permlane16_swap(__float_as_uint(x), __float_as_uint(x), false, false);
  x = __uint_as_float(s[0]) + __uint_as_float(s[1]);
  auto t = __builtin_amdgcn_permlane32_swap(__float_as_uint(x), __float_as_uint(x), false, false);
  return __uint_as_float(t[0]) + __uint_as_float(t[1]);
}
template <int V> struct IC { static constexpr int v = V; };
template <int I, int N, class Fn> DI void sfor(Fn&& f) {
  if constexpr (I < N) { f(IC<I>{}); sfor<I + 1, N>(f); }
}
DI unsigned rfl(unsigned v) { return __builtin_amdgcn_readfirstlane(v); }
DI unsigned opq_v(unsigned x) { asm volatile("" : "+v"(x)); return x; }
DI unsigned opq_s(unsigned x) { asm volatile("" : "+s"(x)); return x; }
template <class T> DI T* opq_p(T* x) { asm volatile("" : "+s"(x)); return x; }
DI bf16x8 ld16(rsrc_t r, unsigned voff, unsigned soff) { return __builtin_bit_cast(bf16x8, __builtin_amdgcn_raw_buffer_load_b128(r, voff, soff, 16)); }
DI u32x4 ld16u(rsrc_t r, unsigned voff, unsigned soff) { return __builtin_amdgcn_raw_buffer_load_b128(r, voff, soff, 16); }
DI u32x2 ld8u(rsrc_t r, unsigned voff, unsigned soff) { return __builtin_amdgcn_raw_buffer_load_b64(r, voff, soff, 16); }
DI void st16(u32x4 v, rsrc_t r, unsigned voff, unsigned soff) {
  __builtin_amdgcn_raw_buffer_store_b128(v, r, voff, soff, 16);
  asm volatile("s_nop 1" :: "v"(v) : "memory");
}
DI void st16_raw(u32x4 v, rsrc_t r, unsigned voff, unsigned soff) { __builtin_amdgcn_raw_buffer_store_b128(v, r, voff, soff, 16); }
template <class... T> DI void st_guard(const T&... v) {
  (..., [](const auto& x) { asm volatile("" :: "v"(x) : "memory"); }(v));
  asm volatile("s_nop 1" ::: "memory");
  (..., [](const auto& x) { asm volatile("" :: "v"(x)); }(v));
}
DI void st8(u32x2 v, rsrc_t r, unsigned voff, unsigned soff) { __builtin_amdgcn_raw_buffer_store_b64(v, r, voff, soff, 16); }
DI void st4(unsigned v, rsrc_t r, unsigned voff, unsigned soff) { __builtin_amdgcn_raw_buffer_store_b32(v, r, voff, soff, 16); }

#define ST(i)
#define STAMP_ARGS
#define STAMP_FWD
#define STAMP_PASS(k)

template <int KS> DI unsigned foff(unsigned row, unsigned col) {
  return ((row >> 4) * KS + (col >> 5)) * 1024u + (((col >> 3) & 3) * 16u + (row & 15)) * 16u + (col & 7) * 2u;
}

extern __shared__ __attribute__((aligned(1024))) char smem[];
DI unsigned lds_base() { return (unsigned)(uintptr_t)(ldsp)smem; }

DI void ln_coef(float s1, float s2, float& mu, float& rs) {
  mu = s1 * (1.f / D_);
  rs = __builtin_amdgcn_rsqf(fmaxf(s2 * (1.f / D_) - mu * mu, 0.f) + EPS);
}

DI void g1_store(const f32x4 (&acc)[2][5], const float (&mu)[2][4], const float (&rs)[2][4], const float (&csv)[5], rsrc_t rab,
                 unsigned P, unsigned ct, unsigned w, unsigned i16, unsigned q) {
  const unsigned kstep = (ct >> 3) * 64 + P * 4 + w;
  const unsigned cq = ((q & 1) << 1) | (q >> 1);
  const unsigned so = ((ct & 7) * 128 + kstep) * 5 * 1024;
  const unsigned vo = (cq * 16 + i16) * 16;
  u32x4 y[5];
#pragma unroll
  for (int f = 0; f < 5; ++f) {
    const f32x2 cs = {csv[f], csv[f]};
    unsigned pk[2][2];
#pragma unroll
    for (int r = 0; r < 2; ++r)
#pragma unroll
      for (int e = 0; e < 2; ++e) {
        const f32x2 t = __builtin_elementwise_fma(f32x2{mu[r][2 * e], mu[r][2 * e + 1]}, cs, f32x2{acc[r][f][2 * e], acc[r][f][2 * e + 1]}) *
                        f32x2{rs[r][2 * e], rs[r][2 * e + 1]};
        pk[r][e] = __builtin_bit_cast(unsigned, __builtin_convertvector(t, bf16x2));
      }
    auto t0 = __builtin_amdgcn_permlane16_swap(pk[0][0], pk[1][0], false, false);
    auto t1 = __builtin_amdgcn_permlane16_swap(pk[0][1], pk[1][1], false, false);
    y[f] = u32x4{t0[0], t1[0], t0[1], t1[1]};
    st16_raw(y[f], rab, vo + (f & 3) * 1024, so + (f >> 2) * 4096);
  }
  st_guard(y[0], y[1], y[2], y[3], y[4]);
}

constexpr int K1_STAGE = 26 * 1024;
constexpr int K1_TR = 5 * K1_STAGE;
constexpr int K1_CS = K1_TR + 5 * 4096;

DI void k1_issue(u32x4 sa, u32x4 sbd, unsigned vo, unsigned la, unsigned lbb, unsigned soa, unsigned sob, unsigned w) {
  glds<0, 0>(sa, vo, la, soa);
  glds<1024, 8 * 1280>(sa, vo, la, soa);
  glds<2048, 16 * 1280>(sa, vo, la, soa);
  glds<3072, 24 * 1280>(sa, vo, la, soa);
  glds<0, 0>(sbd, vo, lbb, sob);
  glds<4096, 32 * 1280>(sbd, vo, lbb, sob);
  if (w < 2) glds<8192, 64 * 1280>(sbd, vo, lbb, sob);
}
struct RtUnit { bool first; unsigned ks, g; };
DI RtUnit rt_unit(unsigned u) {
  RtUnit r;
  r.first = u < 540;
  if (r.first) { r.ks = u / 27u; r.g = u - r.ks * 27u; }
  else { const unsigned v = u - 540; r.ks = v / 10u; r.g = v - r.ks * 10u; }
  return r;
}
DI void retile_issue(unsigned u, const u16* w1, const u16* w2, unsigned ldst, unsigned lane) {
  const RtUnit t = rt_unit(u);
  const unsigned ld = t.first ? F_ * 2 : D_ * 2;
  u32x4 srd = make_srd(t.first ? w1 : w2, D_ * F_ * 2);
  const unsigned vo = (lane >> 3) * ld + (lane & 7) * 16;
  const unsigned so = t.ks * 32 * ld + t.g * 128;
  glds<0, 0>(srd, vo, ldst, so);
  glds<1024, 0>(srd, vo, ldst, so + 8 * ld);
  glds<2048, 0>(srd, vo, ldst, so + 16 * ld);
  glds<3072, 0>(srd, vo, ldst, so + 24 * ld);
}
DI unsigned w1_off(unsigned rt, unsigned ks) {
  const unsigned c = rt / 7u, f = rt - c * 7, g = (c & 1) ? (f == 0 ? 6 : f - 1) : f;
  return WS_W1 + (c * 140 + (g < 4 ? ks * 4 + g : 80 + ks * 3 + g - 4)) * 1024;
}
DI void retile_finish(unsigned u, unsigned f0, rsrc_t rws, unsigned ldst, unsigned lane) {
  const RtUnit t = rt_unit(u);
  const unsigned i = lane & 15, q = lane >> 4;
  const unsigned base = ldst + (8 * q + (i >> 2)) * 128 + (i & 3) * 8 + f0 * 32;
  u32x4 v[2];
#pragma unroll
  for (int f = 0; f < 2; ++f) {
    s16x4 lo = __builtin_amdgcn_ds_read_tr16_b64_v4i16((LDS_AS s16x4*)(uintptr_t)(base + f * 32));
    s16x4 hi = __builtin_amdgcn_ds_read_tr16_b64_v4i16((LDS_AS s16x4*)(uintptr_t)(base + f * 32 + 512));
    u32x2 l2 = __builtin_bit_cast(u32x2, lo), h2 = __builtin_bit_cast(u32x2, hi);
    v[f] = u32x4{l2[0], l2[1], h2[0], h2[1]};
  }
  const unsigned rt = t.g * 4 + f0;
  if (t.first) {
    st16(v[0], rws, lane * 16, w1_off(rt, t.ks));
    st16(v[1], rws, lane * 16, w1_off(rt + 1, t.ks));
    float s[2];
#pragma unroll
    for (int f = 0; f < 2; ++f)
      s[f] = xsum16_32(((bflo(v[f][0]) + bfhi(v[f][0])) + (bflo(v[f][1]) + bfhi(v[f][1]))) + ((bflo(v[f][2]) + bfhi(v[f][2])) + (bflo(v[f][3]) + bfhi(v[f][3]))));
    st4(__float_as_uint(q == 0 ? s[0] : s[1]), rws, lane < 32 ? lane * 4 : OOB, WS_CS1 + (t.ks * F_ + rt * 16) * 4);
  } else {
    const unsigned c0 = rt / 5u, c1 = (rt + 1) / 5u;
    st16(v[0], rws, lane * 16, WS_W2 + ((c0 * 54 + t.ks) * 5 + (rt - c0 * 5)) * 1024);
    st16(v[1], rws, lane * 16, WS_W2 + ((c1 * 54 + t.ks) * 5 + (rt + 1 - c1 * 5)) * 1024);
  }
}

#define ASLEEP 0
#define W2SLEEP 0
#define WDSLEEP 0
#define WARM_X 0
#define WARM_W 0
#define WARM_H 0
#define WARM_S 0
#define WARM_0 0
#define WS1 3
#define WS2 6
#define ROT1 1
#define ROT2 0
#define ROT3 0
#define ROT4 1
#define ROT5 0
#define PUB1 12
#define LSLEEP 20
#define RGAP 10
#define RSLEEP 0
#define ASLEEP2 40
#define RDPAT 1
#define LAZY5 0
#define LAZY2 0
#define WDPRE 6
#define CODEWARM 1
#define OUTPLAIN 1
#define VMPEEK 0
#define PDELAY 18
#define WSLEEP 52
#define SSLEEP 46
constexpr int KM_LDS = 160 * 1024;
constexpr int KM_CS1 = KM_LDS - 1024;
constexpr int G2_A = 80 * 1024, G2_AS = 16 * 1024, G2_BS = 20 * 1024;
constexpr int G4_LATE = 80 * 1024;

DI gu64* flag_ptr(char* ws, unsigned f) { return (gu64*)(ws + WS_FLG + f * 128); }
DI void raise(char* ws, unsigned f, u64 epoch, unsigned lane) {
  if (lane < 16) __hip_atomic_store(flag_ptr(ws, f) + lane, epoch, RLX_AGENT);
}
DI void publish(char* ws, unsigned f, u64 epoch, unsigned lane) {
  wait_vm<0>();
  raise(ws, f, epoch, lane);
}
#define P3DELAY 0
#define P5DELAY0 4
#define P5DELAY 50
#define POLLATOM 0
#define PSL 1
DI void poll0(char* ws, unsigned f, unsigned n, u64 epoch, unsigned lane) {
  gu64* p = flag_ptr(ws, f);
  bool ok = lane >= n;
#pragma unroll 1
  for (unsigned s = 0; s < (1u << 16); ++s) {
    if (!ok) ok = (POLLATOM ? __hip_atomic_fetch_or(p, 0ull, RLX_AGENT) : __hip_atomic_load(p, RLX_AGENT)) == epoch;
    if (__all(ok)) break;
    __builtin_amdgcn_s_sleep(PSL);
  }
  asm volatile("" ::: "memory");
}
DI void poll(char* ws, unsigned f, unsigned n, u64 epoch, unsigned lane) {
  __builtin_amdgcn_s_sleep(PDELAY);
  poll0(ws, f, n, epoch, lane);
}
DI void warm(unsigned ldump, u32x4 srd, unsigned voff, unsigned soff) {
  asm volatile("s_mov_b32 m0, %0\n\ts_nop 0\n\tbuffer_load_dword %1, %2, %3 offen lds" :: "s"(ldump), "v"(voff), "s"(srd), "s"(soff) : "memory");
}
template <int N> DI void glds_run(u32x4 srd, unsigned voff, unsigned l, unsigned so) {
#pragma unroll
  for (int g = 0; g < N / 4; ++g) glds_lin<4>(srd, voff, l + g * 4096, so + g * 4096);
  if constexpr (N % 4 == 3) glds_lin<3>(srd, voff, l + (N / 4) * 4096, so + (N / 4) * 4096);
  else {
#pragma unroll
    for (int g = N / 4 * 4; g < N; ++g) glds_lin<1>(srd, voff, l + g * 1024, so + g * 1024);
  }
}
DI void glds4_sc1(u32x4 srd, unsigned voff, unsigned l, unsigned so) {
  asm volatile("s_mov_b32 m0, %0\n\ts_nop 0\n\tbuffer_load_dwordx4 %1, %2, %3 offen sc1 lds\n\tbuffer_load_dwordx4 %1, %2, %3 offen offset:1024 sc1 lds\n\t"
               "buffer_load_dwordx4 %1, %2, %3 offen offset:2048 sc1 lds\n\tbuffer_load_dwordx4 %1, %2, %3 offen offset:3072 sc1 lds"
               :: "s"(l), "v"(voff), "s"(srd), "s"(so) : "memory");
}
DI void glds1_sc1(u32x4 srd, unsigned voff, unsigned l, unsigned so) {
  asm volatile("s_mov_b32 m0, %0\n\ts_nop 0\n\tbuffer_load_dwordx4 %1, %2, %3 offen sc1 lds" :: "s"(l), "v"(voff), "s"(srd), "s"(so) : "memory");
}
template <int N> DI void glds_run_sc1(u32x4 srd, unsigned voff, unsigned l, unsigned so) {
#pragma unroll
  for (int g = 0; g < N / 4; ++g) glds4_sc1(srd, voff, l + g * 4096, so + g * 4096);
#pragma unroll
  for (int g = N / 4 * 4; g < N; ++g) glds1_sc1(srd, voff, l + g * 1024, so + g * 1024);
}

struct ResTile { u32x4 r0, r1; u32x2 r2; };
struct TileOff { unsigned a0, a1, a2; };
DI TileOff tile_offsets(bool rm, unsigned row, unsigned ct, unsigned q) {
  const unsigned cA = ct * 80 + 16 * (q & 1) + 8 * (q >> 1), cS = ct * 80 + 64 + 4 * q;
  TileOff t;
  if (rm) { t.a0 = (row * D_ + cA) * 2; t.a1 = t.a0 + 64; t.a2 = (row * D_ + cS) * 2; }
  else { t.a0 = foff<20>(row, cA); t.a1 = foff<20>(row, cA + 32); t.a2 = foff<20>(row, cS); }
  asm volatile("" : "+v"(t.a0), "+v"(t.a1), "+v"(t.a2));
  return t;
}
DI ResTile load_res(rsrc_t rr, const TileOff& t) {
  ResTile rt;
  rt.r0 = ld16u(rr, t.a0, 0); rt.r1 = ld16u(rr, t.a1, 0); rt.r2 = ld8u(rr, t.a2, 0);
  return rt;
}
DI void finish_tile(f32x4 (&v)[5], const ResTile& rt, rsrc_t rout, const TileOff& o, bool stats, rsrc_t rst, unsigned sto) {
#pragma unroll
  for (int p = 0; p < 2; ++p)
#pragma unroll
    for (int r = 0; r < 4; ++r) {
      auto t = __builtin_amdgcn_permlane16_swap(__float_as_uint(v[2 * p][r]), __float_as_uint(v[2 * p + 1][r]), false, false);
      v[2 * p][r] = __uint_as_float(t[0]); v[2 * p + 1][r] = __uint_as_float(t[1]);
    }
  u32x4 y0, y1; u32x2 y2;
#pragma unroll
  for (int j = 0; j < 2; ++j) {
    y0[j] = pk2(v[0][2 * j] + bflo(rt.r0[j]), v[0][2 * j + 1] + bfhi(rt.r0[j]));
    y0[2 + j] = pk2(v[1][2 * j] + bflo(rt.r0[2 + j]), v[1][2 * j + 1] + bfhi(rt.r0[2 + j]));
    y1[j] = pk2(v[2][2 * j] + bflo(rt.r1[j]), v[2][2 * j + 1] + bfhi(rt.r1[j]));
    y1[2 + j] = pk2(v[3][2 * j] + bflo(rt.r1[2 + j]), v[3][2 * j + 1] + bfhi(rt.r1[2 + j]));
    y2[j] = pk2(v[4][2 * j] + bflo(rt.r2[j]), v[4][2 * j + 1] + bfhi(rt.r2[j]));
  }
  if (stats || !OUTPLAIN) {
    st16_raw(y0, rout, o.a0, 0); st16_raw(y1, rout, o.a1, 0); st8(y2, rout, o.a2, 0);
  } else {
    __builtin_amdgcn_raw_buffer_store_b128(y0, rout, o.a0, 0, 0);
    __builtin_amdgcn_raw_buffer_store_b128(y1, rout, o.a1, 0, 0);
    __builtin_amdgcn_raw_buffer_store_b64(y2, rout, o.a2, 0, 0);
  }
  st_guard(y0, y1);
  if (stats) {
    float s1a = 0.f, s1b = 0.f, s2a = 0.f, s2b = 0.f;
#pragma unroll
    for (int j = 0; j < 4; ++j) {
      s1a = dot2(y0[j], 0x3f803f80u, s1a); s2a = dot2(y0[j], y0[j], s2a);
      s1b = dot2(y1[j], 0x3f803f80u, s1b); s2b = dot2(y1[j], y1[j], s2b);
    }
    s1a = dot2(y2[0], 0x3f803f80u, s1a); s2a = dot2(y2[0], y2[0], s2a);
    s1b = dot2(y2[1], 0x3f803f80u, s1b); s2b = dot2(y2[1], y2[1], s2b);
    const float s1 = xsum16_32(s1a + s1b), s2 = xsum16_32(s2a + s2b);
    st8(u32x2{__float_as_uint(s1), __float_as_uint(s2)}, rst, sto, 0);
  }
}

DI constexpr unsigned wd_pos(int c) { return c < 8 ? 10 * c : 60 + 10 * c; }
DI constexpr int a_when(int k) { return k < 3 ? -1 : (k < 17 ? (k - 3) / 2 : k - 10); }

constexpr int ORD[14] = {0, 1, 4, 5, 7, 8, 11, 12, 2, 3, 6, 9, 10, 13};
DI constexpr int g4_late(int t) { return (t >= 4 && t < 7) || t >= 11; }
DI constexpr int g4_slot(int t) { return (t < 4 ? t : (t < 7 ? t - 4 : (t < 11 ? t - 7 : t - 11))) * 5120; }

#define PHASE_IDS \
  const unsigned lane = opq_v(tid0) & 63, w = opq_s(w0), bid = opq_s(blockIdx.x); \
  const unsigned xcd = bid & 7, idx = bid >> 3, i16 = lane & 15, q = lane >> 4; \
  char* const ws = opq_p(ws_); \
  (void)xcd; (void)idx; (void)i16; (void)q;
#define IDS_G2 const unsigned kq = xcd & 3, P = (xcd >> 2) * 8 + (idx >> 2), c4 = idx & 3;
#define G2_ROT 0u
#define G2_LA(h) (lb + G2_A + ((w >> 1) * 4 + ((2 * (w & 1) + (h) + 3 - kq) & 3)) * 2048)
#define IDS_G3 const unsigned P = (xcd >> 1) * 4 + (idx >> 3), ct = (xcd & 1) * 8 + (idx & 7);
#define G4PAIR 1
#define C8_OF (G4PAIR ? (xcd & 1) * 4 + (idx & 3) : idx & 7)
#define IDS_G4 const unsigned rt64 = G4PAIR ? (xcd >> 1) * 8 + (idx >> 2) : xcd * 4 + (idx >> 3), c8 = C8_OF;
#define WARM_STRIP(rx, sox) \
  ({ const unsigned n_ = (idx & 7) * 40 + lane; \
     __builtin_amdgcn_raw_buffer_load_b32(rx, lane < 40 ? (((n_ >> 3) & 1) * 20 + (n_ >> 4)) * 1024 + (n_ & 7) * 128 : OOB, sox, 16); })
#define KROT(k) ((k) + rot >= 20 ? (k) + rot - 20 : (k) + rot)
#define LDX(r, k) ld16(rx, lane * 16, sox + (r) * 20480 + KROT(k) * 1024)

__global__ __launch_bounds__(512, 2) void k_all(const u16* x0_, const u16* Sre_, const u16* Sim_, const u16* Dre_, const u16* Dim_,
                                               const u16* w1_, const u16* w2_, char* ws_, u16* out_ STAMP_ARGS) {
  ST(0);
  const unsigned tid0 = threadIdx.x, wall = rfl(tid0 >> 6), w0 = wall & 3;
  const unsigned lb = lds_base();
  const char* code; unsigned code_len;
  asm volatile("s_getpc_b64 %0\n.Lcw_begin:\n\ts_mov_b32 %1, .Lcw_end-.Lcw_begin" : "=s"(code), "=s"(code_len));
  u64 epoch;

  if (wall >= 4) {
    const unsigned sink = lb + K1_CS + 1024;
    unsigned ep0, ep1;
    asm volatile("global_load_dword %0, %2, %3 sc1\n\tglobal_load_dword %1, %2, %3 offset:4 sc1"
                 : "=&v"(ep0), "=&v"(ep1) : "v"(WS_FLG + FL_E * 128), "s"(ws_) : "memory");
    {
      PHASE_IDS
      IDS_G3
      const u32x4 sa = make_srd(opq_p(x0_), S_ * D_ * 2), sbd = make_srd(ct < 8 ? opq_p(Dre_) : opq_p(Dim_), D_ * D_ * 2);
      const unsigned vo = (lane >> 3) * 1280 + (((lane & 7) ^ (lane >> 3)) << 4);
      const unsigned soa = (P * 128 + w * 32) * 1280, sob = ((ct & 7) * 80 + w * 8) * 1280;
      const unsigned la = lb + w * 4096, lbb = lb + 16384 + w * 1024;
      const unsigned u0 = bid * 4 + w, u1 = 1024 + bid;
      const bool extra = w == 3 && bid < 56;
      const unsigned rot = ROT1 ? ((idx & 7) + 3 * (idx >> 3)) % 10u : 0;
      auto ko = [&](unsigned s) { const unsigned c = s + rot; return (c >= 10 ? c - 10 : c) * 128; };
      const rsrc_t rws = make_rsrc(ws, WS_END);
      asm volatile("s_nop 4" ::: "memory");
      if (WARM_0) {
        const unsigned r0 = lane / 10u, id = ((idx >> 3) * 4 + w) * 50 + lane, r1 = id / 10u;
        warm(sink, sa, lane < 40 ? r0 * 1280 + (lane - r0 * 10) * 128 : OOB, (P * 128 + (idx & 7) * 16 + w * 4) * 1280);
        warm(sink, sbd, lane < 50 ? r1 * 1280 + (id - r1 * 10) * 128 : OOB, (ct & 7) * 80 * 1280);
      }
      k1_issue(sa, sbd, vo, la, lbb, soa + ko(0), sob + ko(0), w);
      k1_issue(sa, sbd, vo, la + K1_STAGE, lbb + K1_STAGE, soa + ko(1), sob + ko(1), w);
      if (CODEWARM && w == 3) warm(sink, make_srd(code, code_len), lane < 16 ? idx * 2048 + lane * 128 : OOB, 0);
      ST(8);
      wait_vm<6>();
      {
        unsigned lo, hi;
        asm volatile("v_readfirstlane_b32 %0, %2\n\tv_readfirstlane_b32 %1, %3\n\ts_nop 4" : "=s"(lo), "=s"(hi) : "v"(ep0), "v"(ep1));
        epoch = (u64)lo + ((u64)hi << 32) + 1;
      }
      bar();
      k1_issue(sa, sbd, vo, la + 2 * K1_STAGE, lbb + 2 * K1_STAGE, soa + ko(2), sob + ko(2), w);
      const u32x4 ss = make_srd((xcd & 2) ? Sim_ : Sre_, S_ * S_ * 2);
#define WPAT 0
      const unsigned vs = WPAT ? (lane >> 4) * 4096 + (lane & 15) * 128 : (lane & 31) * 4096 + (lane >> 5) * 128;
      const unsigned sos = ((xcd >> 2) * 1024 + idx * 32) * 4096 + (xcd & 1) * 2048 + (WPAT ? w * 8 * 4096 : w * 256);
      unsigned so = 0;
#pragma unroll 1
      for (int i = 0; i < 10; ++i) {
        if (i == 0) wait_vm<6>(); else if (i == 1) wait_vm<2 * 6>(); else if (i < 6) wait_vm<3 * 6>();
        else if (i == 6) wait_vm<2 * 6 + 4>(); else if (i == 7) wait_vm<6 + 4>(); else wait_vm<4 + (WS2 >= 4) + (WS1 >= 4)>();
        bar();
        if (i == 0) {
          k1_issue(sa, sbd, vo, la + 3 * K1_STAGE, lbb + 3 * K1_STAGE, soa + ko(3), sob + ko(3), w);
          k1_issue(sa, sbd, vo, la + 4 * K1_STAGE, lbb + 4 * K1_STAGE, soa + ko(4), sob + ko(4), w);
        } else if (i == 1) {
          k1_issue(sa, sbd, vo, la, lbb, soa + ko(5), sob + ko(5), w);
          k1_issue(sa, sbd, vo, la + K1_STAGE, lbb + K1_STAGE, soa + ko(6), sob + ko(6), w);
        } else if (i < 5) k1_issue(sa, sbd, vo, la + so, lbb + so, soa + ko(i + 5), sob + ko(i + 5), w);
        so = so == 4 * K1_STAGE ? 0 : so + K1_STAGE;
        if (i == WS1) warm(sink, ss, vs, sos);
        if (i == WS2) warm(sink, ss, vs, sos + (WPAT ? 4 * 4096 : 1024));
        if (i == 5) {
          retile_issue(u0, w1_, w2_, lb + K1_TR + w * 4096, lane);
          if (extra) retile_issue(u1, w1_, w2_, lb + K1_TR + 4 * 4096, lane);
        }
      }
      ST(9);
      bar();
    }
    bf16x8 wr[10][5];
#pragma unroll
    for (int t = 0; t < 10; ++t)
#pragma unroll
      for (int f = 0; f < 5; ++f) wr[t][f] = bf16x8{};
#pragma unroll 1
    for (int L = 0; L < 2; ++L) {
      const int sbase = L * 20;
      (void)sbase;
      {
        PHASE_IDS
        IDS_G2
        const u32x4 sa = make_srd(kq < 2 ? opq_p(Sre_) : opq_p(Sim_), S_ * S_ * 2);
        const u32x4 sb_ = make_srd(ws + (L ? WS_AB2 : WS_AB1), SZ_AB);
        const unsigned va = (lane >> 3) * 4096 + (((lane & 7) ^ (lane >> 3)) << 4), vb = lane * 16;
        const unsigned soa = (P * 128 + w * 32) * 4096 + (kq & 1) * 2048;
        const unsigned sob = ((2 * c4 + (w >> 1)) * 128 + kq * 32 + (w & 1)) * 5 * 1024;
        const unsigned la = lb + G2_A + w * 4096, lbb = lb + w * 5120;
        const unsigned la0 = G2_LA(0), la1 = G2_LA(1);
        const unsigned ct = (xcd & 1) * 8 + (idx & 7);
        const unsigned rot = G2_ROT;
        asm volatile("s_nop 4" ::: "memory");
        auto issue_a = [&](unsigned l, unsigned so) {
          glds<0, 0>(sa, va, la0 + l, so); glds<1024, 8 * 4096>(sa, va, la0 + l, so);
          glds<0, 16 * 4096>(sa, va, la1 + l, so); glds<1024, 24 * 4096>(sa, va, la1 + l, so);
        };
        if (L == 0) {
#pragma unroll
          for (int s = 0; s < 3; ++s) issue_a(s * G2_AS, soa + s * 128);
          const unsigned u0 = bid * 4 + w, u1 = 1024 + bid;
          const rsrc_t rws = make_rsrc(ws, WS_END);
          wait_vm<12>();
          retile_finish(u0, 0, rws, lb + K1_TR + w * 4096, lane);
          retile_finish(u0, 2, rws, lb + K1_TR + w * 4096, lane);
          if (w == 3 && bid < 56) {
            retile_finish(u1, 0, rws, lb + K1_TR + 4 * 4096, lane);
            retile_finish(u1, 2, rws, lb + K1_TR + 4 * 4096, lane);
          }
          if (P5DELAY0) __builtin_amdgcn_s_sleep(P5DELAY0);
        } else {
          issue_a(3 * G2_AS, soa + 3 * 128);
          __builtin_amdgcn_s_sleep(P5DELAY);
        }
        ST(sbase + 1);
        poll0(ws, FL_5 + ((L * 4 + kq) * 4 + c4) * 64 + ((i16 >> 1) * 2 + (w >> 1)) * 4 + (w & 1) + 2 * (i16 & 1), 16, epoch, lane);
        wait_vm<0>();
        ST(sbase + 2);
        glds_run_sc1<5>(sb_, vb, lbb, sob + rot * 10240);
        glds_run_sc1<5>(sb_, vb, lbb + G2_BS, sob + ((1 + rot) & 15) * 10240);
        if (L == 0) {
          raise(ws, FL_W + bid * 4 + w, epoch, lane);
          if (w == 3 && bid < 56) raise(ws, FL_W + 1024 + bid, epoch, lane);
          wait_vm<6>();
        } else wait_vm<5>();
        bar();
        glds_run_sc1<5>(sb_, vb, lbb + 2 * G2_BS, sob + ((2 + rot) & 15) * 10240);
        if (L == 0) issue_a(3 * G2_AS, soa + ((3 + rot) & 15) * 128);
        glds_run_sc1<5>(sb_, vb, lbb + 3 * G2_BS, sob + ((3 + rot) & 15) * 10240);
        ST(sbase + 3);
        const u32x4 sw = make_srd(ws + WS_W1, SZ_W1);
        const unsigned sow = ct * 140 * 1024;
        unsigned fw, fw0 = 0, fw1 = 0;
        {
          const unsigned n = w * 42 + (lane < 42 ? lane : 0), k3 = n / 3u, g3 = (7 * ct >> 2) + (n - k3 * 3), n2 = n - 60;
          fw = FL_W + (n < 60 ? k3 * 27 + (g3 < 26 ? g3 : 26) : 540 + (n2 >> 1) * 10 + (5 * C8_OF >> 2) + (n2 & 1));
        }
#pragma unroll 1
        for (int s = 0; s < 16; ++s) {
          if (s == 0) wait_vm<10>();
          if (s == 1) wait_vm<14>();
          wait_vm<18>();
          if (s == 9 && L == 0) {
            asm volatile("" : "+v"(fw0), "+v"(fw1));
            if (!__all(fw0 == (unsigned)epoch && fw1 == (unsigned)(epoch >> 32))) poll0(ws, fw, 42, epoch, lane);
          }
          bar();
          const unsigned sl = s & 3;
          if (s < 12) {
            issue_a(sl * G2_AS, soa + ((s + 4 + rot) & 15) * 128);
            glds_run_sc1<5>(sb_, vb, lbb + sl * G2_BS, sob + ((s + 4 + rot) & 15) * 10240);
            if (s == 6 && L == 0)
              asm volatile("global_load_dword %0, %2, %3 sc1\n\tglobal_load_dword %1, %2, %3 offset:4 sc1"
                           : "+v"(fw0), "+v"(fw1) : "v"(WS_FLG + fw * 128), "s"(ws) : "memory");
          } else {
            glds_run_sc1<5>(sw, vb, lbb + sl * G2_BS, sow + w * 5120 + sl * G2_BS);
            if (s < 15) glds_run_sc1<4>(sw, vb, la + sl * G2_AS, sow + G2_A + w * 4096 + sl * G2_AS);
            else glds_run_sc1<3>(sw, vb, lb + G2_A + 3 * G2_AS + w * 3072, sow + G2_A + 3 * G2_AS + w * 3072);
          }
        }
        ST(sbase + 4);
      }
      {
        PHASE_IDS
        IDS_G4
        const unsigned ct = (xcd & 1) * 8 + (idx & 7);
        if (w >= 2) {
          const rsrc_t rc = make_rsrc(ws + WS_CS1, 20 * F_ * 4);
          const unsigned c = (w - 2) * 64 + lane, col = ct * 112 + c;
          const unsigned vo = c < 112 && col < F_ ? col * 4 : OOB;
          float csp = 0.f;
#pragma unroll
          for (int k = 0; k < 20; ++k) csp += __uint_as_float(__builtin_amdgcn_raw_buffer_load_b32(rc, vo, k * F_ * 4, 16));
          lds_wr1(lb + KM_CS1 + c * 4, csp);
        }
        const unsigned so = (c8 * 54 + w * 14) * 5 * 1024;
        wait_vm<0>();
        if (L == 0) {
          if (RSLEEP) __builtin_amdgcn_s_sleep(RSLEEP);
          const rsrc_t rw = make_rsrc(ws + WS_W2, SZ_W2);
          const unsigned vl = lane * 16, vz = lane * 16 + (w < 3 ? 0u : OOB);
          sfor<0, 50>([&](auto N) {
            constexpr int t = 4 + N.v / 5, n = ORD[t] * 5 + N.v % 5;
            wr[t - 4][N.v % 5] = ld16(rw, (ORD[t] < 12 ? vl : vz) + (n & 3) * 1024, so + (n >> 2) * 4096);
            if constexpr (RGAP && N.v % 5 == 4 && N.v < 49) { sb(); __builtin_amdgcn_s_sleep(RGAP); sb(); }
          });
          sb();
        }
        wait_lgkm0();
        ST(sbase + 5);
        bar();
        bar();
        const u32x4 s2 = make_srd(ws + WS_W2, SZ_W2);
        const unsigned sg = rfl(so);
        asm volatile("s_nop 4" ::: "memory");
        glds_run_sc1<10>(s2, lane * 16, lb + w * 20480, sg);
        glds_run_sc1<10>(s2, lane * 16, lb + w * 20480 + 10240, sg + 4 * 5120);
        wait_vm<0>();
        ST(sbase + 6);
        const unsigned pe = lb + w * 20480 + lane * 16, pl = lb + G4_LATE + w * 15360 + lane * 16;
        auto put = [&](auto T0, auto T1) {
          sfor<T0.v * 5, T1.v * 5>([&](auto N) {
            constexpr int t = N.v / 5, f = N.v % 5;
            *(LDS_AS bf16x8*)(ldsp)(uintptr_t)((g4_late(t) ? pl : pe) + g4_slot(t) + f * 1024) = wr[t - 4][f];
          });
          wait_lgkm0();
        };
        bar();
        put(IC<4>{}, IC<7>{});
        bar();
        put(IC<7>{}, IC<11>{});
        bar();
        put(IC<11>{}, IC<14>{});
        bar();
      }
      if (L) { bar(); break; }
      {
        PHASE_IDS
        IDS_G3
        const u32x4 sd = make_srd(ct < 8 ? opq_p(Dre_) : opq_p(Dim_), D_ * D_ * 2);
        const unsigned vo = (lane >> 3) * 1280 + (((lane & 7) ^ (lane >> 3)) << 4);
        const unsigned so = (ct & 7) * 80 * 1280;
        sfor<0, 10>([&](auto G) { glds<G.v * 1024, G.v * 8 * 1280>(sd, vo, lb + 20 * w * 1024, so + 2 * w * 128); });
        sfor<0, WDPRE>([&](auto G) { glds<G.v * 1024, G.v * 8 * 1280>(sd, vo, lb + (20 * w + 10) * 1024, so + (2 * w + 1) * 128); });
        bar();
        sfor<WDPRE, 10>([&](auto G) { glds<G.v * 1024, G.v * 8 * 1280>(sd, vo, lb + (20 * w + 10) * 1024, so + (2 * w + 1) * 128); });
        sfor<0, 5>([&](auto G) { glds<G.v * 1024, G.v * 8 * 1280>(sd, vo, lb + (140 + w * 5) * 1024, so + (8 + (w >> 1)) * 128 + (w & 1) * 40 * 1280); });
        wait_vm<0>();
        ST(7);
        bar();
      }
      {
        PHASE_IDS
        IDS_G2
        const u32x4 sa = make_srd(kq < 2 ? opq_p(Sre_) : opq_p(Sim_), S_ * S_ * 2);
        const unsigned va = (lane >> 3) * 4096 + (((lane & 7) ^ (lane >> 3)) << 4);
        const unsigned soa = (P * 128 + w * 32) * 4096 + (kq & 1) * 2048;
        const unsigned rot = G2_ROT;
        __builtin_amdgcn_s_sleep(ASLEEP2);
#pragma unroll
        for (int s = 0; s < 3; ++s) {
          const unsigned l0 = G2_LA(0) + s * G2_AS, l1 = G2_LA(1) + s * G2_AS, so = soa + ((s + rot) & 15) * 128;
          glds<0, 0>(sa, va, l0, so); glds<1024, 8 * 4096>(sa, va, l0, so);
          glds<0, 16 * 4096>(sa, va, l1, so); glds<1024, 24 * 4096>(sa, va, l1, so);
        }
        bar();
      }
    }
    asm volatile(".Lcw_end:");
    return;
  }

  {
    const u64 e = __hip_atomic_load(flag_ptr(ws_, FL_E), RLX_AGENT);
    epoch = (u64)rfl((unsigned)e) + ((u64)rfl((unsigned)(e >> 32)) << 32) + 1;
  }
#pragma unroll 1
  for (int L = 0; L < 2; ++L) {
    const int sbase = L * 20;
    (void)sbase;
    {
      PHASE_IDS
      IDS_G3
      f32x4 acc[2][5];
      float mu[2][4], rs[2][4], csv[5];
      const float sign = ct < 8 ? 1.f : -1.f;
      if (L == 0) {
        const unsigned ra0 = lb + w * 4096 + i16 * 128 + ((q ^ (lane & 7)) << 4);
        const unsigned rb0 = lb + 16384 + i16 * 128 + ((q ^ (lane & 7)) << 4);
        const f32x4 z4 = {0.f, 0.f, 0.f, 0.f};
        f32x4 cs0 = z4, cs1 = z4, rs_[2] = {z4, z4}, rq_[2] = {z4, z4};
#pragma unroll
        for (int r = 0; r < 2; ++r)
#pragma unroll
          for (int f = 0; f < 5; ++f) acc[r][f] = z4;
        bf16x8 fr[2][8];
        bf16x8 one = __builtin_bit_cast(bf16x8, u32x4{0x3f803f80u, 0x3f803f80u, 0x3f803f80u, 0x3f803f80u});
        auto half = [&](auto KK, unsigned so) {
          constexpr int kk = KK.v;
          const unsigned pa = (ra0 ^ ((kk ^ 1) * 64)) + so, pb = (rb0 ^ ((kk ^ 1) * 64)) + so;
          sfor<0, 16>([&](auto M) {
            constexpr int m = M.v;
            if constexpr (m < 10) mma(acc[m & 1][m >> 1], fr[kk][m & 1], fr[kk][2 + (m >> 1)]);
            else if constexpr (m == 10) mma(cs0, one, fr[kk][7]);
            else if constexpr (m == 11) mma(cs1, one, fr[kk][6]);
            else if constexpr (m < 14) mma(rs_[m - 12], fr[kk][m - 12], one);
            else mma(rq_[m - 14], fr[kk][m - 14], fr[kk][m - 14]);
            constexpr int n = m >> 1;
            if constexpr ((m & 1) == 0) fr[kk ^ 1][n] = n < 2 ? lds_rd(pa + n * 2048) : (n < 7 ? lds_rd(pb + (n - 2) * 2048) : lds_rd(pb + w * 2048));
            sb();
          });
        };
        asm volatile("s_nop 1" : "+v"(cs0), "+v"(cs1), "+v"(rs_[0]), "+v"(rs_[1]), "+v"(rq_[0]), "+v"(rq_[1]), "+v"(one));
        fence_all(acc);
        bar();
        ST(38);
        fr[0][0] = lds_rd(ra0); fr[0][1] = lds_rd(ra0 + 2048);
#pragma unroll
        for (int f = 0; f < 5; ++f) fr[0][2 + f] = lds_rd(rb0 + f * 2048);
        fr[0][7] = lds_rd(rb0 + w * 2048);
        sb();
        unsigned so = 0;
#pragma unroll 1
        for (int i = 0; i < 10; ++i) {
          half(IC<0>{}, so);
          wait_lgkm0();
          bar();
          so = so == 4 * K1_STAGE ? 0 : so + K1_STAGE;
          half(IC<1>{}, so);
        }
        fence_all(acc);
        asm volatile("" : "+v"(cs0), "+v"(cs1), "+v"(rs_[0]), "+v"(rs_[1]), "+v"(rq_[0]), "+v"(rq_[1]));
        const unsigned csb = lb + K1_CS;
        if (q == 0) {
          lds_wr1(csb + (w * 16 + i16) * 4, cs0[0]);
          if (w == 0) lds_wr1(csb + (64 + i16) * 4, cs1[0]);
        }
        wait_lgkm0();
        bar();
        ST(18);
#pragma unroll
        for (int f = 0; f < 5; ++f) csv[f] = lds_rd1(csb + (f * 16 + i16) * 4);
#pragma unroll
        for (int r = 0; r < 2; ++r)
#pragma unroll
          for (int j = 0; j < 4; ++j) {
            float m_, r_;
            ln_coef(rs_[r][j], __shfl(rq_[r][j], q * 20 + j), m_, r_);
            mu[r][j] = -m_; rs[r][j] = sign * r_;
          }
        if (w == 0) {
          const rsrc_t rc = make_rsrc(ws + WS_CSD, 256 * 320);
          st4(__float_as_uint(q == 0 ? csv[0] : (q == 1 ? csv[1] : (q == 2 ? csv[2] : csv[3]))), rc, lane * 4, bid * 320);
          st4(__float_as_uint(csv[4]), rc, q == 0 ? 256 + lane * 4 : OOB, bid * 320);
        }
      } else {
        {
          const rsrc_t rc = make_rsrc(ws + WS_CSD, 256 * 320);
#pragma unroll
          for (int f = 0; f < 5; ++f) csv[f] = __uint_as_float(__builtin_amdgcn_raw_buffer_load_b32(rc, (f * 16 + i16) * 4, bid * 320, 16));
        }
        poll(ws, FL_4 + P * 64 + w * 16 + i16, 16, epoch, lane);
        ST(17);
        const rsrc_t rx = make_rsrc(ws + WS_X2, SZ_X);
        const unsigned sox = (P * 8 + w * 2) * 20 * 1024;
        const unsigned rot = ROT5 ? 2 * ((idx & 7) + ((idx & 7) >> 2)) : 0;
        auto wdp = [&](int c) { const unsigned cc = c + (rot >> 1) >= 10 ? c + (rot >> 1) - 10 : c + (rot >> 1);
                                return (cc < 8 ? 10 * cc : 60 + 10 * cc) * 1024; };
        bf16x8 a[20][2];
        unsigned wsink = WARM_X ? WARM_STRIP(rx, sox) : 0u;
#pragma unroll
        for (int k = 0; k < 3; ++k) { a[k][0] = LDX(0, k); a[k][1] = LDX(1, k); }
        const rsrc_t rst = make_rsrc(ws + WS_ST2, SZ_ST);
        const unsigned sto = ((lane >> 5) * 4 * S_ + P * 128 + w * 32 + (lane & 31)) * 8;
        const u32x2 sp0 = ld8u(rst, sto, 0), sp1 = ld8u(rst, sto, S_ * 8), sp2 = ld8u(rst, sto, 2 * S_ * 8), sp3 = ld8u(rst, sto, 3 * S_ * 8);
        sb();
        bar();
        bf16x8 b[2][5];
        const unsigned rb = lb + i16 * 128 + ((q ^ (lane & 7)) << 4);
#pragma unroll
        for (int f = 0; f < 5; ++f) b[0][f] = lds_rd(rb + wdp(0) + f * 2048);
        sb();
        sfor<0, 20>([&](auto K) {
          constexpr int k = K.v;
          const unsigned pb = (rb ^ (((k + 1) & 1) * 64)) + wdp(((k + 1) >> 1) % 10);
          sfor<0, 10>([&](auto M) {
            constexpr int f = M.v >> 1, r = M.v & 1;
            if constexpr (k == 0) mma0(acc[r][f], a[0][r], b[0][f]); else mma(acc[r][f], a[k][r], b[k & 1][f]);
            if constexpr (k + 1 < 20 && r == 0) b[(k + 1) & 1][f] = lds_rd(pb + f * 2048);
            if constexpr (r == 1 && f < 4) {
              constexpr int kn = k < 7 ? 2 * k + 3 + (f >> 1) : (f < 2 ? k + 10 : 20);
              if constexpr (kn < 20) a[kn][f & 1] = LDX(f & 1, kn);
            }
            sb();
          });
          if constexpr (k == 0) ST(40);
          if constexpr (k == 4) ST(41);
          if constexpr (k == 9) ST(42);
        });
        asm volatile("" :: "v"(wsink));
        fence_all(acc);
        bar();
        ST(39);
        float s1 = (__uint_as_float(sp0[0]) + __uint_as_float(sp1[0])) + (__uint_as_float(sp2[0]) + __uint_as_float(sp3[0]));
        float s2 = (__uint_as_float(sp0[1]) + __uint_as_float(sp1[1])) + (__uint_as_float(sp2[1]) + __uint_as_float(sp3[1]));
        auto ta = __builtin_amdgcn_permlane32_swap(__float_as_uint(s1), __float_as_uint(s1), false, false);
        auto tb = __builtin_amdgcn_permlane32_swap(__float_as_uint(s2), __float_as_uint(s2), false, false);
        float m, rr;
        ln_coef(__uint_as_float(ta[0]) + __uint_as_float(ta[1]), __uint_as_float(tb[0]) + __uint_as_float(tb[1]), m, rr);
        m = -m; rr *= sign;
#pragma unroll
        for (int r = 0; r < 2; ++r)
#pragma unroll
          for (int e = 0; e < 4; ++e) { mu[r][e] = __shfl(m, r * 16 + q * 4 + e); rs[r][e] = __shfl(rr, r * 16 + q * 4 + e); }
      }
      g1_store(acc, mu, rs, csv, make_rsrc(ws + (L ? WS_AB2 : WS_AB1), SZ_AB), P, ct, w, i16, q);
      ST(L + 19);
      publish(ws, FL_5 + ((L * 4 + (ct >> 3) * 2 + (P >> 3)) * 4 + ((ct & 7) >> 1)) * 64 + ((P & 7) * 2 + (ct & 1)) * 4 + w, epoch, lane);
    }
    {
      PHASE_IDS
      IDS_G2
      const unsigned m = w >> 1, n = w & 1;
      const unsigned ra = lb + G2_A + (m * 64 + i16) * 128 + ((q ^ (lane & 7)) << 4);
      const unsigned rb = lb + n * 10240 + lane * 16;
      const unsigned row = P * 128 + m * 64 + kq * 16 + i16, ct = 2 * c4 + n;
      const TileOff ro = tile_offsets(L == 0, row, ct, q), oo = tile_offsets(false, row, ct, q);
      const rsrc_t rxp = make_rsrc(ws + WS_XP + L * SZ_XP, SZ_XP);
      const unsigned xo = ((P * 4 + c4) * 48 + w) * XSLOT;
      f32x4 acc[4][5];
#pragma unroll
      for (int i = 0; i < 4; ++i)
#pragma unroll
        for (int f = 0; f < 5; ++f) acc[i][f] = f32x4{0.f, 0.f, 0.f, 0.f};
      bf16x8 fa[2][4], fb[2][5];
      auto half = [&](auto KK, unsigned sl) {
        constexpr int kk = KK.v;
        const unsigned pa = (ra ^ ((kk ^ 1) * 64)) + sl * G2_AS, pb = rb + (kk ^ 1) * 5120 + sl * G2_BS;
        sfor<0, 20>([&](auto M) {
          constexpr int mm = M.v;
          mma(acc[mm / 5][mm % 5], fb[kk][mm % 5], fa[kk][mm / 5]);
          if constexpr (mm < 5) fb[kk ^ 1][mm] = lds_rd(pb + mm * 1024);
          else if constexpr (mm < 9) fa[kk ^ 1][mm - 5] = lds_rd(pa + (mm - 5) * 2048);
          sb();
        });
      };
      if (L) poll0(ws, FL_4 + P * 64 + (m * 4 + kq) * 8 + ct, 1, epoch, lane);
      ResTile rt = load_res(make_rsrc(L ? (const void*)(ws + WS_X2) : (const void*)opq_p(x0_), SZ_X), ro);
      fence_all(acc);
      bar();
      ST(sbase + 1);
#pragma unroll
      for (int f = 0; f < 5; ++f) fb[0][f] = lds_rd(rb + f * 1024);
#pragma unroll
      for (int i = 0; i < 4; ++i) fa[0][i] = lds_rd(ra + i * 2048);
      sb();
#pragma unroll 1
      for (int s = 0; s < 15; ++s) {
        half(IC<0>{}, s & 3);
        wait_lgkm0();
        bar();
        half(IC<1>{}, (s + 1) & 3);
      }
      half(IC<0>{}, 3);
      wait_lgkm0();
      bar();
      {
        unsigned p[3][10];
        auto pack = [&](auto N) {
          constexpr int i = N.v / 5, f = N.v % 5;
          asm volatile("" : "+v"(acc[i][f]));
          p[i][2 * f] = pk2(acc[i][f][0], acc[i][f][1]); p[i][2 * f + 1] = pk2(acc[i][f][2], acc[i][f][3]);
          const unsigned o = xo + (((kq + 1 + i) & 3) * 3 + 2 - i) * 4 * XSLOT;
          if constexpr (f == 1) st16(u32x4{p[i][0], p[i][1], p[i][2], p[i][3]}, rxp, lane * 16, o);
          if constexpr (f == 3) st16(u32x4{p[i][4], p[i][5], p[i][6], p[i][7]}, rxp, lane * 16 + 1024, o);
          if constexpr (f == 4) st8(u32x2{p[i][8], p[i][9]}, rxp, lane * 8 + 2048, o);
        };
        sfor<0, 20>([&](auto M) {
          constexpr int mm = M.v;
          mma(acc[mm / 5][mm % 5], fb[1][mm % 5], fa[1][mm / 5]);
          if constexpr (mm >= 8) pack(IC<mm - 8>{});
          sb();
        });
        ST(sbase + 2);
        pack(IC<12>{}); pack(IC<13>{}); pack(IC<14>{});
      }
      f32x4 (&v)[5] = acc[3];
      fence_acc(v);
      const unsigned fx = FL_X + ((L * 64 + P * 4 + c4) * 4 + w) * 4;
      ST(sbase + 3);
      publish(ws, fx + kq, epoch, lane);
      ST(sbase + 4);
      poll(ws, fx + ((kq + 1 + lane) & 3), 3, epoch, lane);
      ST(sbase + 5);
      const unsigned xi = xo + kq * 12 * XSLOT;
      u32x4 g0[3], g1[3]; u32x2 g2[3];
#pragma unroll
      for (int s = 0; s < 3; ++s) {
        g0[s] = ld16u(rxp, lane * 16, xi + s * 4 * XSLOT);
        g1[s] = ld16u(rxp, lane * 16 + 1024, xi + s * 4 * XSLOT);
        g2[s] = ld8u(rxp, lane * 8 + 2048, xi + s * 4 * XSLOT);
      }
      sb();
#pragma unroll
      for (int s = 0; s < 3; ++s) {
        v[0] += f32x4{bflo(g0[s][0]), bfhi(g0[s][0]), bflo(g0[s][1]), bfhi(g0[s][1])};
        v[1] += f32x4{bflo(g0[s][2]), bfhi(g0[s][2]), bflo(g0[s][3]), bfhi(g0[s][3])};
        v[2] += f32x4{bflo(g1[s][0]), bfhi(g1[s][0]), bflo(g1[s][1]), bfhi(g1[s][1])};
        v[3] += f32x4{bflo(g1[s][2]), bfhi(g1[s][2]), bflo(g1[s][3]), bfhi(g1[s][3])};
        v[4] += f32x4{bflo(g2[s][0]), bfhi(g2[s][0]), bflo(g2[s][1]), bfhi(g2[s][1])};
      }
      finish_tile(v, rt, make_rsrc(ws + (L ? WS_X3 : WS_X1), SZ_X), oo, true, make_rsrc(ws + (L ? WS_ST3 : WS_ST1), SZ_ST),
                  q == 0 ? (ct * S_ + row) * 8 : OOB);
      ST(sbase + 6);
      publish(ws, FL_2 + (L * 16 + P) * 64 + (m * 4 + kq) * 8 + ct, epoch, lane);
      ST(sbase + 7);
    }
    u32x4 y2[2]; u32x2 y1[2];
    {
      PHASE_IDS
      IDS_G3
      typedef __attribute__((ext_vector_type(2))) short s16x2;
      poll(ws, FL_2 + (L * 16 + P) * 64 + w * 16 + i16, 16, epoch, lane);
      ST(sbase + 8);
      const rsrc_t rx = make_rsrc(ws + (L ? WS_X3 : WS_X1), SZ_X);
      const rsrc_t rst = make_rsrc(ws + (L ? WS_ST3 : WS_ST1), SZ_ST);
      const unsigned sox = (P * 8 + w * 2) * 20 * 1024;
      const unsigned rot = ROT3 ? (5 * (idx & 7)) >> 1 : 0;
      const unsigned par = ct & 1;
      bf16x8 a[20][2];
#pragma unroll
      for (int k = 0; k < 3; ++k) { a[k][0] = LDX(0, k); a[k][1] = LDX(1, k); }
      const unsigned sto = (2 * q * S_ + P * 128 + w * 32 + i16) * 8;
      u32x2 sa0, sa1, sb0, sb1;
      sb();
      bar();
      f32x4 acc[2][4];
      bf16x8 b[2][4];
      const unsigned rb = lb + lane * 16;
#pragma unroll
      for (int f = 0; f < 4; ++f) b[0][f] = lds_rd(rb + rot * 4096 + f * 1024);
      sb();
      sfor<0, 20>([&](auto K) {
        constexpr int k = K.v;
        const unsigned pb = k + 1 < 20 ? rb + KROT((k + 1) % 20) * 4096 : rb + 80 * 1024 + rot * 3072;
        if constexpr (k == 9) { sa0 = ld8u(rst, sto, 0); sa1 = ld8u(rst, sto, S_ * 8); sb0 = ld8u(rst, sto + 128, 0); sb1 = ld8u(rst, sto + 128, S_ * 8); }
        sfor<0, 8>([&](auto M) {
          constexpr int f = M.v >> 1, r = M.v & 1;
          if constexpr (k == 0) mma0(acc[r][f], b[0][f], a[0][r]); else mma(acc[r][f], b[k & 1][f], a[k][r]);
          if constexpr (r == 0 && (k + 1 < 20 || f < 3)) b[(k + 1) & 1][f] = lds_rd(pb + f * 1024);
          if constexpr (r == 1 && 2 * k + 3 + (f >> 1) < 20) a[2 * k + 3 + (f >> 1)][f & 1] = LDX(f & 1, 2 * k + 3 + (f >> 1));
          sb();
        });
        if constexpr (k == 0) ST(sbase + 9);
        if constexpr (k == 3) ST(51 + L * 5);
        if constexpr (k == 7) ST(52 + L * 5);
        if constexpr (k == 11) ST(53 + L * 5);
        if constexpr (k == 15) ST(54 + L * 5);
        if constexpr (k == 19) ST(55 + L * 5);
      });
      float mu[2], rs[2];
      ln_coef(xsum16_32(__uint_as_float(sa0[0]) + __uint_as_float(sa1[0])), xsum16_32(__uint_as_float(sa0[1]) + __uint_as_float(sa1[1])), mu[0], rs[0]);
      ln_coef(xsum16_32(__uint_as_float(sb0[0]) + __uint_as_float(sb1[0])), xsum16_32(__uint_as_float(sb0[1]) + __uint_as_float(sb1[1])), mu[1], rs[1]);
      auto act = [&](const f32x4& c, int r, const f32x4& cs, unsigned (&pk)[2]) {
        const f32x2 nm = {-mu[r], -mu[r]}, r2 = {rs[r], rs[r]};
#pragma unroll
        for (int e = 0; e < 2; ++e) {
          const f32x2 z = __builtin_elementwise_fma(nm, f32x2{cs[2 * e], cs[2 * e + 1]}, f32x2{c[2 * e], c[2 * e + 1]}) * r2;
          const s16x2 hb = __builtin_bit_cast(s16x2, __builtin_convertvector(z, bf16x2));
          pk[e] = __builtin_bit_cast(unsigned, __builtin_elementwise_max(hb, s16x2{0, 0}));
        }
      };
      auto pair = [&](const unsigned (&u)[2], const unsigned (&v)[2]) {
        auto t0 = __builtin_amdgcn_permlane16_swap(u[0], v[0], false, false);
        auto t1 = __builtin_amdgcn_permlane16_swap(u[1], v[1], false, false);
        return u32x4{t0[0], t1[0], t0[1], t1[1]};
      };
      const unsigned csb = lb + KM_CS1 + q * 16;
      asm volatile("s_nop 7\n\ts_nop 1" : "+v"(acc[0][0]), "+v"(acc[0][1]), "+v"(acc[0][2]), "+v"(acc[0][3]),
                                          "+v"(acc[1][0]), "+v"(acc[1][1]), "+v"(acc[1][2]), "+v"(acc[1][3]));
      wait_lgkm0();
      bar();
      ST(43 + L * 4);
      {
        const rsrc_t rh = make_rsrc(ws + (L ? WS_H2 : WS_H1), SZ_H);
        const unsigned vo = ((((q & 1) << 1) | (q >> 1)) * 16 + i16) * 16;
        const unsigned so = ((P * 8 + w * 2) * 54 + 7 * (ct >> 1) + 4 * par) * 1024;
        u32x4 y[2][2];
#pragma unroll
        for (int p = 0; p < 2; ++p) {
          const f32x4 c0 = lds_rdf(csb + (2 * p + par) * 64), c1 = lds_rdf(csb + (2 * p + 1 + par) * 64);
#pragma unroll
          for (int r = 0; r < 2; ++r) {
            unsigned u[2], v[2];
            act(acc[r][2 * p], r, c0, u); act(acc[r][2 * p + 1], r, c1, v);
            y[p][r] = pair(u, v);
            st16_raw(y[p][r], rh, p == 1 && ct == 15 ? OOB : vo, so + (r * 54 + p) * 1024);
          }
        }
        st_guard(y[0][0], y[0][1], y[1][0], y[1][1]);
      }
      ST(44 + L * 4);
      bool up = false;
      sb();
      sfor<0, 20>([&](auto K) {
        constexpr int k = K.v;
        const unsigned pb = rb + 80 * 1024 + KROT((k + 1) % 20) * 3072;
        sfor<0, 6>([&](auto M) {
          constexpr int f = M.v >> 1, r = M.v & 1;
          if constexpr (k == 0) mma0(acc[r][f], b[0][f], a[0][r]); else mma(acc[r][f], b[k & 1][f], a[k][r]);
          if constexpr (r == 0 && k + 1 < 20) b[(k + 1) & 1][f] = lds_rd(pb + f * 1024);
          sb();
        });
        if constexpr (VMPEEK) {
          if constexpr (k >= 6) {
            if (!up && (k == 19 || (__builtin_amdgcn_s_getreg((3 << 11) | 7) | __builtin_amdgcn_s_getreg((1 << 11) | (30 << 6) | 7)) == 0)) {
              publish(ws, FL_3 + (L * 32 + P) * 64 + w * 16 + ct, epoch, lane);
              up = true;
            }
          }
        } else if constexpr (k == PUB1) publish(ws, FL_3 + (L * 32 + P) * 64 + w * 16 + ct, epoch, lane);
      });
      const f32x4 c0 = lds_rdf(csb + (4 + par) * 64), c1 = lds_rdf(csb + (5 + par) * 64), c2 = lds_rdf(csb + (par ? 0 : 6) * 64);
      asm volatile("s_nop 7\n\ts_nop 1" : "+v"(acc[0][0]), "+v"(acc[0][1]), "+v"(acc[0][2]), "+v"(acc[1][0]), "+v"(acc[1][1]), "+v"(acc[1][2]));
      ST(sbase + 10);
#pragma unroll
      for (int r = 0; r < 2; ++r) {
        unsigned u[2], v[2], s[2];
        act(acc[r][0], r, c0, u); act(acc[r][1], r, c1, v); act(acc[r][2], r, c2, s);
        y2[r] = pair(u, v);
        y1[r] = u32x2{s[0], s[1]};
      }
      asm volatile("" : "+v"(y2[0]), "+v"(y2[1]), "+v"(y1[0]), "+v"(y1[1]));
      ST(sbase + 11);
    }
    {
      PHASE_IDS
      IDS_G4
      const unsigned row = rt64 * 64 + w * 16 + i16;
      const rsrc_t rh = make_rsrc(ws + (L ? WS_H2 : WS_H1), SZ_H);
      const unsigned soh = (rt64 * 4 * 54 + w * 14) * 1024;
      const unsigned vl = lane * 16, vz = lane * 16 + (w < 3 ? 0u : OOB);
      bf16x8 a[14][4], b[2][5];
      unsigned sh[4];
#pragma unroll
      for (int i = 0; i < 4; ++i) sh[i] = soh + ((w + 1 + i) & 3) * 54 * 1024;
#define LDH(t, i) a[t][i] = ld16(rh, (ORD[t] < 12 ? vl : vz) + (ORD[t] & 3) * 1024, sh[i] + (ORD[t] >> 2) * 4096)
      const TileOff ro = tile_offsets(false, row, c8, q), oo = tile_offsets(L != 0, row, c8, q);
      const unsigned f3 = FL_3 + (L * 32 + (rt64 >> 1)) * 64 + ((rt64 & 1) * 2 + (lane & 1)) * 16 + 4 * w + ((lane >> 1) & 3);
      if (P3DELAY) __builtin_amdgcn_s_sleep(P3DELAY);
      poll0(ws, f3, 8, epoch, lane);
      ST(sbase + 13);
      sfor<0, 12>([&](auto N) { LDH(N.v / 4, N.v % 4); });
      sb();
      {
        const unsigned P = (xcd >> 1) * 4 + (idx >> 3), ct = (xcd & 1) * 8 + (idx & 7), par = ct & 1;
        const unsigned vo = ((((q & 1) << 1) | (q >> 1)) * 16 + i16) * 16;
        const unsigned v1 = ((2 * par + (q >> 1)) * 16 + i16) * 16 + (q & 1) * 8;
        const unsigned so = ((P * 8 + w * 2) * 54 + 7 * (ct >> 1)) * 1024;
#pragma unroll
        for (int r = 0; r < 2; ++r) {
          st16_raw(y2[r], rh, ct == 15 ? OOB : vo, so + (r * 54 + 2 + 4 * par) * 1024);
          st8(y1[r], rh, v1, so + (r * 54 + 3) * 1024);
        }
        st_guard(y2[0], y2[1]);
      }
      ResTile rt;
      u64 fl2;
      const rsrc_t rr = make_rsrc(ws + (L ? WS_X3 : WS_X1), SZ_X);
      sb();
      bar();
      f32x4 acc[4][5];
      const unsigned rbe = lb + w * 20480 + lane * 16, rbl = lb + G4_LATE + w * 15360 + lane * 16;
#pragma unroll
      for (int f = 0; f < 5; ++f) b[0][f] = lds_rd(rbe + f * 1024);
      sb();
      sfor<0, 14>([&](auto T) {
        constexpr int t = T.v;
        constexpr int ta = t < 5 ? t + 3 : (t == 5 ? -1 : (t == 6 ? 8 : (t + 3 < 14 ? t + 3 : -1)));
        if constexpr (t == 3 || t == 6 || t == 10) { wait_lgkm0(); bar(); }
        if constexpr (t == 6) { if (!__all(fl2 == epoch)) poll0(ws, f3 + 16 * 64, 8, epoch, lane); }
        sfor<0, 20>([&](auto M) {
          constexpr int m = M.v, i = m / 5, f = m % 5;
          if constexpr (t == 0) mma0(acc[i][f], b[0][f], a[0][i]); else mma(acc[i][f], b[t & 1][f], a[t][i]);
          if constexpr (t + 1 < 14 && (m & 1) == 1 && m < 10)
            b[(t + 1) & 1][m >> 1] = lds_rd((g4_late(t + 1) ? rbl : rbe) + g4_slot(t + 1) + (m >> 1) * 1024);
          if constexpr (ta >= 0 && (m & 1) == 0 && m < 8) LDH(ta, m >> 1);
          if constexpr (t == 6 && (m & 1) == 0 && m >= 8 && m < 16) LDH(9, (m - 8) >> 1);
          if constexpr (t == 2 && m == 10) rt = load_res(rr, ro);
          if constexpr (t == 4 && m == 18) fl2 = __hip_atomic_load(flag_ptr(ws, f3 + 16 * 64), RLX_AGENT);
          if constexpr (t == 13 && m >= 8) {
            asm volatile("" : "+v"(acc[(m - 8) / 5][(m - 8) % 5]));
            lds_wrf(rbl + (m - 8) * 1024, acc[(m - 8) / 5][(m - 8) % 5]);
          }
          sb();
        });
        if constexpr (t == 2) {
          wait_vm<15>();
          raise(ws, FL_3 + (L * 32 + 16 + (xcd >> 1) * 4 + (idx >> 3)) * 64 + w * 16 + (xcd & 1) * 8 + (idx & 7), epoch, lane);
          ST(sbase + 12);
        }
        if constexpr (t == 0) ST(sbase + 14);
        if constexpr (t == 3) ST(45 + L * 4);
        if constexpr (t == 6) ST(46 + L * 4);
      });
      ST(sbase + 15);
#pragma unroll
      for (int f = 2; f < 5; ++f) { asm volatile("" : "+v"(acc[2][f])); lds_wrf(rbl + (10 + f) * 1024, acc[2][f]); }
      f32x4 (&v)[5] = acc[3];
      fence_acc(v);
      wait_lgkm0();
      bar();
#pragma unroll
      for (int s = 1; s < 4; ++s) {
        const unsigned src = lb + G4_LATE + ((w + s) & 3) * 15360 + (3 - s) * 5 * 1024 + lane * 16;
#pragma unroll
        for (int f = 0; f < 5; ++f) v[f] += lds_rdf(src + f * 1024);
      }
      finish_tile(v, rt, L ? make_rsrc(opq_p(out_), SZ_X) : make_rsrc(ws + WS_X2, SZ_X), oo, L == 0, make_rsrc(ws + WS_ST2, SZ_ST),
                  q == 0 ? (c8 * S_ + row) * 8 : OOB);
      ST(sbase + 16);
      if (L == 0) publish(ws, FL_4 + (rt64 >> 1) * 64 + ((rt64 & 1) * 4 + w) * 8 + c8, epoch, lane);
    }
  }
  if (blockIdx.x == 0 && w0 == 0) raise(ws_, FL_E, epoch, tid0);
  ST(37);
}

extern "C" void kernel_launch(void* const* d_in, const int* in_sizes, int n_in, void* d_out, int out_size, void* d_ws,
                              size_t ws_size, hipStream_t stream) {
  char* ws = (char*)d_ws;
  (void)hipFuncSetAttribute((const void*)k_all, hipFuncAttributeMaxDynamicSharedMemorySize, KM_LDS);
  k_all<<<256, 512, KM_LDS, stream>>>((const u16*)d_in[0], (const u16*)d_in[1], (const u16*)d_in[2], (const u16*)d_in[3],
                                     (const u16*)d_in[4], (const u16*)d_in[6], (const u16*)d_in[7], ws, (u16*)d_out STAMP_PASS(0));
}
```

```cpp
#include <hip/hip_runtime.h>
#include <hip/hip_bf16.h>
#include <stdint.h>

#define DI __device__ __forceinline__
typedef unsigned short u16;
typedef __attribute__((ext_vector_type(8))) __bf16 bf16x8;
typedef __attribute__((ext_vector_type(2))) __bf16 bf16x2;
typedef __attribute__((ext_vector_type(4))) float f32x4;
typedef __attribute__((ext_vector_type(2))) float f32x2;
typedef __attribute__((ext_vector_type(4))) unsigned u32x4;
typedef __attribute__((ext_vector_type(2))) unsigned u32x2;
typedef __attribute__((ext_vector_type(4))) short s16x4;
#define LDS_AS __attribute__((address_space(3)))
#define GLB_AS __attribute__((address_space(1)))
typedef LDS_AS char* ldsp;
typedef GLB_AS unsigned gu32;
typedef unsigned long long u64;
typedef GLB_AS u64 gu64;
typedef __amdgpu_buffer_rsrc_t rsrc_t;
#define RLX_AGENT __ATOMIC_RELAXED, __HIP_MEMORY_SCOPE_AGENT

constexpr int S_ = 2048, D_ = 640, F_ = 1728;
constexpr float EPS = 1e-5f;
constexpr unsigned OOB = 0x7f000000u;

constexpr unsigned SZ_AB = 8 * 128 * 5 * 1024;
constexpr unsigned SZ_X = 128 * 20 * 1024;
constexpr unsigned SZ_H = 128 * 54 * 1024;
constexpr unsigned SZ_W1 = 16 * 140 * 1024;
constexpr unsigned SZ_W2 = 8 * 54 * 5 * 1024;
constexpr unsigned SZ_ST = 2048 * 64;
constexpr unsigned WS_AB1 = 0;
constexpr unsigned WS_AB2 = WS_AB1 + SZ_AB;
constexpr unsigned WS_X1 = WS_AB2 + SZ_AB;
constexpr unsigned WS_X2 = WS_X1 + SZ_X;
constexpr unsigned WS_X3 = WS_X2 + SZ_X;
constexpr unsigned WS_H1 = WS_X3 + SZ_X;
constexpr unsigned WS_H2 = WS_H1 + SZ_H;
constexpr unsigned WS_W1 = WS_H2 + SZ_H;
constexpr unsigned WS_W2 = WS_W1 + SZ_W1;
constexpr unsigned WS_ST1 = WS_W2 + SZ_W2;
constexpr unsigned WS_ST2 = WS_ST1 + SZ_ST;
constexpr unsigned WS_ST3 = WS_ST2 + SZ_ST;
constexpr unsigned WS_CS1 = WS_ST3 + SZ_ST;
constexpr unsigned WS_CSD = WS_CS1 + 20 * 1728 * 4;
constexpr unsigned XSLOT = 2560;
constexpr unsigned SZ_XP = 256 * 12 * XSLOT;
constexpr unsigned WS_XP = (WS_CSD + 256 * 320 + 4095) & ~4095u;
constexpr unsigned WS_FLG = WS_XP + 2 * SZ_XP;
constexpr unsigned FL_X = 0;
constexpr unsigned FL_2 = FL_X + 2048;
constexpr unsigned FL_3 = FL_2 + 2048;
constexpr unsigned FL_4 = FL_3 + 4096;
constexpr unsigned FL_5 = FL_4 + 1024;
constexpr unsigned FL_W = FL_5 + 2048;
constexpr unsigned FL_E = FL_W + 1088;
constexpr unsigned WS_END = WS_FLG + (FL_E + 1) * 128;

DI u32x4 make_srd(const void* p, unsigned bytes) {
  uint64_t a = (uint64_t)p;
  u32x4 r;
  r[0] = (unsigned)a; r[1] = (unsigned)(a >> 32) & 0xffffu; r[2] = bytes; r[3] = 0x00020000u;
  return r;
}
DI rsrc_t make_rsrc(const void* p, unsigned bytes) {
  return __builtin_amdgcn_make_buffer_rsrc((void*)p, (short)0, (int)bytes, 0x00020000);
}
template <int LOFF, int GOFF>
DI void glds(u32x4 srd, unsigned voff, unsigned ldsb, unsigned soff) {
  unsigned tmp;
  asm volatile("s_add_u32 m0, %1, %2\n\ts_add_u32 %0, %3, %4\n\tbuffer_load_dwordx4 %5, %6, %0 offen lds"
               : "=&s"(tmp)
               : "s"(ldsb), "i"(LOFF), "s"(soff), "i"(GOFF), "v"(voff), "s"(srd)
               : "memory", "scc");
}
template <int N> DI void glds_lin(u32x4 srd, unsigned voff, unsigned ldsb, unsigned soff) {
  if constexpr (N == 4)
    asm volatile("s_mov_b32 m0, %0\n\ts_nop 0\n\tbuffer_load_dwordx4 %1, %2, %3 offen lds\n\tbuffer_load_dwordx4 %1, %2, %3 offen offset:1024 lds\n\t"
                 "buffer_load_dwordx4 %1, %2, %3 offen offset:2048 lds\n\tbuffer_load_dwordx4 %1, %2, %3 offen offset:3072 lds"
                 :: "s"(ldsb), "v"(voff), "s"(srd), "s"(soff) : "memory");
  else if constexpr (N == 3)
    asm volatile("s_mov_b32 m0, %0\n\ts_nop 0\n\tbuffer_load_dwordx4 %1, %2, %3 offen lds\n\tbuffer_load_dwordx4 %1, %2, %3 offen offset:1024 lds\n\t"
                 "buffer_load_dwordx4 %1, %2, %3 offen offset:2048 lds"
                 :: "s"(ldsb), "v"(voff), "s"(srd), "s"(soff) : "memory");
  else
    asm volatile("s_mov_b32 m0, %0\n\ts_nop 0\n\tbuffer_load_dwordx4 %1, %2, %3 offen lds"
                 :: "s"(ldsb), "v"(voff), "s"(srd), "s"(soff) : "memory");
}
template <int IMM> DI void ldv(bf16x8& d, u32x4 srd, unsigned voff, unsigned soff) {
  asm volatile("buffer_load_dwordx4 %0, %1, %2, %3 offen offset:%4 sc1" : "=v"(d) : "v"(voff), "s"(srd), "s"(soff), "i"(IMM) : "memory");
}
template <int N> DI void wait_vm() { asm volatile("s_waitcnt vmcnt(%0)" ::"i"(N) : "memory"); }
template <int N> DI void wait_vm5(bf16x8 (&b)[5]) {
  asm volatile("s_waitcnt vmcnt(%5)" : "+v"(b[0]), "+v"(b[1]), "+v"(b[2]), "+v"(b[3]), "+v"(b[4]) : "i"(N) : "memory");
}
DI void wait_lgkm0() { asm volatile("s_waitcnt lgkmcnt(0)" ::: "memory"); }
DI void sb() { __builtin_amdgcn_sched_barrier(0); }
DI void bar() { asm volatile("s_barrier" ::: "memory"); }

DI bf16x8 lds_rd(unsigned addr) { return *(const LDS_AS bf16x8*)(ldsp)(uintptr_t)addr; }
DI f32x4 lds_rdf(unsigned addr) { return *(const LDS_AS f32x4*)(ldsp)(uintptr_t)addr; }
DI void lds_wrf(unsigned addr, f32x4 v) { *(LDS_AS f32x4*)(ldsp)(uintptr_t)addr = v; }
DI float lds_rd1(unsigned addr) { return *(const LDS_AS float*)(ldsp)(uintptr_t)addr; }
DI void lds_wr1(unsigned addr, float v) { *(LDS_AS float*)(ldsp)(uintptr_t)addr = v; }
DI f32x4 mfma(bf16x8 a, bf16x8 b, f32x4 c) { return __builtin_amdgcn_mfma_f32_16x16x32_bf16(a, b, c, 0, 0, 0); }
DI void mma(f32x4& c, const bf16x8& a, const bf16x8& b) {
  asm volatile("v_mfma_f32_16x16x32_bf16 %0, %1, %2, %0" : "+v"(c) : "v"(a), "v"(b));
}
DI void mma0(f32x4& c, const bf16x8& a, const bf16x8& b) {
  asm volatile("v_mfma_f32_16x16x32_bf16 %0, %1, %2, 0" : "=&v"(c) : "v"(a), "v"(b));
}
template <int N> DI void fence_acc(f32x4 (&a)[N]) {
  if constexpr (N == 5) asm volatile("s_nop 7\n\ts_nop 1" : "+v"(a[0]), "+v"(a[1]), "+v"(a[2]), "+v"(a[3]), "+v"(a[4]));
  else asm volatile("s_nop 7\n\ts_nop 1" : "+v"(a[0]), "+v"(a[1]), "+v"(a[2]), "+v"(a[3]), "+v"(a[4]), "+v"(a[5]), "+v"(a[6]));
}
template <int N> DI void pin_acc(f32x4 (&a)[N]) {
  if constexpr (N == 5) asm volatile("" : "+v"(a[0]), "+v"(a[1]), "+v"(a[2]), "+v"(a[3]), "+v"(a[4]));
  else asm volatile("" : "+v"(a[0]), "+v"(a[1]), "+v"(a[2]), "+v"(a[3]), "+v"(a[4]), "+v"(a[5]), "+v"(a[6]));
}
template <int M, int N> DI void fence_all(f32x4 (&a)[M][N]) {
#pragma unroll
  for (int i = 1; i < M; ++i) pin_acc(a[i]);
  fence_acc(a[0]);
#pragma unroll
  for (int i = 1; i < M; ++i) pin_acc(a[i]);
}
DI unsigned pk2(float a, float b) {
  f32x2 v = {a, b};
  return __builtin_bit_cast(unsigned, __builtin_convertvector(v, bf16x2));
}
DI float bflo(unsigned u) { return __uint_as_float(u << 16); }
DI float bfhi(unsigned u) { return __uint_as_float(u & 0xffff0000u); }
DI float dot2(unsigned a, unsigned b, float c) {
  return __builtin_amdgcn_fdot2_f32_bf16(__builtin_bit_cast(bf16x2, a), __builtin_bit_cast(bf16x2, b), c, false);
}
DI float xsum16_32(float x) {
  auto s = __builtin_amdgcn_permlane16_swap(__float_as_uint(x), __float_as_uint(x), false, false);
  x = __uint_as_float(s[0]) + __uint_as_float(s[1]);
  auto t = __builtin_amdgcn_permlane32_swap(__float_as_uint(x), __float_as_uint(x), false, false);
  return __uint_as_float(t[0]) + __uint_as_float(t[1]);
}
template <int V> struct IC { static constexpr int v = V; };
template <int I, int N, class Fn> DI void sfor(Fn&& f) {
  if constexpr (I < N) { f(IC<I>{}); sfor<I + 1, N>(f); }
}
DI unsigned rfl(unsigned v) { return __builtin_amdgcn_readfirstlane(v); }
DI unsigned opq_v(unsigned x) { asm volatile("" : "+v"(x)); return x; }
DI unsigned opq_s(unsigned x) { asm volatile("" : "+s"(x)); return x; }
template <class T> DI T* opq_p(T* x) { asm volatile("" : "+s"(x)); return x; }
DI bf16x8 ld16(rsrc_t r, unsigned voff, unsigned soff) { return __builtin_bit_cast(bf16x8, __builtin_amdgcn_raw_buffer_load_b128(r, voff, soff, 16)); }
DI u32x4 ld16u(rsrc_t r, unsigned voff, unsigned soff) { return __builtin_amdgcn_raw_buffer_load_b128(r, voff, soff, 16); }
DI u32x2 ld8u(rsrc_t r, unsigned voff, unsigned soff) { return __builtin_amdgcn_raw_buffer_load_b64(r, voff, soff, 16); }
DI void st16(u32x4 v, rsrc_t r, unsigned voff, unsigned soff) {
  __builtin_amdgcn_raw_buffer_store_b128(v, r, voff, soff, 16);
  asm volatile("s_nop 1" :: "v"(v) : "memory");
}
DI void st16_raw(u32x4 v, rsrc_t r, unsigned voff, unsigned soff) { __builtin_amdgcn_raw_buffer_store_b128(v, r, voff, soff, 16); }
template <class... T> DI void st_guard(const T&... v) {
  (..., [](const auto& x) { asm volatile("" :: "v"(x) : "memory"); }(v));
  asm volatile("s_nop 1" ::: "memory");
  (..., [](const auto& x) { asm volatile("" :: "v"(x)); }(v));
}
DI void st8(u32x2 v, rsrc_t r, unsigned voff, unsigned soff) { __builtin_amdgcn_raw_buffer_store_b64(v, r, voff, soff, 16); }
DI void st4(unsigned v, rsrc_t r, unsigned voff, unsigned soff) { __builtin_amdgcn_raw_buffer_store_b32(v, r, voff, soff, 16); }

#define ST(i)
#define STAMP_ARGS
#define STAMP_FWD
#define STAMP_PASS(k)

template <int KS> DI unsigned foff(unsigned row, unsigned col) {
  return ((row >> 4) * KS + (col >> 5)) * 1024u + (((col >> 3) & 3) * 16u + (row & 15)) * 16u + (col & 7) * 2u;
}

extern __shared__ __attribute__((aligned(1024))) char smem[];
DI unsigned lds_base() { return (unsigned)(uintptr_t)(ldsp)smem; }

DI void ln_coef(float s1, float s2, float& mu, float& rs) {
  mu = s1 * (1.f / D_);
  rs = __builtin_amdgcn_rsqf(fmaxf(s2 * (1.f / D_) - mu * mu, 0.f) + EPS);
}

DI void g1_store(const f32x4 (&acc)[2][5], const float (&mu)[2][4], const float (&rs)[2][4], const float (&csv)[5], rsrc_t rab,
                 unsigned P, unsigned ct, unsigned w, unsigned i16, unsigned q) {
  const unsigned kstep = (ct >> 3) * 64 + P * 4 + w;
  const unsigned cq = ((q & 1) << 1) | (q >> 1);
  const unsigned so = ((ct & 7) * 128 + kstep) * 5 * 1024;
  const unsigned vo = (cq * 16 + i16) * 16;
  u32x4 y[5];
#pragma unroll
  for (int f = 0; f < 5; ++f) {
    const f32x2 cs = {csv[f], csv[f]};
    unsigned pk[2][2];
#pragma unroll
    for (int r = 0; r < 2; ++r)
#pragma unroll
      for (int e = 0; e < 2; ++e) {
        const f32x2 t = __builtin_elementwise_fma(f32x2{mu[r][2 * e], mu[r][2 * e + 1]}, cs, f32x2{acc[r][f][2 * e], acc[r][f][2 * e + 1]}) *
                        f32x2{rs[r][2 * e], rs[r][2 * e + 1]};
        pk[r][e] = __builtin_bit_cast(unsigned, __builtin_convertvector(t, bf16x2));
      }
    auto t0 = __builtin_amdgcn_permlane16_swap(pk[0][0], pk[1][0], false, false);
    auto t1 = __builtin_amdgcn_permlane16_swap(pk[0][1], pk[1][1], false, false);
    y[f] = u32x4{t0[0], t1[0], t0[1], t1[1]};
    st16_raw(y[f], rab, vo + (f & 3) * 1024, so + (f >> 2) * 4096);
  }
  st_guard(y[0], y[1], y[2], y[3], y[4]);
}

constexpr int K1_STAGE = 26 * 1024;
constexpr int K1_TR = 5 * K1_STAGE;
constexpr int K1_CS = K1_TR + 5 * 4096;

DI void k1_issue(u32x4 sa, u32x4 sbd, unsigned vo, unsigned la, unsigned lbb, unsigned soa, unsigned sob, unsigned w) {
  glds<0, 0>(sa, vo, la, soa);
  glds<1024, 8 * 1280>(sa, vo, la, soa);
  glds<2048, 16 * 1280>(sa, vo, la, soa);
  glds<3072, 24 * 1280>(sa, vo, la, soa);
  glds<0, 0>(sbd, vo, lbb, sob);
  glds<4096, 32 * 1280>(sbd, vo, lbb, sob);
  if (w < 2) glds<8192, 64 * 1280>(sbd, vo, lbb, sob);
}
struct RtUnit { bool first; unsigned ks, g; };
DI RtUnit rt_unit(unsigned u) {
  RtUnit r;
  r.first = u < 540;
  if (r.first) { r.ks = u / 27u; r.g = u - r.ks * 27u; }
  else { const unsigned v = u - 540; r.ks = v / 10u; r.g = v - r.ks * 10u; }
  return r;
}
DI void retile_issue(unsigned u, const u16* w1, const u16* w2, unsigned ldst, unsigned lane) {
  const RtUnit t = rt_unit(u);
  const unsigned ld = t.first ? F_ * 2 : D_ * 2;
  u32x4 srd = make_srd(t.first ? w1 : w2, D_ * F_ * 2);
  const unsigned vo = (lane >> 3) * ld + (lane & 7) * 16;
  const unsigned so = t.ks * 32 * ld + t.g * 128;
  glds<0, 0>(srd, vo, ldst, so);
  glds<1024, 0>(srd, vo, ldst, so + 8 * ld);
  glds<2048, 0>(srd, vo, ldst, so + 16 * ld);
  glds<3072, 0>(srd, vo, ldst, so + 24 * ld);
}
DI unsigned w1_off(unsigned rt, unsigned ks) {
  const unsigned c = rt / 7u, f = rt - c * 7, g = (c & 1) ? (f == 0 ? 6 : f - 1) : f;
  return WS_W1 + (c * 140 + (g < 4 ? ks * 4 + g : 80 + ks * 3 + g - 4)) * 1024;
}
DI void retile_finish(unsigned u, unsigned f0, rsrc_t rws, unsigned ldst, unsigned lane) {
  const RtUnit t = rt_unit(u);
  const unsigned i = lane & 15, q = lane >> 4;
  const unsigned base = ldst + (8 * q + (i >> 2)) * 128 + (i & 3) * 8 + f0 * 32;
  u32x4 v[2];
#pragma unroll
  for (int f = 0; f < 2; ++f) {
    s16x4 lo = __builtin_amdgcn_ds_read_tr16_b64_v4i16((LDS_AS s16x4*)(uintptr_t)(base + f * 32));
    s16x4 hi = __builtin_amdgcn_ds_read_tr16_b64_v4i16((LDS_AS s16x4*)(uintptr_t)(base + f * 32 + 512));
    u32x2 l2 = __builtin_bit_cast(u32x2, lo), h2 = __builtin_bit_cast(u32x2, hi);
    v[f] = u32x4{l2[0], l2[1], h2[0], h2[1]};
  }
  const unsigned rt = t.g * 4 + f0;
  if (t.first) {
    st16(v[0], rws, lane * 16, w1_off(rt, t.ks));
    st16(v[1], rws, lane * 16, w1_off(rt + 1, t.ks));
    float s[2];
#pragma unroll
    for (int f = 0; f < 2; ++f)
      s[f] = xsum16_32(((bflo(v[f][0]) + bfhi(v[f][0])) + (bflo(v[f][1]) + bfhi(v[f][1]))) + ((bflo(v[f][2]) + bfhi(v[f][2])) + (bflo(v[f][3]) + bfhi(v[f][3]))));
    st4(__float_as_uint(q == 0 ? s[0] : s[1]), rws, lane < 32 ? lane * 4 : OOB, WS_CS1 + (t.ks * F_ + rt * 16) * 4);
  } else {
    const unsigned c0 = rt / 5u, c1 = (rt + 1) / 5u;
    st16(v[0], rws, lane * 16, WS_W2 + ((c0 * 54 + t.ks) * 5 + (rt - c0 * 5)) * 1024);
    st16(v[1], rws, lane * 16, WS_W2 + ((c1 * 54 + t.ks) * 5 + (rt + 1 - c1 * 5)) * 1024);
  }
}

#define ASLEEP 0
#define W2SLEEP 0
#define WDSLEEP 0
#define WARM_X 0
#define WARM_W 0
#define WARM_H 0
#define WARM_S 0
#define WARM_0 0
#define WS1 3
#define WS2 6
#define ROT1 1
#define ROT2 0
#define ROT3 0
#define ROT4 1
#define ROT5 0
#define PUB1 13
#define LSLEEP 20
#define RGAP 10
#define RSLEEP 0
#define ASLEEP2 40
#define RDPAT 1
#define LAZY5 0
#define LAZY2 0
#define WDPRE 6
#define CODEWARM 1
#define OUTPLAIN 1
#define PDELAY 18
#define WSLEEP 52
#define SSLEEP 46
constexpr int KM_LDS = 160 * 1024;
constexpr int KM_CS1 = KM_LDS - 1024;
constexpr int G2_A = 80 * 1024, G2_AS = 16 * 1024, G2_BS = 20 * 1024;
constexpr int G4_LATE = 80 * 1024;

DI gu64* flag_ptr(char* ws, unsigned f) { return (gu64*)(ws + WS_FLG + f * 128); }
DI void raise(char* ws, unsigned f, u64 epoch, unsigned lane) {
  if (lane < 16) __hip_atomic_store(flag_ptr(ws, f) + lane, epoch, RLX_AGENT);
}
DI void publish(char* ws, unsigned f, u64 epoch, unsigned lane) {
  wait_vm<0>();
  raise(ws, f, epoch, lane);
}
#define PD4 13
#define PDX 15
#define PD2 30
#define G4INIT 2
#define B0N 2
#define P3DELAY 0
#define P5DELAY0 4
#define P5DELAY 44
#define ONEPOLL 0
#define PSL 1
DI void poll0(char* ws, unsigned f, unsigned n, u64 epoch, unsigned lane) {
  gu64* p = flag_ptr(ws, f);
  bool ok = lane >= n;
  u64 look = ~0ull;
#pragma unroll 1
  for (unsigned s = 0; s < (1u << 16); ++s) {
    if (!ok && ((look >> lane) & 1)) ok = __hip_atomic_load(p, RLX_AGENT) == epoch;
    const u64 waiting = __ballot(!ok);
    if (waiting == 0) break;
    if (ONEPOLL) look = (waiting & look) ? (waiting & look & -(waiting & look)) : ~0ull;
    if (!ONEPOLL || look != ~0ull) __builtin_amdgcn_s_sleep(PSL);
  }
  asm volatile("" ::: "memory");
}
template <int DELAY> DI void poll(char* ws, unsigned f, unsigned n, u64 epoch, unsigned lane) {
  __builtin_amdgcn_s_sleep(DELAY);
  poll0(ws, f, n, epoch, lane);
}
DI void warm(unsigned ldump, u32x4 srd, unsigned voff, unsigned soff) {
  asm volatile("s_mov_b32 m0, %0\n\ts_nop 0\n\tbuffer_load_dword %1, %2, %3 offen lds" :: "s"(ldump), "v"(voff), "s"(srd), "s"(soff) : "memory");
}
template <int N> DI void glds_run(u32x4 srd, unsigned voff, unsigned l, unsigned so) {
#pragma unroll
  for (int g = 0; g < N / 4; ++g) glds_lin<4>(srd, voff, l + g * 4096, so + g * 4096);
  if constexpr (N % 4 == 3) glds_lin<3>(srd, voff, l + (N / 4) * 4096, so + (N / 4) * 4096);
  else {
#pragma unroll
    for (int g = N / 4 * 4; g < N; ++g) glds_lin<1>(srd, voff, l + g * 1024, so + g * 1024);
  }
}
DI void glds4_sc1(u32x4 srd, unsigned voff, unsigned l, unsigned so) {
  asm volatile("s_mov_b32 m0, %0\n\ts_nop 0\n\tbuffer_load_dwordx4 %1, %2, %3 offen sc1 lds\n\tbuffer_load_dwordx4 %1, %2, %3 offen offset:1024 sc1 lds\n\t"
               "buffer_load_dwordx4 %1, %2, %3 offen offset:2048 sc1 lds\n\tbuffer_load_dwordx4 %1, %2, %3 offen offset:3072 sc1 lds"
               :: "s"(l), "v"(voff), "s"(srd), "s"(so) : "memory");
}
DI void glds1_sc1(u32x4 srd, unsigned voff, unsigned l, unsigned so) {
  asm volatile("s_mov_b32 m0, %0\n\ts_nop 0\n\tbuffer_load_dwordx4 %1, %2, %3 offen sc1 lds" :: "s"(l), "v"(voff), "s"(srd), "s"(so) : "memory");
}
template <int N> DI void glds_run_sc1(u32x4 srd, unsigned voff, unsigned l, unsigned so) {
#pragma unroll
  for (int g = 0; g < N / 4; ++g) glds4_sc1(srd, voff, l + g * 4096, so + g * 4096);
#pragma unroll
  for (int g = N / 4 * 4; g < N; ++g) glds1_sc1(srd, voff, l + g * 1024, so + g * 1024);
}

struct ResTile { u32x4 r0, r1; u32x2 r2; };
struct TileOff { unsigned a0, a1, a2; };
DI TileOff tile_offsets(bool rm, unsigned row, unsigned ct, unsigned q) {
  const unsigned cA = ct * 80 + 16 * (q & 1) + 8 * (q >> 1), cS = ct * 80 + 64 + 4 * q;
  TileOff t;
  if (rm) { t.a0 = (row * D_ + cA) * 2; t.a1 = t.a0 + 64; t.a2 = (row * D_ + cS) * 2; }
  else { t.a0 = foff<20>(row, cA); t.a1 = foff<20>(row, cA + 32); t.a2 = foff<20>(row, cS); }
  asm volatile("" : "+v"(t.a0), "+v"(t.a1), "+v"(t.a2));
  return t;
}
DI ResTile load_res(rsrc_t rr, const TileOff& t) {
  ResTile rt;
  rt.r0 = ld16u(rr, t.a0, 0); rt.r1 = ld16u(rr, t.a1, 0); rt.r2 = ld8u(rr, t.a2, 0);
  return rt;
}
DI void finish_tile(f32x4 (&v)[5], const ResTile& rt, rsrc_t rout, const TileOff& o, bool stats, rsrc_t rst, unsigned sto) {
#pragma unroll
  for (int p = 0; p < 2; ++p)
#pragma unroll
    for (int r = 0; r < 4; ++r) {
      auto t = __builtin_amdgcn_permlane16_swap(__float_as_uint(v[2 * p][r]), __float_as_uint(v[2 * p + 1][r]), false, false);
      v[2 * p][r] = __uint_as_float(t[0]); v[2 * p + 1][r] = __uint_as_float(t[1]);
    }
  u32x4 y0, y1; u32x2 y2;
#pragma unroll
  for (int j = 0; j < 2; ++j) {
    y0[j] = pk2(v[0][2 * j] + bflo(rt.r0[j]), v[0][2 * j + 1] + bfhi(rt.r0[j]));
    y0[2 + j] = pk2(v[1][2 * j] + bflo(rt.r0[2 + j]), v[1][2 * j + 1] + bfhi(rt.r0[2 + j]));
    y1[j] = pk2(v[2][2 * j] + bflo(rt.r1[j]), v[2][2 * j + 1] + bfhi(rt.r1[j]));
    y1[2 + j] = pk2(v[3][2 * j] + bflo(rt.r1[2 + j]), v[3][2 * j + 1] + bfhi(rt.r1[2 + j]));
    y2[j] = pk2(v[4][2 * j] + bflo(rt.r2[j]), v[4][2 * j + 1] + bfhi(rt.r2[j]));
  }
  if (stats || !OUTPLAIN) {
    st16_raw(y0, rout, o.a0, 0); st16_raw(y1, rout, o.a1, 0); st8(y2, rout, o.a2, 0);
  } else {
    __builtin_amdgcn_raw_buffer_store_b128(y0, rout, o.a0, 0, 0);
    __builtin_amdgcn_raw_buffer_store_b128(y1, rout, o.a1, 0, 0);
    __builtin_amdgcn_raw_buffer_store_b64(y2, rout, o.a2, 0, 0);
  }
  st_guard(y0, y1);
  if (stats) {
    float s1a = 0.f, s1b = 0.f, s2a = 0.f, s2b = 0.f;
#pragma unroll
    for (int j = 0; j < 4; ++j) {
      s1a = dot2(y0[j], 0x3f803f80u, s1a); s2a = dot2(y0[j], y0[j], s2a);
      s1b = dot2(y1[j], 0x3f803f80u, s1b); s2b = dot2(y1[j], y1[j], s2b);
    }
    s1a = dot2(y2[0], 0x3f803f80u, s1a); s2a = dot2(y2[0], y2[0], s2a);
    s1b = dot2(y2[1], 0x3f803f80u, s1b); s2b = dot2(y2[1], y2[1], s2b);
    const float s1 = xsum16_32(s1a + s1b), s2 = xsum16_32(s2a + s2b);
    st8(u32x2{__float_as_uint(s1), __float_as_uint(s2)}, rst, sto, 0);
  }
}

DI constexpr unsigned wd_pos(int c) { return c < 8 ? 10 * c : 60 + 10 * c; }
DI constexpr int a_when(int k) { return k < 3 ? -1 : (k < 17 ? (k - 3) / 2 : k - 10); }

constexpr int ORD[14] = {0, 1, 4, 5, 7, 8, 11, 12, 2, 3, 6, 9, 10, 13};
DI constexpr int g4_late(int t) { return (t >= 4 && t < 7) || t >= 11; }
DI constexpr int g4_slot(int t) { return (t < 4 ? t : (t < 7 ? t - 4 : (t < 11 ? t - 7 : t - 11))) * 5120; }

#define PHASE_IDS \
  const unsigned lane = opq_v(tid0) & 63, w = opq_s(w0), bid = opq_s(blockIdx.x); \
  const unsigned xcd = bid & 7, idx = bid >> 3, i16 = lane & 15, q = lane >> 4; \
  char* const ws = opq_p(ws_); \
  (void)xcd; (void)idx; (void)i16; (void)q;
#define IDS_G2 const unsigned kq = xcd & 3, P = (xcd >> 2) * 8 + (idx >> 2), c4 = idx & 3;
#define G2_ROT 0u
#define G2_LA(h) (lb + G2_A + ((w >> 1) * 4 + ((2 * (w & 1) + (h) + 3 - kq) & 3)) * 2048)
#define IDS_G3 const unsigned P = (xcd >> 1) * 4 + (idx >> 3), ct = (xcd & 1) * 8 + (idx & 7);
#define G4PAIR 1
#define C8_OF (G4PAIR ? (xcd & 1) * 4 + (idx & 3) : idx & 7)
#define IDS_G4 const unsigned rt64 = G4PAIR ? (xcd >> 1) * 8 + (idx >> 2) : xcd * 4 + (idx >> 3), c8 = C8_OF;
#define WARM_STRIP(rx, sox) \
  ({ const unsigned n_ = (idx & 7) * 40 + lane; \
     __builtin_amdgcn_raw_buffer_load_b32(rx, lane < 40 ? (((n_ >> 3) & 1) * 20 + (n_ >> 4)) * 1024 + (n_ & 7) * 128 : OOB, sox, 16); })
#define KROT(k) ((k) + rot >= 20 ? (k) + rot - 20 : (k) + rot)
#define LDX(r, k) ld16(rx, lane * 16, sox + (r) * 20480 + KROT(k) * 1024)

__global__ __launch_bounds__(512, 2) void k_all(const u16* x0_, const u16* Sre_, const u16* Sim_, const u16* Dre_, const u16* Dim_,
                                               const u16* w1_, const u16* w2_, char* ws_, u16* out_ STAMP_ARGS) {
  ST(0);
  const unsigned tid0 = threadIdx.x, wall = rfl(tid0 >> 6), w0 = wall & 3;
  const unsigned lb = lds_base();
  const char* code; unsigned code_len;
  asm volatile("s_getpc_b64 %0\n.Lcw_begin:\n\ts_mov_b32 %1, .Lcw_end-.Lcw_begin" : "=s"(code), "=s"(code_len));
  u64 epoch;

  if (wall >= 4) {
    const unsigned sink = lb + K1_CS + 1024;
    unsigned ep0, ep1;
    asm volatile("global_load_dword %0, %2, %3 sc1\n\tglobal_load_dword %1, %2, %3 offset:4 sc1"
                 : "=&v"(ep0), "=&v"(ep1) : "v"(WS_FLG + FL_E * 128), "s"(ws_) : "memory");
    {
      PHASE_IDS
      IDS_G3
      const u32x4 sa = make_srd(opq_p(x0_), S_ * D_ * 2), sbd = make_srd(ct < 8 ? opq_p(Dre_) : opq_p(Dim_), D_ * D_ * 2);
      const unsigned vo = (lane >> 3) * 1280 + (((lane & 7) ^ (lane >> 3)) << 4);
      const unsigned soa = (P * 128 + w * 32) * 1280, sob = ((ct & 7) * 80 + w * 8) * 1280;
      const unsigned la = lb + w * 4096, lbb = lb + 16384 + w * 1024;
      const unsigned u0 = bid * 4 + w, u1 = 1024 + bid;
      const bool extra = w == 3 && bid < 56;
      const unsigned rot = ROT1 ? ((idx & 7) + 3 * (idx >> 3)) % 10u : 0;
      auto ko = [&](unsigned s) { const unsigned c = s + rot; return (c >= 10 ? c - 10 : c) * 128; };
      const rsrc_t rws = make_rsrc(ws, WS_END);
      asm volatile("s_nop 4" ::: "memory");
      if (WARM_0) {
        const unsigned r0 = lane / 10u, id = ((idx >> 3) * 4 + w) * 50 + lane, r1 = id / 10u;
        warm(sink, sa, lane < 40 ? r0 * 1280 + (lane - r0 * 10) * 128 : OOB, (P * 128 + (idx & 7) * 16 + w * 4) * 1280);
        warm(sink, sbd, lane < 50 ? r1 * 1280 + (id - r1 * 10) * 128 : OOB, (ct & 7) * 80 * 1280);
      }
      k1_issue(sa, sbd, vo, la, lbb, soa + ko(0), sob + ko(0), w);
      k1_issue(sa, sbd, vo, la + K1_STAGE, lbb + K1_STAGE, soa + ko(1), sob + ko(1), w);
      if (CODEWARM && w == 3) warm(sink, make_srd(code, code_len), lane < 16 ? idx * 2048 + lane * 128 : OOB, 0);
      ST(8);
      wait_vm<6>();
      {
        unsigned lo, hi;
        asm volatile("v_readfirstlane_b32 %0, %2\n\tv_readfirstlane_b32 %1, %3\n\ts_nop 4" : "=s"(lo), "=s"(hi) : "v"(ep0), "v"(ep1));
        epoch = (u64)lo + ((u64)hi << 32) + 1;
      }
      bar();
      k1_issue(sa, sbd, vo, la + 2 * K1_STAGE, lbb + 2 * K1_STAGE, soa + ko(2), sob + ko(2), w);
      const u32x4 ss = make_srd((xcd & 2) ? Sim_ : Sre_, S_ * S_ * 2);
#define WPAT 0
      const unsigned vs = WPAT ? (lane >> 4) * 4096 + (lane & 15) * 128 : (lane & 31) * 4096 + (lane >> 5) * 128;
      const unsigned sos = ((xcd >> 2) * 1024 + idx * 32) * 4096 + (xcd & 1) * 2048 + (WPAT ? w * 8 * 4096 : w * 256);
      unsigned so = 0;
#pragma unroll 1
      for (int i = 0; i < 10; ++i) {
        if (i == 0) wait_vm<6>(); else if (i == 1) wait_vm<2 * 6>(); else if (i < 6) wait_vm<3 * 6>();
        else if (i == 6) wait_vm<2 * 6 + 4>(); else if (i == 7) wait_vm<6 + 4>(); else wait_vm<4 + (WS2 >= 4) + (WS1 >= 4)>();
        bar();
        if (i == 0) {
          k1_issue(sa, sbd, vo, la + 3 * K1_STAGE, lbb + 3 * K1_STAGE, soa + ko(3), sob + ko(3), w);
          k1_issue(sa, sbd, vo, la + 4 * K1_STAGE, lbb + 4 * K1_STAGE, soa + ko(4), sob + ko(4), w);
        } else if (i == 1) {
          k1_issue(sa, sbd, vo, la, lbb, soa + ko(5), sob + ko(5), w);
          k1_issue(sa, sbd, vo, la + K1_STAGE, lbb + K1_STAGE, soa + ko(6), sob + ko(6), w);
        } else if (i < 5) k1_issue(sa, sbd, vo, la + so, lbb + so, soa + ko(i + 5), sob + ko(i + 5), w);
        so = so == 4 * K1_STAGE ? 0 : so + K1_STAGE;
        if (i == WS1) warm(sink, ss, vs, sos);
        if (i == WS2) warm(sink, ss, vs, sos + (WPAT ? 4 * 4096 : 1024));
        if (i == 5) {
          retile_issue(u0, w1_, w2_, lb + K1_TR + w * 4096, lane);
          if (extra) retile_issue(u1, w1_, w2_, lb + K1_TR + 4 * 4096, lane);
        }
      }
      ST(9);
      bar();
    }
    bf16x8 wr[10][5];
#pragma unroll
    for (int t = 0; t < 10; ++t)
#pragma unroll
      for (int f = 0; f < 5; ++f) wr[t][f] = bf16x8{};
#pragma unroll 1
    for (int L = 0; L < 2; ++L) {
      const int sbase = L * 20;
      (void)sbase;
      {
        PHASE_IDS
        IDS_G2
        const u32x4 sa = make_srd(kq < 2 ? opq_p(Sre_) : opq_p(Sim_), S_ * S_ * 2);
        const u32x4 sb_ = make_srd(ws + (L ? WS_AB2 : WS_AB1), SZ_AB);
        const unsigned va = (lane >> 3) * 4096 + (((lane & 7) ^ (lane >> 3)) << 4), vb = lane * 16;
        const unsigned soa = (P * 128 + w * 32) * 4096 + (kq & 1) * 2048;
        const unsigned sob = ((2 * c4 + (w >> 1)) * 128 + kq * 32 + (w & 1)) * 5 * 1024;
        const unsigned la = lb + G2_A + w * 4096, lbb = lb + w * 5120;
        const unsigned la0 = G2_LA(0), la1 = G2_LA(1);
        const unsigned ct = (xcd & 1) * 8 + (idx & 7);
        const unsigned rot = G2_ROT;
        asm volatile("s_nop 4" ::: "memory");
        auto issue_a = [&](unsigned l, unsigned so) {
          glds<0, 0>(sa, va, la0 + l, so); glds<1024, 8 * 4096>(sa, va, la0 + l, so);
          glds<0, 16 * 4096>(sa, va, la1 + l, so); glds<1024, 24 * 4096>(sa, va, la1 + l, so);
        };
        if (L == 0) {
#pragma unroll
          for (int s = 0; s < 3; ++s) issue_a(s * G2_AS, soa + s * 128);
          const unsigned u0 = bid * 4 + w, u1 = 1024 + bid;
          const rsrc_t rws = make_rsrc(ws, WS_END);
          wait_vm<12>();
          retile_finish(u0, 0, rws, lb + K1_TR + w * 4096, lane);
          retile_finish(u0, 2, rws, lb + K1_TR + w * 4096, lane);
          if (w == 3 && bid < 56) {
            retile_finish(u1, 0, rws, lb + K1_TR + 4 * 4096, lane);
            retile_finish(u1, 2, rws, lb + K1_TR + 4 * 4096, lane);
          }
          if (P5DELAY0) __builtin_amdgcn_s_sleep(P5DELAY0);
        } else {
          issue_a(3 * G2_AS, soa + 3 * 128);
          __builtin_amdgcn_s_sleep(P5DELAY);
        }
        ST(sbase + 1);
        poll0(ws, FL_5 + ((L * 4 + kq) * 4 + c4) * 64 + ((i16 >> 1) * 2 + (w >> 1)) * 4 + (w & 1) + 2 * (i16 & 1), 16, epoch, lane);
        wait_vm<0>();
        ST(sbase + 2);
        glds_run_sc1<5>(sb_, vb, lbb, sob + rot * 10240);
        if (B0N == 2) glds_run_sc1<5>(sb_, vb, lbb + G2_BS, sob + ((1 + rot) & 15) * 10240);
        if (L == 0) {
          raise(ws, FL_W + bid * 4 + w, epoch, lane);
          if (w == 3 && bid < 56) raise(ws, FL_W + 1024 + bid, epoch, lane);
          wait_vm<B0N == 2 ? 6 : 1>();
        } else wait_vm<B0N == 2 ? 5 : 0>();
        bar();
        if (B0N == 1) glds_run_sc1<5>(sb_, vb, lbb + G2_BS, sob + ((1 + rot) & 15) * 10240);
        glds_run_sc1<5>(sb_, vb, lbb + 2 * G2_BS, sob + ((2 + rot) & 15) * 10240);
        if (L == 0) issue_a(3 * G2_AS, soa + ((3 + rot) & 15) * 128);
        glds_run_sc1<5>(sb_, vb, lbb + 3 * G2_BS, sob + ((3 + rot) & 15) * 10240);
        ST(sbase + 3);
        const u32x4 sw = make_srd(ws + WS_W1, SZ_W1);
        const unsigned sow = ct * 140 * 1024;
        unsigned fw, fw0 = 0, fw1 = 0;
        {
          const unsigned n = w * 42 + (lane < 42 ? lane : 0), k3 = n / 3u, g3 = (7 * ct >> 2) + (n - k3 * 3), n2 = n - 60;
          fw = FL_W + (n < 60 ? k3 * 27 + (g3 < 26 ? g3 : 26) : 540 + (n2 >> 1) * 10 + (5 * C8_OF >> 2) + (n2 & 1));
        }
#pragma unroll 1
        for (int s = 0; s < 16; ++s) {
          if (s == 0) wait_vm<10>();
          if (s == 1) wait_vm<14>();
          wait_vm<18>();
          if (s == 9 && L == 0) {
            asm volatile("" : "+v"(fw0), "+v"(fw1));
            if (!__all(fw0 == (unsigned)epoch && fw1 == (unsigned)(epoch >> 32))) poll0(ws, fw, 42, epoch, lane);
          }
          bar();
          const unsigned sl = s & 3;
          if (s < 12) {
            issue_a(sl * G2_AS, soa + ((s + 4 + rot) & 15) * 128);
            glds_run_sc1<5>(sb_, vb, lbb + sl * G2_BS, sob + ((s + 4 + rot) & 15) * 10240);
            if (s == 6 && L == 0)
              asm volatile("global_load_dword %0, %2, %3 sc1\n\tglobal_load_dword %1, %2, %3 offset:4 sc1"
                           : "+v"(fw0), "+v"(fw1) : "v"(WS_FLG + fw * 128), "s"(ws) : "memory");
          } else {
            glds_run_sc1<5>(sw, vb, lbb + sl * G2_BS, sow + w * 5120 + sl * G2_BS);
            if (s < 15) glds_run_sc1<4>(sw, vb, la + sl * G2_AS, sow + G2_A + w * 4096 + sl * G2_AS);
            else glds_run_sc1<3>(sw, vb, lb + G2_A + 3 * G2_AS + w * 3072, sow + G2_A + 3 * G2_AS + w * 3072);
          }
        }
        ST(sbase + 4);
      }
      {
        PHASE_IDS
        IDS_G4
        const unsigned ct = (xcd & 1) * 8 + (idx & 7);
        if (w >= 2) {
          const rsrc_t rc = make_rsrc(ws + WS_CS1, 20 * F_ * 4);
          const unsigned c = (w - 2) * 64 + lane, col = ct * 112 + c;
          const unsigned vo = c < 112 && col < F_ ? col * 4 : OOB;
          float csp = 0.f;
#pragma unroll
          for (int k = 0; k < 20; ++k) csp += __uint_as_float(__builtin_amdgcn_raw_buffer_load_b32(rc, vo, k * F_ * 4, 16));
          lds_wr1(lb + KM_CS1 + c * 4, csp);
        }
        const unsigned so = (c8 * 54 + w * 14) * 5 * 1024;
        wait_vm<0>();
        if (L == 0) {
          if (RSLEEP) __builtin_amdgcn_s_sleep(RSLEEP);
          const rsrc_t rw = make_rsrc(ws + WS_W2, SZ_W2);
          const unsigned vl = lane * 16, vz = lane * 16 + (w < 3 ? 0u : OOB);
          sfor<0, 50>([&](auto N) {
            constexpr int t = 4 + N.v / 5, n = ORD[t] * 5 + N.v % 5;
            wr[t - 4][N.v % 5] = ld16(rw, (ORD[t] < 12 ? vl : vz) + (n & 3) * 1024, so + (n >> 2) * 4096);
            if constexpr (RGAP && N.v % 5 == 4 && N.v < 49) { sb(); __builtin_amdgcn_s_sleep(RGAP); sb(); }
          });
          sb();
        }
        wait_lgkm0();
        ST(sbase + 5);
        bar();
        bar();
        const u32x4 s2 = make_srd(ws + WS_W2, SZ_W2);
        const unsigned sg = rfl(so);
        asm volatile("s_nop 4" ::: "memory");
        glds_run_sc1<10>(s2, lane * 16, lb + w * 20480, sg);
        glds_run_sc1<10>(s2, lane * 16, lb + w * 20480 + 10240, sg + 4 * 5120);
        wait_vm<0>();
        ST(sbase + 6);
        const unsigned pe = lb + w * 20480 + lane * 16, pl = lb + G4_LATE + w * 15360 + lane * 16;
        auto put = [&](auto T0, auto T1) {
          sfor<T0.v * 5, T1.v * 5>([&](auto N) {
            constexpr int t = N.v / 5, f = N.v % 5;
            *(LDS_AS bf16x8*)(ldsp)(uintptr_t)((g4_late(t) ? pl : pe) + g4_slot(t) + f * 1024) = wr[t - 4][f];
          });
          wait_lgkm0();
        };
        bar();
        put(IC<4>{}, IC<7>{});
        bar();
        put(IC<7>{}, IC<11>{});
        bar();
        put(IC<11>{}, IC<14>{});
        bar();
      }
      if (L) { bar(); break; }
      {
        PHASE_IDS
        IDS_G3
        const u32x4 sd = make_srd(ct < 8 ? opq_p(Dre_) : opq_p(Dim_), D_ * D_ * 2);
        const unsigned vo = (lane >> 3) * 1280 + (((lane & 7) ^ (lane >> 3)) << 4);
        const unsigned so = (ct & 7) * 80 * 1280;
        sfor<0, 10>([&](auto G) { glds<G.v * 1024, G.v * 8 * 1280>(sd, vo, lb + 20 * w * 1024, so + 2 * w * 128); });
        sfor<0, WDPRE>([&](auto G) { glds<G.v * 1024, G.v * 8 * 1280>(sd, vo, lb + (20 * w + 10) * 1024, so + (2 * w + 1) * 128); });
        bar();
        sfor<WDPRE, 10>([&](auto G) { glds<G.v * 1024, G.v * 8 * 1280>(sd, vo, lb + (20 * w + 10) * 1024, so + (2 * w + 1) * 128); });
        sfor<0, 5>([&](auto G) { glds<G.v * 1024, G.v * 8 * 1280>(sd, vo, lb + (140 + w * 5) * 1024, so + (8 + (w >> 1)) * 128 + (w & 1) * 40 * 1280); });
        wait_vm<0>();
        ST(7);
        bar();
      }
      {
        PHASE_IDS
        IDS_G2
        const u32x4 sa = make_srd(kq < 2 ? opq_p(Sre_) : opq_p(Sim_), S_ * S_ * 2);
        const unsigned va = (lane >> 3) * 4096 + (((lane & 7) ^ (lane >> 3)) << 4);
        const unsigned soa = (P * 128 + w * 32) * 4096 + (kq & 1) * 2048;
        const unsigned rot = G2_ROT;
        __builtin_amdgcn_s_sleep(ASLEEP2);
#pragma unroll
        for (int s = 0; s < 3; ++s) {
          const unsigned l0 = G2_LA(0) + s * G2_AS, l1 = G2_LA(1) + s * G2_AS, so = soa + ((s + rot) & 15) * 128;
          glds<0, 0>(sa, va, l0, so); glds<1024, 8 * 4096>(sa, va, l0, so);
          glds<0, 16 * 4096>(sa, va, l1, so); glds<1024, 24 * 4096>(sa, va, l1, so);
        }
        bar();
      }
    }
    asm volatile(".Lcw_end:");
    return;
  }

  {
    const u64 e = __hip_atomic_load(flag_ptr(ws_, FL_E), RLX_AGENT);
    epoch = (u64)rfl((unsigned)e) + ((u64)rfl((unsigned)(e >> 32)) << 32) + 1;
  }
#pragma unroll 1
  for (int L = 0; L < 2; ++L) {
    const int sbase = L * 20;
    (void)sbase;
    {
      PHASE_IDS
      IDS_G3
      f32x4 acc[2][5];
      float mu[2][4], rs[2][4], csv[5];
      const float sign = ct < 8 ? 1.f : -1.f;
      if (L == 0) {
        const unsigned ra0 = lb + w * 4096 + i16 * 128 + ((q ^ (lane & 7)) << 4);
        const unsigned rb0 = lb + 16384 + i16 * 128 + ((q ^ (lane & 7)) << 4);
        const f32x4 z4 = {0.f, 0.f, 0.f, 0.f};
        f32x4 cs0 = z4, cs1 = z4, rs_[2] = {z4, z4}, rq_[2] = {z4, z4};
#pragma unroll
        for (int r = 0; r < 2; ++r)
#pragma unroll
          for (int f = 0; f < 5; ++f) acc[r][f] = z4;
        bf16x8 fr[2][8];
        bf16x8 one = __builtin_bit_cast(bf16x8, u32x4{0x3f803f80u, 0x3f803f80u, 0x3f803f80u, 0x3f803f80u});
        auto half = [&](auto KK, unsigned so) {
          constexpr int kk = KK.v;
          const unsigned pa = (ra0 ^ ((kk ^ 1) * 64)) + so, pb = (rb0 ^ ((kk ^ 1) * 64)) + so;
          sfor<0, 16>([&](auto M) {
            constexpr int m = M.v;
            if constexpr (m < 10) mma(acc[m & 1][m >> 1], fr[kk][m & 1], fr[kk][2 + (m >> 1)]);
            else if constexpr (m == 10) mma(cs0, one, fr[kk][7]);
            else if constexpr (m == 11) mma(cs1, one, fr[kk][6]);
            else if constexpr (m < 14) mma(rs_[m - 12], fr[kk][m - 12], one);
            else mma(rq_[m - 14], fr[kk][m - 14], fr[kk][m - 14]);
            constexpr int n = m >> 1;
            if constexpr ((m & 1) == 0) fr[kk ^ 1][n] = n < 2 ? lds_rd(pa + n * 2048) : (n < 7 ? lds_rd(pb + (n - 2) * 2048) : lds_rd(pb + w * 2048));
            sb();
          });
        };
        asm volatile("s_nop 1" : "+v"(cs0), "+v"(cs1), "+v"(rs_[0]), "+v"(rs_[1]), "+v"(rq_[0]), "+v"(rq_[1]), "+v"(one));
        fence_all(acc);
        bar();
        ST(38);
        fr[0][0] = lds_rd(ra0); fr[0][1] = lds_rd(ra0 + 2048);
#pragma unroll
        for (int f = 0; f < 5; ++f) fr[0][2 + f] = lds_rd(rb0 + f * 2048);
        fr[0][7] = lds_rd(rb0 + w * 2048);
        sb();
        unsigned so = 0;
#pragma unroll 1
        for (int i = 0; i < 10; ++i) {
          half(IC<0>{}, so);
          wait_lgkm0();
          bar();
          so = so == 4 * K1_STAGE ? 0 : so + K1_STAGE;
          half(IC<1>{}, so);
        }
        fence_all(acc);
        asm volatile("" : "+v"(cs0), "+v"(cs1), "+v"(rs_[0]), "+v"(rs_[1]), "+v"(rq_[0]), "+v"(rq_[1]));
        const unsigned csb = lb + K1_CS;
        if (q == 0) {
          lds_wr1(csb + (w * 16 + i16) * 4, cs0[0]);
          if (w == 0) lds_wr1(csb + (64 + i16) * 4, cs1[0]);
        }
        wait_lgkm0();
        bar();
        ST(18);
#pragma unroll
        for (int f = 0; f < 5; ++f) csv[f] = lds_rd1(csb + (f * 16 + i16) * 4);
#pragma unroll
        for (int r = 0; r < 2; ++r)
#pragma unroll
          for (int j = 0; j < 4; ++j) {
            float m_, r_;
            ln_coef(rs_[r][j], __shfl(rq_[r][j], q * 20 + j), m_, r_);
            mu[r][j] = -m_; rs[r][j] = sign * r_;
          }
        if (w == 0) {
          const rsrc_t rc = make_rsrc(ws + WS_CSD, 256 * 320);
          st4(__float_as_uint(q == 0 ? csv[0] : (q == 1 ? csv[1] : (q == 2 ? csv[2] : csv[3]))), rc, lane * 4, bid * 320);
          st4(__float_as_uint(csv[4]), rc, q == 0 ? 256 + lane * 4 : OOB, bid * 320);
        }
      } else {
        {
          const rsrc_t rc = make_rsrc(ws + WS_CSD, 256 * 320);
#pragma unroll
          for (int f = 0; f < 5; ++f) csv[f] = __uint_as_float(__builtin_amdgcn_raw_buffer_load_b32(rc, (f * 16 + i16) * 4, bid * 320, 16));
        }
        poll<PD4>(ws, FL_4 + P * 64 + w * 16 + i16, 16, epoch, lane);
        ST(17);
        const rsrc_t rx = make_rsrc(ws + WS_X2, SZ_X);
        const unsigned sox = (P * 8 + w * 2) * 20 * 1024;
        const unsigned rot = ROT5 ? 2 * ((idx & 7) + ((idx & 7) >> 2)) : 0;
        auto wdp = [&](int c) { const unsigned cc = c + (rot >> 1) >= 10 ? c + (rot >> 1) - 10 : c + (rot >> 1);
                                return (cc < 8 ? 10 * cc : 60 + 10 * cc) * 1024; };
        bf16x8 a[20][2];
        unsigned wsink = WARM_X ? WARM_STRIP(rx, sox) : 0u;
#pragma unroll
        for (int k = 0; k < 3; ++k) { a[k][0] = LDX(0, k); a[k][1] = LDX(1, k); }
        const rsrc_t rst = make_rsrc(ws + WS_ST2, SZ_ST);
        const unsigned sto = ((lane >> 5) * 4 * S_ + P * 128 + w * 32 + (lane & 31)) * 8;
        const u32x2 sp0 = ld8u(rst, sto, 0), sp1 = ld8u(rst, sto, S_ * 8), sp2 = ld8u(rst, sto, 2 * S_ * 8), sp3 = ld8u(rst, sto, 3 * S_ * 8);
        sb();
        bar();
        bf16x8 b[2][5];
        const unsigned rb = lb + i16 * 128 + ((q ^ (lane & 7)) << 4);
#pragma unroll
        for (int f = 0; f < 5; ++f) b[0][f] = lds_rd(rb + wdp(0) + f * 2048);
        sb();
        sfor<0, 20>([&](auto K) {
          constexpr int k = K.v;
          const unsigned pb = (rb ^ (((k + 1) & 1) * 64)) + wdp(((k + 1) >> 1) % 10);
          sfor<0, 10>([&](auto M) {
            constexpr int f = M.v >> 1, r = M.v & 1;
            if constexpr (k == 0) mma0(acc[r][f], a[0][r], b[0][f]); else mma(acc[r][f], a[k][r], b[k & 1][f]);
            if constexpr (k + 1 < 20 && r == 0) b[(k + 1) & 1][f] = lds_rd(pb + f * 2048);
            if constexpr (r == 1 && f < 4) {
              constexpr int kn = k < 7 ? 2 * k + 3 + (f >> 1) : (f < 2 ? k + 10 : 20);
              if constexpr (kn < 20) a[kn][f & 1] = LDX(f & 1, kn);
            }
            sb();
          });
          if constexpr (k == 0) ST(40);
          if constexpr (k == 4) ST(41);
          if constexpr (k == 9) ST(42);
        });
        asm volatile("" :: "v"(wsink));
        fence_all(acc);
        bar();
        ST(39);
        float s1 = (__uint_as_float(sp0[0]) + __uint_as_float(sp1[0])) + (__uint_as_float(sp2[0]) + __uint_as_float(sp3[0]));
        float s2 = (__uint_as_float(sp0[1]) + __uint_as_float(sp1[1])) + (__uint_as_float(sp2[1]) + __uint_as_float(sp3[1]));
        auto ta = __builtin_amdgcn_permlane32_swap(__float_as_uint(s1), __float_as_uint(s1), false, false);
        auto tb = __builtin_amdgcn_permlane32_swap(__float_as_uint(s2), __float_as_uint(s2), false, false);
        float m, rr;
        ln_coef(__uint_as_float(ta[0]) + __uint_as_float(ta[1]), __uint_as_float(tb[0]) + __uint_as_float(tb[1]), m, rr);
        m = -m; rr *= sign;
#pragma unroll
        for (int r = 0; r < 2; ++r)
#pragma unroll
          for (int e = 0; e < 4; ++e) { mu[r][e] = __shfl(m, r * 16 + q * 4 + e); rs[r][e] = __shfl(rr, r * 16 + q * 4 + e); }
      }
      g1_store(acc, mu, rs, csv, make_rsrc(ws + (L ? WS_AB2 : WS_AB1), SZ_AB), P, ct, w, i16, q);
      ST(L + 19);
      publish(ws, FL_5 + ((L * 4 + (ct >> 3) * 2 + (P >> 3)) * 4 + ((ct & 7) >> 1)) * 64 + ((P & 7) * 2 + (ct & 1)) * 4 + w, epoch, lane);
    }
    {
      PHASE_IDS
      IDS_G2
      const unsigned m = w >> 1, n = w & 1;
      const unsigned ra = lb + G2_A + (m * 64 + i16) * 128 + ((q ^ (lane & 7)) << 4);
      const unsigned rb = lb + n * 10240 + lane * 16;
      const unsigned row = P * 128 + m * 64 + kq * 16 + i16, ct = 2 * c4 + n;
      const TileOff ro = tile_offsets(L == 0, row, ct, q), oo = tile_offsets(false, row, ct, q);
      const rsrc_t rxp = make_rsrc(ws + WS_XP + L * SZ_XP, SZ_XP);
      const unsigned xo = ((P * 4 + c4) * 48 + w) * XSLOT;
      f32x4 acc[4][5];
#pragma unroll
      for (int i = 0; i < 4; ++i)
#pragma unroll
        for (int f = 0; f < 5; ++f) acc[i][f] = f32x4{0.f, 0.f, 0.f, 0.f};
      bf16x8 fa[2][4], fb[2][5];
      unsigned ra1 = ra ^ 64, rb2 = rb + 2 * G2_BS;
      asm volatile("" : "+v"(ra1), "+v"(rb2));
      auto half = [&](auto KK, auto SL) {
        constexpr int kk = KK.v, sl = SL.v;
        sfor<0, 20>([&](auto M) {
          constexpr int mm = M.v;
          mma(acc[mm / 5][mm % 5], fb[kk][mm % 5], fa[kk][mm / 5]);
          if constexpr (mm < 5) fb[kk ^ 1][mm] = lds_rd((sl < 2 ? rb : rb2) + (sl & 1) * G2_BS + (kk ^ 1) * 5120 + mm * 1024);
          else if constexpr (mm < 9) fa[kk ^ 1][mm - 5] = lds_rd((kk ? ra : ra1) + sl * G2_AS + (mm - 5) * 2048);
          sb();
        });
      };
      if (L) poll0(ws, FL_4 + P * 64 + (m * 4 + kq) * 8 + ct, 1, epoch, lane);
      ResTile rt = load_res(make_rsrc(L ? (const void*)(ws + WS_X2) : (const void*)opq_p(x0_), SZ_X), ro);
      fence_all(acc);
      bar();
      ST(sbase + 1);
#pragma unroll
      for (int f = 0; f < 5; ++f) fb[0][f] = lds_rd(rb + f * 1024);
#pragma unroll
      for (int i = 0; i < 4; ++i) fa[0][i] = lds_rd(ra + i * 2048);
      sb();
      auto stage = [&](auto U) {
        half(IC<0>{}, U);
        wait_lgkm0();
        bar();
        half(IC<1>{}, IC<(U.v + 1) & 3>{});
      };
#pragma unroll 1
      for (int g = 0; g < 3; ++g) sfor<0, 4>(stage);
      sfor<0, 3>(stage);
      half(IC<0>{}, IC<3>{});
      wait_lgkm0();
      bar();
      {
        unsigned p[3][10];
        auto pack = [&](auto N) {
          constexpr int i = N.v / 5, f = N.v % 5;
          asm volatile("" : "+v"(acc[i][f]));
          p[i][2 * f] = pk2(acc[i][f][0], acc[i][f][1]); p[i][2 * f + 1] = pk2(acc[i][f][2], acc[i][f][3]);
          const unsigned o = xo + (((kq + 1 + i) & 3) * 3 + 2 - i) * 4 * XSLOT;
          if constexpr (f == 1) st16(u32x4{p[i][0], p[i][1], p[i][2], p[i][3]}, rxp, lane * 16, o);
          if constexpr (f == 3) st16(u32x4{p[i][4], p[i][5], p[i][6], p[i][7]}, rxp, lane * 16 + 1024, o);
          if constexpr (f == 4) st8(u32x2{p[i][8], p[i][9]}, rxp, lane * 8 + 2048, o);
        };
        sfor<0, 20>([&](auto M) {
          constexpr int mm = M.v;
          mma(acc[mm / 5][mm % 5], fb[1][mm % 5], fa[1][mm / 5]);
          if constexpr (mm >= 8) pack(IC<mm - 8>{});
          sb();
        });
        ST(sbase + 2);
        pack(IC<12>{}); pack(IC<13>{}); pack(IC<14>{});
      }
      f32x4 (&v)[5] = acc[3];
      fence_acc(v);
      const unsigned fx = FL_X + ((L * 64 + P * 4 + c4) * 4 + w) * 4;
      ST(sbase + 3);
      publish(ws, fx + kq, epoch, lane);
      ST(sbase + 4);
      poll<PDX>(ws, fx + ((kq + 1 + lane) & 3), 3, epoch, lane);
      ST(sbase + 5);
      const unsigned xi = xo + kq * 12 * XSLOT;
      u32x4 g0[3], g1[3]; u32x2 g2[3];
#pragma unroll
      for (int s = 0; s < 3; ++s) {
        g0[s] = ld16u(rxp, lane * 16, xi + s * 4 * XSLOT);
        g1[s] = ld16u(rxp, lane * 16 + 1024, xi + s * 4 * XSLOT);
        g2[s] = ld8u(rxp, lane * 8 + 2048, xi + s * 4 * XSLOT);
      }
      sb();
#pragma unroll
      for (int s = 0; s < 3; ++s) {
        v[0] += f32x4{bflo(g0[s][0]), bfhi(g0[s][0]), bflo(g0[s][1]), bfhi(g0[s][1])};
        v[1] += f32x4{bflo(g0[s][2]), bfhi(g0[s][2]), bflo(g0[s][3]), bfhi(g0[s][3])};
        v[2] += f32x4{bflo(g1[s][0]), bfhi(g1[s][0]), bflo(g1[s][1]), bfhi(g1[s][1])};
        v[3] += f32x4{bflo(g1[s][2]), bfhi(g1[s][2]), bflo(g1[s][3]), bfhi(g1[s][3])};
        v[4] += f32x4{bflo(g2[s][0]), bfhi(g2[s][0]), bflo(g2[s][1]), bfhi(g2[s][1])};
      }
      finish_tile(v, rt, make_rsrc(ws + (L ? WS_X3 : WS_X1), SZ_X), oo, true, make_rsrc(ws + (L ? WS_ST3 : WS_ST1), SZ_ST),
                  q == 0 ? (ct * S_ + row) * 8 : OOB);
      ST(sbase + 6);
      publish(ws, FL_2 + (L * 16 + P) * 64 + (m * 4 + kq) * 8 + ct, epoch, lane);
      ST(sbase + 7);
    }
    u32x4 y2[2]; u32x2 y1[2];
    {
      PHASE_IDS
      IDS_G3
      typedef __attribute__((ext_vector_type(2))) short s16x2;
      poll<PD2>(ws, FL_2 + (L * 16 + P) * 64 + w * 16 + i16, 16, epoch, lane);
      ST(sbase + 8);
      const rsrc_t rx = make_rsrc(ws + (L ? WS_X3 : WS_X1), SZ_X);
      const rsrc_t rst = make_rsrc(ws + (L ? WS_ST3 : WS_ST1), SZ_ST);
      const unsigned sox = (P * 8 + w * 2) * 20 * 1024;
      const unsigned rot = ROT3 ? (5 * (idx & 7)) >> 1 : 0;
      const unsigned par = ct & 1;
      bf16x8 a[20][2];
#pragma unroll
      for (int k = 0; k < 3; ++k) { a[k][0] = LDX(0, k); a[k][1] = LDX(1, k); }
      const unsigned sto = (2 * q * S_ + P * 128 + w * 32 + i16) * 8;
      u32x2 sa0, sa1, sb0, sb1;
      sb();
      bar();
      f32x4 acc[2][4];
      bf16x8 b[2][4];
      const unsigned rb = lb + lane * 16;
#pragma unroll
      for (int f = 0; f < 4; ++f) b[0][f] = lds_rd(rb + rot * 4096 + f * 1024);
      sb();
      sfor<0, 20>([&](auto K) {
        constexpr int k = K.v;
        const unsigned pb = k + 1 < 20 ? rb + KROT((k + 1) % 20) * 4096 : rb + 80 * 1024 + rot * 3072;
        if constexpr (k == 9) { sa0 = ld8u(rst, sto, 0); sa1 = ld8u(rst, sto, S_ * 8); sb0 = ld8u(rst, sto + 128, 0); sb1 = ld8u(rst, sto + 128, S_ * 8); }
        sfor<0, 8>([&](auto M) {
          constexpr int f = M.v >> 1, r = M.v & 1;
          if constexpr (k == 0) mma0(acc[r][f], b[0][f], a[0][r]); else mma(acc[r][f], b[k & 1][f], a[k][r]);
          if constexpr (r == 0 && (k + 1 < 20 || f < 3)) b[(k + 1) & 1][f] = lds_rd(pb + f * 1024);
          if constexpr (r == 1 && 2 * k + 3 + (f >> 1) < 20) a[2 * k + 3 + (f >> 1)][f & 1] = LDX(f & 1, 2 * k + 3 + (f >> 1));
          sb();
        });
        if constexpr (k == 0) ST(sbase + 9);
        if constexpr (k == 3) ST(51 + L * 5);
        if constexpr (k == 7) ST(52 + L * 5);
        if constexpr (k == 11) ST(53 + L * 5);
        if constexpr (k == 15) ST(54 + L * 5);
        if constexpr (k == 19) ST(55 + L * 5);
      });
      float mu[2], rs[2];
      ln_coef(xsum16_32(__uint_as_float(sa0[0]) + __uint_as_float(sa1[0])), xsum16_32(__uint_as_float(sa0[1]) + __uint_as_float(sa1[1])), mu[0], rs[0]);
      ln_coef(xsum16_32(__uint_as_float(sb0[0]) + __uint_as_float(sb1[0])), xsum16_32(__uint_as_float(sb0[1]) + __uint_as_float(sb1[1])), mu[1], rs[1]);
      auto act = [&](const f32x4& c, int r, const f32x4& cs, unsigned (&pk)[2]) {
        const f32x2 nm = {-mu[r], -mu[r]}, r2 = {rs[r], rs[r]};
#pragma unroll
        for (int e = 0; e < 2; ++e) {
          const f32x2 z = __builtin_elementwise_fma(nm, f32x2{cs[2 * e], cs[2 * e + 1]}, f32x2{c[2 * e], c[2 * e + 1]}) * r2;
          const s16x2 hb = __builtin_bit_cast(s16x2, __builtin_convertvector(z, bf16x2));
          pk[e] = __builtin_bit_cast(unsigned, __builtin_elementwise_max(hb, s16x2{0, 0}));
        }
      };
      auto pair = [&](const unsigned (&u)[2], const unsigned (&v)[2]) {
        auto t0 = __builtin_amdgcn_permlane16_swap(u[0], v[0], false, false);
        auto t1 = __builtin_amdgcn_permlane16_swap(u[1], v[1], false, false);
        return u32x4{t0[0], t1[0], t0[1], t1[1]};
      };
      const unsigned csb = lb + KM_CS1 + q * 16;
      f32x4 c1s[4];
#pragma unroll
      for (int f = 0; f < 4; ++f) c1s[f] = lds_rdf(csb + (f + par) * 64);
      asm volatile("s_nop 7\n\ts_nop 1" : "+v"(acc[0][0]), "+v"(acc[0][1]), "+v"(acc[0][2]), "+v"(acc[0][3]),
                                          "+v"(acc[1][0]), "+v"(acc[1][1]), "+v"(acc[1][2]), "+v"(acc[1][3]));
      wait_lgkm0();
      bar();
      ST(43 + L * 4);
      {
        const rsrc_t rh = make_rsrc(ws + (L ? WS_H2 : WS_H1), SZ_H);
        const unsigned vo = ((((q & 1) << 1) | (q >> 1)) * 16 + i16) * 16;
        const unsigned so = ((P * 8 + w * 2) * 54 + 7 * (ct >> 1) + 4 * par) * 1024;
        u32x4 y[2][2];
#pragma unroll
        for (int p = 0; p < 2; ++p) {
          const f32x4 c0 = c1s[2 * p], c1 = c1s[2 * p + 1];
#pragma unroll
          for (int r = 0; r < 2; ++r) {
            unsigned u[2], v[2];
            act(acc[r][2 * p], r, c0, u); act(acc[r][2 * p + 1], r, c1, v);
            y[p][r] = pair(u, v);
            st16_raw(y[p][r], rh, p == 1 && ct == 15 ? OOB : vo, so + (r * 54 + p) * 1024);
          }
        }
        st_guard(y[0][0], y[0][1], y[1][0], y[1][1]);
      }
      ST(44 + L * 4);
      sb();
      sfor<0, 20>([&](auto K) {
        constexpr int k = K.v;
        const unsigned pb = rb + 80 * 1024 + KROT((k + 1) % 20) * 3072;
        sfor<0, 6>([&](auto M) {
          constexpr int f = M.v >> 1, r = M.v & 1;
          if constexpr (k == 0) mma0(acc[r][f], b[0][f], a[0][r]); else mma(acc[r][f], b[k & 1][f], a[k][r]);
          if constexpr (r == 0 && k + 1 < 20) b[(k + 1) & 1][f] = lds_rd(pb + f * 1024);
          sb();
        });
        if constexpr (k == PUB1) publish(ws, FL_3 + (L * 32 + P) * 64 + w * 16 + ct, epoch, lane);
      });
      const f32x4 c0 = lds_rdf(csb + (4 + par) * 64), c1 = lds_rdf(csb + (5 + par) * 64), c2 = lds_rdf(csb + (par ? 0 : 6) * 64);
      asm volatile("s_nop 7\n\ts_nop 1" : "+v"(acc[0][0]), "+v"(acc[0][1]), "+v"(acc[0][2]), "+v"(acc[1][0]), "+v"(acc[1][1]), "+v"(acc[1][2]));
      ST(sbase + 10);
#pragma unroll
      for (int r = 0; r < 2; ++r) {
        unsigned u[2], v[2], s[2];
        act(acc[r][0], r, c0, u); act(acc[r][1], r, c1, v); act(acc[r][2], r, c2, s);
        y2[r] = pair(u, v);
        y1[r] = u32x2{s[0], s[1]};
      }
      asm volatile("" : "+v"(y2[0]), "+v"(y2[1]), "+v"(y1[0]), "+v"(y1[1]));
      ST(sbase + 11);
    }
    {
      PHASE_IDS
      IDS_G4
      const unsigned row = rt64 * 64 + w * 16 + i16;
      const rsrc_t rh = make_rsrc(ws + (L ? WS_H2 : WS_H1), SZ_H);
      const unsigned soh = (rt64 * 4 * 54 + w * 14) * 1024;
      const unsigned vl = lane * 16, vz = lane * 16 + (w < 3 ? 0u : OOB);
      bf16x8 a[14][4], b[2][5];
      unsigned sh[4];
#pragma unroll
      for (int i = 0; i < 4; ++i) sh[i] = soh + ((w + 1 + i) & 3) * 54 * 1024;
#define LDH(t, i) a[t][i] = ld16(rh, (ORD[t] < 12 ? vl : vz) + (ORD[t] & 3) * 1024, sh[i] + (ORD[t] >> 2) * 4096)
      const TileOff ro = tile_offsets(false, row, c8, q), oo = tile_offsets(L != 0, row, c8, q);
      const unsigned f3 = FL_3 + (L * 32 + (rt64 >> 1)) * 64 + ((rt64 & 1) * 2 + (lane & 1)) * 16 + 4 * w + ((lane >> 1) & 3);
      if (P3DELAY) __builtin_amdgcn_s_sleep(P3DELAY);
      poll0(ws, f3, 8, epoch, lane);
      ST(sbase + 13);
      sfor<0, 4 * G4INIT>([&](auto N) { LDH(N.v / 4, N.v % 4); });
      sb();
      {
        const unsigned P = (xcd >> 1) * 4 + (idx >> 3), ct = (xcd & 1) * 8 + (idx & 7), par = ct & 1;
        const unsigned vo = ((((q & 1) << 1) | (q >> 1)) * 16 + i16) * 16;
        const unsigned v1 = ((2 * par + (q >> 1)) * 16 + i16) * 16 + (q & 1) * 8;
        const unsigned so = ((P * 8 + w * 2) * 54 + 7 * (ct >> 1)) * 1024;
#pragma unroll
        for (int r = 0; r < 2; ++r) {
          st16_raw(y2[r], rh, ct == 15 ? OOB : vo, so + (r * 54 + 2 + 4 * par) * 1024);
          st8(y1[r], rh, v1, so + (r * 54 + 3) * 1024);
        }
        st_guard(y2[0], y2[1]);
      }
      ResTile rt;
      u64 fl2;
      const rsrc_t rr = make_rsrc(ws + (L ? WS_X3 : WS_X1), SZ_X);
      sb();
      bar();
      f32x4 acc[4][5];
      const unsigned rbe = lb + w * 20480 + lane * 16, rbl = lb + G4_LATE + w * 15360 + lane * 16;
#pragma unroll
      for (int f = 0; f < 5; ++f) b[0][f] = lds_rd(rbe + f * 1024);
      sb();
      sfor<0, 14>([&](auto T) {
        constexpr int t = T.v;
        constexpr int ta = t < 5 ? (G4INIT == 2 && t == 0 ? 2 : t + 3) : (t == 5 ? -1 : (t == 6 ? 8 : (t + 3 < 14 ? t + 3 : -1)));
        if constexpr (t == 3 || t == 6 || t == 10) { wait_lgkm0(); bar(); }
        if constexpr (t == 6) { if (!__all(fl2 == epoch)) poll0(ws, f3 + 16 * 64, 8, epoch, lane); }
        sfor<0, 20>([&](auto M) {
          constexpr int m = M.v, i = m / 5, f = m % 5;
          if constexpr (t == 0) mma0(acc[i][f], b[0][f], a[0][i]); else mma(acc[i][f], b[t & 1][f], a[t][i]);
          if constexpr (t + 1 < 14 && (m & 1) == 1 && m < 10)
            b[(t + 1) & 1][m >> 1] = lds_rd((g4_late(t + 1) ? rbl : rbe) + g4_slot(t + 1) + (m >> 1) * 1024);
          if constexpr (ta >= 0 && (m & 1) == 0 && m < 8) LDH(ta, m >> 1);
          if constexpr (t == 6 && (m & 1) == 0 && m >= 8 && m < 16) LDH(9, (m - 8) >> 1);
          if constexpr (G4INIT == 2 && t == 0 && (m & 1) == 0 && m >= 8 && m < 16) LDH(3, (m - 8) >> 1);
          if constexpr (t == 2 && m == 10) rt = load_res(rr, ro);
          if constexpr (t == 4 && m == 18) fl2 = __hip_atomic_load(flag_ptr(ws, f3 + 16 * 64), RLX_AGENT);
          if constexpr (t == 13 && m >= 8) {
            asm volatile("" : "+v"(acc[(m - 8) / 5][(m - 8) % 5]));
            lds_wrf(rbl + (m - 8) * 1024, acc[(m - 8) / 5][(m - 8) % 5]);
          }
          sb();
        });
        if constexpr (t == 2) {
          wait_vm<15 + (G4INIT == 2 ? 4 : 0)>();
          raise(ws, FL_3 + (L * 32 + 16 + (xcd >> 1) * 4 + (idx >> 3)) * 64 + w * 16 + (xcd & 1) * 8 + (idx & 7), epoch, lane);
          ST(sbase + 12);
        }
        if constexpr (t == 0) ST(sbase + 14);
        if constexpr (t == 3) ST(45 + L * 4);
        if constexpr (t == 6) ST(46 + L * 4);
      });
      ST(sbase + 15);
#pragma unroll
      for (int f = 2; f < 5; ++f) { asm volatile("" : "+v"(acc[2][f])); lds_wrf(rbl + (10 + f) * 1024, acc[2][f]); }
      f32x4 (&v)[5] = acc[3];
      fence_acc(v);
      wait_lgkm0();
      bar();
#pragma unroll
      for (int s = 1; s < 4; ++s) {
        const unsigned src = lb + G4_LATE + ((w + s) & 3) * 15360 + (3 - s) * 5 * 1024 + lane * 16;
#pragma unroll
        for (int f = 0; f < 5; ++f) v[f] += lds_rdf(src + f * 1024);
      }
      finish_tile(v, rt, L ? make_rsrc(opq_p(out_), SZ_X) : make_rsrc(ws + WS_X2, SZ_X), oo, L == 0, make_rsrc(ws + WS_ST2, SZ_ST),
                  q == 0 ? (c8 * S_ + row) * 8 : OOB);
      ST(sbase + 16);
      if (L == 0) publish(ws, FL_4 + (rt64 >> 1) * 64 + ((rt64 & 1) * 4 + w) * 8 + c8, epoch, lane);
    }
  }
  if (blockIdx.x == 0 && w0 == 0) raise(ws_, FL_E, epoch, tid0);
  ST(37);
}

extern "C" void kernel_launch(void* const* d_in, const int* in_sizes, int n_in, void* d_out, int out_size, void* d_ws,
                              size_t ws_size, hipStream_t stream) {
  char* ws = (char*)d_ws;
  (void)hipFuncSetAttribute((const void*)k_all, hipFuncAttributeMaxDynamicSharedMemorySize, KM_LDS);
  k_all<<<256, 512, KM_LDS, stream>>>((const u16*)d_in[0], (const u16*)d_in[1], (const u16*)d_in[2], (const u16*)d_in[3],
                                     (const u16*)d_in[4], (const u16*)d_in[6], (const u16*)d_in[7], ws, (u16*)d_out STAMP_PASS(0));
}
```

```cpp
#include <hip/hip_runtime.h>
#include <hip/hip_bf16.h>
#include <stdint.h>

#define DI __device__ __forceinline__
typedef unsigned short u16;
typedef __attribute__((ext_vector_type(8))) __bf16 bf16x8;
typedef __attribute__((ext_vector_type(2))) __bf16 bf16x2;
typedef __attribute__((ext_vector_type(4))) float f32x4;
typedef __attribute__((ext_vector_type(2))) float f32x2;
typedef __attribute__((ext_vector_type(4))) unsigned u32x4;
typedef __attribute__((ext_vector_type(2))) unsigned u32x2;
typedef __attribute__((ext_vector_type(4))) short s16x4;
#define LDS_AS __attribute__((address_space(3)))
#define GLB_AS __attribute__((address_space(1)))
typedef LDS_AS char* ldsp;
typedef unsigned long long u64;
typedef GLB_AS u64 gu64;
typedef __amdgpu_buffer_rsrc_t rsrc_t;
#define RLX_AGENT __ATOMIC_RELAXED, __HIP_MEMORY_SCOPE_AGENT

constexpr int S_ = 2048, D_ = 640, F_ = 1728;
constexpr float EPS = 1e-5f;
constexpr unsigned OOB = 0x7f000000u;

constexpr unsigned SZ_AB = 8 * 128 * 5 * 1024;
constexpr unsigned SZ_X = 128 * 20 * 1024;
constexpr unsigned SZ_H = 128 * 54 * 1024;
constexpr unsigned SZ_W1 = 16 * 140 * 1024;
constexpr unsigned SZ_W2 = 8 * 54 * 5 * 1024;
constexpr unsigned SZ_ST = 2048 * 64;
constexpr unsigned WS_AB1 = 0;
constexpr unsigned WS_AB2 = WS_AB1 + SZ_AB;
constexpr unsigned WS_X1 = WS_AB2 + SZ_AB;
constexpr unsigned WS_X2 = WS_X1 + SZ_X;
constexpr unsigned WS_X3 = WS_X2 + SZ_X;
constexpr unsigned WS_H1 = WS_X3 + SZ_X;
constexpr unsigned WS_H2 = WS_H1 + SZ_H;
constexpr unsigned WS_W1 = WS_H2 + SZ_H;
constexpr unsigned WS_W2 = WS_W1 + SZ_W1;
constexpr unsigned WS_ST1 = WS_W2 + SZ_W2;
constexpr unsigned WS_ST2 = WS_ST1 + SZ_ST;
constexpr unsigned WS_ST3 = WS_ST2 + SZ_ST;
constexpr unsigned WS_CS1 = WS_ST3 + SZ_ST;
constexpr unsigned WS_CSD = WS_CS1 + 20 * 1728 * 4;
constexpr unsigned XSLOT = 2560;
constexpr unsigned SZ_XP = 256 * 12 * XSLOT;
constexpr unsigned WS_XP = (WS_CSD + 256 * 320 + 4095) & ~4095u;
constexpr unsigned WS_FLG = WS_XP + 2 * SZ_XP;
constexpr unsigned FL_X = 0;
constexpr unsigned FL_2 = FL_X + 2048;
constexpr unsigned FL_3 = FL_2 + 2048;
constexpr unsigned FL_4 = FL_3 + 4096;
constexpr unsigned FL_5 = FL_4 + 1024;
constexpr unsigned FL_W = FL_5 + 2048;
constexpr unsigned FL_E = FL_W + 1088;
constexpr unsigned WS_END = WS_FLG + (FL_E + 1) * 128;

DI u32x4 make_srd(const void* p, unsigned bytes) {
  uint64_t a = (uint64_t)p;
  u32x4 r;
  r[0] = (unsigned)a; r[1] = (unsigned)(a >> 32) & 0xffffu; r[2] = bytes; r[3] = 0x00020000u;
  return r;
}
DI rsrc_t make_rsrc(const void* p, unsigned bytes) {
  return __builtin_amdgcn_make_buffer_rsrc((void*)p, (short)0, (int)bytes, 0x00020000);
}
template <int LOFF, int GOFF>
DI void glds(u32x4 srd, unsigned voff, unsigned ldsb, unsigned soff) {
  unsigned tmp;
  asm volatile("s_add_u32 m0, %1, %2\n\ts_add_u32 %0, %3, %4\n\tbuffer_load_dwordx4 %5, %6, %0 offen lds"
               : "=&s"(tmp)
               : "s"(ldsb), "i"(LOFF), "s"(soff), "i"(GOFF), "v"(voff), "s"(srd)
               : "memory", "scc");
}
template <int N> DI void wait_vm() { asm volatile("s_waitcnt vmcnt(%0)" ::"i"(N) : "memory"); }
DI void wait_lgkm0() { asm volatile("s_waitcnt lgkmcnt(0)" ::: "memory"); }
DI void sb() { __builtin_amdgcn_sched_barrier(0); }
DI void bar() { asm volatile("s_barrier" ::: "memory"); }

DI bf16x8 lds_rd(unsigned addr) { return *(const LDS_AS bf16x8*)(ldsp)(uintptr_t)addr; }
DI f32x4 lds_rdf(unsigned addr) { return *(const LDS_AS f32x4*)(ldsp)(uintptr_t)addr; }
DI void lds_wrf(unsigned addr, f32x4 v) { *(LDS_AS f32x4*)(ldsp)(uintptr_t)addr = v; }
DI float lds_rd1(unsigned addr) { return *(const LDS_AS float*)(ldsp)(uintptr_t)addr; }
DI void lds_wr1(unsigned addr, float v) { *(LDS_AS float*)(ldsp)(uintptr_t)addr = v; }
DI void mma(f32x4& c, const bf16x8& a, const bf16x8& b) {
  asm volatile("v_mfma_f32_16x16x32_bf16 %0, %1, %2, %0" : "+v"(c) : "v"(a), "v"(b));
}
DI void mma0(f32x4& c, const bf16x8& a, const bf16x8& b) {
  asm volatile("v_mfma_f32_16x16x32_bf16 %0, %1, %2, 0" : "=&v"(c) : "v"(a), "v"(b));
}
DI void fence_acc(f32x4 (&a)[5]) { asm volatile("s_nop 7\n\ts_nop 1" : "+v"(a[0]), "+v"(a[1]), "+v"(a[2]), "+v"(a[3]), "+v"(a[4])); }
DI void pin_acc(f32x4 (&a)[5]) { asm volatile("" : "+v"(a[0]), "+v"(a[1]), "+v"(a[2]), "+v"(a[3]), "+v"(a[4])); }
template <int M> DI void fence_all(f32x4 (&a)[M][5]) {
#pragma unroll
  for (int i = 1; i < M; ++i) pin_acc(a[i]);
  fence_acc(a[0]);
#pragma unroll
  for (int i = 1; i < M; ++i) pin_acc(a[i]);
}
DI unsigned pk2(float a, float b) {
  f32x2 v = {a, b};
  return __builtin_bit_cast(unsigned, __builtin_convertvector(v, bf16x2));
}
DI float bflo(unsigned u) { return __uint_as_float(u << 16); }
DI float bfhi(unsigned u) { return __uint_as_float(u & 0xffff0000u); }
DI float dot2(unsigned a, unsigned b, float c) {
  return __builtin_amdgcn_fdot2_f32_bf16(__builtin_bit_cast(bf16x2, a), __builtin_bit_cast(bf16x2, b), c, false);
}
DI float xsum16_32(float x) {
  auto s = __builtin_amdgcn_permlane16_swap(__float_as_uint(x), __float_as_uint(x), false, false);
  x = __uint_as_float(s[0]) + __uint_as_float(s[1]);
  auto t = __builtin_amdgcn_permlane32_swap(__float_as_uint(x), __float_as_uint(x), false, false);
  return __uint_as_float(t[0]) + __uint_as_float(t[1]);
}
template <int V> struct IC { static constexpr int v = V; };
template <int I, int N, class Fn> DI void sfor(Fn&& f) {
  if constexpr (I < N) { f(IC<I>{}); sfor<I + 1, N>(f); }
}
DI unsigned rfl(unsigned v) { return __builtin_amdgcn_readfirstlane(v); }
DI unsigned opq_v(unsigned x) { asm volatile("" : "+v"(x)); return x; }
DI unsigned opq_s(unsigned x) { asm volatile("" : "+s"(x)); return x; }
template <class T> DI T* opq_p(T* x) { asm volatile("" : "+s"(x)); return x; }
DI bf16x8 ld16(rsrc_t r, unsigned voff, unsigned soff) { return __builtin_bit_cast(bf16x8, __builtin_amdgcn_raw_buffer_load_b128(r, voff, soff, 16)); }
DI u32x4 ld16u(rsrc_t r, unsigned voff, unsigned soff) { return __builtin_amdgcn_raw_buffer_load_b128(r, voff, soff, 16); }
DI u32x2 ld8u(rsrc_t r, unsigned voff, unsigned soff) { return __builtin_amdgcn_raw_buffer_load_b64(r, voff, soff, 16); }
DI void st16(u32x4 v, rsrc_t r, unsigned voff, unsigned soff) {
  __builtin_amdgcn_raw_buffer_store_b128(v, r, voff, soff, 16);
  asm volatile("s_nop 1" :: "v"(v) : "memory");
}
DI void st16_raw(u32x4 v, rsrc_t r, unsigned voff, unsigned soff) { __builtin_amdgcn_raw_buffer_store_b128(v, r, voff, soff, 16); }
template <class... T> DI void st_guard(const T&... v) {
  (..., [](const auto& x) { asm volatile("" :: "v"(x) : "memory"); }(v));
  asm volatile("s_nop 1" ::: "memory");
  (..., [](const auto& x) { asm volatile("" :: "v"(x)); }(v));
}
DI void st8(u32x2 v, rsrc_t r, unsigned voff, unsigned soff) { __builtin_amdgcn_raw_buffer_store_b64(v, r, voff, soff, 16); }
DI void st4(unsigned v, rsrc_t r, unsigned voff, unsigned soff) { __builtin_amdgcn_raw_buffer_store_b32(v, r, voff, soff, 16); }

#define ST(i)
#define STAMP_ARGS
#define STAMP_PASS(k)

template <int KS> DI unsigned foff(unsigned row, unsigned col) {
  return ((row >> 4) * KS + (col >> 5)) * 1024u + (((col >> 3) & 3) * 16u + (row & 15)) * 16u + (col & 7) * 2u;
}

extern __shared__ __attribute__((aligned(1024))) char smem[];
DI unsigned lds_base() { return (unsigned)(uintptr_t)(ldsp)smem; }

DI void ln_coef(float s1, float s2, float& mu, float& rs) {
  mu = s1 * (1.f / D_);
  rs = __builtin_amdgcn_rsqf(fmaxf(s2 * (1.f / D_) - mu * mu, 0.f) + EPS);
}

DI void g1_store(const f32x4 (&acc)[2][5], const float (&mu)[2][4], const float (&rs)[2][4], const float (&csv)[5], rsrc_t rab,
                 unsigned P, unsigned ct, unsigned w, unsigned i16, unsigned q) {
  const unsigned kstep = (ct >> 3) * 64 + P * 4 + w;
  const unsigned cq = ((q & 1) << 1) | (q >> 1);
  const unsigned so = ((ct & 7) * 128 + kstep) * 5 * 1024;
  const unsigned vo = (cq * 16 + i16) * 16;
  u32x4 y[5];
#pragma unroll
  for (int f = 0; f < 5; ++f) {
    const f32x2 cs = {csv[f], csv[f]};
    unsigned pk[2][2];
#pragma unroll
    for (int r = 0; r < 2; ++r)
#pragma unroll
      for (int e = 0; e < 2; ++e) {
        const f32x2 t = __builtin_elementwise_fma(f32x2{mu[r][2 * e], mu[r][2 * e + 1]}, cs, f32x2{acc[r][f][2 * e], acc[r][f][2 * e + 1]}) *
                        f32x2{rs[r][2 * e], rs[r][2 * e + 1]};
        pk[r][e] = __builtin_bit_cast(unsigned, __builtin_convertvector(t, bf16x2));
      }
    auto t0 = __builtin_amdgcn_permlane16_swap(pk[0][0], pk[1][0], false, false);
    auto t1 = __builtin_amdgcn_permlane16_swap(pk[0][1], pk[1][1], false, false);
    y[f] = u32x4{t0[0], t1[0], t0[1], t1[1]};
    st16_raw(y[f], rab, vo + (f & 3) * 1024, so + (f >> 2) * 4096);
  }
  st_guard(y[0], y[1], y[2], y[3], y[4]);
}

constexpr int K1_STAGE = 26 * 1024;
constexpr int K1_TR = 5 * K1_STAGE;
constexpr int K1_CS = K1_TR + 5 * 4096;

DI void k1_issue(u32x4 sa, u32x4 sbd, unsigned vo, unsigned la, unsigned lbb, unsigned soa, unsigned sob, unsigned w) {
  glds<0, 0>(sa, vo, la, soa);
  glds<1024, 8 * 1280>(sa, vo, la, soa);
  glds<2048, 16 * 1280>(sa, vo, la, soa);
  glds<3072, 24 * 1280>(sa, vo, la, soa);
  glds<0, 0>(sbd, vo, lbb, sob);
  glds<4096, 32 * 1280>(sbd, vo, lbb, sob);
  if (w < 2) glds<8192, 64 * 1280>(sbd, vo, lbb, sob);
}
struct RtUnit { bool first; unsigned ks, g; };
DI RtUnit rt_unit(unsigned u) {
  RtUnit r;
  r.first = u < 540;
  if (r.first) { r.ks = u / 27u; r.g = u - r.ks * 27u; }
  else { const unsigned v = u - 540; r.ks = v / 10u; r.g = v - r.ks * 10u; }
  return r;
}
DI void retile_issue(unsigned u, const u16* w1, const u16* w2, unsigned ldst, unsigned lane) {
  const RtUnit t = rt_unit(u);
  const unsigned ld = t.first ? F_ * 2 : D_ * 2;
  u32x4 srd = make_srd(t.first ? w1 : w2, D_ * F_ * 2);
  const unsigned vo = (lane >> 3) * ld + (lane & 7) * 16;
  const unsigned so = t.ks * 32 * ld + t.g * 128;
  glds<0, 0>(srd, vo, ldst, so);
  glds<1024, 0>(srd, vo, ldst, so + 8 * ld);
  glds<2048, 0>(srd, vo, ldst, so + 16 * ld);
  glds<3072, 0>(srd, vo, ldst, so + 24 * ld);
}
DI unsigned w1_off(unsigned rt, unsigned ks) {
  const unsigned c = rt / 7u, f = rt - c * 7, g = (c & 1) ? (f == 0 ? 6 : f - 1) : f;
  return WS_W1 + (c * 140 + (g < 4 ? ks * 4 + g : 80 + ks * 3 + g - 4)) * 1024;
}
DI void retile_finish(unsigned u, unsigned f0, rsrc_t rws, unsigned ldst, unsigned lane) {
  const RtUnit t = rt_unit(u);
  const unsigned i = lane & 15, q = lane >> 4;
  const unsigned base = ldst + (8 * q + (i >> 2)) * 128 + (i & 3) * 8 + f0 * 32;
  u32x4 v[2];
#pragma unroll
  for (int f = 0; f < 2; ++f) {
    s16x4 lo = __builtin_amdgcn_ds_read_tr16_b64_v4i16((LDS_AS s16x4*)(uintptr_t)(base + f * 32));
    s16x4 hi = __builtin_amdgcn_ds_read_tr16_b64_v4i16((LDS_AS s16x4*)(uintptr_t)(base + f * 32 + 512));
    u32x2 l2 = __builtin_bit_cast(u32x2, lo), h2 = __builtin_bit_cast(u32x2, hi);
    v[f] = u32x4{l2[0], l2[1], h2[0], h2[1]};
  }
  const unsigned rt = t.g * 4 + f0;
  if (t.first) {
    st16(v[0], rws, lane * 16, w1_off(rt, t.ks));
    st16(v[1], rws, lane * 16, w1_off(rt + 1, t.ks));
    float s[2];
#pragma unroll
    for (int f = 0; f < 2; ++f)
      s[f] = xsum16_32(((bflo(v[f][0]) + bfhi(v[f][0])) + (bflo(v[f][1]) + bfhi(v[f][1]))) + ((bflo(v[f][2]) + bfhi(v[f][2])) + (bflo(v[f][3]) + bfhi(v[f][3]))));
    st4(__float_as_uint(q == 0 ? s[0] : s[1]), rws, lane < 32 ? lane * 4 : OOB, WS_CS1 + (t.ks * F_ + rt * 16) * 4);
  } else {
    const unsigned c0 = rt / 5u, c1 = (rt + 1) / 5u;
    st16(v[0], rws, lane * 16, WS_W2 + ((c0 * 54 + t.ks) * 5 + (rt - c0 * 5)) * 1024);
    st16(v[1], rws, lane * 16, WS_W2 + ((c1 * 54 + t.ks) * 5 + (rt + 1 - c1 * 5)) * 1024);
  }
}

constexpr int WS1 = 3, WS2 = 6;
constexpr int PUB1 = 13;
constexpr int RGAP = 10, ASLEEP2 = 40, WDPRE = 6;
constexpr int PD4 = 13, PDX = 15, PD2 = 30, P5DELAY0 = 4, P5DELAY = 44;

constexpr int KM_LDS = 160 * 1024;
constexpr int KM_CS1 = KM_LDS - 1024;
constexpr int G2_A = 80 * 1024, G2_AS = 16 * 1024, G2_BS = 20 * 1024;
constexpr int G4_LATE = 80 * 1024;

DI gu64* flag_ptr(char* ws, unsigned f) { return (gu64*)(ws + WS_FLG + f * 128); }
DI void raise(char* ws, unsigned f, u64 epoch, unsigned lane) {
  if (lane < 16) __hip_atomic_store(flag_ptr(ws, f) + lane, epoch, RLX_AGENT);
}
DI void publish(char* ws, unsigned f, u64 epoch, unsigned lane) {
  wait_vm<0>();
  raise(ws, f, epoch, lane);
}
DI void poll0(char* ws, unsigned f, unsigned n, u64 epoch, unsigned lane) {
  gu64* p = flag_ptr(ws, f);
  bool ok = lane >= n;
#pragma unroll 1
  for (unsigned s = 0; s < (1u << 16); ++s) {
    if (!ok) ok = __hip_atomic_load(p, RLX_AGENT) == epoch;
    if (__all(ok)) break;
    __builtin_amdgcn_s_sleep(1);
  }
  asm volatile("" ::: "memory");
}
template <int DELAY> DI void poll(char* ws, unsigned f, unsigned n, u64 epoch, unsigned lane) {
  __builtin_amdgcn_s_sleep(DELAY);
  poll0(ws, f, n, epoch, lane);
}
DI void warm(unsigned ldump, u32x4 srd, unsigned voff, unsigned soff) {
  asm volatile("s_mov_b32 m0, %0\n\ts_nop 0\n\tbuffer_load_dword %1, %2, %3 offen lds" :: "s"(ldump), "v"(voff), "s"(srd), "s"(soff) : "memory");
}
DI void glds4_sc1(u32x4 srd, unsigned voff, unsigned l, unsigned so) {
  asm volatile("s_mov_b32 m0, %0\n\ts_nop 0\n\tbuffer_load_dwordx4 %1, %2, %3 offen sc1 lds\n\tbuffer_load_dwordx4 %1, %2, %3 offen offset:1024 sc1 lds\n\t"
               "buffer_load_dwordx4 %1, %2, %3 offen offset:2048 sc1 lds\n\tbuffer_load_dwordx4 %1, %2, %3 offen offset:3072 sc1 lds"
               :: "s"(l), "v"(voff), "s"(srd), "s"(so) : "memory");
}
DI void glds1_sc1(u32x4 srd, unsigned voff, unsigned l, unsigned so) {
  asm volatile("s_mov_b32 m0, %0\n\ts_nop 0\n\tbuffer_load_dwordx4 %1, %2, %3 offen sc1 lds" :: "s"(l), "v"(voff), "s"(srd), "s"(so) : "memory");
}
template <int N> DI void glds_run_sc1(u32x4 srd, unsigned voff, unsigned l, unsigned so) {
#pragma unroll
  for (int g = 0; g < N / 4; ++g) glds4_sc1(srd, voff, l + g * 4096, so + g * 4096);
#pragma unroll
  for (int g = N / 4 * 4; g < N; ++g) glds1_sc1(srd, voff, l + g * 1024, so + g * 1024);
}

struct ResTile { u32x4 r0, r1; u32x2 r2; };
struct TileOff { unsigned a0, a1, a2; };
DI TileOff tile_offsets(bool rm, unsigned row, unsigned ct, unsigned q) {
  const unsigned cA = ct * 80 + 16 * (q & 1) + 8 * (q >> 1), cS = ct * 80 + 64 + 4 * q;
  TileOff t;
  if (rm) { t.a0 = (row * D_ + cA) * 2; t.a1 = t.a0 + 64; t.a2 = (row * D_ + cS) * 2; }
  else { t.a0 = foff<20>(row, cA); t.a1 = foff<20>(row, cA + 32); t.a2 = foff<20>(row, cS); }
  asm volatile("" : "+v"(t.a0), "+v"(t.a1), "+v"(t.a2));
  return t;
}
DI ResTile load_res(rsrc_t rr, const TileOff& t) {
  ResTile rt;
  rt.r0 = ld16u(rr, t.a0, 0); rt.r1 = ld16u(rr, t.a1, 0); rt.r2 = ld8u(rr, t.a2, 0);
  return rt;
}
DI void finish_tile(f32x4 (&v)[5], const ResTile& rt, rsrc_t rout, const TileOff& o, bool stats, rsrc_t rst, unsigned sto) {
#pragma unroll
  for (int p = 0; p < 2; ++p)
#pragma unroll
    for (int r = 0; r < 4; ++r) {
      auto t = __builtin_amdgcn_permlane16_swap(__float_as_uint(v[2 * p][r]), __float_as_uint(v[2 * p + 1][r]), false, false);
      v[2 * p][r] = __uint_as_float(t[0]); v[2 * p + 1][r] = __uint_as_float(t[1]);
    }
  u32x4 y0, y1; u32x2 y2;
#pragma unroll
  for (int j = 0; j < 2; ++j) {
    y0[j] = pk2(v[0][2 * j] + bflo(rt.r0[j]), v[0][2 * j + 1] + bfhi(rt.r0[j]));
    y0[2 + j] = pk2(v[1][2 * j] + bflo(rt.r0[2 + j]), v[1][2 * j + 1] + bfhi(rt.r0[2 + j]));
    y1[j] = pk2(v[2][2 * j] + bflo(rt.r1[j]), v[2][2 * j + 1] + bfhi(rt.r1[j]));
    y1[2 + j] = pk2(v[3][2 * j] + bflo(rt.r1[2 + j]), v[3][2 * j + 1] + bfhi(rt.r1[2 + j]));
    y2[j] = pk2(v[4][2 * j] + bflo(rt.r2[j]), v[4][2 * j + 1] + bfhi(rt.r2[j]));
  }
  if (stats) {
    st16_raw(y0, rout, o.a0, 0); st16_raw(y1, rout, o.a1, 0); st8(y2, rout, o.a2, 0);
  } else {
    __builtin_amdgcn_raw_buffer_store_b128(y0, rout, o.a0, 0, 0);
    __builtin_amdgcn_raw_buffer_store_b128(y1, rout, o.a1, 0, 0);
    __builtin_amdgcn_raw_buffer_store_b64(y2, rout, o.a2, 0, 0);
  }
  st_guard(y0, y1);
  if (stats) {
    float s1a = 0.f, s1b = 0.f, s2a = 0.f, s2b = 0.f;
#pragma unroll
    for (int j = 0; j < 4; ++j) {
      s1a = dot2(y0[j], 0x3f803f80u, s1a); s2a = dot2(y0[j], y0[j], s2a);
      s1b = dot2(y1[j], 0x3f803f80u, s1b); s2b = dot2(y1[j], y1[j], s2b);
    }
    s1a = dot2(y2[0], 0x3f803f80u, s1a); s2a = dot2(y2[0], y2[0], s2a);
    s1b = dot2(y2[1], 0x3f803f80u, s1b); s2b = dot2(y2[1], y2[1], s2b);
    const float s1 = xsum16_32(s1a + s1b), s2 = xsum16_32(s2a + s2b);
    st8(u32x2{__float_as_uint(s1), __float_as_uint(s2)}, rst, sto, 0);
  }
}

DI constexpr unsigned wd_pos(int c) { return c < 8 ? 10 * c : 60 + 10 * c; }
constexpr int ORD[14] = {0, 1, 4, 5, 7, 8, 11, 12, 2, 3, 6, 9, 10, 13};
DI constexpr int g4_late(int t) { return (t >= 4 && t < 7) || t >= 11; }
DI constexpr int g4_slot(int t) { return (t < 4 ? t : (t < 7 ? t - 4 : (t < 11 ? t - 7 : t - 11))) * 5120; }

#define PHASE_IDS \
  const unsigned lane = opq_v(tid0) & 63, w = opq_s(w0), bid = opq_s(blockIdx.x); \
  const unsigned xcd = bid & 7, idx = bid >> 3, i16 = lane & 15, q = lane >> 4; \
  char* const ws = opq_p(ws_); \
  (void)xcd; (void)idx; (void)i16; (void)q;
#define IDS_G2 const unsigned kq = xcd & 3, P = (xcd >> 2) * 8 + (idx >> 2), c4 = idx & 3;
#define G2_LA(h) (lb + G2_A + ((w >> 1) * 4 + ((2 * (w & 1) + (h) + 3 - kq) & 3)) * 2048)
#define IDS_G3 const unsigned P = (xcd >> 1) * 4 + (idx >> 3), ct = (xcd & 1) * 8 + (idx & 7);
#define C8_OF ((xcd & 1) * 4 + (idx & 3))
#define IDS_G4 const unsigned rt64 = (xcd >> 1) * 8 + (idx >> 2), c8 = C8_OF;
#define LDX(r, k) ld16(rx, lane * 16 + ((k) & 3) * 1024, sox + (r) * 20480 + ((k) >> 2) * 4096)

__global__ __launch_bounds__(512, 2) void k_all(const u16* x0_, const u16* Sre_, const u16* Sim_, const u16* Dre_, const u16* Dim_,
                                               const u16* w1_, const u16* w2_, char* ws_, u16* out_ STAMP_ARGS) {
  ST(0);
  const unsigned tid0 = threadIdx.x, wall = rfl(tid0 >> 6), w0 = wall & 3;
  const unsigned lb = lds_base();
  const char* code; unsigned code_len;
  asm volatile("s_getpc_b64 %0\n.Lcw_begin:\n\ts_mov_b32 %1, .Lcw_end-.Lcw_begin" : "=s"(code), "=s"(code_len));
  u64 epoch;

  if (__builtin_expect(wall >= 4, 1)) {
    const unsigned sink = lb + K1_CS + 1024;
    unsigned ep0, ep1;
    asm volatile("global_load_dword %0, %2, %3 sc1\n\tglobal_load_dword %1, %2, %3 offset:4 sc1"
                 : "=&v"(ep0), "=&v"(ep1) : "v"(WS_FLG + FL_E * 128), "s"(ws_) : "memory");
    {
      PHASE_IDS
      IDS_G3
      const u32x4 sa = make_srd(opq_p(x0_), S_ * D_ * 2), sbd = make_srd(ct < 8 ? opq_p(Dre_) : opq_p(Dim_), D_ * D_ * 2);
      const unsigned vo = (lane >> 3) * 1280 + (((lane & 7) ^ (lane >> 3)) << 4);
      const unsigned soa = (P * 128 + w * 32) * 1280, sob = ((ct & 7) * 80 + w * 8) * 1280;
      const unsigned la = lb + w * 4096, lbb = lb + 16384 + w * 1024;
      const unsigned u0 = bid * 4 + w, u1 = 1024 + bid;
      const bool extra = w == 3 && bid < 56;
      const unsigned rot = ((idx & 7) + 3 * (idx >> 3)) % 10u;
      auto ko = [&](unsigned s) { const unsigned c = s + rot; return (c >= 10 ? c - 10 : c) * 128; };
      asm volatile("s_nop 4" ::: "memory");
      k1_issue(sa, sbd, vo, la, lbb, soa + ko(0), sob + ko(0), w);
      k1_issue(sa, sbd, vo, la + K1_STAGE, lbb + K1_STAGE, soa + ko(1), sob + ko(1), w);
      if (w == 3) warm(sink, make_srd(code, code_len), lane < 16 ? idx * 2048 + lane * 128 : OOB, 0);
      ST(8);
      wait_vm<6>();
      {
        unsigned lo, hi;
        asm volatile("v_readfirstlane_b32 %0, %2\n\tv_readfirstlane_b32 %1, %3\n\ts_nop 4" : "=s"(lo), "=s"(hi) : "v"(ep0), "v"(ep1));
        epoch = (u64)lo + ((u64)hi << 32) + 1;
      }
      bar();
      k1_issue(sa, sbd, vo, la + 2 * K1_STAGE, lbb + 2 * K1_STAGE, soa + ko(2), sob + ko(2), w);
      const u32x4 ss = make_srd((xcd & 2) ? Sim_ : Sre_, S_ * S_ * 2);
      const unsigned vs = (lane & 31) * 4096 + (lane >> 5) * 128;
      const unsigned sos = ((xcd >> 2) * 1024 + idx * 32) * 4096 + (xcd & 1) * 2048 + w * 256;
      unsigned so = 0;
#pragma unroll 1
      for (int i = 0; i < 10; ++i) {
        if (i == 0) wait_vm<6>(); else if (i == 1) wait_vm<2 * 6>(); else if (i < 6) wait_vm<3 * 6>();
        else if (i == 6) wait_vm<2 * 6 + 4>(); else if (i == 7) wait_vm<6 + 4>(); else wait_vm<4 + (WS2 >= 4 && WS2 < 8) + (WS1 >= 4 && WS1 < 8)>();
        bar();
        if (i == 0) {
          k1_issue(sa, sbd, vo, la + 3 * K1_STAGE, lbb + 3 * K1_STAGE, soa + ko(3), sob + ko(3), w);
          k1_issue(sa, sbd, vo, la + 4 * K1_STAGE, lbb + 4 * K1_STAGE, soa + ko(4), sob + ko(4), w);
        } else if (i == 1) {
          k1_issue(sa, sbd, vo, la, lbb, soa + ko(5), sob + ko(5), w);
          k1_issue(sa, sbd, vo, la + K1_STAGE, lbb + K1_STAGE, soa + ko(6), sob + ko(6), w);
        } else if (i < 5) k1_issue(sa, sbd, vo, la + so, lbb + so, soa + ko(i + 5), sob + ko(i + 5), w);
        so = so == 4 * K1_STAGE ? 0 : so + K1_STAGE;
        if (i == WS1) warm(sink, ss, vs, sos);
        if (i == WS2) warm(sink, ss, vs, sos + 1024);
        if (i == 5) {
          retile_issue(u0, w1_, w2_, lb + K1_TR + w * 4096, lane);
          if (extra) retile_issue(u1, w1_, w2_, lb + K1_TR + 4 * 4096, lane);
        }
      }
      ST(9);
      bar();
    }
    bf16x8 wr[10][5];
#pragma unroll
    for (int t = 0; t < 10; ++t)
#pragma unroll
      for (int f = 0; f < 5; ++f) wr[t][f] = bf16x8{};
#pragma unroll 1
    for (int L = 0; L < 2; ++L) {
      const int sbase = L * 20;
      (void)sbase;
      {
        PHASE_IDS
        IDS_G2
        const u32x4 sa = make_srd(kq < 2 ? opq_p(Sre_) : opq_p(Sim_), S_ * S_ * 2);
        const u32x4 sb_ = make_srd(ws + (L ? WS_AB2 : WS_AB1), SZ_AB);
        const unsigned va = (lane >> 3) * 4096 + (((lane & 7) ^ (lane >> 3)) << 4), vb = lane * 16;
        const unsigned soa = (P * 128 + w * 32) * 4096 + (kq & 1) * 2048;
        const unsigned sob = ((2 * c4 + (w >> 1)) * 128 + kq * 32 + (w & 1)) * 5 * 1024;
        const unsigned la = lb + G2_A + w * 4096, lbb = lb + w * 5120;
        const unsigned la0 = G2_LA(0), la1 = G2_LA(1);
        const unsigned ct = (xcd & 1) * 8 + (idx & 7);
        asm volatile("s_nop 4" ::: "memory");
        auto issue_a = [&](unsigned l, unsigned so) {
          glds<0, 0>(sa, va, la0 + l, so); glds<1024, 8 * 4096>(sa, va, la0 + l, so);
          glds<0, 16 * 4096>(sa, va, la1 + l, so); glds<1024, 24 * 4096>(sa, va, la1 + l, so);
        };
        if (L == 0) {
#pragma unroll
          for (int s = 0; s < 3; ++s) issue_a(s * G2_AS, soa + s * 128);
          const unsigned u0 = bid * 4 + w, u1 = 1024 + bid;
          const rsrc_t rws = make_rsrc(ws, WS_END);
          wait_vm<12>();
          retile_finish(u0, 0, rws, lb + K1_TR + w * 4096, lane);
          retile_finish(u0, 2, rws, lb + K1_TR + w * 4096, lane);
          if (w == 3 && bid < 56) {
            retile_finish(u1, 0, rws, lb + K1_TR + 4 * 4096, lane);
            retile_finish(u1, 2, rws, lb + K1_TR + 4 * 4096, lane);
          }
          __builtin_amdgcn_s_sleep(P5DELAY0);
        } else {
          issue_a(3 * G2_AS, soa + 3 * 128);
          __builtin_amdgcn_s_sleep(P5DELAY);
        }
        ST(sbase + 1);
        poll0(ws, FL_5 + ((L * 4 + kq) * 4 + c4) * 64 + ((i16 >> 1) * 2 + (w >> 1)) * 4 + (w & 1) + 2 * (i16 & 1), 16, epoch, lane);
        wait_vm<0>();
        ST(sbase + 2);
        glds_run_sc1<5>(sb_, vb, lbb, sob);
        glds_run_sc1<5>(sb_, vb, lbb + G2_BS, sob + (1) * 10240);
        if (L == 0) {
          raise(ws, FL_W + bid * 4 + w, epoch, lane);
          if (w == 3 && bid < 56) raise(ws, FL_W + 1024 + bid, epoch, lane);
          wait_vm<6>();
        } else wait_vm<5>();
        bar();
        glds_run_sc1<5>(sb_, vb, lbb + 2 * G2_BS, sob + (2) * 10240);
        if (L == 0) issue_a(3 * G2_AS, soa + (3) * 128);
        glds_run_sc1<5>(sb_, vb, lbb + 3 * G2_BS, sob + (3) * 10240);
        ST(sbase + 3);
        const u32x4 sw = make_srd(ws + WS_W1, SZ_W1);
        const unsigned sow = ct * 140 * 1024;
        unsigned fw, fw0 = 0, fw1 = 0;
        {
          const unsigned n = w * 42 + (lane < 42 ? lane : 0), k3 = n / 3u, g3 = (7 * ct >> 2) + (n - k3 * 3), n2 = n - 60;
          fw = FL_W + (n < 60 ? k3 * 27 + (g3 < 26 ? g3 : 26) : 540 + (n2 >> 1) * 10 + (5 * C8_OF >> 2) + (n2 & 1));
        }
#pragma unroll 1
        for (int s = 0; s < 16; ++s) {
          if (s == 0) wait_vm<10>();
          if (s == 1) wait_vm<14>();
          wait_vm<18>();
          if (s == 9 && L == 0) {
            asm volatile("" : "+v"(fw0), "+v"(fw1));
            if (!__all(fw0 == (unsigned)epoch && fw1 == (unsigned)(epoch >> 32))) poll0(ws, fw, 42, epoch, lane);
          }
          bar();
          const unsigned sl = s & 3;
          if (s < 12) {
            issue_a(sl * G2_AS, soa + (s + 4) * 128);
            glds_run_sc1<5>(sb_, vb, lbb + sl * G2_BS, sob + (s + 4) * 10240);
            if (s == 6 && L == 0)
              asm volatile("global_load_dword %0, %2, %3 sc1\n\tglobal_load_dword %1, %2, %3 offset:4 sc1"
                           : "+v"(fw0), "+v"(fw1) : "v"(WS_FLG + fw * 128), "s"(ws) : "memory");
          } else {
            glds_run_sc1<5>(sw, vb, lbb + sl * G2_BS, sow + w * 5120 + sl * G2_BS);
            if (s < 15) glds_run_sc1<4>(sw, vb, la + sl * G2_AS, sow + G2_A + w * 4096 + sl * G2_AS);
            else glds_run_sc1<3>(sw, vb, lb + G2_A + 3 * G2_AS + w * 3072, sow + G2_A + 3 * G2_AS + w * 3072);
          }
        }
        ST(sbase + 4);
      }
      {
        PHASE_IDS
        IDS_G4
        const unsigned ct = (xcd & 1) * 8 + (idx & 7);
        if (w >= 2) {
          const rsrc_t rc = make_rsrc(ws + WS_CS1, 20 * F_ * 4);
          const unsigned c = (w - 2) * 64 + lane, col = ct * 112 + c;
          const unsigned vo = c < 112 && col < F_ ? col * 4 : OOB;
          float csp = 0.f;
#pragma unroll
          for (int k = 0; k < 20; ++k) csp += __uint_as_float(__builtin_amdgcn_raw_buffer_load_b32(rc, vo, k * F_ * 4, 16));
          lds_wr1(lb + KM_CS1 + c * 4, csp);
        }
        const unsigned so = (c8 * 54 + w * 14) * 5 * 1024;
        wait_vm<0>();
        if (L == 0) {
          const rsrc_t rw = make_rsrc(ws + WS_W2, SZ_W2);
          const unsigned vl = lane * 16, vz = lane * 16 + (w < 3 ? 0u : OOB);
          sfor<0, 50>([&](auto N) {
            constexpr int t = 4 + N.v / 5, n = ORD[t] * 5 + N.v % 5;
            wr[t - 4][N.v % 5] = ld16(rw, (ORD[t] < 12 ? vl : vz) + (n & 3) * 1024, so + (n >> 2) * 4096);
            if constexpr (N.v % 5 == 4 && N.v < 49) { sb(); __builtin_amdgcn_s_sleep(RGAP); sb(); }
          });
          sb();
        }
        wait_lgkm0();
        ST(sbase + 5);
        bar();
        bar();
        const u32x4 s2 = make_srd(ws + WS_W2, SZ_W2);
        const unsigned sg = rfl(so);
        asm volatile("s_nop 4" ::: "memory");
        glds_run_sc1<10>(s2, lane * 16, lb + w * 20480, sg);
        glds_run_sc1<10>(s2, lane * 16, lb + w * 20480 + 10240, sg + 4 * 5120);
        wait_vm<0>();
        ST(sbase + 6);
        const unsigned pe = lb + w * 20480 + lane * 16, pl = lb + G4_LATE + w * 15360 + lane * 16;
        auto put = [&](auto T0, auto T1) {
          sfor<T0.v * 5, T1.v * 5>([&](auto N) {
            constexpr int t = N.v / 5, f = N.v % 5;
            *(LDS_AS bf16x8*)(ldsp)(uintptr_t)((g4_late(t) ? pl : pe) + g4_slot(t) + f * 1024) = wr[t - 4][f];
          });
          wait_lgkm0();
        };
        bar();
        put(IC<4>{}, IC<7>{});
        bar();
        put(IC<7>{}, IC<11>{});
        bar();
        put(IC<11>{}, IC<14>{});
        bar();
      }
      if (L) { bar(); break; }
      {
        PHASE_IDS
        IDS_G3
        const u32x4 sd = make_srd(ct < 8 ? opq_p(Dre_) : opq_p(Dim_), D_ * D_ * 2);
        const unsigned vo = (lane >> 3) * 1280 + (((lane & 7) ^ (lane >> 3)) << 4);
        const unsigned so = (ct & 7) * 80 * 1280;
        sfor<0, 10>([&](auto G) { glds<G.v * 1024, G.v * 8 * 1280>(sd, vo, lb + 20 * w * 1024, so + 2 * w * 128); });
        sfor<0, WDPRE>([&](auto G) { glds<G.v * 1024, G.v * 8 * 1280>(sd, vo, lb + (20 * w + 10) * 1024, so + (2 * w + 1) * 128); });
        bar();
        sfor<WDPRE, 10>([&](auto G) { glds<G.v * 1024, G.v * 8 * 1280>(sd, vo, lb + (20 * w + 10) * 1024, so + (2 * w + 1) * 128); });
        sfor<0, 5>([&](auto G) { glds<G.v * 1024, G.v * 8 * 1280>(sd, vo, lb + (140 + w * 5) * 1024, so + (8 + (w >> 1)) * 128 + (w & 1) * 40 * 1280); });
        wait_vm<0>();
        ST(7);
        bar();
      }
      {
        PHASE_IDS
        IDS_G2
        const u32x4 sa = make_srd(kq < 2 ? opq_p(Sre_) : opq_p(Sim_), S_ * S_ * 2);
        const unsigned va = (lane >> 3) * 4096 + (((lane & 7) ^ (lane >> 3)) << 4);
        const unsigned soa = (P * 128 + w * 32) * 4096 + (kq & 1) * 2048;
        __builtin_amdgcn_s_sleep(ASLEEP2);
#pragma unroll
        for (int s = 0; s < 3; ++s) {
          const unsigned l0 = G2_LA(0) + s * G2_AS, l1 = G2_LA(1) + s * G2_AS, so = soa + (s) * 128;
          glds<0, 0>(sa, va, l0, so); glds<1024, 8 * 4096>(sa, va, l0, so);
          glds<0, 16 * 4096>(sa, va, l1, so); glds<1024, 24 * 4096>(sa, va, l1, so);
        }
        bar();
      }
    }
    return;
  }

  {
    const u64 e = __hip_atomic_load(flag_ptr(ws_, FL_E), RLX_AGENT);
    epoch = (u64)rfl((unsigned)e) + ((u64)rfl((unsigned)(e >> 32)) << 32) + 1;
  }
#pragma unroll 1
  for (int L = 0; L < 2; ++L) {
    const int sbase = L * 20;
    (void)sbase;
    {
      PHASE_IDS
      IDS_G3
      f32x4 acc[2][5];
      float mu[2][4], rs[2][4], csv[5];
      const float sign = ct < 8 ? 1.f : -1.f;
      if (L == 0) {
        const unsigned ra0 = lb + w * 4096 + i16 * 128 + ((q ^ (lane & 7)) << 4);
        const unsigned rb0 = lb + 16384 + i16 * 128 + ((q ^ (lane & 7)) << 4);
        const f32x4 z4 = {0.f, 0.f, 0.f, 0.f};
        f32x4 cs0 = z4, cs1 = z4, rs_[2] = {z4, z4}, rq_[2] = {z4, z4};
#pragma unroll
        for (int r = 0; r < 2; ++r)
#pragma unroll
          for (int f = 0; f < 5; ++f) acc[r][f] = z4;
        bf16x8 fr[2][8];
        bf16x8 one = __builtin_bit_cast(bf16x8, u32x4{0x3f803f80u, 0x3f803f80u, 0x3f803f80u, 0x3f803f80u});
        auto half = [&](auto KK, unsigned so) {
          constexpr int kk = KK.v;
          const unsigned pa = (ra0 ^ ((kk ^ 1) * 64)) + so, pb = (rb0 ^ ((kk ^ 1) * 64)) + so;
          sfor<0, 16>([&](auto M) {
            constexpr int m = M.v;
            if constexpr (m < 10) mma(acc[m & 1][m >> 1], fr[kk][m & 1], fr[kk][2 + (m >> 1)]);
            else if constexpr (m == 10) mma(cs0, one, fr[kk][7]);
            else if constexpr (m == 11) mma(cs1, one, fr[kk][6]);
            else if constexpr (m < 14) mma(rs_[m - 12], fr[kk][m - 12], one);
            else mma(rq_[m - 14], fr[kk][m - 14], fr[kk][m - 14]);
            constexpr int n = m >> 1;
            if constexpr ((m & 1) == 0) fr[kk ^ 1][n] = n < 2 ? lds_rd(pa + n * 2048) : (n < 7 ? lds_rd(pb + (n - 2) * 2048) : lds_rd(pb + w * 2048));
            sb();
          });
        };
        asm volatile("s_nop 1" : "+v"(cs0), "+v"(cs1), "+v"(rs_[0]), "+v"(rs_[1]), "+v"(rq_[0]), "+v"(rq_[1]), "+v"(one));
        fence_all(acc);
        bar();
        ST(38);
        fr[0][0] = lds_rd(ra0); fr[0][1] = lds_rd(ra0 + 2048);
#pragma unroll
        for (int f = 0; f < 5; ++f) fr[0][2 + f] = lds_rd(rb0 + f * 2048);
        fr[0][7] = lds_rd(rb0 + w * 2048);
        sb();
        unsigned so = 0;
#pragma unroll 1
        for (int i = 0; i < 10; ++i) {
          half(IC<0>{}, so);
          wait_lgkm0();
          bar();
          so = so == 4 * K1_STAGE ? 0 : so + K1_STAGE;
          half(IC<1>{}, so);
        }
        fence_all(acc);
        asm volatile("" : "+v"(cs0), "+v"(cs1), "+v"(rs_[0]), "+v"(rs_[1]), "+v"(rq_[0]), "+v"(rq_[1]));
        const unsigned csb = lb + K1_CS;
        if (q == 0) {
          lds_wr1(csb + (w * 16 + i16) * 4, cs0[0]);
          if (w == 0) lds_wr1(csb + (64 + i16) * 4, cs1[0]);
        }
        wait_lgkm0();
        bar();
        ST(18);
#pragma unroll
        for (int f = 0; f < 5; ++f) csv[f] = lds_rd1(csb + (f * 16 + i16) * 4);
#pragma unroll
        for (int r = 0; r < 2; ++r)
#pragma unroll
          for (int j = 0; j < 4; ++j) {
            float m_, r_;
            ln_coef(rs_[r][j], __shfl(rq_[r][j], q * 20 + j), m_, r_);
            mu[r][j] = -m_; rs[r][j] = sign * r_;
          }
        if (w == 0) {
          const rsrc_t rc = make_rsrc(ws + WS_CSD, 256 * 320);
          st4(__float_as_uint(q == 0 ? csv[0] : (q == 1 ? csv[1] : (q == 2 ? csv[2] : csv[3]))), rc, lane * 4, bid * 320);
          st4(__float_as_uint(csv[4]), rc, q == 0 ? 256 + lane * 4 : OOB, bid * 320);
        }
      } else {
        {
          const rsrc_t rc = make_rsrc(ws + WS_CSD, 256 * 320);
#pragma unroll
          for (int f = 0; f < 5; ++f) csv[f] = __uint_as_float(__builtin_amdgcn_raw_buffer_load_b32(rc, (f * 16 + i16) * 4, bid * 320, 16));
        }
        poll<PD4>(ws, FL_4 + P * 64 + w * 16 + i16, 16, epoch, lane);
        ST(17);
        const rsrc_t rx = make_rsrc(ws + WS_X2, SZ_X);
        const unsigned sox = (P * 8 + w * 2) * 20 * 1024;
        bf16x8 a[20][2];
#pragma unroll
        for (int k = 0; k < 3; ++k) { a[k][0] = LDX(0, k); a[k][1] = LDX(1, k); }
        const rsrc_t rst = make_rsrc(ws + WS_ST2, SZ_ST);
        const unsigned sto = ((lane >> 5) * 4 * S_ + P * 128 + w * 32 + (lane & 31)) * 8;
        const u32x2 sp0 = ld8u(rst, sto, 0), sp1 = ld8u(rst, sto, S_ * 8), sp2 = ld8u(rst, sto, 2 * S_ * 8), sp3 = ld8u(rst, sto, 3 * S_ * 8);
        sb();
        bar();
        bf16x8 b[2][5];
        const unsigned rb = lb + i16 * 128 + ((q ^ (lane & 7)) << 4);
#pragma unroll
        for (int f = 0; f < 5; ++f) b[0][f] = lds_rd(rb + wd_pos(0) * 1024 + f * 2048);
        sb();
        sfor<0, 20>([&](auto K) {
          constexpr int k = K.v;
          sfor<0, 10>([&](auto M) {
            constexpr int f = M.v >> 1, r = M.v & 1;
            if constexpr (k == 0) mma0(acc[r][f], a[0][r], b[0][f]); else mma(acc[r][f], a[k][r], b[k & 1][f]);
            if constexpr (k + 1 < 20 && r == 0) b[(k + 1) & 1][f] = lds_rd((rb ^ (((k + 1) & 1) * 64)) + wd_pos((k + 1) >> 1) * 1024 + f * 2048);
            if constexpr (r == 1 && f < 4) {
              constexpr int kn = k < 7 ? 2 * k + 3 + (f >> 1) : (f < 2 ? k + 10 : 20);
              if constexpr (kn < 20) a[kn][f & 1] = LDX(f & 1, kn);
            }
            sb();
          });
          if constexpr (k == 0) ST(40);
          if constexpr (k == 4) ST(41);
          if constexpr (k == 9) ST(42);
        });
        fence_all(acc);
        bar();
        ST(39);
        float s1 = (__uint_as_float(sp0[0]) + __uint_as_float(sp1[0])) + (__uint_as_float(sp2[0]) + __uint_as_float(sp3[0]));
        float s2 = (__uint_as_float(sp0[1]) + __uint_as_float(sp1[1])) + (__uint_as_float(sp2[1]) + __uint_as_float(sp3[1]));
        auto ta = __builtin_amdgcn_permlane32_swap(__float_as_uint(s1), __float_as_uint(s1), false, false);
        auto tb = __builtin_amdgcn_permlane32_swap(__float_as_uint(s2), __float_as_uint(s2), false, false);
        float m, rr;
        ln_coef(__uint_as_float(ta[0]) + __uint_as_float(ta[1]), __uint_as_float(tb[0]) + __uint_as_float(tb[1]), m, rr);
        m = -m; rr *= sign;
#pragma unroll
        for (int r = 0; r < 2; ++r)
#pragma unroll
          for (int e = 0; e < 4; ++e) { mu[r][e] = __shfl(m, r * 16 + q * 4 + e); rs[r][e] = __shfl(rr, r * 16 + q * 4 + e); }
      }
      g1_store(acc, mu, rs, csv, make_rsrc(ws + (L ? WS_AB2 : WS_AB1), SZ_AB), P, ct, w, i16, q);
      ST(L + 19);
      publish(ws, FL_5 + ((L * 4 + (ct >> 3) * 2 + (P >> 3)) * 4 + ((ct & 7) >> 1)) * 64 + ((P & 7) * 2 + (ct & 1)) * 4 + w, epoch, lane);
    }
    {
      PHASE_IDS
      IDS_G2
      const unsigned m = w >> 1, n = w & 1;
      const unsigned ra = lb + G2_A + (m * 64 + i16) * 128 + ((q ^ (lane & 7)) << 4);
      const unsigned rb = lb + n * 10240 + lane * 16;
      const unsigned row = P * 128 + m * 64 + kq * 16 + i16, ct = 2 * c4 + n;
      const TileOff ro = tile_offsets(L == 0, row, ct, q), oo = tile_offsets(false, row, ct, q);
      const rsrc_t rxp = make_rsrc(ws + WS_XP + L * SZ_XP, SZ_XP);
      const unsigned xo = ((P * 4 + c4) * 48 + w) * XSLOT;
      f32x4 acc[4][5];
#pragma unroll
      for (int i = 0; i < 4; ++i)
#pragma unroll
        for (int f = 0; f < 5; ++f) acc[i][f] = f32x4{0.f, 0.f, 0.f, 0.f};
      bf16x8 fa[2][4], fb[2][5];
      unsigned ra1 = ra ^ 64, rb2 = rb + 2 * G2_BS;
      asm volatile("" : "+v"(ra1), "+v"(rb2));
      auto half = [&](auto KK, auto SL) {
        constexpr int kk = KK.v, sl = SL.v;
        sfor<0, 20>([&](auto M) {
          constexpr int mm = M.v;
          mma(acc[mm / 5][mm % 5], fb[kk][mm % 5], fa[kk][mm / 5]);
          if constexpr (mm < 5) fb[kk ^ 1][mm] = lds_rd((sl < 2 ? rb : rb2) + (sl & 1) * G2_BS + (kk ^ 1) * 5120 + mm * 1024);
          else if constexpr (mm < 9) fa[kk ^ 1][mm - 5] = lds_rd((kk ? ra : ra1) + sl * G2_AS + (mm - 5) * 2048);
          sb();
        });
      };
      if (L) poll0(ws, FL_4 + P * 64 + (m * 4 + kq) * 8 + ct, 1, epoch, lane);
      ResTile rt = load_res(make_rsrc(L ? (const void*)(ws + WS_X2) : (const void*)opq_p(x0_), SZ_X), ro);
      fence_all(acc);
      bar();
      ST(sbase + 1);
#pragma unroll
      for (int f = 0; f < 5; ++f) fb[0][f] = lds_rd(rb + f * 1024);
#pragma unroll
      for (int i = 0; i < 4; ++i) fa[0][i] = lds_rd(ra + i * 2048);
      sb();
      auto stage = [&](auto U) {
        half(IC<0>{}, U);
        wait_lgkm0();
        bar();
        half(IC<1>{}, IC<(U.v + 1) & 3>{});
      };
#pragma unroll 1
      for (int g = 0; g < 3; ++g) sfor<0, 4>(stage);
      sfor<0, 3>(stage);
      half(IC<0>{}, IC<3>{});
      wait_lgkm0();
      bar();
      {
        unsigned p[3][10];
        auto pack = [&](auto N) {
          constexpr int i = N.v / 5, f = N.v % 5;
          asm volatile("" : "+v"(acc[i][f]));
          p[i][2 * f] = pk2(acc[i][f][0], acc[i][f][1]); p[i][2 * f + 1] = pk2(acc[i][f][2], acc[i][f][3]);
          const unsigned o = xo + (((kq + 1 + i) & 3) * 3 + 2 - i) * 4 * XSLOT;
          if constexpr (f == 1) st16(u32x4{p[i][0], p[i][1], p[i][2], p[i][3]}, rxp, lane * 16, o);
          if constexpr (f == 3) st16(u32x4{p[i][4], p[i][5], p[i][6], p[i][7]}, rxp, lane * 16 + 1024, o);
          if constexpr (f == 4) st8(u32x2{p[i][8], p[i][9]}, rxp, lane * 8 + 2048, o);
        };
        sfor<0, 20>([&](auto M) {
          constexpr int mm = M.v;
          mma(acc[mm / 5][mm % 5], fb[1][mm % 5], fa[1][mm / 5]);
          if constexpr (mm >= 8) pack(IC<mm - 8>{});
          sb();
        });
        ST(sbase + 2);
        pack(IC<12>{}); pack(IC<13>{}); pack(IC<14>{});
      }
      f32x4 (&v)[5] = acc[3];
      fence_acc(v);
      const unsigned fx = FL_X + ((L * 64 + P * 4 + c4) * 4 + w) * 4;
      ST(sbase + 3);
      publish(ws, fx + kq, epoch, lane);
      ST(sbase + 4);
      poll<PDX>(ws, fx + ((kq + 1 + lane) & 3), 3, epoch, lane);
      ST(sbase + 5);
      const unsigned xi = xo + kq * 12 * XSLOT;
      u32x4 g0[3], g1[3]; u32x2 g2[3];
#pragma unroll
      for (int s = 0; s < 3; ++s) {
        g0[s] = ld16u(rxp, lane * 16, xi + s * 4 * XSLOT);
        g1[s] = ld16u(rxp, lane * 16 + 1024, xi + s * 4 * XSLOT);
        g2[s] = ld8u(rxp, lane * 8 + 2048, xi + s * 4 * XSLOT);
      }
      sb();
#pragma unroll
      for (int s = 0; s < 3; ++s) {
        v[0] += f32x4{bflo(g0[s][0]), bfhi(g0[s][0]), bflo(g0[s][1]), bfhi(g0[s][1])};
        v[1] += f32x4{bflo(g0[s][2]), bfhi(g0[s][2]), bflo(g0[s][3]), bfhi(g0[s][3])};
        v[2] += f32x4{bflo(g1[s][0]), bfhi(g1[s][0]), bflo(g1[s][1]), bfhi(g1[s][1])};
        v[3] += f32x4{bflo(g1[s][2]), bfhi(g1[s][2]), bflo(g1[s][3]), bfhi(g1[s][3])};
        v[4] += f32x4{bflo(g2[s][0]), bfhi(g2[s][0]), bflo(g2[s][1]), bfhi(g2[s][1])};
      }
      finish_tile(v, rt, make_rsrc(ws + (L ? WS_X3 : WS_X1), SZ_X), oo, true, make_rsrc(ws + (L ? WS_ST3 : WS_ST1), SZ_ST),
                  q == 0 ? (ct * S_ + row) * 8 : OOB);
      ST(sbase + 6);
      publish(ws, FL_2 + (L * 16 + P) * 64 + (m * 4 + kq) * 8 + ct, epoch, lane);
      ST(sbase + 7);
    }
    u32x4 y2[2]; u32x2 y1[2];
    {
      PHASE_IDS
      IDS_G3
      typedef __attribute__((ext_vector_type(2))) short s16x2;
      poll<PD2>(ws, FL_2 + (L * 16 + P) * 64 + w * 16 + i16, 16, epoch, lane);
      ST(sbase + 8);
      const rsrc_t rx = make_rsrc(ws + (L ? WS_X3 : WS_X1), SZ_X);
      const rsrc_t rst = make_rsrc(ws + (L ? WS_ST3 : WS_ST1), SZ_ST);
      const unsigned sox = (P * 8 + w * 2) * 20 * 1024;
      const unsigned par = ct & 1;
      bf16x8 a[20][2];
#pragma unroll
      for (int k = 0; k < 3; ++k) { a[k][0] = LDX(0, k); a[k][1] = LDX(1, k); }
      const unsigned sto = (2 * q * S_ + P * 128 + w * 32 + i16) * 8;
      u32x2 sa0, sa1, sb0, sb1;
      sb();
      bar();
      f32x4 acc[2][4];
      bf16x8 b[2][4];
      const unsigned rb = lb + lane * 16;
#pragma unroll
      for (int f = 0; f < 4; ++f) b[0][f] = lds_rd(rb + f * 1024);
      sb();
      sfor<0, 20>([&](auto K) {
        constexpr int k = K.v;
        const unsigned pb = k + 1 < 20 ? rb + (k + 1) * 4096 : rb + 80 * 1024;
        if constexpr (k == 9) { sa0 = ld8u(rst, sto, 0); sa1 = ld8u(rst, sto, S_ * 8); sb0 = ld8u(rst, sto + 128, 0); sb1 = ld8u(rst, sto + 128, S_ * 8); }
        sfor<0, 8>([&](auto M) {
          constexpr int f = M.v >> 1, r = M.v & 1;
          if constexpr (k == 0) mma0(acc[r][f], b[0][f], a[0][r]); else mma(acc[r][f], b[k & 1][f], a[k][r]);
          if constexpr (r == 0 && (k + 1 < 20 || f < 3)) b[(k + 1) & 1][f] = lds_rd(pb + f * 1024);
          if constexpr (r == 1 && 2 * k + 3 + (f >> 1) < 20) a[2 * k + 3 + (f >> 1)][f & 1] = LDX(f & 1, 2 * k + 3 + (f >> 1));
          sb();
        });
        if constexpr (k == 0) ST(sbase + 9);
        if constexpr (k == 3) ST(51 + L * 5);
        if constexpr (k == 7) ST(52 + L * 5);
        if constexpr (k == 11) ST(53 + L * 5);
        if constexpr (k == 15) ST(54 + L * 5);
        if constexpr (k == 19) ST(55 + L * 5);
      });
      float mu[2], rs[2];
      ln_coef(xsum16_32(__uint_as_float(sa0[0]) + __uint_as_float(sa1[0])), xsum16_32(__uint_as_float(sa0[1]) + __uint_as_float(sa1[1])), mu[0], rs[0]);
      ln_coef(xsum16_32(__uint_as_float(sb0[0]) + __uint_as_float(sb1[0])), xsum16_32(__uint_as_float(sb0[1]) + __uint_as_float(sb1[1])), mu[1], rs[1]);
      auto act = [&](const f32x4& c, int r, const f32x4& cs, unsigned (&pk)[2]) {
        const f32x2 nm = {-mu[r], -mu[r]}, r2 = {rs[r], rs[r]};
#pragma unroll
        for (int e = 0; e < 2; ++e) {
          const f32x2 z = __builtin_elementwise_fma(nm, f32x2{cs[2 * e], cs[2 * e + 1]}, f32x2{c[2 * e], c[2 * e + 1]}) * r2;
          const s16x2 hb = __builtin_bit_cast(s16x2, __builtin_convertvector(z, bf16x2));
          pk[e] = __builtin_bit_cast(unsigned, __builtin_elementwise_max(hb, s16x2{0, 0}));
        }
      };
      auto pair = [&](const unsigned (&u)[2], const unsigned (&v)[2]) {
        auto t0 = __builtin_amdgcn_permlane16_swap(u[0], v[0], false, false);
        auto t1 = __builtin_amdgcn_permlane16_swap(u[1], v[1], false, false);
        return u32x4{t0[0], t1[0], t0[1], t1[1]};
      };
      const unsigned csb = lb + KM_CS1 + q * 16;
      f32x4 c1s[4];
#pragma unroll
      for (int f = 0; f < 4; ++f) c1s[f] = lds_rdf(csb + (f + par) * 64);
      asm volatile("s_nop 7\n\ts_nop 1" : "+v"(acc[0][0]), "+v"(acc[0][1]), "+v"(acc[0][2]), "+v"(acc[0][3]),
                                          "+v"(acc[1][0]), "+v"(acc[1][1]), "+v"(acc[1][2]), "+v"(acc[1][3]));
      wait_lgkm0();
      bar();
      ST(43 + L * 4);
      {
        const rsrc_t rh = make_rsrc(ws + (L ? WS_H2 : WS_H1), SZ_H);
        const unsigned vo = ((((q & 1) << 1) | (q >> 1)) * 16 + i16) * 16;
        const unsigned so = ((P * 8 + w * 2) * 54 + 7 * (ct >> 1) + 4 * par) * 1024;
        u32x4 y[2][2];
#pragma unroll
        for (int p = 0; p < 2; ++p) {
          const f32x4 c0 = c1s[2 * p], c1 = c1s[2 * p + 1];
#pragma unroll
          for (int r = 0; r < 2; ++r) {
            unsigned u[2], v[2];
            act(acc[r][2 * p], r, c0, u); act(acc[r][2 * p + 1], r, c1, v);
            y[p][r] = pair(u, v);
            st16_raw(y[p][r], rh, p == 1 && ct == 15 ? OOB : vo, so + (r * 54 + p) * 1024);
          }
        }
        st_guard(y[0][0], y[0][1], y[1][0], y[1][1]);
      }
      ST(44 + L * 4);
      sb();
      sfor<0, 20>([&](auto K) {
        constexpr int k = K.v;
        const unsigned pb = rb + 80 * 1024 + (k + 1) * 3072;
        sfor<0, 6>([&](auto M) {
          constexpr int f = M.v >> 1, r = M.v & 1;
          if constexpr (k == 0) mma0(acc[r][f], b[0][f], a[0][r]); else mma(acc[r][f], b[k & 1][f], a[k][r]);
          if constexpr (r == 0 && k + 1 < 20) b[(k + 1) & 1][f] = lds_rd(pb + f * 1024);
          sb();
        });
        if constexpr (k == PUB1) publish(ws, FL_3 + (L * 32 + P) * 64 + w * 16 + ct, epoch, lane);
      });
      const f32x4 c0 = lds_rdf(csb + (4 + par) * 64), c1 = lds_rdf(csb + (5 + par) * 64), c2 = lds_rdf(csb + (par ? 0 : 6) * 64);
      asm volatile("s_nop 7\n\ts_nop 1" : "+v"(acc[0][0]), "+v"(acc[0][1]), "+v"(acc[0][2]), "+v"(acc[1][0]), "+v"(acc[1][1]), "+v"(acc[1][2]));
      ST(sbase + 10);
#pragma unroll
      for (int r = 0; r < 2; ++r) {
        unsigned u[2], v[2], s[2];
        act(acc[r][0], r, c0, u); act(acc[r][1], r, c1, v); act(acc[r][2], r, c2, s);
        y2[r] = pair(u, v);
        y1[r] = u32x2{s[0], s[1]};
      }
      asm volatile("" : "+v"(y2[0]), "+v"(y2[1]), "+v"(y1[0]), "+v"(y1[1]));
      ST(sbase + 11);
    }
    {
      PHASE_IDS
      IDS_G4
      const unsigned row = rt64 * 64 + w * 16 + i16;
      const rsrc_t rh = make_rsrc(ws + (L ? WS_H2 : WS_H1), SZ_H);
      const unsigned soh = (rt64 * 4 * 54 + w * 14) * 1024;
      const unsigned vl = lane * 16, vz = lane * 16 + (w < 3 ? 0u : OOB);
      bf16x8 a[14][4], b[2][5];
      unsigned sh[4];
#pragma unroll
      for (int i = 0; i < 4; ++i) sh[i] = soh + ((w + 1 + i) & 3) * 54 * 1024;
#define LDH(t, i) a[t][i] = ld16(rh, (ORD[t] < 12 ? vl : vz) + (ORD[t] & 3) * 1024, sh[i] + (ORD[t] >> 2) * 4096)
      const TileOff ro = tile_offsets(false, row, c8, q), oo = tile_offsets(L != 0, row, c8, q);
      const unsigned f3 = FL_3 + (L * 32 + (rt64 >> 1)) * 64 + ((rt64 & 1) * 2 + (lane & 1)) * 16 + 4 * w + ((lane >> 1) & 3);
      poll0(ws, f3, 8, epoch, lane);
      ST(sbase + 13);
      sfor<0, 8>([&](auto N) { LDH(N.v / 4, N.v % 4); });
      sb();
      {
        const unsigned P = (xcd >> 1) * 4 + (idx >> 3), ct = (xcd & 1) * 8 + (idx & 7), par = ct & 1;
        const unsigned vo = ((((q & 1) << 1) | (q >> 1)) * 16 + i16) * 16;
        const unsigned v1 = ((2 * par + (q >> 1)) * 16 + i16) * 16 + (q & 1) * 8;
        const unsigned so = ((P * 8 + w * 2) * 54 + 7 * (ct >> 1)) * 1024;
#pragma unroll
        for (int r = 0; r < 2; ++r) {
          st16_raw(y2[r], rh, ct == 15 ? OOB : vo, so + (r * 54 + 2 + 4 * par) * 1024);
          st8(y1[r], rh, v1, so + (r * 54 + 3) * 1024);
        }
        st_guard(y2[0], y2[1]);
      }
      ResTile rt;
      u64 fl2;
      const rsrc_t rr = make_rsrc(ws + (L ? WS_X3 : WS_X1), SZ_X);
      sb();
      bar();
      f32x4 acc[4][5];
      const unsigned rbe = lb + w * 20480 + lane * 16, rbl = lb + G4_LATE + w * 15360 + lane * 16;
#pragma unroll
      for (int f = 0; f < 5; ++f) b[0][f] = lds_rd(rbe + f * 1024);
      sb();
      sfor<0, 14>([&](auto T) {
        constexpr int t = T.v;
        constexpr int ta = t < 5 ? (t == 0 ? 2 : t + 3) : (t == 5 ? -1 : (t == 6 ? 8 : (t + 3 < 14 ? t + 3 : -1)));
        if constexpr (t == 3 || t == 6 || t == 10) { wait_lgkm0(); bar(); }
        if constexpr (t == 6) { if (!__all(fl2 == epoch)) poll0(ws, f3 + 16 * 64, 8, epoch, lane); }
        sfor<0, 20>([&](auto M) {
          constexpr int m = M.v, i = m / 5, f = m % 5;
          if constexpr (t == 0) mma0(acc[i][f], b[0][f], a[0][i]); else mma(acc[i][f], b[t & 1][f], a[t][i]);
          if constexpr (t + 1 < 14 && (m & 1) == 1 && m < 10)
            b[(t + 1) & 1][m >> 1] = lds_rd((g4_late(t + 1) ? rbl : rbe) + g4_slot(t + 1) + (m >> 1) * 1024);
          if constexpr (ta >= 0 && (m & 1) == 0 && m < 8) LDH(ta, m >> 1);
          if constexpr (t == 6 && (m & 1) == 0 && m >= 8 && m < 16) LDH(9, (m - 8) >> 1);
          if constexpr (t == 0 && (m & 1) == 0 && m >= 8 && m < 16) LDH(3, (m - 8) >> 1);
          if constexpr (t == 2 && m == 10) rt = load_res(rr, ro);
          if constexpr (t == 4 && m == 18) fl2 = __hip_atomic_load(flag_ptr(ws, f3 + 16 * 64), RLX_AGENT);
          if constexpr (t == 13 && m >= 8) {
            asm volatile("" : "+v"(acc[(m - 8) / 5][(m - 8) % 5]));
            lds_wrf(rbl + (m - 8) * 1024, acc[(m - 8) / 5][(m - 8) % 5]);
          }
          sb();
        });
        if constexpr (t == 2) {
          wait_vm<19>();
          raise(ws, FL_3 + (L * 32 + 16 + (xcd >> 1) * 4 + (idx >> 3)) * 64 + w * 16 + (xcd & 1) * 8 + (idx & 7), epoch, lane);
          ST(sbase + 12);
        }
        if constexpr (t == 0) ST(sbase + 14);
        if constexpr (t == 3) ST(45 + L * 4);
        if constexpr (t == 6) ST(46 + L * 4);
      });
      ST(sbase + 15);
#pragma unroll
      for (int f = 2; f < 5; ++f) { asm volatile("" : "+v"(acc[2][f])); lds_wrf(rbl + (10 + f) * 1024, acc[2][f]); }
      f32x4 (&v)[5] = acc[3];
      fence_acc(v);
      wait_lgkm0();
      bar();
#pragma unroll
      for (int s = 1; s < 4; ++s) {
        const unsigned src = lb + G4_LATE + ((w + s) & 3) * 15360 + (3 - s) * 5 * 1024 + lane * 16;
#pragma unroll
        for (int f = 0; f < 5; ++f) v[f] += lds_rdf(src + f * 1024);
      }
      finish_tile(v, rt, L ? make_rsrc(opq_p(out_), SZ_X) : make_rsrc(ws + WS_X2, SZ_X), oo, L == 0, make_rsrc(ws + WS_ST2, SZ_ST),
                  q == 0 ? (c8 * S_ + row) * 8 : OOB);
      ST(sbase + 16);
      if (L == 0) publish(ws, FL_4 + (rt64 >> 1) * 64 + ((rt64 & 1) * 4 + w) * 8 + c8, epoch, lane);
    }
  }
  asm volatile(".Lcw_end:");
  if (blockIdx.x == 0 && w0 == 0) raise(ws_, FL_E, epoch, tid0);
  ST(37);
}

extern "C" void kernel_launch(void* const* d_in, const int* in_sizes, int n_in, void* d_out, int out_size, void* d_ws,
                              size_t ws_size, hipStream_t stream) {
  char* ws = (char*)d_ws;
  (void)hipFuncSetAttribute((const void*)k_all, hipFuncAttributeMaxDynamicSharedMemorySize, KM_LDS);
  k_all<<<256, 512, KM_LDS, stream>>>((const u16*)d_in[0], (const u16*)d_in[1], (const u16*)d_in[2], (const u16*)d_in[3],
                                     (const u16*)d_in[4], (const u16*)d_in[6], (const u16*)d_in[7], ws, (u16*)d_out STAMP_PASS(0));
}
```

```cpp
#include <hip/hip_runtime.h>
#include <hip/hip_bf16.h>
#include <stdint.h>

#define DI __device__ __forceinline__
typedef unsigned short u16;
typedef __attribute__((ext_vector_type(8))) __bf16 bf16x8;
typedef __attribute__((ext_vector_type(2))) __bf16 bf16x2;
typedef __attribute__((ext_vector_type(4))) float f32x4;
typedef __attribute__((ext_vector_type(2))) float f32x2;
typedef __attribute__((ext_vector_type(4))) unsigned u32x4;
typedef __attribute__((ext_vector_type(2))) unsigned u32x2;
typedef __attribute__((ext_vector_type(4))) short s16x4;
#define LDS_AS __attribute__((address_space(3)))
#define GLB_AS __attribute__((address_space(1)))
typedef LDS_AS char* ldsp;
typedef unsigned long long u64;
typedef GLB_AS u64 gu64;
typedef __amdgpu_buffer_rsrc_t rsrc_t;
#define RLX_AGENT __ATOMIC_RELAXED, __HIP_MEMORY_SCOPE_AGENT

constexpr int S_ = 2048, D_ = 640, F_ = 1728;
constexpr float EPS = 1e-5f;
constexpr unsigned OOB = 0x7f000000u;

constexpr unsigned SZ_AB = 8 * 128 * 5 * 1024;
constexpr unsigned SZ_X = 128 * 20 * 1024;
constexpr unsigned SZ_H = 128 * 54 * 1024;
constexpr unsigned SZ_W1 = 16 * 140 * 1024;
constexpr unsigned SZ_W2 = 8 * 54 * 5 * 1024;
constexpr unsigned SZ_ST = 2048 * 64;
constexpr unsigned WS_AB1 = 0;
constexpr unsigned WS_AB2 = WS_AB1 + SZ_AB;
constexpr unsigned WS_X1 = WS_AB2 + SZ_AB;
constexpr unsigned WS_X2 = WS_X1 + SZ_X;
constexpr unsigned WS_X3 = WS_X2 + SZ_X;
constexpr unsigned WS_H1 = WS_X3 + SZ_X;
constexpr unsigned WS_H2 = WS_H1 + SZ_H;
constexpr unsigned WS_W1 = WS_H2 + SZ_H;
constexpr unsigned WS_W2 = WS_W1 + SZ_W1;
constexpr unsigned WS_ST1 = WS_W2 + SZ_W2;
constexpr unsigned WS_ST2 = WS_ST1 + SZ_ST;
constexpr unsigned WS_ST3 = WS_ST2 + SZ_ST;
constexpr unsigned WS_CS1 = WS_ST3 + SZ_ST;
constexpr unsigned WS_CSD = WS_CS1 + 20 * 1728 * 4;
constexpr unsigned XSLOT = 2560;
constexpr unsigned SZ_XP = 256 * 12 * XSLOT;
constexpr unsigned WS_XP = (WS_CSD + 256 * 320 + 4095) & ~4095u;
constexpr unsigned WS_FLG = WS_XP + 2 * SZ_XP;
constexpr unsigned FL_X = 0;
constexpr unsigned FL_2 = FL_X + 2048;
constexpr unsigned FL_3 = FL_2 + 2048;
constexpr unsigned FL_4 = FL_3 + 4096;
constexpr unsigned FL_5 = FL_4 + 1024;
constexpr unsigned FL_W = FL_5 + 3072;
constexpr unsigned FL_E = FL_W + 1088;
constexpr unsigned WS_END = WS_FLG + (FL_E + 1) * 128;

DI u32x4 make_srd(const void* p, unsigned bytes) {
  uint64_t a = (uint64_t)p;
  u32x4 r;
  r[0] = (unsigned)a; r[1] = (unsigned)(a >> 32) & 0xffffu; r[2] = bytes; r[3] = 0x00020000u;
  return r;
}
DI rsrc_t make_rsrc(const void* p, unsigned bytes) {
  return __builtin_amdgcn_make_buffer_rsrc((void*)p, (short)0, (int)bytes, 0x00020000);
}
template <int LOFF, int GOFF>
DI void glds(u32x4 srd, unsigned voff, unsigned ldsb, unsigned soff) {
  unsigned tmp;
  asm volatile("s_add_u32 m0, %1, %2\n\ts_add_u32 %0, %3, %4\n\tbuffer_load_dwordx4 %5, %6, %0 offen lds"
               : "=&s"(tmp)
               : "s"(ldsb), "i"(LOFF), "s"(soff), "i"(GOFF), "v"(voff), "s"(srd)
               : "memory", "scc");
}
template <int N> DI void wait_vm() { asm volatile("s_waitcnt vmcnt(%0)" ::"i"(N) : "memory"); }
DI void wait_lgkm0() { asm volatile("s_waitcnt lgkmcnt(0)" ::: "memory"); }
DI void sb() { __builtin_amdgcn_sched_barrier(0); }
DI void bar() { asm volatile("s_barrier" ::: "memory"); }

DI bf16x8 lds_rd(unsigned addr) { return *(const LDS_AS bf16x8*)(ldsp)(uintptr_t)addr; }
DI f32x4 lds_rdf(unsigned addr) { return *(const LDS_AS f32x4*)(ldsp)(uintptr_t)addr; }
DI void lds_wrf(unsigned addr, f32x4 v) { *(LDS_AS f32x4*)(ldsp)(uintptr_t)addr = v; }
DI float lds_rd1(unsigned addr) { return *(const LDS_AS float*)(ldsp)(uintptr_t)addr; }
DI void lds_wr1(unsigned addr, float v) { *(LDS_AS float*)(ldsp)(uintptr_t)addr = v; }
DI void mma(f32x4& c, const bf16x8& a, const bf16x8& b) {
  asm volatile("v_mfma_f32_16x16x32_bf16 %0, %1, %2, %0" : "+v"(c) : "v"(a), "v"(b));
}
DI void mma0(f32x4& c, const bf16x8& a, const bf16x8& b) {
  asm volatile("v_mfma_f32_16x16x32_bf16 %0, %1, %2, 0" : "=&v"(c) : "v"(a), "v"(b));
}
DI void fence_acc(f32x4 (&a)[5]) { asm volatile("s_nop 7\n\ts_nop 1" : "+v"(a[0]), "+v"(a[1]), "+v"(a[2]), "+v"(a[3]), "+v"(a[4])); }
DI void pin_acc(f32x4 (&a)[5]) { asm volatile("" : "+v"(a[0]), "+v"(a[1]), "+v"(a[2]), "+v"(a[3]), "+v"(a[4])); }
template <int M> DI void fence_all(f32x4 (&a)[M][5]) {
#pragma unroll
  for (int i = 1; i < M; ++i) pin_acc(a[i]);
  fence_acc(a[0]);
#pragma unroll
  for (int i = 1; i < M; ++i) pin_acc(a[i]);
}
DI unsigned pk2(float a, float b) {
  f32x2 v = {a, b};
  return __builtin_bit_cast(unsigned, __builtin_convertvector(v, bf16x2));
}
DI float bflo(unsigned u) { return __uint_as_float(u << 16); }
DI float bfhi(unsigned u) { return __uint_as_float(u & 0xffff0000u); }
DI float dot2(unsigned a, unsigned b, float c) {
  return __builtin_amdgcn_fdot2_f32_bf16(__builtin_bit_cast(bf16x2, a), __builtin_bit_cast(bf16x2, b), c, false);
}
DI float xsum16_32(float x) {
  auto s = __builtin_amdgcn_permlane16_swap(__float_as_uint(x), __float_as_uint(x), false, false);
  x = __uint_as_float(s[0]) + __uint_as_float(s[1]);
  auto t = __builtin_amdgcn_permlane32_swap(__float_as_uint(x), __float_as_uint(x), false, false);
  return __uint_as_float(t[0]) + __uint_as_float(t[1]);
}
template <int V> struct IC { static constexpr int v = V; };
template <int I, int N, class Fn> DI void sfor(Fn&& f) {
  if constexpr (I < N) { f(IC<I>{}); sfor<I + 1, N>(f); }
}
DI unsigned rfl(unsigned v) { return __builtin_amdgcn_readfirstlane(v); }
DI unsigned opq_v(unsigned x) { asm volatile("" : "+v"(x)); return x; }
DI unsigned opq_s(unsigned x) { asm volatile("" : "+s"(x)); return x; }
template <class T> DI T* opq_p(T* x) { asm volatile("" : "+s"(x)); return x; }
DI bf16x8 ld16(rsrc_t r, unsigned voff, unsigned soff) { return __builtin_bit_cast(bf16x8, __builtin_amdgcn_raw_buffer_load_b128(r, voff, soff, 16)); }
DI u32x4 ld16u(rsrc_t r, unsigned voff, unsigned soff) { return __builtin_amdgcn_raw_buffer_load_b128(r, voff, soff, 16); }
DI u32x2 ld8u(rsrc_t r, unsigned voff, unsigned soff) { return __builtin_amdgcn_raw_buffer_load_b64(r, voff, soff, 16); }
DI void st16(u32x4 v, rsrc_t r, unsigned voff, unsigned soff) {
  __builtin_amdgcn_raw_buffer_store_b128(v, r, voff, soff, 16);
  asm volatile("s_nop 1" :: "v"(v) : "memory");
}
DI void st16_raw(u32x4 v, rsrc_t r, unsigned voff, unsigned soff) { __builtin_amdgcn_raw_buffer_store_b128(v, r, voff, soff, 16); }
template <class... T> DI void st_guard(const T&... v) {
  (..., [](const auto& x) { asm volatile("" :: "v"(x) : "memory"); }(v));
  asm volatile("s_nop 1" ::: "memory");
  (..., [](const auto& x) { asm volatile("" :: "v"(x)); }(v));
}
DI void st8(u32x2 v, rsrc_t r, unsigned voff, unsigned soff) { __builtin_amdgcn_raw_buffer_store_b64(v, r, voff, soff, 16); }
DI void st4(unsigned v, rsrc_t r, unsigned voff, unsigned soff) { __builtin_amdgcn_raw_buffer_store_b32(v, r, voff, soff, 16); }

#define ST(i)
#define STAMP_ARGS
#define STAMP_PASS(k)

template <int KS> DI unsigned foff(unsigned row, unsigned col) {
  return ((row >> 4) * KS + (col >> 5)) * 1024u + (((col >> 3) & 3) * 16u + (row & 15)) * 16u + (col & 7) * 2u;
}

extern __shared__ __attribute__((aligned(1024))) char smem[];
DI unsigned lds_base() { return (unsigned)(uintptr_t)(ldsp)smem; }

DI void ln_coef(float s1, float s2, float& mu, float& rs) {
  mu = s1 * (1.f / D_);
  rs = __builtin_amdgcn_rsqf(fmaxf(s2 * (1.f / D_) - mu * mu, 0.f) + EPS);
}

DI void g1_store(const f32x4 (&acc)[2][5], const float (&mu)[2][4], const float (&rs)[2][4], const float (&csv)[5], rsrc_t rab,
                 unsigned P, unsigned ct, unsigned w, unsigned i16, unsigned q) {
  const unsigned kstep = (ct >> 3) * 64 + P * 4 + w;
  const unsigned cq = ((q & 1) << 1) | (q >> 1);
  const unsigned so = ((ct & 7) * 128 + kstep) * 5 * 1024;
  const unsigned vo = (cq * 16 + i16) * 16;
  u32x4 y[5];
#pragma unroll
  for (int f = 0; f < 5; ++f) {
    const f32x2 cs = {csv[f], csv[f]};
    unsigned pk[2][2];
#pragma unroll
    for (int r = 0; r < 2; ++r)
#pragma unroll
      for (int e = 0; e < 2; ++e) {
        const f32x2 t = __builtin_elementwise_fma(f32x2{mu[r][2 * e], mu[r][2 * e + 1]}, cs, f32x2{acc[r][f][2 * e], acc[r][f][2 * e + 1]}) *
                        f32x2{rs[r][2 * e], rs[r][2 * e + 1]};
        pk[r][e] = __builtin_bit_cast(unsigned, __builtin_convertvector(t, bf16x2));
      }
    auto t0 = __builtin_amdgcn_permlane16_swap(pk[0][0], pk[1][0], false, false);
    auto t1 = __builtin_amdgcn_permlane16_swap(pk[0][1], pk[1][1], false, false);
    y[f] = u32x4{t0[0], t1[0], t0[1], t1[1]};
    st16_raw(y[f], rab, vo + (f & 3) * 1024, so + (f >> 2) * 4096);
  }
  st_guard(y[0], y[1], y[2], y[3], y[4]);
}

constexpr int K1_STAGE = 26 * 1024;
constexpr int K1_TR = 5 * K1_STAGE;
constexpr int K1_CS = K1_TR + 5 * 4096;

DI void k1_issue(u32x4 sa, u32x4 sbd, unsigned vo, unsigned la, unsigned lbb, unsigned soa, unsigned sob, unsigned w) {
  glds<0, 0>(sa, vo, la, soa);
  glds<1024, 8 * 1280>(sa, vo, la, soa);
  glds<2048, 16 * 1280>(sa, vo, la, soa);
  glds<3072, 24 * 1280>(sa, vo, la, soa);
  glds<0, 0>(sbd, vo, lbb, sob);
  glds<4096, 32 * 1280>(sbd, vo, lbb, sob);
  if (w < 2) glds<8192, 64 * 1280>(sbd, vo, lbb, sob);
}
struct RtUnit { bool first; unsigned ks, g; };
DI RtUnit rt_unit(unsigned u) {
  RtUnit r;
  r.first = u < 540;
  if (r.first) { r.ks = u / 27u; r.g = u - r.ks * 27u; }
  else { const unsigned v = u - 540; r.ks = v / 10u; r.g = v - r.ks * 10u; }
  return r;
}
DI void retile_issue(unsigned u, const u16* w1, const u16* w2, unsigned ldst, unsigned lane) {
  const RtUnit t = rt_unit(u);
  const unsigned ld = t.first ? F_ * 2 : D_ * 2;
  u32x4 srd = make_srd(t.first ? w1 : w2, D_ * F_ * 2);
  const unsigned vo = (lane >> 3) * ld + (lane & 7) * 16;
  const unsigned so = t.ks * 32 * ld + t.g * 128;
  glds<0, 0>(srd, vo, ldst, so);
  glds<1024, 0>(srd, vo, ldst, so + 8 * ld);
  glds<2048, 0>(srd, vo, ldst, so + 16 * ld);
  glds<3072, 0>(srd, vo, ldst, so + 24 * ld);
}
DI unsigned w1_off(unsigned rt, unsigned ks) {
  const unsigned c = rt / 7u, f = rt - c * 7, g = (c & 1) ? (f == 0 ? 6 : f - 1) : f;
  return WS_W1 + (c * 140 + (g < 4 ? ks * 4 + g : 80 + ks * 3 + g - 4)) * 1024;
}
DI void retile_finish(unsigned u, unsigned f0, rsrc_t rws, unsigned ldst, unsigned lane) {
  const RtUnit t = rt_unit(u);
  const unsigned i = lane & 15, q = lane >> 4;
  const unsigned base = ldst + (8 * q + (i >> 2)) * 128 + (i & 3) * 8 + f0 * 32;
  u32x4 v[2];
#pragma unroll
  for (int f = 0; f < 2; ++f) {
    s16x4 lo = __builtin_amdgcn_ds_read_tr16_b64_v4i16((LDS_AS s16x4*)(uintptr_t)(base + f * 32));
    s16x4 hi = __builtin_amdgcn_ds_read_tr16_b64_v4i16((LDS_AS s16x4*)(uintptr_t)(base + f * 32 + 512));
    u32x2 l2 = __builtin_bit_cast(u32x2, lo), h2 = __builtin_bit_cast(u32x2, hi);
    v[f] = u32x4{l2[0], l2[1], h2[0], h2[1]};
  }
  const unsigned rt = t.g * 4 + f0;
  if (t.first) {
    st16(v[0], rws, lane * 16, w1_off(rt, t.ks));
    st16(v[1], rws, lane * 16, w1_off(rt + 1, t.ks));
    float s[2];
#pragma unroll
    for (int f = 0; f < 2; ++f)
      s[f] = xsum16_32(((bflo(v[f][0]) + bfhi(v[f][0])) + (bflo(v[f][1]) + bfhi(v[f][1]))) + ((bflo(v[f][2]) + bfhi(v[f][2])) + (bflo(v[f][3]) + bfhi(v[f][3]))));
    st4(__float_as_uint(q == 0 ? s[0] : s[1]), rws, lane < 32 ? lane * 4 : OOB, WS_CS1 + (t.ks * F_ + rt * 16) * 4);
  } else {
    const unsigned c0 = rt / 5u, c1 = (rt + 1) / 5u;
    st16(v[0], rws, lane * 16, WS_W2 + ((c0 * 54 + t.ks) * 5 + (rt - c0 * 5)) * 1024);
    st16(v[1], rws, lane * 16, WS_W2 + ((c1 * 54 + t.ks) * 5 + (rt + 1 - c1 * 5)) * 1024);
  }
}

#define P5D 1
#define PUBG 11
#define KP3 3
#define KP1 3
constexpr int WS1 = 3, WS2 = 6;
constexpr int PUB1 = 13;
constexpr int RGAP = 10, ASLEEP2 = 40, WDPRE = 6;
constexpr int PD4 = 13, PDX = 15, PD2 = 30, P5DELAY0 = 4, P5DELAY = P5D;

constexpr int KM_LDS = 160 * 1024;
constexpr int KM_CS1 = KM_LDS - 1024;
constexpr int G2_A = 80 * 1024, G2_AS = 16 * 1024, G2_BS = 20 * 1024;
constexpr int G4_LATE = 80 * 1024;

DI gu64* flag_ptr(char* ws, unsigned f) { return (gu64*)(ws + WS_FLG + f * 128); }
DI void raise(char* ws, unsigned f, u64 epoch, unsigned lane) {
  if (lane < 16) __hip_atomic_store(flag_ptr(ws, f) + lane, epoch, RLX_AGENT);
}
DI void publish(char* ws, unsigned f, u64 epoch, unsigned lane) {
  wait_vm<0>();
  raise(ws, f, epoch, lane);
}
DI void poll0(char* ws, unsigned f, unsigned n, u64 epoch, unsigned lane) {
  gu64* p = flag_ptr(ws, f);
  bool ok = lane >= n;
#pragma unroll 1
  for (unsigned s = 0; s < (1u << 16); ++s) {
    if (!ok) ok = __hip_atomic_load(p, RLX_AGENT) == epoch;
    if (__all(ok)) break;
    __builtin_amdgcn_s_sleep(1);
  }
  asm volatile("" ::: "memory");
}
template <int DELAY> DI void poll(char* ws, unsigned f, unsigned n, u64 epoch, unsigned lane) {
  __builtin_amdgcn_s_sleep(DELAY);
  poll0(ws, f, n, epoch, lane);
}
DI void warm(unsigned ldump, u32x4 srd, unsigned voff, unsigned soff) {
  asm volatile("s_mov_b32 m0, %0\n\ts_nop 0\n\tbuffer_load_dword %1, %2, %3 offen lds" :: "s"(ldump), "v"(voff), "s"(srd), "s"(soff) : "memory");
}
DI void glds4_sc1(u32x4 srd, unsigned voff, unsigned l, unsigned so) {
  asm volatile("s_mov_b32 m0, %0\n\ts_nop 0\n\tbuffer_load_dwordx4 %1, %2, %3 offen sc1 lds\n\tbuffer_load_dwordx4 %1, %2, %3 offen offset:1024 sc1 lds\n\t"
               "buffer_load_dwordx4 %1, %2, %3 offen offset:2048 sc1 lds\n\tbuffer_load_dwordx4 %1, %2, %3 offen offset:3072 sc1 lds"
               :: "s"(l), "v"(voff), "s"(srd), "s"(so) : "memory");
}
DI void glds1_sc1(u32x4 srd, unsigned voff, unsigned l, unsigned so) {
  asm volatile("s_mov_b32 m0, %0\n\ts_nop 0\n\tbuffer_load_dwordx4 %1, %2, %3 offen sc1 lds" :: "s"(l), "v"(voff), "s"(srd), "s"(so) : "memory");
}
template <int N> DI void glds_run_sc1(u32x4 srd, unsigned voff, unsigned l, unsigned so) {
#pragma unroll
  for (int g = 0; g < N / 4; ++g) glds4_sc1(srd, voff, l + g * 4096, so + g * 4096);
#pragma unroll
  for (int g = N / 4 * 4; g < N; ++g) glds1_sc1(srd, voff, l + g * 1024, so + g * 1024);
}

struct ResTile { u32x4 r0, r1; u32x2 r2; };
struct TileOff { unsigned a0, a1, a2; };
DI TileOff tile_offsets(bool rm, unsigned row, unsigned ct, unsigned q) {
  const unsigned cA = ct * 80 + 16 * (q & 1) + 8 * (q >> 1), cS = ct * 80 + 64 + 4 * q;
  TileOff t;
  if (rm) { t.a0 = (row * D_ + cA) * 2; t.a1 = t.a0 + 64; t.a2 = (row * D_ + cS) * 2; }
  else { t.a0 = foff<20>(row, cA); t.a1 = foff<20>(row, cA + 32); t.a2 = foff<20>(row, cS); }
  asm volatile("" : "+v"(t.a0), "+v"(t.a1), "+v"(t.a2));
  return t;
}
DI ResTile load_res(rsrc_t rr, const TileOff& t) {
  ResTile rt;
  rt.r0 = ld16u(rr, t.a0, 0); rt.r1 = ld16u(rr, t.a1, 0); rt.r2 = ld8u(rr, t.a2, 0);
  return rt;
}
DI void finish_tile(f32x4 (&v)[5], const ResTile& rt, rsrc_t rout, const TileOff& o, bool stats, rsrc_t rst, unsigned sto) {
#pragma unroll
  for (int p = 0; p < 2; ++p)
#pragma unroll
    for (int r = 0; r < 4; ++r) {
      auto t = __builtin_amdgcn_permlane16_swap(__float_as_uint(v[2 * p][r]), __float_as_uint(v[2 * p + 1][r]), false, false);
      v[2 * p][r] = __uint_as_float(t[0]); v[2 * p + 1][r] = __uint_as_float(t[1]);
    }
  u32x4 y0, y1; u32x2 y2;
#pragma unroll
  for (int j = 0; j < 2; ++j) {
    y0[j] = pk2(v[0][2 * j] + bflo(rt.r0[j]), v[0][2 * j + 1] + bfhi(rt.r0[j]));
    y0[2 + j] = pk2(v[1][2 * j] + bflo(rt.r0[2 + j]), v[1][2 * j + 1] + bfhi(rt.r0[2 + j]));
    y1[j] = pk2(v[2][2 * j] + bflo(rt.r1[j]), v[2][2 * j + 1] + bfhi(rt.r1[j]));
    y1[2 + j] = pk2(v[3][2 * j] + bflo(rt.r1[2 + j]), v[3][2 * j + 1] + bfhi(rt.r1[2 + j]));
    y2[j] = pk2(v[4][2 * j] + bflo(rt.r2[j]), v[4][2 * j + 1] + bfhi(rt.r2[j]));
  }
  if (stats) {
    st16_raw(y0, rout, o.a0, 0); st16_raw(y1, rout, o.a1, 0); st8(y2, rout, o.a2, 0);
  } else {
    __builtin_amdgcn_raw_buffer_store_b128(y0, rout, o.a0, 0, 0);
    __builtin_amdgcn_raw_buffer_store_b128(y1, rout, o.a1, 0, 0);
    __builtin_amdgcn_raw_buffer_store_b64(y2, rout, o.a2, 0, 0);
  }
  st_guard(y0, y1);
  if (stats) {
    float s1a = 0.f, s1b = 0.f, s2a = 0.f, s2b = 0.f;
#pragma unroll
    for (int j = 0; j < 4; ++j) {
      s1a = dot2(y0[j], 0x3f803f80u, s1a); s2a = dot2(y0[j], y0[j], s2a);
      s1b = dot2(y1[j], 0x3f803f80u, s1b); s2b = dot2(y1[j], y1[j], s2b);
    }
    s1a = dot2(y2[0], 0x3f803f80u, s1a); s2a = dot2(y2[0], y2[0], s2a);
    s1b = dot2(y2[1], 0x3f803f80u, s1b); s2b = dot2(y2[1], y2[1], s2b);
    const float s1 = xsum16_32(s1a + s1b), s2 = xsum16_32(s2a + s2b);
    st8(u32x2{__float_as_uint(s1), __float_as_uint(s2)}, rst, sto, 0);
  }
}

DI constexpr unsigned wd_pos(int c) { return c < 8 ? 10 * c : 60 + 10 * c; }
constexpr int ORD[14] = {0, 1, 4, 5, 7, 8, 11, 12, 2, 3, 6, 9, 10, 13};
DI constexpr int g4_late(int t) { return (t >= 4 && t < 7) || t >= 11; }
DI constexpr int g4_slot(int t) { return (t < 4 ? t : (t < 7 ? t - 4 : (t < 11 ? t - 7 : t - 11))) * 5120; }

#define PHASE_IDS \
  const unsigned lane = opq_v(tid0) & 63, w = opq_s(w0), bid = opq_s(blockIdx.x); \
  const unsigned xcd = bid & 7, idx = bid >> 3, i16 = lane & 15, q = lane >> 4; \
  char* const ws = opq_p(ws_); \
  (void)xcd; (void)idx; (void)i16; (void)q;
#define IDS_G2 const unsigned kq = xcd & 3, P = (xcd >> 2) * 8 + (idx >> 2), c4 = idx & 3;
#define G2_LA(h) (lb + G2_A + ((w >> 1) * 4 + ((2 * (w & 1) + (h) + 3 - kq) & 3)) * 2048)
#define IDS_G3 const unsigned P = (xcd >> 1) * 4 + (idx >> 3), ct = (xcd & 1) * 8 + (idx & 7);
#define C8_OF ((xcd & 1) * 4 + (idx & 3))
#define IDS_G4 const unsigned rt64 = (xcd >> 1) * 8 + (idx >> 2), c8 = C8_OF;
#define LDX(r, k) ld16(rx, lane * 16 + ((k) & 3) * 1024, sox + (r) * 20480 + ((k) >> 2) * 4096)

__global__ __launch_bounds__(512, 2) void k_all(const u16* x0_, const u16* Sre_, const u16* Sim_, const u16* Dre_, const u16* Dim_,
                                               const u16* w1_, const u16* w2_, char* ws_, u16* out_ STAMP_ARGS) {
  ST(0);
  const unsigned tid0 = threadIdx.x, wall = rfl(tid0 >> 6), w0 = wall & 3;
  const unsigned lb = lds_base();
  const char* code; unsigned code_len;
  asm volatile("s_getpc_b64 %0\n.Lcw_begin:\n\ts_mov_b32 %1, .Lcw_end-.Lcw_begin" : "=s"(code), "=s"(code_len));
  u64 epoch;

  if (__builtin_expect(wall >= 4, 1)) {
    const unsigned sink = lb + K1_CS + 1024;
    unsigned ep0, ep1;
    asm volatile("global_load_dword %0, %2, %3 sc1\n\tglobal_load_dword %1, %2, %3 offset:4 sc1"
                 : "=&v"(ep0), "=&v"(ep1) : "v"(WS_FLG + FL_E * 128), "s"(ws_) : "memory");
    {
      PHASE_IDS
      IDS_G3
      const u32x4 sa = make_srd(opq_p(x0_), S_ * D_ * 2), sbd = make_srd(ct < 8 ? opq_p(Dre_) : opq_p(Dim_), D_ * D_ * 2);
      const unsigned vo = (lane >> 3) * 1280 + (((lane & 7) ^ (lane >> 3)) << 4);
      const unsigned soa = (P * 128 + w * 32) * 1280, sob = ((ct & 7) * 80 + w * 8) * 1280;
      const unsigned la = lb + w * 4096, lbb = lb + 16384 + w * 1024;
      const unsigned u0 = bid * 4 + w, u1 = 1024 + bid;
      const bool extra = w == 3 && bid < 56;
      const unsigned rot = ((idx & 7) + 3 * (idx >> 3)) % 10u;
      auto ko = [&](unsigned s) { const unsigned c = s + rot; return (c >= 10 ? c - 10 : c) * 128; };
      asm volatile("s_nop 4" ::: "memory");
      k1_issue(sa, sbd, vo, la, lbb, soa + ko(0), sob + ko(0), w);
      k1_issue(sa, sbd, vo, la + K1_STAGE, lbb + K1_STAGE, soa + ko(1), sob + ko(1), w);
      if (w == 3) warm(sink, make_srd(code, code_len), lane < 16 ? idx * 2048 + lane * 128 : OOB, 0);
      ST(8);
      wait_vm<6>();
      {
        unsigned lo, hi;
        asm volatile("v_readfirstlane_b32 %0, %2\n\tv_readfirstlane_b32 %1, %3\n\ts_nop 4" : "=s"(lo), "=s"(hi) : "v"(ep0), "v"(ep1));
        epoch = (u64)lo + ((u64)hi << 32) + 1;
      }
      bar();
      k1_issue(sa, sbd, vo, la + 2 * K1_STAGE, lbb + 2 * K1_STAGE, soa + ko(2), sob + ko(2), w);
      const u32x4 ss = make_srd((xcd & 2) ? Sim_ : Sre_, S_ * S_ * 2);
      const unsigned vs = (lane & 31) * 4096 + (lane >> 5) * 128;
      const unsigned sos = ((xcd >> 2) * 1024 + idx * 32) * 4096 + (xcd & 1) * 2048 + w * 256;
      unsigned so = 0;
#pragma unroll 1
      for (int i = 0; i < 10; ++i) {
        if (i == 0) wait_vm<6>(); else if (i == 1) wait_vm<2 * 6>(); else if (i < 6) wait_vm<3 * 6>();
        else if (i == 6) wait_vm<2 * 6 + 4>(); else if (i == 7) wait_vm<6 + 4>(); else wait_vm<4 + (WS2 >= 4 && WS2 < 8) + (WS1 >= 4 && WS1 < 8)>();
        bar();
        if (i == 0) {
          k1_issue(sa, sbd, vo, la + 3 * K1_STAGE, lbb + 3 * K1_STAGE, soa + ko(3), sob + ko(3), w);
          k1_issue(sa, sbd, vo, la + 4 * K1_STAGE, lbb + 4 * K1_STAGE, soa + ko(4), sob + ko(4), w);
        } else if (i == 1) {
          k1_issue(sa, sbd, vo, la, lbb, soa + ko(5), sob + ko(5), w);
          k1_issue(sa, sbd, vo, la + K1_STAGE, lbb + K1_STAGE, soa + ko(6), sob + ko(6), w);
        } else if (i < 5) k1_issue(sa, sbd, vo, la + so, lbb + so, soa + ko(i + 5), sob + ko(i + 5), w);
        so = so == 4 * K1_STAGE ? 0 : so + K1_STAGE;
        if (i == WS1) warm(sink, ss, vs, sos);
        if (i == WS2) warm(sink, ss, vs, sos + 1024);
        if (i == 5) {
          retile_issue(u0, w1_, w2_, lb + K1_TR + w * 4096, lane);
          if (extra) retile_issue(u1, w1_, w2_, lb + K1_TR + 4 * 4096, lane);
        }
      }
      ST(9);
      bar();
    }
    bf16x8 wr[10][5];
#pragma unroll
    for (int t = 0; t < 10; ++t)
#pragma unroll
      for (int f = 0; f < 5; ++f) wr[t][f] = bf16x8{};
#pragma unroll 1
    for (int L = 0; L < 2; ++L) {
      const int sbase = L * 20;
      (void)sbase;
      {
        PHASE_IDS
        IDS_G2
        const u32x4 sa = make_srd(kq < 2 ? opq_p(Sre_) : opq_p(Sim_), S_ * S_ * 2);
        const u32x4 sb_ = make_srd(ws + (L ? WS_AB2 : WS_AB1), SZ_AB);
        const unsigned va = (lane >> 3) * 4096 + (((lane & 7) ^ (lane >> 3)) << 4), vb = lane * 16;
        const unsigned soa = (P * 128 + w * 32) * 4096 + (kq & 1) * 2048;
        const unsigned sob = ((2 * c4 + (w >> 1)) * 128 + kq * 32 + (w & 1)) * 5 * 1024;
        const unsigned la = lb + G2_A + w * 4096, lbb = lb + w * 5120;
        const unsigned la0 = G2_LA(0), la1 = G2_LA(1);
        const unsigned ct = (xcd & 1) * 8 + (idx & 7);
        auto kc = [&](unsigned s) { return L ? 2 * (s & 7) + ((s ^ (s >> 3)) & 1) : s; };
        asm volatile("s_nop 4" ::: "memory");
        auto issue_a = [&](unsigned l, unsigned so) {
          glds<0, 0>(sa, va, la0 + l, so); glds<1024, 8 * 4096>(sa, va, la0 + l, so);
          glds<0, 16 * 4096>(sa, va, la1 + l, so); glds<1024, 24 * 4096>(sa, va, la1 + l, so);
        };
        if (L == 0) {
#pragma unroll
          for (int s = 0; s < 3; ++s) issue_a(s * G2_AS, soa + s * 128);
          const unsigned u0 = bid * 4 + w, u1 = 1024 + bid;
          const rsrc_t rws = make_rsrc(ws, WS_END);
          wait_vm<12>();
          retile_finish(u0, 0, rws, lb + K1_TR + w * 4096, lane);
          retile_finish(u0, 2, rws, lb + K1_TR + w * 4096, lane);
          if (w == 3 && bid < 56) {
            retile_finish(u1, 0, rws, lb + K1_TR + 4 * 4096, lane);
            retile_finish(u1, 2, rws, lb + K1_TR + 4 * 4096, lane);
          }
          __builtin_amdgcn_s_sleep(P5DELAY0);
        } else {
          issue_a(3 * G2_AS, soa + kc(3) * 128);
          __builtin_amdgcn_s_sleep(P5DELAY);
        }
        ST(sbase + 1);
        const unsigned f5 = FL_5 + ((L * 4 + kq) * 4 + c4) * 64 + ((i16 >> 1) * 2 + (w >> 1)) * 4 + (L ? 2 * (w & 1) + (i16 & 1) : (w & 1) + 2 * (i16 & 1));
        poll0(ws, f5, 16, epoch, lane);
        wait_vm<0>();
        ST(sbase + 2);
        glds_run_sc1<5>(sb_, vb, lbb, sob);
        glds_run_sc1<5>(sb_, vb, lbb + G2_BS, sob + kc(1) * 10240);
        if (L == 0) {
          raise(ws, FL_W + bid * 4 + w, epoch, lane);
          if (w == 3 && bid < 56) raise(ws, FL_W + 1024 + bid, epoch, lane);
          wait_vm<6>();
        } else wait_vm<5>();
        bar();
        glds_run_sc1<5>(sb_, vb, lbb + 2 * G2_BS, sob + kc(2) * 10240);
        if (L == 0) issue_a(3 * G2_AS, soa + kc(3) * 128);
        glds_run_sc1<5>(sb_, vb, lbb + 3 * G2_BS, sob + kc(3) * 10240);
        ST(sbase + 3);
        const u32x4 sw = make_srd(ws + WS_W1, SZ_W1);
        const unsigned sow = ct * 140 * 1024;
        unsigned fw, fw0 = 0, fw1 = 0;
        {
          const unsigned n = w * 42 + (lane < 42 ? lane : 0), k3 = n / 3u, g3 = (7 * ct >> 2) + (n - k3 * 3), n2 = n - 60;
          fw = FL_W + (n < 60 ? k3 * 27 + (g3 < 26 ? g3 : 26) : 540 + (n2 >> 1) * 10 + (5 * C8_OF >> 2) + (n2 & 1));
          if (L) fw = f5 + 1024;
        }
#pragma unroll 1
        for (int s = 0; s < 16; ++s) {
          if (s == 0) wait_vm<10>();
          if (s == 1) wait_vm<14>();
          wait_vm<18>();
          if (s == (L ? 4 : 9)) {
            asm volatile("" : "+v"(fw0), "+v"(fw1));
            if (!__all(fw0 == (unsigned)epoch && fw1 == (unsigned)(epoch >> 32))) poll0(ws, fw, 42, epoch, lane);
          }
          bar();
          const unsigned sl = s & 3;
          if (s < 12) {
            issue_a(sl * G2_AS, soa + kc(s + 4) * 128);
            glds_run_sc1<5>(sb_, vb, lbb + sl * G2_BS, sob + kc(s + 4) * 10240);
            if (s == (L ? 1 : 6))
              asm volatile("global_load_dword %0, %2, %3 sc1\n\tglobal_load_dword %1, %2, %3 offset:4 sc1"
                           : "+v"(fw0), "+v"(fw1) : "v"(WS_FLG + fw * 128), "s"(ws) : "memory");
          } else {
            glds_run_sc1<5>(sw, vb, lbb + sl * G2_BS, sow + w * 5120 + sl * G2_BS);
            if (s < 15) glds_run_sc1<4>(sw, vb, la + sl * G2_AS, sow + G2_A + w * 4096 + sl * G2_AS);
            else glds_run_sc1<3>(sw, vb, lb + G2_A + 3 * G2_AS + w * 3072, sow + G2_A + 3 * G2_AS + w * 3072);
          }
        }
        ST(sbase + 4);
      }
      {
        PHASE_IDS
        IDS_G4
        const unsigned ct = (xcd & 1) * 8 + (idx & 7);
        if (w >= 2) {
          const rsrc_t rc = make_rsrc(ws + WS_CS1, 20 * F_ * 4);
          const unsigned c = (w - 2) * 64 + lane, col = ct * 112 + c;
          const unsigned vo = c < 112 && col < F_ ? col * 4 : OOB;
          float csp = 0.f;
#pragma unroll
          for (int k = 0; k < 20; ++k) csp += __uint_as_float(__builtin_amdgcn_raw_buffer_load_b32(rc, vo, k * F_ * 4, 16));
          lds_wr1(lb + KM_CS1 + c * 4, csp);
        }
        const unsigned so = (c8 * 54 + w * 14) * 5 * 1024;
        wait_vm<0>();
        if (L == 0) {
          const rsrc_t rw = make_rsrc(ws + WS_W2, SZ_W2);
          const unsigned vl = lane * 16, vz = lane * 16 + (w < 3 ? 0u : OOB);
          sfor<0, 50>([&](auto N) {
            constexpr int t = 4 + N.v / 5, n = ORD[t] * 5 + N.v % 5;
            wr[t - 4][N.v % 5] = ld16(rw, (ORD[t] < 12 ? vl : vz) + (n & 3) * 1024, so + (n >> 2) * 4096);
            if constexpr (N.v % 5 == 4 && N.v < 49) { sb(); __builtin_amdgcn_s_sleep(RGAP); sb(); }
          });
          sb();
        }
        wait_lgkm0();
        ST(sbase + 5);
        bar();
        bar();
        const u32x4 s2 = make_srd(ws + WS_W2, SZ_W2);
        const unsigned sg = rfl(so);
        asm volatile("s_nop 4" ::: "memory");
        glds_run_sc1<10>(s2, lane * 16, lb + w * 20480, sg);
        glds_run_sc1<10>(s2, lane * 16, lb + w * 20480 + 10240, sg + 4 * 5120);
        wait_vm<0>();
        ST(sbase + 6);
        const unsigned pe = lb + w * 20480 + lane * 16, pl = lb + G4_LATE + w * 15360 + lane * 16;
        auto put = [&](auto T0, auto T1) {
          sfor<T0.v * 5, T1.v * 5>([&](auto N) {
            constexpr int t = N.v / 5, f = N.v % 5;
            *(LDS_AS bf16x8*)(ldsp)(uintptr_t)((g4_late(t) ? pl : pe) + g4_slot(t) + f * 1024) = wr[t - 4][f];
          });
          wait_lgkm0();
        };
        bar();
        put(IC<4>{}, IC<7>{});
        bar();
        put(IC<7>{}, IC<11>{});
        bar();
        put(IC<11>{}, IC<14>{});
        bar();
      }
      if (L) { bar(); break; }
      {
        PHASE_IDS
        IDS_G3
        const u32x4 sd = make_srd(ct < 8 ? opq_p(Dre_) : opq_p(Dim_), D_ * D_ * 2);
        const unsigned vo = (lane >> 3) * 1280 + (((lane & 7) ^ (lane >> 3)) << 4);
        const unsigned so = (ct & 7) * 80 * 1280;
        sfor<0, 10>([&](auto G) { glds<G.v * 1024, G.v * 8 * 1280>(sd, vo, lb + 20 * w * 1024, so + 2 * w * 128); });
        sfor<0, WDPRE>([&](auto G) { glds<G.v * 1024, G.v * 8 * 1280>(sd, vo, lb + (20 * w + 10) * 1024, so + (2 * w + 1) * 128); });
        bar();
        sfor<WDPRE, 10>([&](auto G) { glds<G.v * 1024, G.v * 8 * 1280>(sd, vo, lb + (20 * w + 10) * 1024, so + (2 * w + 1) * 128); });
        sfor<0, 5>([&](auto G) { glds<G.v * 1024, G.v * 8 * 1280>(sd, vo, lb + (140 + w * 5) * 1024, so + (8 + (w >> 1)) * 128 + (w & 1) * 40 * 1280); });
        wait_vm<0>();
        ST(7);
        bar();
      }
      {
        PHASE_IDS
        IDS_G2
        const u32x4 sa = make_srd(kq < 2 ? opq_p(Sre_) : opq_p(Sim_), S_ * S_ * 2);
        const unsigned va = (lane >> 3) * 4096 + (((lane & 7) ^ (lane >> 3)) << 4);
        const unsigned soa = (P * 128 + w * 32) * 4096 + (kq & 1) * 2048;
        __builtin_amdgcn_s_sleep(ASLEEP2);
#pragma unroll
        for (int s = 0; s < 3; ++s) {
          const unsigned l0 = G2_LA(0) + s * G2_AS, l1 = G2_LA(1) + s * G2_AS, so = soa + (2 * s + (s & 1)) * 128;
          glds<0, 0>(sa, va, l0, so); glds<1024, 8 * 4096>(sa, va, l0, so);
          glds<0, 16 * 4096>(sa, va, l1, so); glds<1024, 24 * 4096>(sa, va, l1, so);
        }
        bar();
      }
    }
    return;
  }

  {
    const u64 e = __hip_atomic_load(flag_ptr(ws_, FL_E), RLX_AGENT);
    epoch = (u64)rfl((unsigned)e) + ((u64)rfl((unsigned)(e >> 32)) << 32) + 1;
  }
#pragma unroll 1
  for (int L = 0; L < 2; ++L) {
    const int sbase = L * 20;
    (void)sbase;
    {
      PHASE_IDS
      IDS_G3
      f32x4 acc[2][5];
      float mu[2][4], rs[2][4], csv[5];
      const float sign = ct < 8 ? 1.f : -1.f;
      const unsigned f5 = FL_5 + (((ct >> 3) * 2 + (P >> 3)) * 4 + ((ct & 7) >> 1)) * 64 + ((P & 7) * 2 + (ct & 1)) * 4 + w;
      if (L == 0) {
        const unsigned ra0 = lb + w * 4096 + i16 * 128 + ((q ^ (lane & 7)) << 4);
        const unsigned rb0 = lb + 16384 + i16 * 128 + ((q ^ (lane & 7)) << 4);
        const f32x4 z4 = {0.f, 0.f, 0.f, 0.f};
        f32x4 cs0 = z4, cs1 = z4, rs_[2] = {z4, z4}, rq_[2] = {z4, z4};
#pragma unroll
        for (int r = 0; r < 2; ++r)
#pragma unroll
          for (int f = 0; f < 5; ++f) acc[r][f] = z4;
        bf16x8 fr[2][8];
        bf16x8 one = __builtin_bit_cast(bf16x8, u32x4{0x3f803f80u, 0x3f803f80u, 0x3f803f80u, 0x3f803f80u});
        auto half = [&](auto KK, unsigned so) {
          constexpr int kk = KK.v;
          const unsigned pa = (ra0 ^ ((kk ^ 1) * 64)) + so, pb = (rb0 ^ ((kk ^ 1) * 64)) + so;
          sfor<0, 16>([&](auto M) {
            constexpr int m = M.v;
            if constexpr (m < 10) mma(acc[m & 1][m >> 1], fr[kk][m & 1], fr[kk][2 + (m >> 1)]);
            else if constexpr (m == 10) mma(cs0, one, fr[kk][7]);
            else if constexpr (m == 11) mma(cs1, one, fr[kk][6]);
            else if constexpr (m < 14) mma(rs_[m - 12], fr[kk][m - 12], one);
            else mma(rq_[m - 14], fr[kk][m - 14], fr[kk][m - 14]);
            constexpr int n = m >> 1;
            if constexpr ((m & 1) == 0) fr[kk ^ 1][n] = n < 2 ? lds_rd(pa + n * 2048) : (n < 7 ? lds_rd(pb + (n - 2) * 2048) : lds_rd(pb + w * 2048));
            sb();
          });
        };
        asm volatile("s_nop 1" : "+v"(cs0), "+v"(cs1), "+v"(rs_[0]), "+v"(rs_[1]), "+v"(rq_[0]), "+v"(rq_[1]), "+v"(one));
        fence_all(acc);
        bar();
        ST(38);
        fr[0][0] = lds_rd(ra0); fr[0][1] = lds_rd(ra0 + 2048);
#pragma unroll
        for (int f = 0; f < 5; ++f) fr[0][2 + f] = lds_rd(rb0 + f * 2048);
        fr[0][7] = lds_rd(rb0 + w * 2048);
        sb();
        unsigned so = 0;
#pragma unroll 1
        for (int i = 0; i < 10; ++i) {
          half(IC<0>{}, so);
          wait_lgkm0();
          bar();
          so = so == 4 * K1_STAGE ? 0 : so + K1_STAGE;
          half(IC<1>{}, so);
        }
        fence_all(acc);
        asm volatile("" : "+v"(cs0), "+v"(cs1), "+v"(rs_[0]), "+v"(rs_[1]), "+v"(rq_[0]), "+v"(rq_[1]));
        const unsigned csb = lb + K1_CS;
        if (q == 0) {
          lds_wr1(csb + (w * 16 + i16) * 4, cs0[0]);
          if (w == 0) lds_wr1(csb + (64 + i16) * 4, cs1[0]);
        }
        wait_lgkm0();
        bar();
        ST(18);
#pragma unroll
        for (int f = 0; f < 5; ++f) csv[f] = lds_rd1(csb + (f * 16 + i16) * 4);
#pragma unroll
        for (int r = 0; r < 2; ++r)
#pragma unroll
          for (int j = 0; j < 4; ++j) {
            float m_, r_;
            ln_coef(rs_[r][j], __shfl(rq_[r][j], q * 20 + j), m_, r_);
            mu[r][j] = -m_; rs[r][j] = sign * r_;
          }
        if (w == 0) {
          const rsrc_t rc = make_rsrc(ws + WS_CSD, 256 * 320);
          st4(__float_as_uint(q == 0 ? csv[0] : (q == 1 ? csv[1] : (q == 2 ? csv[2] : csv[3]))), rc, lane * 4, bid * 320);
          st4(__float_as_uint(csv[4]), rc, q == 0 ? 256 + lane * 4 : OOB, bid * 320);
        }
        g1_store(acc, mu, rs, csv, make_rsrc(ws + WS_AB1, SZ_AB), P, ct, w, i16, q);
        ST(19);
        publish(ws, f5, epoch, lane);
      } else {
        {
          const rsrc_t rc = make_rsrc(ws + WS_CSD, 256 * 320);
#pragma unroll
          for (int f = 0; f < 5; ++f) csv[f] = __uint_as_float(__builtin_amdgcn_raw_buffer_load_b32(rc, (f * 16 + i16) * 4, bid * 320, 16));
        }
        const unsigned h0 = P & 1;
        poll<PD4>(ws, FL_4 + P * 64 + ((i16 >> 3) * 4 + w) * 8 + (i16 & 7), 16, epoch, lane);
        ST(17);
        const rsrc_t rx = make_rsrc(ws + WS_X2, SZ_X);
        const unsigned sox0 = (P * 8 + h0 * 4 + w) * 20 * 1024, sox1 = (P * 8 + (h0 ^ 1) * 4 + w) * 20 * 1024;
        bf16x8 a[40];
#define LDA(n) a[n] = ld16(rx, lane * 16 + ((n) & 3) * 1024, ((n) < 20 ? sox0 : sox1) + (((n) % 20) >> 2) * 4096)
        sfor<0, 6>([&](auto N) { LDA(N.v); });
        const rsrc_t rst = make_rsrc(ws + WS_ST2, SZ_ST);
        const unsigned sto = ((lane >> 5) * 4 * S_ + P * 128 + (((lane >> 4) & 1) ^ h0) * 64 + w * 16 + i16) * 8;
        const u32x2 sp0 = ld8u(rst, sto, 0), sp1 = ld8u(rst, sto, S_ * 8), sp2 = ld8u(rst, sto, 2 * S_ * 8), sp3 = ld8u(rst, sto, 3 * S_ * 8);
        sb();
        bar();
        bf16x8 b[2][5];
        const unsigned rb = lb + i16 * 128 + ((q ^ (lane & 7)) << 4);
        const rsrc_t rab = make_rsrc(ws + WS_AB2, SZ_AB);
        const unsigned vo = ((2 * (w & 1) + (q >> 1)) * 16 + i16) * 16 + (q & 1) * 8;
        const unsigned so = ((ct & 7) * 128 + (ct >> 3) * 64 + P * 4 + (w >> 1)) * 5 * 1024;
        auto put = [&](auto H) {
          constexpr int hh = H.v;
#pragma unroll
          for (int f = 0; f < 5; ++f) {
            const f32x2 cs = {csv[f], csv[f]};
            u32x2 y;
#pragma unroll
            for (int e = 0; e < 2; ++e) {
              const f32x2 t = __builtin_elementwise_fma(f32x2{mu[hh][2 * e], mu[hh][2 * e + 1]}, cs, f32x2{acc[hh][f][2 * e], acc[hh][f][2 * e + 1]}) *
                              f32x2{rs[hh][2 * e], rs[hh][2 * e + 1]};
              y[e] = __builtin_bit_cast(unsigned, __builtin_convertvector(t, bf16x2));
            }
            st8(y, rab, vo + (f & 3) * 1024, so + (hh ^ h0) * 10240 + (f >> 2) * 4096);
          }
        };
#pragma unroll
        for (int f = 0; f < 5; ++f) b[0][f] = lds_rd(rb + wd_pos(0) * 1024 + f * 2048);
        sb();
        sfor<0, 40>([&](auto N) {
          constexpr int n = N.v, hh = n / 20, k = n % 20, kn = (k + 1) % 20;
          if constexpr (n == 2 * KP1) {
            float s1 = (__uint_as_float(sp0[0]) + __uint_as_float(sp1[0])) + (__uint_as_float(sp2[0]) + __uint_as_float(sp3[0]));
            float s2 = (__uint_as_float(sp0[1]) + __uint_as_float(sp1[1])) + (__uint_as_float(sp2[1]) + __uint_as_float(sp3[1]));
            auto ta = __builtin_amdgcn_permlane32_swap(__float_as_uint(s1), __float_as_uint(s1), false, false);
            auto tb = __builtin_amdgcn_permlane32_swap(__float_as_uint(s2), __float_as_uint(s2), false, false);
            float m, rr;
            ln_coef(__uint_as_float(ta[0]) + __uint_as_float(ta[1]), __uint_as_float(tb[0]) + __uint_as_float(tb[1]), m, rr);
            m = -m; rr *= sign;
#pragma unroll
            for (int r = 0; r < 2; ++r)
#pragma unroll
              for (int e = 0; e < 4; ++e) { mu[r][e] = __shfl(m, r * 16 + q * 4 + e); rs[r][e] = __shfl(rr, r * 16 + q * 4 + e); }
            asm volatile("" : "+v"(mu[0][0]), "+v"(mu[0][1]), "+v"(mu[0][2]), "+v"(mu[0][3]), "+v"(mu[1][0]), "+v"(mu[1][1]), "+v"(mu[1][2]), "+v"(mu[1][3]),
                              "+v"(rs[0][0]), "+v"(rs[0][1]), "+v"(rs[0][2]), "+v"(rs[0][3]), "+v"(rs[1][0]), "+v"(rs[1][1]), "+v"(rs[1][2]), "+v"(rs[1][3]));
            sb();
          }
          sfor<0, 5>([&](auto F) {
            constexpr int f = F.v;
            if constexpr (k == 0) mma0(acc[hh][f], a[n], b[n & 1][f]); else mma(acc[hh][f], a[n], b[n & 1][f]);
            if constexpr (n + 1 < 40) b[(n + 1) & 1][f] = lds_rd((rb ^ ((kn & 1) * 64)) + wd_pos(kn >> 1) * 1024 + f * 2048);
            if constexpr ((f == 1 || f == 3) && 6 + 2 * n + (f >> 1) < 40) LDA(6 + 2 * n + (f >> 1));
            sb();
          });
          if constexpr (n == 21) { pin_acc(acc[0]); put(IC<0>{}); sb(); }
          if constexpr (n == 20 + PUBG) publish(ws, f5 + 1024, epoch, lane);
          if constexpr (n == 0) ST(40);
          if constexpr (n == 9) ST(41);
          if constexpr (n == 19) ST(42);
        });
        fence_acc(acc[1]);
        bar();
        ST(39);
        put(IC<1>{});
        ST(20);
        publish(ws, f5 + 2048, epoch, lane);
      }
    }
    {
      PHASE_IDS
      IDS_G2
      const unsigned m = w >> 1, n = w & 1;
      const unsigned ra = lb + G2_A + (m * 64 + i16) * 128 + ((q ^ (lane & 7)) << 4);
      const unsigned rb = lb + n * 10240 + lane * 16;
      const unsigned row = P * 128 + m * 64 + kq * 16 + i16, ct = 2 * c4 + n;
      const TileOff ro = tile_offsets(L == 0, row, ct, q), oo = tile_offsets(false, row, ct, q);
      const rsrc_t rxp = make_rsrc(ws + WS_XP + L * SZ_XP, SZ_XP);
      const unsigned xo = ((P * 4 + c4) * 48 + w) * XSLOT;
      f32x4 acc[4][5];
#pragma unroll
      for (int i = 0; i < 4; ++i)
#pragma unroll
        for (int f = 0; f < 5; ++f) acc[i][f] = f32x4{0.f, 0.f, 0.f, 0.f};
      bf16x8 fa[2][4], fb[2][5];
      unsigned ra1 = ra ^ 64, rb2 = rb + 2 * G2_BS;
      asm volatile("" : "+v"(ra1), "+v"(rb2));
      auto half = [&](auto KK, auto SL) {
        constexpr int kk = KK.v, sl = SL.v;
        sfor<0, 20>([&](auto M) {
          constexpr int mm = M.v;
          mma(acc[mm / 5][mm % 5], fb[kk][mm % 5], fa[kk][mm / 5]);
          if constexpr (mm < 5) fb[kk ^ 1][mm] = lds_rd((sl < 2 ? rb : rb2) + (sl & 1) * G2_BS + (kk ^ 1) * 5120 + mm * 1024);
          else if constexpr (mm < 9) fa[kk ^ 1][mm - 5] = lds_rd((kk ? ra : ra1) + sl * G2_AS + (mm - 5) * 2048);
          sb();
        });
      };
      if (L) poll0(ws, FL_4 + P * 64 + (m * 4 + kq) * 8 + ct, 1, epoch, lane);
      ResTile rt = load_res(make_rsrc(L ? (const void*)(ws + WS_X2) : (const void*)opq_p(x0_), SZ_X), ro);
      fence_all(acc);
      bar();
      ST(sbase + 1);
#pragma unroll
      for (int f = 0; f < 5; ++f) fb[0][f] = lds_rd(rb + f * 1024);
#pragma unroll
      for (int i = 0; i < 4; ++i) fa[0][i] = lds_rd(ra + i * 2048);
      sb();
      auto stage = [&](auto U) {
        half(IC<0>{}, U);
        wait_lgkm0();
        bar();
        half(IC<1>{}, IC<(U.v + 1) & 3>{});
      };
#pragma unroll 1
      for (int g = 0; g < 3; ++g) sfor<0, 4>(stage);
      sfor<0, 3>(stage);
      half(IC<0>{}, IC<3>{});
      wait_lgkm0();
      bar();
      {
        unsigned p[3][10];
        u32x4 y[3][2];
        auto pack = [&](auto N) {
          constexpr int i = N.v / 5, f = N.v % 5;
          asm volatile("" : "+v"(acc[i][f]));
          p[i][2 * f] = pk2(acc[i][f][0], acc[i][f][1]); p[i][2 * f + 1] = pk2(acc[i][f][2], acc[i][f][3]);
          const unsigned o = xo + (((kq + 1 + i) & 3) * 3 + 2 - i) * 4 * XSLOT;
          if constexpr (f == 1) { y[i][0] = u32x4{p[i][0], p[i][1], p[i][2], p[i][3]}; st16_raw(y[i][0], rxp, lane * 16, o); }
          if constexpr (f == 3) { y[i][1] = u32x4{p[i][4], p[i][5], p[i][6], p[i][7]}; st16_raw(y[i][1], rxp, lane * 16 + 1024, o); }
          if constexpr (f == 4) st8(u32x2{p[i][8], p[i][9]}, rxp, lane * 8 + 2048, o);
        };
        sfor<0, 20>([&](auto M) {
          constexpr int mm = M.v;
          mma(acc[mm / 5][mm % 5], fb[1][mm % 5], fa[1][mm / 5]);
          if constexpr (mm >= 8) pack(IC<mm - 8>{});
          sb();
        });
        ST(sbase + 2);
        pack(IC<12>{}); pack(IC<13>{}); pack(IC<14>{});
        st_guard(y[0][0], y[0][1], y[1][0], y[1][1], y[2][0], y[2][1]);
      }
      f32x4 (&v)[5] = acc[3];
      fence_acc(v);
      const unsigned fx = FL_X + ((L * 64 + P * 4 + c4) * 4 + w) * 4;
      ST(sbase + 3);
      publish(ws, fx + kq, epoch, lane);
      ST(sbase + 4);
      poll<PDX>(ws, fx + ((kq + 1 + lane) & 3), 3, epoch, lane);
      ST(sbase + 5);
      const unsigned xi = xo + kq * 12 * XSLOT;
      u32x4 g0[3], g1[3]; u32x2 g2[3];
#pragma unroll
      for (int s = 0; s < 3; ++s) {
        g0[s] = ld16u(rxp, lane * 16, xi + s * 4 * XSLOT);
        g1[s] = ld16u(rxp, lane * 16 + 1024, xi + s * 4 * XSLOT);
        g2[s] = ld8u(rxp, lane * 8 + 2048, xi + s * 4 * XSLOT);
      }
      sb();
#pragma unroll
      for (int s = 0; s < 3; ++s) {
        v[0] += f32x4{bflo(g0[s][0]), bfhi(g0[s][0]), bflo(g0[s][1]), bfhi(g0[s][1])};
        v[1] += f32x4{bflo(g0[s][2]), bfhi(g0[s][2]), bflo(g0[s][3]), bfhi(g0[s][3])};
        v[2] += f32x4{bflo(g1[s][0]), bfhi(g1[s][0]), bflo(g1[s][1]), bfhi(g1[s][1])};
        v[3] += f32x4{bflo(g1[s][2]), bfhi(g1[s][2]), bflo(g1[s][3]), bfhi(g1[s][3])};
        v[4] += f32x4{bflo(g2[s][0]), bfhi(g2[s][0]), bflo(g2[s][1]), bfhi(g2[s][1])};
      }
      finish_tile(v, rt, make_rsrc(ws + (L ? WS_X3 : WS_X1), SZ_X), oo, true, make_rsrc(ws + (L ? WS_ST3 : WS_ST1), SZ_ST),
                  q == 0 ? (ct * S_ + row) * 8 : OOB);
      ST(sbase + 6);
      publish(ws, FL_2 + (L * 16 + P) * 64 + (m * 4 + kq) * 8 + ct, epoch, lane);
      ST(sbase + 7);
    }
    u32x4 y2[2]; u32x2 y1[2];
    {
      PHASE_IDS
      IDS_G3
      typedef __attribute__((ext_vector_type(2))) short s16x2;
      poll<PD2>(ws, FL_2 + (L * 16 + P) * 64 + w * 16 + i16, 16, epoch, lane);
      ST(sbase + 8);
      const rsrc_t rx = make_rsrc(ws + (L ? WS_X3 : WS_X1), SZ_X);
      const rsrc_t rst = make_rsrc(ws + (L ? WS_ST3 : WS_ST1), SZ_ST);
      const unsigned sox = (P * 8 + w * 2) * 20 * 1024;
      const unsigned par = ct & 1;
      bf16x8 a[20][2];
#pragma unroll
      for (int k = 0; k < 3; ++k) { a[k][0] = LDX(0, k); a[k][1] = LDX(1, k); }
      const unsigned sto = (2 * q * S_ + P * 128 + w * 32 + i16) * 8;
      const u32x2 sa0 = ld8u(rst, sto, 0), sa1 = ld8u(rst, sto, S_ * 8), sb0 = ld8u(rst, sto + 128, 0), sb1 = ld8u(rst, sto + 128, S_ * 8);
      float mu[2], rs[2];
      sb();
      bar();
      f32x4 acc[2][4];
      bf16x8 b[2][4];
      const unsigned rb = lb + lane * 16;
#pragma unroll
      for (int f = 0; f < 4; ++f) b[0][f] = lds_rd(rb + f * 1024);
      sb();
      sfor<0, 20>([&](auto K) {
        constexpr int k = K.v;
        const unsigned pb = k + 1 < 20 ? rb + (k + 1) * 4096 : rb + 80 * 1024;
        if constexpr (k == KP3) {
          ln_coef(xsum16_32(__uint_as_float(sa0[0]) + __uint_as_float(sa1[0])), xsum16_32(__uint_as_float(sa0[1]) + __uint_as_float(sa1[1])), mu[0], rs[0]);
          ln_coef(xsum16_32(__uint_as_float(sb0[0]) + __uint_as_float(sb1[0])), xsum16_32(__uint_as_float(sb0[1]) + __uint_as_float(sb1[1])), mu[1], rs[1]);
          asm volatile("" : "+v"(mu[0]), "+v"(mu[1]), "+v"(rs[0]), "+v"(rs[1]));
          sb();
        }
        sfor<0, 8>([&](auto M) {
          constexpr int f = M.v >> 1, r = M.v & 1;
          if constexpr (k == 0) mma0(acc[r][f], b[0][f], a[0][r]); else mma(acc[r][f], b[k & 1][f], a[k][r]);
          if constexpr (r == 0 && (k + 1 < 20 || f < 3)) b[(k + 1) & 1][f] = lds_rd(pb + f * 1024);
          if constexpr (r == 1 && 2 * k + 3 + (f >> 1) < 20) a[2 * k + 3 + (f >> 1)][f & 1] = LDX(f & 1, 2 * k + 3 + (f >> 1));
          sb();
        });
        if constexpr (k == 0) ST(sbase + 9);
        if constexpr (k == 3) ST(51 + L * 5);
        if constexpr (k == 7) ST(52 + L * 5);
        if constexpr (k == 11) ST(53 + L * 5);
        if constexpr (k == 15) ST(54 + L * 5);
        if constexpr (k == 19) ST(55 + L * 5);
      });
      auto act = [&](const f32x4& c, int r, const f32x4& cs, unsigned (&pk)[2]) {
        const f32x2 nm = {-mu[r], -mu[r]}, r2 = {rs[r], rs[r]};
#pragma unroll
        for (int e = 0; e < 2; ++e) {
          const f32x2 z = __builtin_elementwise_fma(nm, f32x2{cs[2 * e], cs[2 * e + 1]}, f32x2{c[2 * e], c[2 * e + 1]}) * r2;
          const s16x2 hb = __builtin_bit_cast(s16x2, __builtin_convertvector(z, bf16x2));
          pk[e] = __builtin_bit_cast(unsigned, __builtin_elementwise_max(hb, s16x2{0, 0}));
        }
      };
      auto pair = [&](const unsigned (&u)[2], const unsigned (&v)[2]) {
        auto t0 = __builtin_amdgcn_permlane16_swap(u[0], v[0], false, false);
        auto t1 = __builtin_amdgcn_permlane16_swap(u[1], v[1], false, false);
        return u32x4{t0[0], t1[0], t0[1], t1[1]};
      };
      const unsigned csb = lb + KM_CS1 + q * 16;
      f32x4 c1s[4];
#pragma unroll
      for (int f = 0; f < 4; ++f) c1s[f] = lds_rdf(csb + (f + par) * 64);
      asm volatile("s_nop 7\n\ts_nop 1" : "+v"(acc[0][0]), "+v"(acc[0][1]), "+v"(acc[0][2]), "+v"(acc[0][3]),
                                          "+v"(acc[1][0]), "+v"(acc[1][1]), "+v"(acc[1][2]), "+v"(acc[1][3]));
      wait_lgkm0();
      bar();
      ST(43 + L * 4);
      {
        const rsrc_t rh = make_rsrc(ws + (L ? WS_H2 : WS_H1), SZ_H);
        const unsigned vo = ((((q & 1) << 1) | (q >> 1)) * 16 + i16) * 16;
        const unsigned so = ((P * 8 + w * 2) * 54 + 7 * (ct >> 1) + 4 * par) * 1024;
        u32x4 y[2][2];
#pragma unroll
        for (int p = 0; p < 2; ++p) {
          const f32x4 c0 = c1s[2 * p], c1 = c1s[2 * p + 1];
#pragma unroll
          for (int r = 0; r < 2; ++r) {
            unsigned u[2], v[2];
            act(acc[r][2 * p], r, c0, u); act(acc[r][2 * p + 1], r, c1, v);
            y[p][r] = pair(u, v);
            st16_raw(y[p][r], rh, p == 1 && ct == 15 ? OOB : vo, so + (r * 54 + p) * 1024);
          }
        }
        st_guard(y[0][0], y[0][1], y[1][0], y[1][1]);
      }
      ST(44 + L * 4);
      sb();
      sfor<0, 20>([&](auto K) {
        constexpr int k = K.v;
        const unsigned pb = rb + 80 * 1024 + (k + 1) * 3072;
        sfor<0, 6>([&](auto M) {
          constexpr int f = M.v >> 1, r = M.v & 1;
          if constexpr (k == 0) mma0(acc[r][f], b[0][f], a[0][r]); else mma(acc[r][f], b[k & 1][f], a[k][r]);
          if constexpr (r == 0 && k + 1 < 20) b[(k + 1) & 1][f] = lds_rd(pb + f * 1024);
          sb();
        });
        if constexpr (k == PUB1) publish(ws, FL_3 + (L * 32 + P) * 64 + w * 16 + ct, epoch, lane);
      });
      const f32x4 c0 = lds_rdf(csb + (4 + par) * 64), c1 = lds_rdf(csb + (5 + par) * 64), c2 = lds_rdf(csb + (par ? 0 : 6) * 64);
      asm volatile("s_nop 7\n\ts_nop 1" : "+v"(acc[0][0]), "+v"(acc[0][1]), "+v"(acc[0][2]), "+v"(acc[1][0]), "+v"(acc[1][1]), "+v"(acc[1][2]));
      ST(sbase + 10);
#pragma unroll
      for (int r = 0; r < 2; ++r) {
        unsigned u[2], v[2], s[2];
        act(acc[r][0], r, c0, u); act(acc[r][1], r, c1, v); act(acc[r][2], r, c2, s);
        y2[r] = pair(u, v);
        y1[r] = u32x2{s[0], s[1]};
      }
      asm volatile("" : "+v"(y2[0]), "+v"(y2[1]), "+v"(y1[0]), "+v"(y1[1]));
      ST(sbase + 11);
    }
    {
      PHASE_IDS
      IDS_G4
      const unsigned row = rt64 * 64 + w * 16 + i16;
      const rsrc_t rh = make_rsrc(ws + (L ? WS_H2 : WS_H1), SZ_H);
      const unsigned soh = (rt64 * 4 * 54 + w * 14) * 1024;
      const unsigned vl = lane * 16, vz = lane * 16 + (w < 3 ? 0u : OOB);
      bf16x8 a[14][4], b[2][5];
      unsigned sh[4];
#pragma unroll
      for (int i = 0; i < 4; ++i) sh[i] = soh + ((w + 1 + i) & 3) * 54 * 1024;
#define LDH(t, i) a[t][i] = ld16(rh, (ORD[t] < 12 ? vl : vz) + (ORD[t] & 3) * 1024, sh[i] + (ORD[t] >> 2) * 4096)
      const TileOff ro = tile_offsets(false, row, c8, q), oo = tile_offsets(L != 0, row, c8, q);
      const unsigned f3 = FL_3 + (L * 32 + (rt64 >> 1)) * 64 + ((rt64 & 1) * 2 + (lane & 1)) * 16 + 4 * w + ((lane >> 1) & 3);
      poll0(ws, f3, 8, epoch, lane);
      ST(sbase + 13);
      sfor<0, 8>([&](auto N) { LDH(N.v / 4, N.v % 4); });
      sb();
      {
        const unsigned P = (xcd >> 1) * 4 + (idx >> 3), ct = (xcd & 1) * 8 + (idx & 7), par = ct & 1;
        const unsigned vo = ((((q & 1) << 1) | (q >> 1)) * 16 + i16) * 16;
        const unsigned v1 = ((2 * par + (q >> 1)) * 16 + i16) * 16 + (q & 1) * 8;
        const unsigned so = ((P * 8 + w * 2) * 54 + 7 * (ct >> 1)) * 1024;
#pragma unroll
        for (int r = 0; r < 2; ++r) {
          st16_raw(y2[r], rh, ct == 15 ? OOB : vo, so + (r * 54 + 2 + 4 * par) * 1024);
          st8(y1[r], rh, v1, so + (r * 54 + 3) * 1024);
        }
        st_guard(y2[0], y2[1]);
      }
      ResTile rt;
      u64 fl2;
      const rsrc_t rr = make_rsrc(ws + (L ? WS_X3 : WS_X1), SZ_X);
      sb();
      bar();
      f32x4 acc[4][5];
      const unsigned rbe = lb + w * 20480 + lane * 16, rbl = lb + G4_LATE + w * 15360 + lane * 16;
#pragma unroll
      for (int f = 0; f < 5; ++f) b[0][f] = lds_rd(rbe + f * 1024);
      sb();
      sfor<0, 14>([&](auto T) {
        constexpr int t = T.v;
        constexpr int ta = t < 5 ? (t == 0 ? 2 : t + 3) : (t == 5 ? -1 : (t == 6 ? 8 : (t + 3 < 14 ? t + 3 : -1)));
        if constexpr (t == 3 || t == 6 || t == 10) { wait_lgkm0(); bar(); }
        if constexpr (t == 6) { if (!__all(fl2 == epoch)) poll0(ws, f3 + 16 * 64, 8, epoch, lane); }
        sfor<0, 20>([&](auto M) {
          constexpr int m = M.v, i = m / 5, f = m % 5;
          if constexpr (t == 0) mma0(acc[i][f], b[0][f], a[0][i]); else mma(acc[i][f], b[t & 1][f], a[t][i]);
          if constexpr (t + 1 < 14 && (m & 1) == 1 && m < 10)
            b[(t + 1) & 1][m >> 1] = lds_rd((g4_late(t + 1) ? rbl : rbe) + g4_slot(t + 1) + (m >> 1) * 1024);
          if constexpr (ta >= 0 && (m & 1) == 0 && m < 8) LDH(ta, m >> 1);
          if constexpr (t == 6 && (m & 1) == 0 && m >= 8 && m < 16) LDH(9, (m - 8) >> 1);
          if constexpr (t == 0 && (m & 1) == 0 && m >= 8 && m < 16) LDH(3, (m - 8) >> 1);
          if constexpr (t == 2 && m == 10) rt = load_res(rr, ro);
          if constexpr (t == 4 && m == 18) fl2 = __hip_atomic_load(flag_ptr(ws, f3 + 16 * 64), RLX_AGENT);
          if constexpr (t == 13 && m >= 8) {
            asm volatile("" : "+v"(acc[(m - 8) / 5][(m - 8) % 5]));
            lds_wrf(rbl + (m - 8) * 1024, acc[(m - 8) / 5][(m - 8) % 5]);
          }
          sb();
        });
        if constexpr (t == 2) {
          wait_vm<19>();
          raise(ws, FL_3 + (L * 32 + 16 + (xcd >> 1) * 4 + (idx >> 3)) * 64 + w * 16 + (xcd & 1) * 8 + (idx & 7), epoch, lane);
          ST(sbase + 12);
        }
        if constexpr (t == 0) ST(sbase + 14);
        if constexpr (t == 3) ST(45 + L * 4);
        if constexpr (t == 6) ST(46 + L * 4);
      });
      ST(sbase + 15);
#pragma unroll
      for (int f = 2; f < 5; ++f) { asm volatile("" : "+v"(acc[2][f])); lds_wrf(rbl + (10 + f) * 1024, acc[2][f]); }
      f32x4 (&v)[5] = acc[3];
      fence_acc(v);
      wait_lgkm0();
      bar();
#pragma unroll
      for (int s = 1; s < 4; ++s) {
        const unsigned src = lb + G4_LATE + ((w + s) & 3) * 15360 + (3 - s) * 5 * 1024 + lane * 16;
#pragma unroll
        for (int f = 0; f < 5; ++f) v[f] += lds_rdf(src + f * 1024);
      }
      finish_tile(v, rt, L ? make_rsrc(opq_p(out_), SZ_X) : make_rsrc(ws + WS_X2, SZ_X), oo, L == 0, make_rsrc(ws + WS_ST2, SZ_ST),
                  q == 0 ? (c8 * S_ + row) * 8 : OOB);
      ST(sbase + 16);
      if (L == 0) publish(ws, FL_4 + (rt64 >> 1) * 64 + ((rt64 & 1) * 4 + w) * 8 + c8, epoch, lane);
    }
  }
  asm volatile(".Lcw_end:");
  if (blockIdx.x == 0 && w0 == 0) raise(ws_, FL_E, epoch, tid0);
  ST(37);
}

extern "C" void kernel_launch(void* const* d_in, const int* in_sizes, int n_in, void* d_out, int out_size, void* d_ws,
                              size_t ws_size, hipStream_t stream) {
  char* ws = (char*)d_ws;
  (void)hipFuncSetAttribute((const void*)k_all, hipFuncAttributeMaxDynamicSharedMemorySize, KM_LDS);
  k_all<<<256, 512, KM_LDS, stream>>>((const u16*)d_in[0], (const u16*)d_in[1], (const u16*)d_in[2], (const u16*)d_in[3],
                                     (const u16*)d_in[4], (const u16*)d_in[6], (const u16*)d_in[7], ws, (u16*)d_out STAMP_PASS(0));
}
```

```cpp
#define SEGBLK 5
#define SEGA 0
#define SEGB 0
#include <hip/hip_runtime.h>
#include <hip/hip_bf16.h>
#include <stdint.h>

#define DI __device__ __forceinline__
typedef unsigned short u16;
typedef __attribute__((ext_vector_type(8))) __bf16 bf16x8;
typedef __attribute__((ext_vector_type(2))) __bf16 bf16x2;
typedef __attribute__((ext_vector_type(4))) float f32x4;
typedef __attribute__((ext_vector_type(2))) float f32x2;
typedef __attribute__((ext_vector_type(4))) unsigned u32x4;
typedef __attribute__((ext_vector_type(2))) unsigned u32x2;
typedef __attribute__((ext_vector_type(4))) short s16x4;
#define LDS_AS __attribute__((address_space(3)))
#define GLB_AS __attribute__((address_space(1)))
typedef LDS_AS char* ldsp;
typedef unsigned long long u64;
typedef GLB_AS u64 gu64;
typedef __amdgpu_buffer_rsrc_t rsrc_t;
#define RLX_AGENT __ATOMIC_RELAXED, __HIP_MEMORY_SCOPE_AGENT

constexpr int S_ = 2048, D_ = 640, F_ = 1728;
constexpr float EPS = 1e-5f;
constexpr unsigned OOB = 0x7f000000u;

constexpr unsigned SZ_AB = 8 * 128 * 5 * 1024;
constexpr unsigned SZ_X = 128 * 20 * 1024;
constexpr unsigned SZ_H = 128 * 54 * 1024;
constexpr unsigned SZ_W1 = 16 * 140 * 1024;
constexpr unsigned SZ_W2 = 8 * 54 * 5 * 1024;
constexpr unsigned SZ_ST = 2048 * 64;
constexpr unsigned WS_AB1 = 0;
constexpr unsigned WS_AB2 = WS_AB1 + SZ_AB;
constexpr unsigned WS_X1 = WS_AB2 + SZ_AB;
constexpr unsigned WS_X2 = WS_X1 + SZ_X;
constexpr unsigned WS_X3 = WS_X2 + SZ_X;
constexpr unsigned WS_H1 = WS_X3 + SZ_X;
constexpr unsigned WS_H2 = WS_H1 + SZ_H;
constexpr unsigned WS_W1 = WS_H2 + SZ_H;
constexpr unsigned WS_W2 = WS_W1 + SZ_W1;
constexpr unsigned WS_ST1 = WS_W2 + SZ_W2;
constexpr unsigned WS_ST2 = WS_ST1 + SZ_ST;
constexpr unsigned WS_ST3 = WS_ST2 + SZ_ST;
constexpr unsigned WS_CS1 = WS_ST3 + SZ_ST;
constexpr unsigned WS_CSD = WS_CS1 + 20 * 1728 * 4;
constexpr unsigned XSLOT = 2560;
constexpr unsigned SZ_XP = 256 * 12 * XSLOT;
constexpr unsigned WS_XP = (WS_CSD + 256 * 320 + 4095) & ~4095u;
constexpr unsigned WS_FLG = WS_XP + 2 * SZ_XP;
constexpr unsigned FL_X = 0;
constexpr unsigned FL_2 = FL_X + 2048;
constexpr unsigned FL_3 = FL_2 + 2048;
constexpr unsigned FL_4 = FL_3 + 4096;
constexpr unsigned FL_5 = FL_4 + 1024;
constexpr unsigned FL_W = FL_5 + 3072;
constexpr unsigned FL_E = FL_W + 1088;
constexpr unsigned WS_END = WS_FLG + (FL_E + 1) * 128;

DI u32x4 make_srd(const void* p, unsigned bytes) {
  uint64_t a = (uint64_t)p;
  u32x4 r;
  r[0] = (unsigned)a; r[1] = (unsigned)(a >> 32) & 0xffffu; r[2] = bytes; r[3] = 0x00020000u;
  return r;
}
DI rsrc_t make_rsrc(const void* p, unsigned bytes) {
  return __builtin_amdgcn_make_buffer_rsrc((void*)p, (short)0, (int)bytes, 0x00020000);
}
template <int LOFF, int GOFF>
DI void glds(u32x4 srd, unsigned voff, unsigned ldsb, unsigned soff) {
  unsigned tmp;
  asm volatile("s_add_u32 m0, %1, %2\n\ts_add_u32 %0, %3, %4\n\tbuffer_load_dwordx4 %5, %6, %0 offen lds"
               : "=&s"(tmp)
               : "s"(ldsb), "i"(LOFF), "s"(soff), "i"(GOFF), "v"(voff), "s"(srd)
               : "memory", "scc");
}
template <int N> DI void wait_vm() { asm volatile("s_waitcnt vmcnt(%0)" ::"i"(N) : "memory"); }
DI void wait_lgkm0() { asm volatile("s_waitcnt lgkmcnt(0)" ::: "memory"); }
DI void sb() { __builtin_amdgcn_sched_barrier(0); }
DI void bar() { asm volatile("s_barrier" ::: "memory"); }

DI bf16x8 lds_rd(unsigned addr) { return *(const LDS_AS bf16x8*)(ldsp)(uintptr_t)addr; }
DI f32x4 lds_rdf(unsigned addr) { return *(const LDS_AS f32x4*)(ldsp)(uintptr_t)addr; }
DI void lds_wrf(unsigned addr, f32x4 v) { *(LDS_AS f32x4*)(ldsp)(uintptr_t)addr = v; }
DI float lds_rd1(unsigned addr) { return *(const LDS_AS float*)(ldsp)(uintptr_t)addr; }
DI void lds_wr1(unsigned addr, float v) { *(LDS_AS float*)(ldsp)(uintptr_t)addr = v; }
DI void mma(f32x4& c, const bf16x8& a, const bf16x8& b) {
  asm volatile("v_mfma_f32_16x16x32_bf16 %0, %1, %2, %0" : "+v"(c) : "v"(a), "v"(b));
}
DI void mma0(f32x4& c, const bf16x8& a, const bf16x8& b) {
  asm volatile("v_mfma_f32_16x16x32_bf16 %0, %1, %2, 0" : "=&v"(c) : "v"(a), "v"(b));
}
DI void fence_acc(f32x4 (&a)[5]) { asm volatile("s_nop 7\n\ts_nop 1" : "+v"(a[0]), "+v"(a[1]), "+v"(a[2]), "+v"(a[3]), "+v"(a[4])); }
DI void pin_acc(f32x4 (&a)[5]) { asm volatile("" : "+v"(a[0]), "+v"(a[1]), "+v"(a[2]), "+v"(a[3]), "+v"(a[4])); }
template <int M> DI void fence_all(f32x4 (&a)[M][5]) {
#pragma unroll
  for (int i = 1; i < M; ++i) pin_acc(a[i]);
  fence_acc(a[0]);
#pragma unroll
  for (int i = 1; i < M; ++i) pin_acc(a[i]);
}
DI unsigned pk2(float a, float b) {
  f32x2 v = {a, b};
  return __builtin_bit_cast(unsigned, __builtin_convertvector(v, bf16x2));
}
DI float bflo(unsigned u) { return __uint_as_float(u << 16); }
DI float bfhi(unsigned u) { return __uint_as_float(u & 0xffff0000u); }
DI float dot2(unsigned a, unsigned b, float c) {
  return __builtin_amdgcn_fdot2_f32_bf16(__builtin_bit_cast(bf16x2, a), __builtin_bit_cast(bf16x2, b), c, false);
}
DI float xsum16_32(float x) {
  auto s = __builtin_amdgcn_permlane16_swap(__float_as_uint(x), __float_as_uint(x), false, false);
  x = __uint_as_float(s[0]) + __uint_as_float(s[1]);
  auto t = __builtin_amdgcn_permlane32_swap(__float_as_uint(x), __float_as_uint(x), false, false);
  return __uint_as_float(t[0]) + __uint_as_float(t[1]);
}
template <int V> struct IC { static constexpr int v = V; };
template <int I, int N, class Fn> DI void sfor(Fn&& f) {
  if constexpr (I < N) { f(IC<I>{}); sfor<I + 1, N>(f); }
}
DI unsigned rfl(unsigned v) { return __builtin_amdgcn_readfirstlane(v); }
DI unsigned opq_v(unsigned x) { asm volatile("" : "+v"(x)); return x; }
DI unsigned opq_s(unsigned x) { asm volatile("" : "+s"(x)); return x; }
template <class T> DI T* opq_p(T* x) { asm volatile("" : "+s"(x)); return x; }
DI bf16x8 ld16(rsrc_t r, unsigned voff, unsigned soff) { return __builtin_bit_cast(bf16x8, __builtin_amdgcn_raw_buffer_load_b128(r, voff, soff, 16)); }
DI u32x4 ld16u(rsrc_t r, unsigned voff, unsigned soff) { return __builtin_amdgcn_raw_buffer_load_b128(r, voff, soff, 16); }
DI u32x2 ld8u(rsrc_t r, unsigned voff, unsigned soff) { return __builtin_amdgcn_raw_buffer_load_b64(r, voff, soff, 16); }
DI void st16(u32x4 v, rsrc_t r, unsigned voff, unsigned soff) {
  __builtin_amdgcn_raw_buffer_store_b128(v, r, voff, soff, 16);
  asm volatile("s_nop 1" :: "v"(v) : "memory");
}
DI void st16_raw(u32x4 v, rsrc_t r, unsigned voff, unsigned soff) { __builtin_amdgcn_raw_buffer_store_b128(v, r, voff, soff, 16); }
template <class... T> DI void st_guard(const T&... v) {
  (..., [](const auto& x) { asm volatile("" :: "v"(x) : "memory"); }(v));
  asm volatile("s_nop 1" ::: "memory");
  (..., [](const auto& x) { asm volatile("" :: "v"(x)); }(v));
}
DI void st8(u32x2 v, rsrc_t r, unsigned voff, unsigned soff) { __builtin_amdgcn_raw_buffer_store_b64(v, r, voff, soff, 16); }
DI void st4(unsigned v, rsrc_t r, unsigned voff, unsigned soff) { __builtin_amdgcn_raw_buffer_store_b32(v, r, voff, soff, 16); }

#define ST(i) do { if ((i) == SEGA) tm_a = (unsigned)__builtin_amdgcn_s_memrealtime(); if ((i) == SEGB) tm_b = (unsigned)__builtin_amdgcn_s_memrealtime(); } while (0)
#define STAMP_ARGS
#define STAMP_PASS(k)

template <int KS> DI unsigned foff(unsigned row, unsigned col) {
  return ((row >> 4) * KS + (col >> 5)) * 1024u + (((col >> 3) & 3) * 16u + (row & 15)) * 16u + (col & 7) * 2u;
}

extern __shared__ __attribute__((aligned(1024))) char smem[];
DI unsigned lds_base() { return (unsigned)(uintptr_t)(ldsp)smem; }

DI void ln_coef(float s1, float s2, float& mu, float& rs) {
  mu = s1 * (1.f / D_);
  rs = __builtin_amdgcn_rsqf(fmaxf(s2 * (1.f / D_) - mu * mu, 0.f) + EPS);
}

DI void g1_store(const f32x4 (&acc)[2][5], const float (&mu)[2][4], const float (&rs)[2][4], const float (&csv)[5], rsrc_t rab,
                 unsigned P, unsigned ct, unsigned w, unsigned i16, unsigned q) {
  const unsigned kstep = (ct >> 3) * 64 + P * 4 + w;
  const unsigned cq = ((q & 1) << 1) | (q >> 1);
  const unsigned so = ((ct & 7) * 128 + kstep) * 5 * 1024;
  const unsigned vo = (cq * 16 + i16) * 16;
  u32x4 y[5];
#pragma unroll
  for (int f = 0; f < 5; ++f) {
    const f32x2 cs = {csv[f], csv[f]};
    unsigned pk[2][2];
#pragma unroll
    for (int r = 0; r < 2; ++r)
#pragma unroll
      for (int e = 0; e < 2; ++e) {
        const f32x2 t = __builtin_elementwise_fma(f32x2{mu[r][2 * e], mu[r][2 * e + 1]}, cs, f32x2{acc[r][f][2 * e], acc[r][f][2 * e + 1]}) *
                        f32x2{rs[r][2 * e], rs[r][2 * e + 1]};
        pk[r][e] = __builtin_bit_cast(unsigned, __builtin_convertvector(t, bf16x2));
      }
    auto t0 = __builtin_amdgcn_permlane16_swap(pk[0][0], pk[1][0], false, false);
    auto t1 = __builtin_amdgcn_permlane16_swap(pk[0][1], pk[1][1], false, false);
    y[f] = u32x4{t0[0], t1[0], t0[1], t1[1]};
    st16_raw(y[f], rab, vo + (f & 3) * 1024, so + (f >> 2) * 4096);
  }
  st_guard(y[0], y[1], y[2], y[3], y[4]);
}

constexpr int K1_STAGE = 26 * 1024;
constexpr int K1_TR = 5 * K1_STAGE;
constexpr int K1_CS = K1_TR + 5 * 4096;

DI void k1_issue(u32x4 sa, u32x4 sbd, unsigned vo, unsigned la, unsigned lbb, unsigned soa, unsigned sob, unsigned w) {
  glds<0, 0>(sa, vo, la, soa);
  glds<1024, 8 * 1280>(sa, vo, la, soa);
  glds<2048, 16 * 1280>(sa, vo, la, soa);
  glds<3072, 24 * 1280>(sa, vo, la, soa);
  glds<0, 0>(sbd, vo, lbb, sob);
  glds<4096, 32 * 1280>(sbd, vo, lbb, sob);
  if (w < 2) glds<8192, 64 * 1280>(sbd, vo, lbb, sob);
}
struct RtUnit { bool first; unsigned ks, g; };
DI RtUnit rt_unit(unsigned u) {
  RtUnit r;
  r.first = u < 540;
  if (r.first) { r.ks = u / 27u; r.g = u - r.ks * 27u; }
  else { const unsigned v = u - 540; r.ks = v / 10u; r.g = v - r.ks * 10u; }
  return r;
}
DI void retile_issue(unsigned u, const u16* w1, const u16* w2, unsigned ldst, unsigned lane) {
  const RtUnit t = rt_unit(u);
  const unsigned ld = t.first ? F_ * 2 : D_ * 2;
  u32x4 srd = make_srd(t.first ? w1 : w2, D_ * F_ * 2);
  const unsigned vo = (lane >> 3) * ld + (lane & 7) * 16;
  const unsigned so = t.ks * 32 * ld + t.g * 128;
  glds<0, 0>(srd, vo, ldst, so);
  glds<1024, 0>(srd, vo, ldst, so + 8 * ld);
  glds<2048, 0>(srd, vo, ldst, so + 16 * ld);
  glds<3072, 0>(srd, vo, ldst, so + 24 * ld);
}
DI unsigned w1_off(unsigned rt, unsigned ks) {
  const unsigned c = rt / 7u, f = rt - c * 7, g = (c & 1) ? (f == 0 ? 6 : f - 1) : f;
  return WS_W1 + (c * 140 + (g < 4 ? ks * 4 + g : 80 + ks * 3 + g - 4)) * 1024;
}
DI void retile_finish(unsigned u, unsigned f0, rsrc_t rws, unsigned ldst, unsigned lane) {
  const RtUnit t = rt_unit(u);
  const unsigned i = lane & 15, q = lane >> 4;
  const unsigned base = ldst + (8 * q + (i >> 2)) * 128 + (i & 3) * 8 + f0 * 32;
  u32x4 v[2];
#pragma unroll
  for (int f = 0; f < 2; ++f) {
    s16x4 lo = __builtin_amdgcn_ds_read_tr16_b64_v4i16((LDS_AS s16x4*)(uintptr_t)(base + f * 32));
    s16x4 hi = __builtin_amdgcn_ds_read_tr16_b64_v4i16((LDS_AS s16x4*)(uintptr_t)(base + f * 32 + 512));
    u32x2 l2 = __builtin_bit_cast(u32x2, lo), h2 = __builtin_bit_cast(u32x2, hi);
    v[f] = u32x4{l2[0], l2[1], h2[0], h2[1]};
  }
  const unsigned rt = t.g * 4 + f0;
  if (t.first) {
    st16(v[0], rws, lane * 16, w1_off(rt, t.ks));
    st16(v[1], rws, lane * 16, w1_off(rt + 1, t.ks));
    float s[2];
#pragma unroll
    for (int f = 0; f < 2; ++f)
      s[f] = xsum16_32(((bflo(v[f][0]) + bfhi(v[f][0])) + (bflo(v[f][1]) + bfhi(v[f][1]))) + ((bflo(v[f][2]) + bfhi(v[f][2])) + (bflo(v[f][3]) + bfhi(v[f][3]))));
    st4(__float_as_uint(q == 0 ? s[0] : s[1]), rws, lane < 32 ? lane * 4 : OOB, WS_CS1 + (t.ks * F_ + rt * 16) * 4);
  } else {
    const unsigned c0 = rt / 5u, c1 = (rt + 1) / 5u;
    st16(v[0], rws, lane * 16, WS_W2 + ((c0 * 54 + t.ks) * 5 + (rt - c0 * 5)) * 1024);
    st16(v[1], rws, lane * 16, WS_W2 + ((c1 * 54 + t.ks) * 5 + (rt + 1 - c1 * 5)) * 1024);
  }
}

#define P5D 1
#define PUBG 11
#define KP3 3
#define KP1 3
constexpr int WS1 = 3, WS2 = 6;
constexpr int PUB1 = 13;
constexpr int RGAP = 10, ASLEEP2 = 40, WDPRE = 6;
constexpr int PD4 = 13, PDX = 15, PD2 = 30, P5DELAY0 = 4, P5DELAY = P5D;

constexpr int KM_LDS = 160 * 1024;
constexpr int KM_CS1 = KM_LDS - 1024;
constexpr int G2_A = 80 * 1024, G2_AS = 16 * 1024, G2_BS = 20 * 1024;
constexpr int G4_LATE = 80 * 1024;

DI gu64* flag_ptr(char* ws, unsigned f) { return (gu64*)(ws + WS_FLG + f * 128); }
DI void raise(char* ws, unsigned f, u64 epoch, unsigned lane) {
  if (lane < 16) __hip_atomic_store(flag_ptr(ws, f) + lane, epoch, RLX_AGENT);
}
DI void publish(char* ws, unsigned f, u64 epoch, unsigned lane) {
  wait_vm<0>();
  raise(ws, f, epoch, lane);
}
DI void poll0(char* ws, unsigned f, unsigned n, u64 epoch, unsigned lane) {
  gu64* p = flag_ptr(ws, f);
  bool ok = lane >= n;
#pragma unroll 1
  for (unsigned s = 0; s < (1u << 16); ++s) {
    if (!ok) ok = __hip_atomic_load(p, RLX_AGENT) == epoch;
    if (__all(ok)) break;
    __builtin_amdgcn_s_sleep(1);
  }
  asm volatile("" ::: "memory");
}
template <int DELAY> DI void poll(char* ws, unsigned f, unsigned n, u64 epoch, unsigned lane) {
  __builtin_amdgcn_s_sleep(DELAY);
  poll0(ws, f, n, epoch, lane);
}
DI void warm(unsigned ldump, u32x4 srd, unsigned voff, unsigned soff) {
  asm volatile("s_mov_b32 m0, %0\n\ts_nop 0\n\tbuffer_load_dword %1, %2, %3 offen lds" :: "s"(ldump), "v"(voff), "s"(srd), "s"(soff) : "memory");
}
DI void glds4_sc1(u32x4 srd, unsigned voff, unsigned l, unsigned so) {
  asm volatile("s_mov_b32 m0, %0\n\ts_nop 0\n\tbuffer_load_dwordx4 %1, %2, %3 offen sc1 lds\n\tbuffer_load_dwordx4 %1, %2, %3 offen offset:1024 sc1 lds\n\t"
               "buffer_load_dwordx4 %1, %2, %3 offen offset:2048 sc1 lds\n\tbuffer_load_dwordx4 %1, %2, %3 offen offset:3072 sc1 lds"
               :: "s"(l), "v"(voff), "s"(srd), "s"(so) : "memory");
}
DI void glds1_sc1(u32x4 srd, unsigned voff, unsigned l, unsigned so) {
  asm volatile("s_mov_b32 m0, %0\n\ts_nop 0\n\tbuffer_load_dwordx4 %1, %2, %3 offen sc1 lds" :: "s"(l), "v"(voff), "s"(srd), "s"(so) : "memory");
}
template <int N> DI void glds_run_sc1(u32x4 srd, unsigned voff, unsigned l, unsigned so) {
#pragma unroll
  for (int g = 0; g < N / 4; ++g) glds4_sc1(srd, voff, l + g * 4096, so + g * 4096);
#pragma unroll
  for (int g = N / 4 * 4; g < N; ++g) glds1_sc1(srd, voff, l + g * 1024, so + g * 1024);
}

struct ResTile { u32x4 r0, r1; u32x2 r2; };
struct TileOff { unsigned a0, a1, a2; };
DI TileOff tile_offsets(bool rm, unsigned row, unsigned ct, unsigned q) {
  const unsigned cA = ct * 80 + 16 * (q & 1) + 8 * (q >> 1), cS = ct * 80 + 64 + 4 * q;
  TileOff t;
  if (rm) { t.a0 = (row * D_ + cA) * 2; t.a1 = t.a0 + 64; t.a2 = (row * D_ + cS) * 2; }
  else { t.a0 = foff<20>(row, cA); t.a1 = foff<20>(row, cA + 32); t.a2 = foff<20>(row, cS); }
  asm volatile("" : "+v"(t.a0), "+v"(t.a1), "+v"(t.a2));
  return t;
}
DI ResTile load_res(rsrc_t rr, const TileOff& t) {
  ResTile rt;
  rt.r0 = ld16u(rr, t.a0, 0); rt.r1 = ld16u(rr, t.a1, 0); rt.r2 = ld8u(rr, t.a2, 0);
  return rt;
}
DI void finish_tile(f32x4 (&v)[5], const ResTile& rt, rsrc_t rout, const TileOff& o, bool stats, rsrc_t rst, unsigned sto) {
#pragma unroll
  for (int p = 0; p < 2; ++p)
#pragma unroll
    for (int r = 0; r < 4; ++r) {
      auto t = __builtin_amdgcn_permlane16_swap(__float_as_uint(v[2 * p][r]), __float_as_uint(v[2 * p + 1][r]), false, false);
      v[2 * p][r] = __uint_as_float(t[0]); v[2 * p + 1][r] = __uint_as_float(t[1]);
    }
  u32x4 y0, y1; u32x2 y2;
#pragma unroll
  for (int j = 0; j < 2; ++j) {
    y0[j] = pk2(v[0][2 * j] + bflo(rt.r0[j]), v[0][2 * j + 1] + bfhi(rt.r0[j]));
    y0[2 + j] = pk2(v[1][2 * j] + bflo(rt.r0[2 + j]), v[1][2 * j + 1] + bfhi(rt.r0[2 + j]));
    y1[j] = pk2(v[2][2 * j] + bflo(rt.r1[j]), v[2][2 * j + 1] + bfhi(rt.r1[j]));
    y1[2 + j] = pk2(v[3][2 * j] + bflo(rt.r1[2 + j]), v[3][2 * j + 1] + bfhi(rt.r1[2 + j]));
    y2[j] = pk2(v[4][2 * j] + bflo(rt.r2[j]), v[4][2 * j + 1] + bfhi(rt.r2[j]));
  }
  if (stats) {
    st16_raw(y0, rout, o.a0, 0); st16_raw(y1, rout, o.a1, 0); st8(y2, rout, o.a2, 0);
  } else {
    __builtin_amdgcn_raw_buffer_store_b128(y0, rout, o.a0, 0, 0);
    __builtin_amdgcn_raw_buffer_store_b128(y1, rout, o.a1, 0, 0);
    __builtin_amdgcn_raw_buffer_store_b64(y2, rout, o.a2, 0, 0);
  }
  st_guard(y0, y1);
  if (stats) {
    float s1a = 0.f, s1b = 0.f, s2a = 0.f, s2b = 0.f;
#pragma unroll
    for (int j = 0; j < 4; ++j) {
      s1a = dot2(y0[j], 0x3f803f80u, s1a); s2a = dot2(y0[j], y0[j], s2a);
      s1b = dot2(y1[j], 0x3f803f80u, s1b); s2b = dot2(y1[j], y1[j], s2b);
    }
    s1a = dot2(y2[0], 0x3f803f80u, s1a); s2a = dot2(y2[0], y2[0], s2a);
    s1b = dot2(y2[1], 0x3f803f80u, s1b); s2b = dot2(y2[1], y2[1], s2b);
    const float s1 = xsum16_32(s1a + s1b), s2 = xsum16_32(s2a + s2b);
    st8(u32x2{__float_as_uint(s1), __float_as_uint(s2)}, rst, sto, 0);
  }
}

DI constexpr unsigned wd_pos(int c) { return c < 8 ? 10 * c : 60 + 10 * c; }
constexpr int ORD[14] = {0, 1, 4, 5, 7, 8, 11, 12, 2, 3, 6, 9, 10, 13};
DI constexpr int g4_late(int t) { return (t >= 4 && t < 7) || t >= 11; }
DI constexpr int g4_slot(int t) { return (t < 4 ? t : (t < 7 ? t - 4 : (t < 11 ? t - 7 : t - 11))) * 5120; }

#define PHASE_IDS \
  const unsigned lane = opq_v(tid0) & 63, w = opq_s(w0), bid = opq_s(blockIdx.x); \
  const unsigned xcd = bid & 7, idx = bid >> 3, i16 = lane & 15, q = lane >> 4; \
  char* const ws = opq_p(ws_); \
  (void)xcd; (void)idx; (void)i16; (void)q;
#define IDS_G2 const unsigned kq = xcd & 3, P = (xcd >> 2) * 8 + (idx >> 2), c4 = idx & 3;
#define G2_LA(h) (lb + G2_A + ((w >> 1) * 4 + ((2 * (w & 1) + (h) + 3 - kq) & 3)) * 2048)
#define IDS_G3 const unsigned P = (xcd >> 1) * 4 + (idx >> 3), ct = (xcd & 1) * 8 + (idx & 7);
#define C8_OF ((xcd & 1) * 4 + (idx & 3))
#define IDS_G4 const unsigned rt64 = (xcd >> 1) * 8 + (idx >> 2), c8 = C8_OF;
#define LDX(r, k) ld16(rx, lane * 16 + ((k) & 3) * 1024, sox + (r) * 20480 + ((k) >> 2) * 4096)

__global__ __launch_bounds__(512, 2) void k_all(const u16* x0_, const u16* Sre_, const u16* Sim_, const u16* Dre_, const u16* Dim_,
                                               const u16* w1_, const u16* w2_, char* ws_, u16* out_ STAMP_ARGS) {
  unsigned tm_a = 0, tm_b = 0;
  ST(0);
  const unsigned tid0 = threadIdx.x, wall = rfl(tid0 >> 6), w0 = wall & 3;
  const unsigned lb = lds_base();
  const char* code; unsigned code_len;
  asm volatile("s_getpc_b64 %0\n.Lcw_begin:\n\ts_mov_b32 %1, .Lcw_end-.Lcw_begin" : "=s"(code), "=s"(code_len));
  u64 epoch;

  if (__builtin_expect(wall >= 4, 1)) {
    const unsigned sink = lb + K1_CS + 1024;
    unsigned ep0, ep1;
    asm volatile("global_load_dword %0, %2, %3 sc1\n\tglobal_load_dword %1, %2, %3 offset:4 sc1"
                 : "=&v"(ep0), "=&v"(ep1) : "v"(WS_FLG + FL_E * 128), "s"(ws_) : "memory");
    {
      PHASE_IDS
      IDS_G3
      const u32x4 sa = make_srd(opq_p(x0_), S_ * D_ * 2), sbd = make_srd(ct < 8 ? opq_p(Dre_) : opq_p(Dim_), D_ * D_ * 2);
      const unsigned vo = (lane >> 3) * 1280 + (((lane & 7) ^ (lane >> 3)) << 4);
      const unsigned soa = (P * 128 + w * 32) * 1280, sob = ((ct & 7) * 80 + w * 8) * 1280;
      const unsigned la = lb + w * 4096, lbb = lb + 16384 + w * 1024;
      const unsigned u0 = bid * 4 + w, u1 = 1024 + bid;
      const bool extra = w == 3 && bid < 56;
      const unsigned rot = ((idx & 7) + 3 * (idx >> 3)) % 10u;
      auto ko = [&](unsigned s) { const unsigned c = s + rot; return (c >= 10 ? c - 10 : c) * 128; };
      asm volatile("s_nop 4" ::: "memory");
      k1_issue(sa, sbd, vo, la, lbb, soa + ko(0), sob + ko(0), w);
      k1_issue(sa, sbd, vo, la + K1_STAGE, lbb + K1_STAGE, soa + ko(1), sob + ko(1), w);
      if (w == 3) warm(sink, make_srd(code, code_len), lane < 16 ? idx * 2048 + lane * 128 : OOB, 0);
      ST(8);
      wait_vm<6>();
      {
        unsigned lo, hi;
        asm volatile("v_readfirstlane_b32 %0, %2\n\tv_readfirstlane_b32 %1, %3\n\ts_nop 4" : "=s"(lo), "=s"(hi) : "v"(ep0), "v"(ep1));
        epoch = (u64)lo + ((u64)hi << 32) + 1;
      }
      bar();
      k1_issue(sa, sbd, vo, la + 2 * K1_STAGE, lbb + 2 * K1_STAGE, soa + ko(2), sob + ko(2), w);
      const u32x4 ss = make_srd((xcd & 2) ? Sim_ : Sre_, S_ * S_ * 2);
      const unsigned vs = (lane & 31) * 4096 + (lane >> 5) * 128;
      const unsigned sos = ((xcd >> 2) * 1024 + idx * 32) * 4096 + (xcd & 1) * 2048 + w * 256;
      unsigned so = 0;
#pragma unroll 1
      for (int i = 0; i < 10; ++i) {
        if (i == 0) wait_vm<6>(); else if (i == 1) wait_vm<2 * 6>(); else if (i < 6) wait_vm<3 * 6>();
        else if (i == 6) wait_vm<2 * 6 + 4>(); else if (i == 7) wait_vm<6 + 4>(); else wait_vm<4 + (WS2 >= 4 && WS2 < 8) + (WS1 >= 4 && WS1 < 8)>();
        bar();
        if (i == 0) {
          k1_issue(sa, sbd, vo, la + 3 * K1_STAGE, lbb + 3 * K1_STAGE, soa + ko(3), sob + ko(3), w);
          k1_issue(sa, sbd, vo, la + 4 * K1_STAGE, lbb + 4 * K1_STAGE, soa + ko(4), sob + ko(4), w);
        } else if (i == 1) {
          k1_issue(sa, sbd, vo, la, lbb, soa + ko(5), sob + ko(5), w);
          k1_issue(sa, sbd, vo, la + K1_STAGE, lbb + K1_STAGE, soa + ko(6), sob + ko(6), w);
        } else if (i < 5) k1_issue(sa, sbd, vo, la + so, lbb + so, soa + ko(i + 5), sob + ko(i + 5), w);
        so = so == 4 * K1_STAGE ? 0 : so + K1_STAGE;
        if (i == WS1) warm(sink, ss, vs, sos);
        if (i == WS2) warm(sink, ss, vs, sos + 1024);
        if (i == 5) {
          retile_issue(u0, w1_, w2_, lb + K1_TR + w * 4096, lane);
          if (extra) retile_issue(u1, w1_, w2_, lb + K1_TR + 4 * 4096, lane);
        }
      }
      ST(9);
      bar();
    }
    bf16x8 wr[10][5];
#pragma unroll
    for (int t = 0; t < 10; ++t)
#pragma unroll
      for (int f = 0; f < 5; ++f) wr[t][f] = bf16x8{};
#pragma unroll 1
    for (int L = 0; L < 2; ++L) {
      const int sbase = L * 20;
      (void)sbase;
      {
        PHASE_IDS
        IDS_G2
        const u32x4 sa = make_srd(kq < 2 ? opq_p(Sre_) : opq_p(Sim_), S_ * S_ * 2);
        const u32x4 sb_ = make_srd(ws + (L ? WS_AB2 : WS_AB1), SZ_AB);
        const unsigned va = (lane >> 3) * 4096 + (((lane & 7) ^ (lane >> 3)) << 4), vb = lane * 16;
        const unsigned soa = (P * 128 + w * 32) * 4096 + (kq & 1) * 2048;
        const unsigned sob = ((2 * c4 + (w >> 1)) * 128 + kq * 32 + (w & 1)) * 5 * 1024;
        const unsigned la = lb + G2_A + w * 4096, lbb = lb + w * 5120;
        const unsigned la0 = G2_LA(0), la1 = G2_LA(1);
        const unsigned ct = (xcd & 1) * 8 + (idx & 7);
        auto kc = [&](unsigned s) { return L ? 2 * (s & 7) + ((s ^ (s >> 3)) & 1) : s; };
        asm volatile("s_nop 4" ::: "memory");
        auto issue_a = [&](unsigned l, unsigned so) {
          glds<0, 0>(sa, va, la0 + l, so); glds<1024, 8 * 4096>(sa, va, la0 + l, so);
          glds<0, 16 * 4096>(sa, va, la1 + l, so); glds<1024, 24 * 4096>(sa, va, la1 + l, so);
        };
        if (L == 0) {
#pragma unroll
          for (int s = 0; s < 3; ++s) issue_a(s * G2_AS, soa + s * 128);
          const unsigned u0 = bid * 4 + w, u1 = 1024 + bid;
          const rsrc_t rws = make_rsrc(ws, WS_END);
          wait_vm<12>();
          retile_finish(u0, 0, rws, lb + K1_TR + w * 4096, lane);
          retile_finish(u0, 2, rws, lb + K1_TR + w * 4096, lane);
          if (w == 3 && bid < 56) {
            retile_finish(u1, 0, rws, lb + K1_TR + 4 * 4096, lane);
            retile_finish(u1, 2, rws, lb + K1_TR + 4 * 4096, lane);
          }
          __builtin_amdgcn_s_sleep(P5DELAY0);
        } else {
          issue_a(3 * G2_AS, soa + kc(3) * 128);
          __builtin_amdgcn_s_sleep(P5DELAY);
        }
        ST(sbase + 1);
        const unsigned f5 = FL_5 + ((L * 4 + kq) * 4 + c4) * 64 + ((i16 >> 1) * 2 + (w >> 1)) * 4 + (L ? 2 * (w & 1) + (i16 & 1) : (w & 1) + 2 * (i16 & 1));
        poll0(ws, f5, 16, epoch, lane);
        wait_vm<0>();
        ST(sbase + 2);
        glds_run_sc1<5>(sb_, vb, lbb, sob);
        glds_run_sc1<5>(sb_, vb, lbb + G2_BS, sob + kc(1) * 10240);
        if (L == 0) {
          raise(ws, FL_W + bid * 4 + w, epoch, lane);
          if (w == 3 && bid < 56) raise(ws, FL_W + 1024 + bid, epoch, lane);
          wait_vm<6>();
        } else wait_vm<5>();
        bar();
        glds_run_sc1<5>(sb_, vb, lbb + 2 * G2_BS, sob + kc(2) * 10240);
        if (L == 0) issue_a(3 * G2_AS, soa + kc(3) * 128);
        glds_run_sc1<5>(sb_, vb, lbb + 3 * G2_BS, sob + kc(3) * 10240);
        ST(sbase + 3);
        const u32x4 sw = make_srd(ws + WS_W1, SZ_W1);
        const unsigned sow = ct * 140 * 1024;
        unsigned fw, fw0 = 0, fw1 = 0;
        {
          const unsigned n = w * 42 + (lane < 42 ? lane : 0), k3 = n / 3u, g3 = (7 * ct >> 2) + (n - k3 * 3), n2 = n - 60;
          fw = FL_W + (n < 60 ? k3 * 27 + (g3 < 26 ? g3 : 26) : 540 + (n2 >> 1) * 10 + (5 * C8_OF >> 2) + (n2 & 1));
          if (L) fw = f5 + 1024;
        }
#pragma unroll 1
        for (int s = 0; s < 16; ++s) {
          if (s == 0) wait_vm<10>();
          if (s == 1) wait_vm<14>();
          wait_vm<18>();
          if (s == (L ? 4 : 9)) {
            asm volatile("" : "+v"(fw0), "+v"(fw1));
            if (!__all(fw0 == (unsigned)epoch && fw1 == (unsigned)(epoch >> 32))) poll0(ws, fw, 42, epoch, lane);
          }
          bar();
          const unsigned sl = s & 3;
          if (s < 12) {
            issue_a(sl * G2_AS, soa + kc(s + 4) * 128);
            glds_run_sc1<5>(sb_, vb, lbb + sl * G2_BS, sob + kc(s + 4) * 10240);
            if (s == (L ? 1 : 6))
              asm volatile("global_load_dword %0, %2, %3 sc1\n\tglobal_load_dword %1, %2, %3 offset:4 sc1"
                           : "+v"(fw0), "+v"(fw1) : "v"(WS_FLG + fw * 128), "s"(ws) : "memory");
          } else {
            glds_run_sc1<5>(sw, vb, lbb + sl * G2_BS, sow + w * 5120 + sl * G2_BS);
            if (s < 15) glds_run_sc1<4>(sw, vb, la + sl * G2_AS, sow + G2_A + w * 4096 + sl * G2_AS);
            else glds_run_sc1<3>(sw, vb, lb + G2_A + 3 * G2_AS + w * 3072, sow + G2_A + 3 * G2_AS + w * 3072);
          }
        }
        ST(sbase + 4);
      }
      {
        PHASE_IDS
        IDS_G4
        const unsigned ct = (xcd & 1) * 8 + (idx & 7);
        if (w >= 2) {
          const rsrc_t rc = make_rsrc(ws + WS_CS1, 20 * F_ * 4);
          const unsigned c = (w - 2) * 64 + lane, col = ct * 112 + c;
          const unsigned vo = c < 112 && col < F_ ? col * 4 : OOB;
          float csp = 0.f;
#pragma unroll
          for (int k = 0; k < 20; ++k) csp += __uint_as_float(__builtin_amdgcn_raw_buffer_load_b32(rc, vo, k * F_ * 4, 16));
          lds_wr1(lb + KM_CS1 + c * 4, csp);
        }
        const unsigned so = (c8 * 54 + w * 14) * 5 * 1024;
        wait_vm<0>();
        if (L == 0) {
          const rsrc_t rw = make_rsrc(ws + WS_W2, SZ_W2);
          const unsigned vl = lane * 16, vz = lane * 16 + (w < 3 ? 0u : OOB);
          sfor<0, 50>([&](auto N) {
            constexpr int t = 4 + N.v / 5, n = ORD[t] * 5 + N.v % 5;
            wr[t - 4][N.v % 5] = ld16(rw, (ORD[t] < 12 ? vl : vz) + (n & 3) * 1024, so + (n >> 2) * 4096);
            if constexpr (N.v % 5 == 4 && N.v < 49) { sb(); __builtin_amdgcn_s_sleep(RGAP); sb(); }
          });
          sb();
        }
        wait_lgkm0();
        ST(sbase + 5);
        bar();
        bar();
        const u32x4 s2 = make_srd(ws + WS_W2, SZ_W2);
        const unsigned sg = rfl(so);
        asm volatile("s_nop 4" ::: "memory");
        glds_run_sc1<10>(s2, lane * 16, lb + w * 20480, sg);
        glds_run_sc1<10>(s2, lane * 16, lb + w * 20480 + 10240, sg + 4 * 5120);
        wait_vm<0>();
        ST(sbase + 6);
        const unsigned pe = lb + w * 20480 + lane * 16, pl = lb + G4_LATE + w * 15360 + lane * 16;
        auto put = [&](auto T0, auto T1) {
          sfor<T0.v * 5, T1.v * 5>([&](auto N) {
            constexpr int t = N.v / 5, f = N.v % 5;
            *(LDS_AS bf16x8*)(ldsp)(uintptr_t)((g4_late(t) ? pl : pe) + g4_slot(t) + f * 1024) = wr[t - 4][f];
          });
          wait_lgkm0();
        };
        bar();
        put(IC<4>{}, IC<7>{});
        bar();
        put(IC<7>{}, IC<11>{});
        bar();
        put(IC<11>{}, IC<14>{});
        bar();
      }
      if (L) { bar(); break; }
      {
        PHASE_IDS
        IDS_G3
        const u32x4 sd = make_srd(ct < 8 ? opq_p(Dre_) : opq_p(Dim_), D_ * D_ * 2);
        const unsigned vo = (lane >> 3) * 1280 + (((lane & 7) ^ (lane >> 3)) << 4);
        const unsigned so = (ct & 7) * 80 * 1280;
        sfor<0, 10>([&](auto G) { glds<G.v * 1024, G.v * 8 * 1280>(sd, vo, lb + 20 * w * 1024, so + 2 * w * 128); });
        sfor<0, WDPRE>([&](auto G) { glds<G.v * 1024, G.v * 8 * 1280>(sd, vo, lb + (20 * w + 10) * 1024, so + (2 * w + 1) * 128); });
        bar();
        sfor<WDPRE, 10>([&](auto G) { glds<G.v * 1024, G.v * 8 * 1280>(sd, vo, lb + (20 * w + 10) * 1024, so + (2 * w + 1) * 128); });
        sfor<0, 5>([&](auto G) { glds<G.v * 1024, G.v * 8 * 1280>(sd, vo, lb + (140 + w * 5) * 1024, so + (8 + (w >> 1)) * 128 + (w & 1) * 40 * 1280); });
        wait_vm<0>();
        ST(7);
        bar();
      }
      {
        PHASE_IDS
        IDS_G2
        const u32x4 sa = make_srd(kq < 2 ? opq_p(Sre_) : opq_p(Sim_), S_ * S_ * 2);
        const unsigned va = (lane >> 3) * 4096 + (((lane & 7) ^ (lane >> 3)) << 4);
        const unsigned soa = (P * 128 + w * 32) * 4096 + (kq & 1) * 2048;
        __builtin_amdgcn_s_sleep(ASLEEP2);
#pragma unroll
        for (int s = 0; s < 3; ++s) {
          const unsigned l0 = G2_LA(0) + s * G2_AS, l1 = G2_LA(1) + s * G2_AS, so = soa + (2 * s + (s & 1)) * 128;
          glds<0, 0>(sa, va, l0, so); glds<1024, 8 * 4096>(sa, va, l0, so);
          glds<0, 16 * 4096>(sa, va, l1, so); glds<1024, 24 * 4096>(sa, va, l1, so);
        }
        bar();
      }
    }
    return;
  }

  {
    const u64 e = __hip_atomic_load(flag_ptr(ws_, FL_E), RLX_AGENT);
    epoch = (u64)rfl((unsigned)e) + ((u64)rfl((unsigned)(e >> 32)) << 32) + 1;
  }
#pragma unroll 1
  for (int L = 0; L < 2; ++L) {
    const int sbase = L * 20;
    (void)sbase;
    {
      PHASE_IDS
      IDS_G3
      f32x4 acc[2][5];
      float mu[2][4], rs[2][4], csv[5];
      const float sign = ct < 8 ? 1.f : -1.f;
      const unsigned f5 = FL_5 + (((ct >> 3) * 2 + (P >> 3)) * 4 + ((ct & 7) >> 1)) * 64 + ((P & 7) * 2 + (ct & 1)) * 4 + w;
      if (L == 0) {
        const unsigned ra0 = lb + w * 4096 + i16 * 128 + ((q ^ (lane & 7)) << 4);
        const unsigned rb0 = lb + 16384 + i16 * 128 + ((q ^ (lane & 7)) << 4);
        const f32x4 z4 = {0.f, 0.f, 0.f, 0.f};
        f32x4 cs0 = z4, cs1 = z4, rs_[2] = {z4, z4}, rq_[2] = {z4, z4};
#pragma unroll
        for (int r = 0; r < 2; ++r)
#pragma unroll
          for (int f = 0; f < 5; ++f) acc[r][f] = z4;
        bf16x8 fr[2][8];
        bf16x8 one = __builtin_bit_cast(bf16x8, u32x4{0x3f803f80u, 0x3f803f80u, 0x3f803f80u, 0x3f803f80u});
        auto half = [&](auto KK, unsigned so) {
          constexpr int kk = KK.v;
          const unsigned pa = (ra0 ^ ((kk ^ 1) * 64)) + so, pb = (rb0 ^ ((kk ^ 1) * 64)) + so;
          sfor<0, 16>([&](auto M) {
            constexpr int m = M.v;
            if constexpr (m < 10) mma(acc[m & 1][m >> 1], fr[kk][m & 1], fr[kk][2 + (m >> 1)]);
            else if constexpr (m == 10) mma(cs0, one, fr[kk][7]);
            else if constexpr (m == 11) mma(cs1, one, fr[kk][6]);
            else if constexpr (m < 14) mma(rs_[m - 12], fr[kk][m - 12], one);
            else mma(rq_[m - 14], fr[kk][m - 14], fr[kk][m - 14]);
            constexpr int n = m >> 1;
            if constexpr ((m & 1) == 0) fr[kk ^ 1][n] = n < 2 ? lds_rd(pa + n * 2048) : (n < 7 ? lds_rd(pb + (n - 2) * 2048) : lds_rd(pb + w * 2048));
            sb();
          });
        };
        asm volatile("s_nop 1" : "+v"(cs0), "+v"(cs1), "+v"(rs_[0]), "+v"(rs_[1]), "+v"(rq_[0]), "+v"(rq_[1]), "+v"(one));
        fence_all(acc);
        bar();
        ST(38);
        fr[0][0] = lds_rd(ra0); fr[0][1] = lds_rd(ra0 + 2048);
#pragma unroll
        for (int f = 0; f < 5; ++f) fr[0][2 + f] = lds_rd(rb0 + f * 2048);
        fr[0][7] = lds_rd(rb0 + w * 2048);
        sb();
        unsigned so = 0;
#pragma unroll 1
        for (int i = 0; i < 10; ++i) {
          half(IC<0>{}, so);
          wait_lgkm0();
          bar();
          so = so == 4 * K1_STAGE ? 0 : so + K1_STAGE;
          half(IC<1>{}, so);
        }
        fence_all(acc);
        asm volatile("" : "+v"(cs0), "+v"(cs1), "+v"(rs_[0]), "+v"(rs_[1]), "+v"(rq_[0]), "+v"(rq_[1]));
        const unsigned csb = lb + K1_CS;
        if (q == 0) {
          lds_wr1(csb + (w * 16 + i16) * 4, cs0[0]);
          if (w == 0) lds_wr1(csb + (64 + i16) * 4, cs1[0]);
        }
        wait_lgkm0();
        bar();
        ST(18);
#pragma unroll
        for (int f = 0; f < 5; ++f) csv[f] = lds_rd1(csb + (f * 16 + i16) * 4);
#pragma unroll
        for (int r = 0; r < 2; ++r)
#pragma unroll
          for (int j = 0; j < 4; ++j) {
            float m_, r_;
            ln_coef(rs_[r][j], __shfl(rq_[r][j], q * 20 + j), m_, r_);
            mu[r][j] = -m_; rs[r][j] = sign * r_;
          }
        if (w == 0) {
          const rsrc_t rc = make_rsrc(ws + WS_CSD, 256 * 320);
          st4(__float_as_uint(q == 0 ? csv[0] : (q == 1 ? csv[1] : (q == 2 ? csv[2] : csv[3]))), rc, lane * 4, bid * 320);
          st4(__float_as_uint(csv[4]), rc, q == 0 ? 256 + lane * 4 : OOB, bid * 320);
        }
        g1_store(acc, mu, rs, csv, make_rsrc(ws + WS_AB1, SZ_AB), P, ct, w, i16, q);
        ST(19);
        publish(ws, f5, epoch, lane);
      } else {
        {
          const rsrc_t rc = make_rsrc(ws + WS_CSD, 256 * 320);
#pragma unroll
          for (int f = 0; f < 5; ++f) csv[f] = __uint_as_float(__builtin_amdgcn_raw_buffer_load_b32(rc, (f * 16 + i16) * 4, bid * 320, 16));
        }
        const unsigned h0 = P & 1;
        poll<PD4>(ws, FL_4 + P * 64 + ((i16 >> 3) * 4 + w) * 8 + (i16 & 7), 16, epoch, lane);
        ST(17);
        const rsrc_t rx = make_rsrc(ws + WS_X2, SZ_X);
        const unsigned sox0 = (P * 8 + h0 * 4 + w) * 20 * 1024, sox1 = (P * 8 + (h0 ^ 1) * 4 + w) * 20 * 1024;
        bf16x8 a[40];
#define LDA(n) a[n] = ld16(rx, lane * 16 + ((n) & 3) * 1024, ((n) < 20 ? sox0 : sox1) + (((n) % 20) >> 2) * 4096)
        sfor<0, 6>([&](auto N) { LDA(N.v); });
        const rsrc_t rst = make_rsrc(ws + WS_ST2, SZ_ST);
        const unsigned sto = ((lane >> 5) * 4 * S_ + P * 128 + (((lane >> 4) & 1) ^ h0) * 64 + w * 16 + i16) * 8;
        const u32x2 sp0 = ld8u(rst, sto, 0), sp1 = ld8u(rst, sto, S_ * 8), sp2 = ld8u(rst, sto, 2 * S_ * 8), sp3 = ld8u(rst, sto, 3 * S_ * 8);
        sb();
        bar();
        bf16x8 b[2][5];
        const unsigned rb = lb + i16 * 128 + ((q ^ (lane & 7)) << 4);
        const rsrc_t rab = make_rsrc(ws + WS_AB2, SZ_AB);
        const unsigned vo = ((2 * (w & 1) + (q >> 1)) * 16 + i16) * 16 + (q & 1) * 8;
        const unsigned so = ((ct & 7) * 128 + (ct >> 3) * 64 + P * 4 + (w >> 1)) * 5 * 1024;
        auto put = [&](auto H) {
          constexpr int hh = H.v;
#pragma unroll
          for (int f = 0; f < 5; ++f) {
            const f32x2 cs = {csv[f], csv[f]};
            u32x2 y;
#pragma unroll
            for (int e = 0; e < 2; ++e) {
              const f32x2 t = __builtin_elementwise_fma(f32x2{mu[hh][2 * e], mu[hh][2 * e + 1]}, cs, f32x2{acc[hh][f][2 * e], acc[hh][f][2 * e + 1]}) *
                              f32x2{rs[hh][2 * e], rs[hh][2 * e + 1]};
              y[e] = __builtin_bit_cast(unsigned, __builtin_convertvector(t, bf16x2));
            }
            st8(y, rab, vo + (f & 3) * 1024, so + (hh ^ h0) * 10240 + (f >> 2) * 4096);
          }
        };
#pragma unroll
        for (int f = 0; f < 5; ++f) b[0][f] = lds_rd(rb + wd_pos(0) * 1024 + f * 2048);
        sb();
        sfor<0, 40>([&](auto N) {
          constexpr int n = N.v, hh = n / 20, k = n % 20, kn = (k + 1) % 20;
          if constexpr (n == 2 * KP1) {
            float s1 = (__uint_as_float(sp0[0]) + __uint_as_float(sp1[0])) + (__uint_as_float(sp2[0]) + __uint_as_float(sp3[0]));
            float s2 = (__uint_as_float(sp0[1]) + __uint_as_float(sp1[1])) + (__uint_as_float(sp2[1]) + __uint_as_float(sp3[1]));
            auto ta = __builtin_amdgcn_permlane32_swap(__float_as_uint(s1), __float_as_uint(s1), false, false);
            auto tb = __builtin_amdgcn_permlane32_swap(__float_as_uint(s2), __float_as_uint(s2), false, false);
            float m, rr;
            ln_coef(__uint_as_float(ta[0]) + __uint_as_float(ta[1]), __uint_as_float(tb[0]) + __uint_as_float(tb[1]), m, rr);
            m = -m; rr *= sign;
#pragma unroll
            for (int r = 0; r < 2; ++r)
#pragma unroll
              for (int e = 0; e < 4; ++e) { mu[r][e] = __shfl(m, r * 16 + q * 4 + e); rs[r][e] = __shfl(rr, r * 16 + q * 4 + e); }
            asm volatile("" : "+v"(mu[0][0]), "+v"(mu[0][1]), "+v"(mu[0][2]), "+v"(mu[0][3]), "+v"(mu[1][0]), "+v"(mu[1][1]), "+v"(mu[1][2]), "+v"(mu[1][3]),
                              "+v"(rs[0][0]), "+v"(rs[0][1]), "+v"(rs[0][2]), "+v"(rs[0][3]), "+v"(rs[1][0]), "+v"(rs[1][1]), "+v"(rs[1][2]), "+v"(rs[1][3]));
            sb();
          }
          sfor<0, 5>([&](auto F) {
            constexpr int f = F.v;
            if constexpr (k == 0) mma0(acc[hh][f], a[n], b[n & 1][f]); else mma(acc[hh][f], a[n], b[n & 1][f]);
            if constexpr (n + 1 < 40) b[(n + 1) & 1][f] = lds_rd((rb ^ ((kn & 1) * 64)) + wd_pos(kn >> 1) * 1024 + f * 2048);
            if constexpr ((f == 1 || f == 3) && 6 + 2 * n + (f >> 1) < 40) LDA(6 + 2 * n + (f >> 1));
            sb();
          });
          if constexpr (n == 21) { pin_acc(acc[0]); put(IC<0>{}); sb(); }
          if constexpr (n == 20 + PUBG) publish(ws, f5 + 1024, epoch, lane);
          if constexpr (n == 0) ST(40);
          if constexpr (n == 9) ST(41);
          if constexpr (n == 19) ST(42);
        });
        fence_acc(acc[1]);
        bar();
        ST(39);
        put(IC<1>{});
        ST(20);
        publish(ws, f5 + 2048, epoch, lane);
      }
    }
    {
      PHASE_IDS
      IDS_G2
      const unsigned m = w >> 1, n = w & 1;
      const unsigned ra = lb + G2_A + (m * 64 + i16) * 128 + ((q ^ (lane & 7)) << 4);
      const unsigned rb = lb + n * 10240 + lane * 16;
      const unsigned row = P * 128 + m * 64 + kq * 16 + i16, ct = 2 * c4 + n;
      const TileOff ro = tile_offsets(L == 0, row, ct, q), oo = tile_offsets(false, row, ct, q);
      const rsrc_t rxp = make_rsrc(ws + WS_XP + L * SZ_XP, SZ_XP);
      const unsigned xo = ((P * 4 + c4) * 48 + w) * XSLOT;
      f32x4 acc[4][5];
#pragma unroll
      for (int i = 0; i < 4; ++i)
#pragma unroll
        for (int f = 0; f < 5; ++f) acc[i][f] = f32x4{0.f, 0.f, 0.f, 0.f};
      bf16x8 fa[2][4], fb[2][5];
      unsigned ra1 = ra ^ 64, rb2 = rb + 2 * G2_BS;
      asm volatile("" : "+v"(ra1), "+v"(rb2));
      auto half = [&](auto KK, auto SL) {
        constexpr int kk = KK.v, sl = SL.v;
        sfor<0, 20>([&](auto M) {
          constexpr int mm = M.v;
          mma(acc[mm / 5][mm % 5], fb[kk][mm % 5], fa[kk][mm / 5]);
          if constexpr (mm < 5) fb[kk ^ 1][mm] = lds_rd((sl < 2 ? rb : rb2) + (sl & 1) * G2_BS + (kk ^ 1) * 5120 + mm * 1024);
          else if constexpr (mm < 9) fa[kk ^ 1][mm - 5] = lds_rd((kk ? ra : ra1) + sl * G2_AS + (mm - 5) * 2048);
          sb();
        });
      };
      if (L) poll0(ws, FL_4 + P * 64 + (m * 4 + kq) * 8 + ct, 1, epoch, lane);
      ResTile rt = load_res(make_rsrc(L ? (const void*)(ws + WS_X2) : (const void*)opq_p(x0_), SZ_X), ro);
      fence_all(acc);
      bar();
      ST(sbase + 1);
#pragma unroll
      for (int f = 0; f < 5; ++f) fb[0][f] = lds_rd(rb + f * 1024);
#pragma unroll
      for (int i = 0; i < 4; ++i) fa[0][i] = lds_rd(ra + i * 2048);
      sb();
      auto stage = [&](auto U) {
        half(IC<0>{}, U);
        wait_lgkm0();
        bar();
        half(IC<1>{}, IC<(U.v + 1) & 3>{});
      };
#pragma unroll 1
      for (int g = 0; g < 3; ++g) sfor<0, 4>(stage);
      sfor<0, 3>(stage);
      half(IC<0>{}, IC<3>{});
      wait_lgkm0();
      bar();
      {
        unsigned p[3][10];
        u32x4 y[3][2];
        auto pack = [&](auto N) {
          constexpr int i = N.v / 5, f = N.v % 5;
          asm volatile("" : "+v"(acc[i][f]));
          p[i][2 * f] = pk2(acc[i][f][0], acc[i][f][1]); p[i][2 * f + 1] = pk2(acc[i][f][2], acc[i][f][3]);
          const unsigned o = xo + (((kq + 1 + i) & 3) * 3 + 2 - i) * 4 * XSLOT;
          if constexpr (f == 1) { y[i][0] = u32x4{p[i][0], p[i][1], p[i][2], p[i][3]}; st16_raw(y[i][0], rxp, lane * 16, o); }
          if constexpr (f == 3) { y[i][1] = u32x4{p[i][4], p[i][5], p[i][6], p[i][7]}; st16_raw(y[i][1], rxp, lane * 16 + 1024, o); }
          if constexpr (f == 4) st8(u32x2{p[i][8], p[i][9]}, rxp, lane * 8 + 2048, o);
        };
        sfor<0, 20>([&](auto M) {
          constexpr int mm = M.v;
          mma(acc[mm / 5][mm % 5], fb[1][mm % 5], fa[1][mm / 5]);
          if constexpr (mm >= 8) pack(IC<mm - 8>{});
          sb();
        });
        ST(sbase + 2);
        pack(IC<12>{}); pack(IC<13>{}); pack(IC<14>{});
        st_guard(y[0][0], y[0][1], y[1][0], y[1][1], y[2][0], y[2][1]);
      }
      f32x4 (&v)[5] = acc[3];
      fence_acc(v);
      const unsigned fx = FL_X + ((L * 64 + P * 4 + c4) * 4 + w) * 4;
      ST(sbase + 3);
      publish(ws, fx + kq, epoch, lane);
      ST(sbase + 4);
      poll<PDX>(ws, fx + ((kq + 1 + lane) & 3), 3, epoch, lane);
      ST(sbase + 5);
      const unsigned xi = xo + kq * 12 * XSLOT;
      u32x4 g0[3], g1[3]; u32x2 g2[3];
#pragma unroll
      for (int s = 0; s < 3; ++s) {
        g0[s] = ld16u(rxp, lane * 16, xi + s * 4 * XSLOT);
        g1[s] = ld16u(rxp, lane * 16 + 1024, xi + s * 4 * XSLOT);
        g2[s] = ld8u(rxp, lane * 8 + 2048, xi + s * 4 * XSLOT);
      }
      sb();
#pragma unroll
      for (int s = 0; s < 3; ++s) {
        v[0] += f32x4{bflo(g0[s][0]), bfhi(g0[s][0]), bflo(g0[s][1]), bfhi(g0[s][1])};
        v[1] += f32x4{bflo(g0[s][2]), bfhi(g0[s][2]), bflo(g0[s][3]), bfhi(g0[s][3])};
        v[2] += f32x4{bflo(g1[s][0]), bfhi(g1[s][0]), bflo(g1[s][1]), bfhi(g1[s][1])};
        v[3] += f32x4{bflo(g1[s][2]), bfhi(g1[s][2]), bflo(g1[s][3]), bfhi(g1[s][3])};
        v[4] += f32x4{bflo(g2[s][0]), bfhi(g2[s][0]), bflo(g2[s][1]), bfhi(g2[s][1])};
      }
      finish_tile(v, rt, make_rsrc(ws + (L ? WS_X3 : WS_X1), SZ_X), oo, true, make_rsrc(ws + (L ? WS_ST3 : WS_ST1), SZ_ST),
                  q == 0 ? (ct * S_ + row) * 8 : OOB);
      ST(sbase + 6);
      publish(ws, FL_2 + (L * 16 + P) * 64 + (m * 4 + kq) * 8 + ct, epoch, lane);
      ST(sbase + 7);
    }
    u32x4 y2[2]; u32x2 y1[2];
    {
      PHASE_IDS
      IDS_G3
      typedef __attribute__((ext_vector_type(2))) short s16x2;
      poll<PD2>(ws, FL_2 + (L * 16 + P) * 64 + w * 16 + i16, 16, epoch, lane);
      ST(sbase + 8);
      const rsrc_t rx = make_rsrc(ws + (L ? WS_X3 : WS_X1), SZ_X);
      const rsrc_t rst = make_rsrc(ws + (L ? WS_ST3 : WS_ST1), SZ_ST);
      const unsigned sox = (P * 8 + w * 2) * 20 * 1024;
      const unsigned par = ct & 1;
      bf16x8 a[20][2];
#pragma unroll
      for (int k = 0; k < 3; ++k) { a[k][0] = LDX(0, k); a[k][1] = LDX(1, k); }
      const unsigned sto = (2 * q * S_ + P * 128 + w * 32 + i16) * 8;
      const u32x2 sa0 = ld8u(rst, sto, 0), sa1 = ld8u(rst, sto, S_ * 8), sb0 = ld8u(rst, sto + 128, 0), sb1 = ld8u(rst, sto + 128, S_ * 8);
      float mu[2], rs[2];
      sb();
      bar();
      f32x4 acc[2][4];
      bf16x8 b[2][4];
      const unsigned rb = lb + lane * 16;
#pragma unroll
      for (int f = 0; f < 4; ++f) b[0][f] = lds_rd(rb + f * 1024);
      sb();
      sfor<0, 20>([&](auto K) {
        constexpr int k = K.v;
        const unsigned pb = k + 1 < 20 ? rb + (k + 1) * 4096 : rb + 80 * 1024;
        if constexpr (k == KP3) {
          ln_coef(xsum16_32(__uint_as_float(sa0[0]) + __uint_as_float(sa1[0])), xsum16_32(__uint_as_float(sa0[1]) + __uint_as_float(sa1[1])), mu[0], rs[0]);
          ln_coef(xsum16_32(__uint_as_float(sb0[0]) + __uint_as_float(sb1[0])), xsum16_32(__uint_as_float(sb0[1]) + __uint_as_float(sb1[1])), mu[1], rs[1]);
          asm volatile("" : "+v"(mu[0]), "+v"(mu[1]), "+v"(rs[0]), "+v"(rs[1]));
          sb();
        }
        sfor<0, 8>([&](auto M) {
          constexpr int f = M.v >> 1, r = M.v & 1;
          if constexpr (k == 0) mma0(acc[r][f], b[0][f], a[0][r]); else mma(acc[r][f], b[k & 1][f], a[k][r]);
          if constexpr (r == 0 && (k + 1 < 20 || f < 3)) b[(k + 1) & 1][f] = lds_rd(pb + f * 1024);
          if constexpr (r == 1 && 2 * k + 3 + (f >> 1) < 20) a[2 * k + 3 + (f >> 1)][f & 1] = LDX(f & 1, 2 * k + 3 + (f >> 1));
          sb();
        });
        if constexpr (k == 0) ST(sbase + 9);
        if constexpr (k == 3) ST(51 + L * 5);
        if constexpr (k == 7) ST(52 + L * 5);
        if constexpr (k == 11) ST(53 + L * 5);
        if constexpr (k == 15) ST(54 + L * 5);
        if constexpr (k == 19) ST(55 + L * 5);
      });
      auto act = [&](const f32x4& c, int r, const f32x4& cs, unsigned (&pk)[2]) {
        const f32x2 nm = {-mu[r], -mu[r]}, r2 = {rs[r], rs[r]};
#pragma unroll
        for (int e = 0; e < 2; ++e) {
          const f32x2 z = __builtin_elementwise_fma(nm, f32x2{cs[2 * e], cs[2 * e + 1]}, f32x2{c[2 * e], c[2 * e + 1]}) * r2;
          const s16x2 hb = __builtin_bit_cast(s16x2, __builtin_convertvector(z, bf16x2));
          pk[e] = __builtin_bit_cast(unsigned, __builtin_elementwise_max(hb, s16x2{0, 0}));
        }
      };
      auto pair = [&](const unsigned (&u)[2], const unsigned (&v)[2]) {
        auto t0 = __builtin_amdgcn_permlane16_swap(u[0], v[0], false, false);
        auto t1 = __builtin_amdgcn_permlane16_swap(u[1], v[1], false, false);
        return u32x4{t0[0], t1[0], t0[1], t1[1]};
      };
      const unsigned csb = lb + KM_CS1 + q * 16;
      f32x4 c1s[4];
#pragma unroll
      for (int f = 0; f < 4; ++f) c1s[f] = lds_rdf(csb + (f + par) * 64);
      asm volatile("s_nop 7\n\ts_nop 1" : "+v"(acc[0][0]), "+v"(acc[0][1]), "+v"(acc[0][2]), "+v"(acc[0][3]),
                                          "+v"(acc[1][0]), "+v"(acc[1][1]), "+v"(acc[1][2]), "+v"(acc[1][3]));
      wait_lgkm0();
      bar();
      ST(43 + L * 4);
      {
        const rsrc_t rh = make_rsrc(ws + (L ? WS_H2 : WS_H1), SZ_H);
        const unsigned vo = ((((q & 1) << 1) | (q >> 1)) * 16 + i16) * 16;
        const unsigned so = ((P * 8 + w * 2) * 54 + 7 * (ct >> 1) + 4 * par) * 1024;
        u32x4 y[2][2];
#pragma unroll
        for (int p = 0; p < 2; ++p) {
          const f32x4 c0 = c1s[2 * p], c1 = c1s[2 * p + 1];
#pragma unroll
          for (int r = 0; r < 2; ++r) {
            unsigned u[2], v[2];
            act(acc[r][2 * p], r, c0, u); act(acc[r][2 * p + 1], r, c1, v);
            y[p][r] = pair(u, v);
            st16_raw(y[p][r], rh, p == 1 && ct == 15 ? OOB : vo, so + (r * 54 + p) * 1024);
          }
        }
        st_guard(y[0][0], y[0][1], y[1][0], y[1][1]);
      }
      ST(44 + L * 4);
      sb();
      sfor<0, 20>([&](auto K) {
        constexpr int k = K.v;
        const unsigned pb = rb + 80 * 1024 + (k + 1) * 3072;
        sfor<0, 6>([&](auto M) {
          constexpr int f = M.v >> 1, r = M.v & 1;
          if constexpr (k == 0) mma0(acc[r][f], b[0][f], a[0][r]); else mma(acc[r][f], b[k & 1][f], a[k][r]);
          if constexpr (r == 0 && k + 1 < 20) b[(k + 1) & 1][f] = lds_rd(pb + f * 1024);
          sb();
        });
        if constexpr (k == PUB1) publish(ws, FL_3 + (L * 32 + P) * 64 + w * 16 + ct, epoch, lane);
      });
      const f32x4 c0 = lds_rdf(csb + (4 + par) * 64), c1 = lds_rdf(csb + (5 + par) * 64), c2 = lds_rdf(csb + (par ? 0 : 6) * 64);
      asm volatile("s_nop 7\n\ts_nop 1" : "+v"(acc[0][0]), "+v"(acc[0][1]), "+v"(acc[0][2]), "+v"(acc[1][0]), "+v"(acc[1][1]), "+v"(acc[1][2]));
      ST(sbase + 10);
#pragma unroll
      for (int r = 0; r < 2; ++r) {
        unsigned u[2], v[2], s[2];
        act(acc[r][0], r, c0, u); act(acc[r][1], r, c1, v); act(acc[r][2], r, c2, s);
        y2[r] = pair(u, v);
        y1[r] = u32x2{s[0], s[1]};
      }
      asm volatile("" : "+v"(y2[0]), "+v"(y2[1]), "+v"(y1[0]), "+v"(y1[1]));
      ST(sbase + 11);
    }
    {
      PHASE_IDS
      IDS_G4
      const unsigned row = rt64 * 64 + w * 16 + i16;
      const rsrc_t rh = make_rsrc(ws + (L ? WS_H2 : WS_H1), SZ_H);
      const unsigned soh = (rt64 * 4 * 54 + w * 14) * 1024;
      const unsigned vl = lane * 16, vz = lane * 16 + (w < 3 ? 0u : OOB);
      bf16x8 a[14][4], b[2][5];
      unsigned sh[4];
#pragma unroll
      for (int i = 0; i < 4; ++i) sh[i] = soh + ((w + 1 + i) & 3) * 54 * 1024;
#define LDH(t, i) a[t][i] = ld16(rh, (ORD[t] < 12 ? vl : vz) + (ORD[t] & 3) * 1024, sh[i] + (ORD[t] >> 2) * 4096)
      const TileOff ro = tile_offsets(false, row, c8, q), oo = tile_offsets(L != 0, row, c8, q);
      const unsigned f3 = FL_3 + (L * 32 + (rt64 >> 1)) * 64 + ((rt64 & 1) * 2 + (lane & 1)) * 16 + 4 * w + ((lane >> 1) & 3);
      poll0(ws, f3, 8, epoch, lane);
      ST(sbase + 13);
      sfor<0, 8>([&](auto N) { LDH(N.v / 4, N.v % 4); });
      sb();
      {
        const unsigned P = (xcd >> 1) * 4 + (idx >> 3), ct = (xcd & 1) * 8 + (idx & 7), par = ct & 1;
        const unsigned vo = ((((q & 1) << 1) | (q >> 1)) * 16 + i16) * 16;
        const unsigned v1 = ((2 * par + (q >> 1)) * 16 + i16) * 16 + (q & 1) * 8;
        const unsigned so = ((P * 8 + w * 2) * 54 + 7 * (ct >> 1)) * 1024;
#pragma unroll
        for (int r = 0; r < 2; ++r) {
          st16_raw(y2[r], rh, ct == 15 ? OOB : vo, so + (r * 54 + 2 + 4 * par) * 1024);
          st8(y1[r], rh, v1, so + (r * 54 + 3) * 1024);
        }
        st_guard(y2[0], y2[1]);
      }
      ResTile rt;
      u64 fl2;
      const rsrc_t rr = make_rsrc(ws + (L ? WS_X3 : WS_X1), SZ_X);
      sb();
      bar();
      f32x4 acc[4][5];
      const unsigned rbe = lb + w * 20480 + lane * 16, rbl = lb + G4_LATE + w * 15360 + lane * 16;
#pragma unroll
      for (int f = 0; f < 5; ++f) b[0][f] = lds_rd(rbe + f * 1024);
      sb();
      sfor<0, 14>([&](auto T) {
        constexpr int t = T.v;
        constexpr int ta = t < 5 ? (t == 0 ? 2 : t + 3) : (t == 5 ? -1 : (t == 6 ? 8 : (t + 3 < 14 ? t + 3 : -1)));
        if constexpr (t == 3 || t == 6 || t == 10) { wait_lgkm0(); bar(); }
        if constexpr (t == 6) { if (!__all(fl2 == epoch)) poll0(ws, f3 + 16 * 64, 8, epoch, lane); }
        sfor<0, 20>([&](auto M) {
          constexpr int m = M.v, i = m / 5, f = m % 5;
          if constexpr (t == 0) mma0(acc[i][f], b[0][f], a[0][i]); else mma(acc[i][f], b[t & 1][f], a[t][i]);
          if constexpr (t + 1 < 14 && (m & 1) == 1 && m < 10)
            b[(t + 1) & 1][m >> 1] = lds_rd((g4_late(t + 1) ? rbl : rbe) + g4_slot(t + 1) + (m >> 1) * 1024);
          if constexpr (ta >= 0 && (m & 1) == 0 && m < 8) LDH(ta, m >> 1);
          if constexpr (t == 6 && (m & 1) == 0 && m >= 8 && m < 16) LDH(9, (m - 8) >> 1);
          if constexpr (t == 0 && (m & 1) == 0 && m >= 8 && m < 16) LDH(3, (m - 8) >> 1);
          if constexpr (t == 2 && m == 10) rt = load_res(rr, ro);
          if constexpr (t == 4 && m == 18) fl2 = __hip_atomic_load(flag_ptr(ws, f3 + 16 * 64), RLX_AGENT);
          if constexpr (t == 13 && m >= 8) {
            asm volatile("" : "+v"(acc[(m - 8) / 5][(m - 8) % 5]));
            lds_wrf(rbl + (m - 8) * 1024, acc[(m - 8) / 5][(m - 8) % 5]);
          }
          sb();
        });
        if constexpr (t == 2) {
          wait_vm<19>();
          raise(ws, FL_3 + (L * 32 + 16 + (xcd >> 1) * 4 + (idx >> 3)) * 64 + w * 16 + (xcd & 1) * 8 + (idx & 7), epoch, lane);
          ST(sbase + 12);
        }
        if constexpr (t == 0) ST(sbase + 14);
        if constexpr (t == 3) ST(45 + L * 4);
        if constexpr (t == 6) ST(46 + L * 4);
      });
      ST(sbase + 15);
#pragma unroll
      for (int f = 2; f < 5; ++f) { asm volatile("" : "+v"(acc[2][f])); lds_wrf(rbl + (10 + f) * 1024, acc[2][f]); }
      f32x4 (&v)[5] = acc[3];
      fence_acc(v);
      wait_lgkm0();
      bar();
#pragma unroll
      for (int s = 1; s < 4; ++s) {
        const unsigned src = lb + G4_LATE + ((w + s) & 3) * 15360 + (3 - s) * 5 * 1024 + lane * 16;
#pragma unroll
        for (int f = 0; f < 5; ++f) v[f] += lds_rdf(src + f * 1024);
      }
      finish_tile(v, rt, L ? make_rsrc(opq_p(out_), SZ_X) : make_rsrc(ws + WS_X2, SZ_X), oo, L == 0, make_rsrc(ws + WS_ST2, SZ_ST),
                  q == 0 ? (c8 * S_ + row) * 8 : OOB);
      ST(sbase + 16);
      if (L == 0) publish(ws, FL_4 + (rt64 >> 1) * 64 + ((rt64 & 1) * 4 + w) * 8 + c8, epoch, lane);
    }
  }
  asm volatile(".Lcw_end:");
  if (blockIdx.x == 0 && w0 == 0) raise(ws_, FL_E, epoch, tid0);
  ST(37);
  if (blockIdx.x == SEGBLK && w0 == 0 && SEGA != SEGB) {
    unsigned t5 = (unsigned)__builtin_amdgcn_s_memrealtime();
    unsigned d = (tm_b - tm_a) * 8;
    if (d > 100000) d = 100000;
    while ((unsigned)__builtin_amdgcn_s_memrealtime() - t5 < d) __builtin_amdgcn_s_sleep(1);
  }
}

extern "C" void kernel_launch(void* const* d_in, const int* in_sizes, int n_in, void* d_out, int out_size, void* d_ws,
                              size_t ws_size, hipStream_t stream) {
  char* ws = (char*)d_ws;
  (void)hipFuncSetAttribute((const void*)k_all, hipFuncAttributeMaxDynamicSharedMemorySize, KM_LDS);
  k_all<<<256, 512, KM_LDS, stream>>>((const u16*)d_in[0], (const u16*)d_in[1], (const u16*)d_in[2], (const u16*)d_in[3],
                                     (const u16*)d_in[4], (const u16*)d_in[6], (const u16*)d_in[7], ws, (u16*)d_out STAMP_PASS(0));
}
```

```cpp
#include <hip/hip_runtime.h>
#include <hip/hip_bf16.h>
#include <stdint.h>

#define DI __device__ __forceinline__
typedef unsigned short u16;
typedef __attribute__((ext_vector_type(8))) __bf16 bf16x8;
typedef __attribute__((ext_vector_type(2))) __bf16 bf16x2;
typedef __attribute__((ext_vector_type(4))) float f32x4;
typedef __attribute__((ext_vector_type(2))) float f32x2;
typedef __attribute__((ext_vector_type(4))) unsigned u32x4;
typedef __attribute__((ext_vector_type(2))) unsigned u32x2;
typedef __attribute__((ext_vector_type(4))) short s16x4;
#define LDS_AS __attribute__((address_space(3)))
#define GLB_AS __attribute__((address_space(1)))
typedef LDS_AS char* ldsp;
typedef unsigned long long u64;
typedef GLB_AS u64 gu64;
typedef __amdgpu_buffer_rsrc_t rsrc_t;
#define RLX_AGENT __ATOMIC_RELAXED, __HIP_MEMORY_SCOPE_AGENT

constexpr int S_ = 2048, D_ = 640, F_ = 1728;
constexpr float EPS = 1e-5f;
constexpr unsigned OOB = 0x7f000000u;

constexpr unsigned SZ_AB = 8 * 128 * 5 * 1024;
constexpr unsigned SZ_X = 128 * 20 * 1024;
constexpr unsigned SZ_H = 128 * 54 * 1024;
constexpr unsigned SZ_W1 = 16 * 140 * 1024;
constexpr unsigned SZ_W2 = 8 * 54 * 5 * 1024;
constexpr unsigned SZ_ST = 2048 * 64;
constexpr unsigned WS_AB1 = 0;
constexpr unsigned WS_AB2 = WS_AB1 + SZ_AB;
constexpr unsigned WS_X1 = WS_AB2 + SZ_AB;
constexpr unsigned WS_X2 = WS_X1 + SZ_X;
constexpr unsigned WS_X3 = WS_X2 + SZ_X;
constexpr unsigned WS_H1 = WS_X3 + SZ_X;
constexpr unsigned WS_H2 = WS_H1 + SZ_H;
constexpr unsigned WS_W1 = WS_H2 + SZ_H;
constexpr unsigned WS_W2 = WS_W1 + SZ_W1;
constexpr unsigned WS_ST1 = WS_W2 + SZ_W2;
constexpr unsigned WS_ST2 = WS_ST1 + SZ_ST;
constexpr unsigned WS_ST3 = WS_ST2 + SZ_ST;
constexpr unsigned WS_CS1 = WS_ST3 + SZ_ST;
constexpr unsigned WS_CSD = WS_CS1 + 20 * 1728 * 4;
constexpr unsigned XSLOT = 2560;
constexpr unsigned SZ_XP = 256 * 12 * XSLOT;
constexpr unsigned WS_XP = (WS_CSD + 256 * 320 + 4095) & ~4095u;
constexpr unsigned WS_FLG = WS_XP + 2 * SZ_XP;
constexpr unsigned FL_X = 0;
constexpr unsigned FL_2 = FL_X + 2048;
constexpr unsigned FL_3 = FL_2 + 2048;
constexpr unsigned FL_4 = FL_3 + 4096;
constexpr unsigned FL_5 = FL_4 + 1024;
constexpr unsigned FL_W = FL_5 + 3072;
constexpr unsigned FL_E = FL_W + 1088;
constexpr unsigned WS_END = WS_FLG + (FL_E + 1) * 128;

DI u32x4 make_srd(const void* p, unsigned bytes) {
  uint64_t a = (uint64_t)p;
  u32x4 r;
  r[0] = (unsigned)a; r[1] = (unsigned)(a >> 32) & 0xffffu; r[2] = bytes; r[3] = 0x00020000u;
  return r;
}
DI rsrc_t make_rsrc(const void* p, unsigned bytes) {
  return __builtin_amdgcn_make_buffer_rsrc((void*)p, (short)0, (int)bytes, 0x00020000);
}
template <int LOFF, int GOFF>
DI void glds(u32x4 srd, unsigned voff, unsigned ldsb, unsigned soff) {
  unsigned tmp;
  asm volatile("s_add_u32 m0, %1, %2\n\ts_add_u32 %0, %3, %4\n\tbuffer_load_dwordx4 %5, %6, %0 offen lds"
               : "=&s"(tmp)
               : "s"(ldsb), "i"(LOFF), "s"(soff), "i"(GOFF), "v"(voff), "s"(srd)
               : "memory", "scc");
}
template <int N> DI void wait_vm() { asm volatile("s_waitcnt vmcnt(%0)" ::"i"(N) : "memory"); }
DI void wait_lgkm0() { asm volatile("s_waitcnt lgkmcnt(0)" ::: "memory"); }
DI void sb() { __builtin_amdgcn_sched_barrier(0); }
DI void bar() { asm volatile("s_barrier" ::: "memory"); }

DI bf16x8 lds_rd(unsigned addr) { return *(const LDS_AS bf16x8*)(ldsp)(uintptr_t)addr; }
DI f32x4 lds_rdf(unsigned addr) { return *(const LDS_AS f32x4*)(ldsp)(uintptr_t)addr; }
DI void lds_wrf(unsigned addr, f32x4 v) { *(LDS_AS f32x4*)(ldsp)(uintptr_t)addr = v; }
DI float lds_rd1(unsigned addr) { return *(const LDS_AS float*)(ldsp)(uintptr_t)addr; }
DI void lds_wr1(unsigned addr, float v) { *(LDS_AS float*)(ldsp)(uintptr_t)addr = v; }
DI void mma(f32x4& c, const bf16x8& a, const bf16x8& b) {
  asm volatile("v_mfma_f32_16x16x32_bf16 %0, %1, %2, %0" : "+v"(c) : "v"(a), "v"(b));
}
DI void mma0(f32x4& c, const bf16x8& a, const bf16x8& b) {
  asm volatile("v_mfma_f32_16x16x32_bf16 %0, %1, %2, 0" : "=&v"(c) : "v"(a), "v"(b));
}
DI void fence_acc(f32x4 (&a)[5]) { asm volatile("s_nop 7\n\ts_nop 1" : "+v"(a[0]), "+v"(a[1]), "+v"(a[2]), "+v"(a[3]), "+v"(a[4])); }
DI void pin_acc(f32x4 (&a)[5]) { asm volatile("" : "+v"(a[0]), "+v"(a[1]), "+v"(a[2]), "+v"(a[3]), "+v"(a[4])); }
template <int M> DI void fence_all(f32x4 (&a)[M][5]) {
#pragma unroll
  for (int i = 1; i < M; ++i) pin_acc(a[i]);
  fence_acc(a[0]);
#pragma unroll
  for (int i = 1; i < M; ++i) pin_acc(a[i]);
}
DI unsigned pk2(float a, float b) {
  f32x2 v = {a, b};
  return __builtin_bit_cast(unsigned, __builtin_convertvector(v, bf16x2));
}
DI float bflo(unsigned u) { return __uint_as_float(u << 16); }
DI float bfhi(unsigned u) { return __uint_as_float(u & 0xffff0000u); }
DI float dot2(unsigned a, unsigned b, float c) {
  return __builtin_amdgcn_fdot2_f32_bf16(__builtin_bit_cast(bf16x2, a), __builtin_bit_cast(bf16x2, b), c, false);
}
DI float xsum16_32(float x) {
  auto s = __builtin_amdgcn_permlane16_swap(__float_as_uint(x), __float_as_uint(x), false, false);
  x = __uint_as_float(s[0]) + __uint_as_float(s[1]);
  auto t = __builtin_amdgcn_permlane32_swap(__float_as_uint(x), __float_as_uint(x), false, false);
  return __uint_as_float(t[0]) + __uint_as_float(t[1]);
}
template <int V> struct IC { static constexpr int v = V; };
template <int I, int N, class Fn> DI void sfor(Fn&& f) {
  if constexpr (I < N) { f(IC<I>{}); sfor<I + 1, N>(f); }
}
DI unsigned rfl(unsigned v) { return __builtin_amdgcn_readfirstlane(v); }
DI unsigned opq_v(unsigned x) { asm volatile("" : "+v"(x)); return x; }
DI unsigned opq_s(unsigned x) { asm volatile("" : "+s"(x)); return x; }
template <class T> DI T* opq_p(T* x) { asm volatile("" : "+s"(x)); return x; }
DI bf16x8 ld16(rsrc_t r, unsigned voff, unsigned soff) { return __builtin_bit_cast(bf16x8, __builtin_amdgcn_raw_buffer_load_b128(r, voff, soff, 16)); }
DI u32x4 ld16u(rsrc_t r, unsigned voff, unsigned soff) { return __builtin_amdgcn_raw_buffer_load_b128(r, voff, soff, 16); }
DI u32x2 ld8u(rsrc_t r, unsigned voff, unsigned soff) { return __builtin_amdgcn_raw_buffer_load_b64(r, voff, soff, 16); }
DI void st16(u32x4 v, rsrc_t r, unsigned voff, unsigned soff) {
  __builtin_amdgcn_raw_buffer_store_b128(v, r, voff, soff, 16);
  asm volatile("s_nop 1" :: "v"(v) : "memory");
}
DI void st16_raw(u32x4 v, rsrc_t r, unsigned voff, unsigned soff) { __builtin_amdgcn_raw_buffer_store_b128(v, r, voff, soff, 16); }
template <class... T> DI void st_guard(const T&... v) {
  (..., [](const auto& x) { asm volatile("" :: "v"(x) : "memory"); }(v));
  asm volatile("s_nop 1" ::: "memory");
  (..., [](const auto& x) { asm volatile("" :: "v"(x)); }(v));
}
DI void st8(u32x2 v, rsrc_t r, unsigned voff, unsigned soff) { __builtin_amdgcn_raw_buffer_store_b64(v, r, voff, soff, 16); }
DI void st4(unsigned v, rsrc_t r, unsigned voff, unsigned soff) { __builtin_amdgcn_raw_buffer_store_b32(v, r, voff, soff, 16); }

template <int KS> DI unsigned foff(unsigned row, unsigned col) {
  return ((row >> 4) * KS + (col >> 5)) * 1024u + (((col >> 3) & 3) * 16u + (row & 15)) * 16u + (col & 7) * 2u;
}

extern __shared__ __attribute__((aligned(1024))) char smem[];
DI unsigned lds_base() { return (unsigned)(uintptr_t)(ldsp)smem; }

DI void ln_coef(float s1, float s2, float& mu, float& rs) {
  mu = s1 * (1.f / D_);
  rs = __builtin_amdgcn_rsqf(fmaxf(s2 * (1.f / D_) - mu * mu, 0.f) + EPS);
}

DI void g1_store(const f32x4 (&acc)[2][5], const float (&mu)[2][4], const float (&rs)[2][4], const float (&csv)[5], rsrc_t rab,
                 unsigned P, unsigned ct, unsigned w, unsigned i16, unsigned q) {
  const unsigned kstep = (ct >> 3) * 64 + P * 4 + w;
  const unsigned cq = ((q & 1) << 1) | (q >> 1);
  const unsigned so = ((ct & 7) * 128 + kstep) * 5 * 1024;
  const unsigned vo = (cq * 16 + i16) * 16;
  u32x4 y[5];
#pragma unroll
  for (int f = 0; f < 5; ++f) {
    const f32x2 cs = {csv[f], csv[f]};
    unsigned pk[2][2];
#pragma unroll
    for (int r = 0; r < 2; ++r)
#pragma unroll
      for (int e = 0; e < 2; ++e) {
        const f32x2 t = __builtin_elementwise_fma(f32x2{mu[r][2 * e], mu[r][2 * e + 1]}, cs, f32x2{acc[r][f][2 * e], acc[r][f][2 * e + 1]}) *
                        f32x2{rs[r][2 * e], rs[r][2 * e + 1]};
        pk[r][e] = __builtin_bit_cast(unsigned, __builtin_convertvector(t, bf16x2));
      }
    auto t0 = __builtin_amdgcn_permlane16_swap(pk[0][0], pk[1][0], false, false);
    auto t1 = __builtin_amdgcn_permlane16_swap(pk[0][1], pk[1][1], false, false);
    y[f] = u32x4{t0[0], t1[0], t0[1], t1[1]};
    st16_raw(y[f], rab, vo + (f & 3) * 1024, so + (f >> 2) * 4096);
  }
  st_guard(y[0], y[1], y[2], y[3], y[4]);
}

constexpr int K1_STAGE = 26 * 1024;
constexpr int K1_TR = 5 * K1_STAGE;
constexpr int K1_CS = K1_TR + 5 * 4096;

DI void k1_issue(u32x4 sa, u32x4 sbd, unsigned vo, unsigned la, unsigned lbb, unsigned soa, unsigned sob, unsigned w) {
  glds<0, 0>(sa, vo, la, soa);
  glds<1024, 8 * 1280>(sa, vo, la, soa);
  glds<2048, 16 * 1280>(sa, vo, la, soa);
  glds<3072, 24 * 1280>(sa, vo, la, soa);
  glds<0, 0>(sbd, vo, lbb, sob);
  glds<4096, 32 * 1280>(sbd, vo, lbb, sob);
  if (w < 2) glds<8192, 64 * 1280>(sbd, vo, lbb, sob);
}
struct RtUnit { bool first; unsigned ks, g; };
DI RtUnit rt_unit(unsigned u) {
  RtUnit r;
  r.first = u < 540;
  if (r.first) { r.ks = u / 27u; r.g = u - r.ks * 27u; }
  else { const unsigned v = u - 540; r.ks = v / 10u; r.g = v - r.ks * 10u; }
  return r;
}
DI void retile_issue(unsigned u, const u16* w1, const u16* w2, unsigned ldst, unsigned lane) {
  const RtUnit t = rt_unit(u);
  const unsigned ld = t.first ? F_ * 2 : D_ * 2;
  u32x4 srd = make_srd(t.first ? w1 : w2, D_ * F_ * 2);
  const unsigned vo = (lane >> 3) * ld + (lane & 7) * 16;
  const unsigned so = t.ks * 32 * ld + t.g * 128;
  glds<0, 0>(srd, vo, ldst, so);
  glds<1024, 0>(srd, vo, ldst, so + 8 * ld);
  glds<2048, 0>(srd, vo, ldst, so + 16 * ld);
  glds<3072, 0>(srd, vo, ldst, so + 24 * ld);
}
DI unsigned w1_off(unsigned rt, unsigned ks) {
  const unsigned c = rt / 7u, f = rt - c * 7, g = (c & 1) ? (f == 0 ? 6 : f - 1) : f;
  return WS_W1 + (c * 140 + (g < 4 ? ks * 4 + g : 80 + ks * 3 + g - 4)) * 1024;
}
DI void retile_finish(unsigned u, unsigned f0, rsrc_t rws, unsigned ldst, unsigned lane) {
  const RtUnit t = rt_unit(u);
  const unsigned i = lane & 15, q = lane >> 4;
  const unsigned base = ldst + (8 * q + (i >> 2)) * 128 + (i & 3) * 8 + f0 * 32;
  u32x4 v[2];
#pragma unroll
  for (int f = 0; f < 2; ++f) {
    s16x4 lo = __builtin_amdgcn_ds_read_tr16_b64_v4i16((LDS_AS s16x4*)(uintptr_t)(base + f * 32));
    s16x4 hi = __builtin_amdgcn_ds_read_tr16_b64_v4i16((LDS_AS s16x4*)(uintptr_t)(base + f * 32 + 512));
    u32x2 l2 = __builtin_bit_cast(u32x2, lo), h2 = __builtin_bit_cast(u32x2, hi);
    v[f] = u32x4{l2[0], l2[1], h2[0], h2[1]};
  }
  const unsigned rt = t.g * 4 + f0;
  if (t.first) {
    st16(v[0], rws, lane * 16, w1_off(rt, t.ks));
    st16(v[1], rws, lane * 16, w1_off(rt + 1, t.ks));
    float s[2];
#pragma unroll
    for (int f = 0; f < 2; ++f)
      s[f] = xsum16_32(((bflo(v[f][0]) + bfhi(v[f][0])) + (bflo(v[f][1]) + bfhi(v[f][1]))) + ((bflo(v[f][2]) + bfhi(v[f][2])) + (bflo(v[f][3]) + bfhi(v[f][3]))));
    st4(__float_as_uint(q == 0 ? s[0] : s[1]), rws, lane < 32 ? lane * 4 : OOB, WS_CS1 + (t.ks * F_ + rt * 16) * 4);
  } else {
    const unsigned c0 = rt / 5u, c1 = (rt + 1) / 5u;
    st16(v[0], rws, lane * 16, WS_W2 + ((c0 * 54 + t.ks) * 5 + (rt - c0 * 5)) * 1024);
    st16(v[1], rws, lane * 16, WS_W2 + ((c1 * 54 + t.ks) * 5 + (rt + 1 - c1 * 5)) * 1024);
  }
}

constexpr int WS1 = 3, WS2 = 6;
constexpr int PUB1 = 13;
constexpr int PUBG = 11;
constexpr int KP3 = 3, KP1 = 3;
constexpr int RGAP = 10, ASLEEP2 = 40, WDPRE = 6;
constexpr int PD4 = 13, PDX = 15, PD2 = 30, P5DELAY0 = 4, P5DELAY = 1;

constexpr int KM_LDS = 160 * 1024;
constexpr int KM_CS1 = KM_LDS - 1024;
constexpr int G2_A = 80 * 1024, G2_AS = 16 * 1024, G2_BS = 20 * 1024;
constexpr int G4_LATE = 80 * 1024;

DI gu64* flag_ptr(char* ws, unsigned f) { return (gu64*)(ws + WS_FLG + f * 128); }
DI void raise(char* ws, unsigned f, u64 epoch, unsigned lane) {
  if (lane < 16) __hip_atomic_store(flag_ptr(ws, f) + lane, epoch, RLX_AGENT);
}
DI void publish(char* ws, unsigned f, u64 epoch, unsigned lane) {
  wait_vm<0>();
  raise(ws, f, epoch, lane);
}
DI void poll0(char* ws, unsigned f, unsigned n, u64 epoch, unsigned lane) {
  gu64* p = flag_ptr(ws, f);
  bool ok = lane >= n;
#pragma unroll 1
  for (unsigned s = 0; s < (1u << 16); ++s) {
    if (!ok) ok = __hip_atomic_load(p, RLX_AGENT) == epoch;
    if (__all(ok)) break;
    __builtin_amdgcn_s_sleep(1);
  }
  asm volatile("" ::: "memory");
}
template <int DELAY> DI void poll(char* ws, unsigned f, unsigned n, u64 epoch, unsigned lane) {
  __builtin_amdgcn_s_sleep(DELAY);
  poll0(ws, f, n, epoch, lane);
}
DI void warm(unsigned ldump, u32x4 srd, unsigned voff, unsigned soff) {
  asm volatile("s_mov_b32 m0, %0\n\ts_nop 0\n\tbuffer_load_dword %1, %2, %3 offen lds" :: "s"(ldump), "v"(voff), "s"(srd), "s"(soff) : "memory");
}
DI void glds4_sc1(u32x4 srd, unsigned voff, unsigned l, unsigned so) {
  asm volatile("s_mov_b32 m0, %0\n\ts_nop 0\n\tbuffer_load_dwordx4 %1, %2, %3 offen sc1 lds\n\tbuffer_load_dwordx4 %1, %2, %3 offen offset:1024 sc1 lds\n\t"
               "buffer_load_dwordx4 %1, %2, %3 offen offset:2048 sc1 lds\n\tbuffer_load_dwordx4 %1, %2, %3 offen offset:3072 sc1 lds"
               :: "s"(l), "v"(voff), "s"(srd), "s"(so) : "memory");
}
DI void glds1_sc1(u32x4 srd, unsigned voff, unsigned l, unsigned so) {
  asm volatile("s_mov_b32 m0, %0\n\ts_nop 0\n\tbuffer_load_dwordx4 %1, %2, %3 offen sc1 lds" :: "s"(l), "v"(voff), "s"(srd), "s"(so) : "memory");
}
template <int N> DI void glds_run_sc1(u32x4 srd, unsigned voff, unsigned l, unsigned so) {
#pragma unroll
  for (int g = 0; g < N / 4; ++g) glds4_sc1(srd, voff, l + g * 4096, so + g * 4096);
#pragma unroll
  for (int g = N / 4 * 4; g < N; ++g) glds1_sc1(srd, voff, l + g * 1024, so + g * 1024);
}

struct ResTile { u32x4 r0, r1; u32x2 r2; };
struct TileOff { unsigned a0, a1, a2; };
DI TileOff tile_offsets(bool rm, unsigned row, unsigned ct, unsigned q) {
  const unsigned cA = ct * 80 + 16 * (q & 1) + 8 * (q >> 1), cS = ct * 80 + 64 + 4 * q;
  TileOff t;
  if (rm) { t.a0 = (row * D_ + cA) * 2; t.a1 = t.a0 + 64; t.a2 = (row * D_ + cS) * 2; }
  else { t.a0 = foff<20>(row, cA); t.a1 = foff<20>(row, cA + 32); t.a2 = foff<20>(row, cS); }
  asm volatile("" : "+v"(t.a0), "+v"(t.a1), "+v"(t.a2));
  return t;
}
DI ResTile load_res(rsrc_t rr, const TileOff& t) {
  ResTile rt;
  rt.r0 = ld16u(rr, t.a0, 0); rt.r1 = ld16u(rr, t.a1, 0); rt.r2 = ld8u(rr, t.a2, 0);
  return rt;
}
DI void finish_tile(f32x4 (&v)[5], const ResTile& rt, rsrc_t rout, const TileOff& o, bool stats, rsrc_t rst, unsigned sto) {
#pragma unroll
  for (int p = 0; p < 2; ++p)
#pragma unroll
    for (int r = 0; r < 4; ++r) {
      auto t = __builtin_amdgcn_permlane16_swap(__float_as_uint(v[2 * p][r]), __float_as_uint(v[2 * p + 1][r]), false, false);
      v[2 * p][r] = __uint_as_float(t[0]); v[2 * p + 1][r] = __uint_as_float(t[1]);
    }
  u32x4 y0, y1; u32x2 y2;
#pragma unroll
  for (int j = 0; j < 2; ++j) {
    y0[j] = pk2(v[0][2 * j] + bflo(rt.r0[j]), v[0][2 * j + 1] + bfhi(rt.r0[j]));
    y0[2 + j] = pk2(v[1][2 * j] + bflo(rt.r0[2 + j]), v[1][2 * j + 1] + bfhi(rt.r0[2 + j]));
    y1[j] = pk2(v[2][2 * j] + bflo(rt.r1[j]), v[2][2 * j + 1] + bfhi(rt.r1[j]));
    y1[2 + j] = pk2(v[3][2 * j] + bflo(rt.r1[2 + j]), v[3][2 * j + 1] + bfhi(rt.r1[2 + j]));
    y2[j] = pk2(v[4][2 * j] + bflo(rt.r2[j]), v[4][2 * j + 1] + bfhi(rt.r2[j]));
  }
  if (stats) {
    st16_raw(y0, rout, o.a0, 0); st16_raw(y1, rout, o.a1, 0); st8(y2, rout, o.a2, 0);
  } else {
    __builtin_amdgcn_raw_buffer_store_b128(y0, rout, o.a0, 0, 0);
    __builtin_amdgcn_raw_buffer_store_b128(y1, rout, o.a1, 0, 0);
    __builtin_amdgcn_raw_buffer_store_b64(y2, rout, o.a2, 0, 0);
  }
  st_guard(y0, y1);
  if (stats) {
    float s1a = 0.f, s1b = 0.f, s2a = 0.f, s2b = 0.f;
#pragma unroll
    for (int j = 0; j < 4; ++j) {
      s1a = dot2(y0[j], 0x3f803f80u, s1a); s2a = dot2(y0[j], y0[j], s2a);
      s1b = dot2(y1[j], 0x3f803f80u, s1b); s2b = dot2(y1[j], y1[j], s2b);
    }
    s1a = dot2(y2[0], 0x3f803f80u, s1a); s2a = dot2(y2[0], y2[0], s2a);
    s1b = dot2(y2[1], 0x3f803f80u, s1b); s2b = dot2(y2[1], y2[1], s2b);
    const float s1 = xsum16_32(s1a + s1b), s2 = xsum16_32(s2a + s2b);
    st8(u32x2{__float_as_uint(s1), __float_as_uint(s2)}, rst, sto, 0);
  }
}

DI constexpr unsigned wd_pos(int c) { return c < 8 ? 10 * c : 60 + 10 * c; }
constexpr int ORD[14] = {0, 1, 4, 5, 7, 8, 11, 12, 2, 3, 6, 9, 10, 13};
DI constexpr int g4_late(int t) { return (t >= 4 && t < 7) || t >= 11; }
DI constexpr int g4_slot(int t) { return (t < 4 ? t : (t < 7 ? t - 4 : (t < 11 ? t - 7 : t - 11))) * 5120; }

#define PHASE_IDS \
  const unsigned lane = opq_v(tid0) & 63, w = opq_s(w0), bid = opq_s(blockIdx.x); \
  const unsigned xcd = bid & 7, idx = bid >> 3, i16 = lane & 15, q = lane >> 4; \
  char* const ws = opq_p(ws_); \
  (void)xcd; (void)idx; (void)i16; (void)q; (void)ws;
#define IDS_G2 const unsigned kq = xcd & 3, P = (xcd >> 2) * 8 + (idx >> 2), c4 = idx & 3;
#define G2_LA(h) (lb + G2_A + ((w >> 1) * 4 + ((2 * (w & 1) + (h) + 3 - kq) & 3)) * 2048)
#define IDS_G3 const unsigned P = (xcd >> 1) * 4 + (idx >> 3), ct = (xcd & 1) * 8 + (idx & 7);
#define C8_OF ((xcd & 1) * 4 + (idx & 3))
#define IDS_G4 const unsigned rt64 = (xcd >> 1) * 8 + (idx >> 2), c8 = C8_OF;
#define LDX(r, k) ld16(rx, lane * 16 + ((k) & 3) * 1024, sox + (r) * 20480 + ((k) >> 2) * 4096)

__global__ __launch_bounds__(512, 2) void k_all(const u16* x0_, const u16* Sre_, const u16* Sim_, const u16* Dre_, const u16* Dim_,
                                               const u16* w1_, const u16* w2_, char* ws_, u16* out_) {
  const unsigned tid0 = threadIdx.x, wall = rfl(tid0 >> 6), w0 = wall & 3;
  const unsigned lb = lds_base();
  const char* code; unsigned code_len;
  asm volatile("s_getpc_b64 %0\n.Lcw_begin:\n\ts_mov_b32 %1, .Lcw_end-.Lcw_begin" : "=s"(code), "=s"(code_len));
  u64 epoch;

  if (__builtin_expect(wall >= 4, 1)) {
    const unsigned sink = lb + K1_CS + 1024;
    unsigned ep0, ep1;
    asm volatile("global_load_dword %0, %2, %3 sc1\n\tglobal_load_dword %1, %2, %3 offset:4 sc1"
                 : "=&v"(ep0), "=&v"(ep1) : "v"(WS_FLG + FL_E * 128), "s"(ws_) : "memory");
    {
      PHASE_IDS
      IDS_G3
      const u32x4 sa = make_srd(opq_p(x0_), S_ * D_ * 2), sbd = make_srd(ct < 8 ? opq_p(Dre_) : opq_p(Dim_), D_ * D_ * 2);
      const unsigned vo = (lane >> 3) * 1280 + (((lane & 7) ^ (lane >> 3)) << 4);
      const unsigned soa = (P * 128 + w * 32) * 1280, sob = ((ct & 7) * 80 + w * 8) * 1280;
      const unsigned la = lb + w * 4096, lbb = lb + 16384 + w * 1024;
      const unsigned u0 = bid * 4 + w, u1 = 1024 + bid;
      const bool extra = w == 3 && bid < 56;
      const unsigned rot = ((idx & 7) + 3 * (idx >> 3)) % 10u;
      auto ko = [&](unsigned s) { const unsigned c = s + rot; return (c >= 10 ? c - 10 : c) * 128; };
      asm volatile("s_nop 4" ::: "memory");
      k1_issue(sa, sbd, vo, la, lbb, soa + ko(0), sob + ko(0), w);
      k1_issue(sa, sbd, vo, la + K1_STAGE, lbb + K1_STAGE, soa + ko(1), sob + ko(1), w);
      if (w == 3) warm(sink, make_srd(code, code_len), lane < 16 ? idx * 2048 + lane * 128 : OOB, 0);
      wait_vm<6>();
      {
        unsigned lo, hi;
        asm volatile("v_readfirstlane_b32 %0, %2\n\tv_readfirstlane_b32 %1, %3\n\ts_nop 4" : "=s"(lo), "=s"(hi) : "v"(ep0), "v"(ep1));
        epoch = (u64)lo + ((u64)hi << 32) + 1;
      }
      bar();
      k1_issue(sa, sbd, vo, la + 2 * K1_STAGE, lbb + 2 * K1_STAGE, soa + ko(2), sob + ko(2), w);
      const u32x4 ss = make_srd((xcd & 2) ? Sim_ : Sre_, S_ * S_ * 2);
      const unsigned vs = (lane & 31) * 4096 + (lane >> 5) * 128;
      const unsigned sos = ((xcd >> 2) * 1024 + idx * 32) * 4096 + (xcd & 1) * 2048 + w * 256;
      unsigned so = 0;
#pragma unroll 1
      for (int i = 0; i < 10; ++i) {
        if (i == 0) wait_vm<6>(); else if (i == 1) wait_vm<2 * 6>(); else if (i < 6) wait_vm<3 * 6>();
        else if (i == 6) wait_vm<2 * 6 + 4>(); else if (i == 7) wait_vm<6 + 4>(); else wait_vm<4 + (WS2 >= 4 && WS2 < 8) + (WS1 >= 4 && WS1 < 8)>();
        bar();
        if (i == 0) {
          k1_issue(sa, sbd, vo, la + 3 * K1_STAGE, lbb + 3 * K1_STAGE, soa + ko(3), sob + ko(3), w);
          k1_issue(sa, sbd, vo, la + 4 * K1_STAGE, lbb + 4 * K1_STAGE, soa + ko(4), sob + ko(4), w);
        } else if (i == 1) {
          k1_issue(sa, sbd, vo, la, lbb, soa + ko(5), sob + ko(5), w);
          k1_issue(sa, sbd, vo, la + K1_STAGE, lbb + K1_STAGE, soa + ko(6), sob + ko(6), w);
        } else if (i < 5) k1_issue(sa, sbd, vo, la + so, lbb + so, soa + ko(i + 5), sob + ko(i + 5), w);
        so = so == 4 * K1_STAGE ? 0 : so + K1_STAGE;
        if (i == WS1) warm(sink, ss, vs, sos);
        if (i == WS2) warm(sink, ss, vs, sos + 1024);
        if (i == 5) {
          retile_issue(u0, w1_, w2_, lb + K1_TR + w * 4096, lane);
          if (extra) retile_issue(u1, w1_, w2_, lb + K1_TR + 4 * 4096, lane);
        }
      }
      bar();
    }
    bf16x8 wr[10][5];
#pragma unroll
    for (int t = 0; t < 10; ++t)
#pragma unroll
      for (int f = 0; f < 5; ++f) wr[t][f] = bf16x8{};
#pragma unroll 1
    for (int L = 0; L < 2; ++L) {
      {
        PHASE_IDS
        IDS_G2
        const u32x4 sa = make_srd(kq < 2 ? opq_p(Sre_) : opq_p(Sim_), S_ * S_ * 2);
        const u32x4 sb_ = make_srd(ws + (L ? WS_AB2 : WS_AB1), SZ_AB);
        const unsigned va = (lane >> 3) * 4096 + (((lane & 7) ^ (lane >> 3)) << 4), vb = lane * 16;
        const unsigned soa = (P * 128 + w * 32) * 4096 + (kq & 1) * 2048;
        const unsigned sob = ((2 * c4 + (w >> 1)) * 128 + kq * 32 + (w & 1)) * 5 * 1024;
        const unsigned la = lb + G2_A + w * 4096, lbb = lb + w * 5120;
        const unsigned la0 = G2_LA(0), la1 = G2_LA(1);
        const unsigned ct = (xcd & 1) * 8 + (idx & 7);
        auto kc = [&](unsigned s) { return L ? 2 * (s & 7) + ((s ^ (s >> 3)) & 1) : s; };
        asm volatile("s_nop 4" ::: "memory");
        auto issue_a = [&](unsigned l, unsigned so) {
          glds<0, 0>(sa, va, la0 + l, so); glds<1024, 8 * 4096>(sa, va, la0 + l, so);
          glds<0, 16 * 4096>(sa, va, la1 + l, so); glds<1024, 24 * 4096>(sa, va, la1 + l, so);
        };
        if (L == 0) {
#pragma unroll
          for (int s = 0; s < 3; ++s) issue_a(s * G2_AS, soa + s * 128);
          const unsigned u0 = bid * 4 + w, u1 = 1024 + bid;
          const rsrc_t rws = make_rsrc(ws, WS_END);
          wait_vm<12>();
          retile_finish(u0, 0, rws, lb + K1_TR + w * 4096, lane);
          retile_finish(u0, 2, rws, lb + K1_TR + w * 4096, lane);
          if (w == 3 && bid < 56) {
            retile_finish(u1, 0, rws, lb + K1_TR + 4 * 4096, lane);
            retile_finish(u1, 2, rws, lb + K1_TR + 4 * 4096, lane);
          }
          __builtin_amdgcn_s_sleep(P5DELAY0);
        } else {
          issue_a(3 * G2_AS, soa + kc(3) * 128);
          __builtin_amdgcn_s_sleep(P5DELAY);
        }
        const unsigned f5 = FL_5 + ((L * 4 + kq) * 4 + c4) * 64 + ((i16 >> 1) * 2 + (w >> 1)) * 4 + (L ? 2 * (w & 1) + (i16 & 1) : (w & 1) + 2 * (i16 & 1));
        poll0(ws, f5, 16, epoch, lane);
        wait_vm<0>();
        glds_run_sc1<5>(sb_, vb, lbb, sob);
        glds_run_sc1<5>(sb_, vb, lbb + G2_BS, sob + kc(1) * 10240);
        if (L == 0) {
          raise(ws, FL_W + bid * 4 + w, epoch, lane);
          if (w == 3 && bid < 56) raise(ws, FL_W + 1024 + bid, epoch, lane);
          wait_vm<6>();
        } else wait_vm<5>();
        bar();
        glds_run_sc1<5>(sb_, vb, lbb + 2 * G2_BS, sob + kc(2) * 10240);
        if (L == 0) issue_a(3 * G2_AS, soa + kc(3) * 128);
        glds_run_sc1<5>(sb_, vb, lbb + 3 * G2_BS, sob + kc(3) * 10240);
        const u32x4 sw = make_srd(ws + WS_W1, SZ_W1);
        const unsigned sow = ct * 140 * 1024;
        unsigned fw, fw0 = 0, fw1 = 0;
        {
          const unsigned n = w * 42 + (lane < 42 ? lane : 0), k3 = n / 3u, g3 = (7 * ct >> 2) + (n - k3 * 3), n2 = n - 60;
          fw = FL_W + (n < 60 ? k3 * 27 + (g3 < 26 ? g3 : 26) : 540 + (n2 >> 1) * 10 + (5 * C8_OF >> 2) + (n2 & 1));
          if (L) fw = f5 + 1024;
        }
#pragma unroll 1
        for (int s = 0; s < 16; ++s) {
          if (s == 0) wait_vm<10>();
          if (s == 1) wait_vm<14>();
          wait_vm<18>();
          if (s == (L ? 4 : 9)) {
            asm volatile("" : "+v"(fw0), "+v"(fw1));
            if (!__all(fw0 == (unsigned)epoch && fw1 == (unsigned)(epoch >> 32))) poll0(ws, fw, 42, epoch, lane);
          }
          bar();
          const unsigned sl = s & 3;
          if (s < 12) {
            issue_a(sl * G2_AS, soa + kc(s + 4) * 128);
            glds_run_sc1<5>(sb_, vb, lbb + sl * G2_BS, sob + kc(s + 4) * 10240);
            if (s == (L ? 1 : 6))
              asm volatile("global_load_dword %0, %2, %3 sc1\n\tglobal_load_dword %1, %2, %3 offset:4 sc1"
                           : "+v"(fw0), "+v"(fw1) : "v"(WS_FLG + fw * 128), "s"(ws) : "memory");
          } else {
            glds_run_sc1<5>(sw, vb, lbb + sl * G2_BS, sow + w * 5120 + sl * G2_BS);
            if (s < 15) glds_run_sc1<4>(sw, vb, la + sl * G2_AS, sow + G2_A + w * 4096 + sl * G2_AS);
            else glds_run_sc1<3>(sw, vb, lb + G2_A + 3 * G2_AS + w * 3072, sow + G2_A + 3 * G2_AS + w * 3072);
          }
        }
      }
      {
        PHASE_IDS
        IDS_G4
        const unsigned ct = (xcd & 1) * 8 + (idx & 7);
        if (w >= 2) {
          const rsrc_t rc = make_rsrc(ws + WS_CS1, 20 * F_ * 4);
          const unsigned c = (w - 2) * 64 + lane, col = ct * 112 + c;
          const unsigned vo = c < 112 && col < F_ ? col * 4 : OOB;
          float csp = 0.f;
#pragma unroll
          for (int k = 0; k < 20; ++k) csp += __uint_as_float(__builtin_amdgcn_raw_buffer_load_b32(rc, vo, k * F_ * 4, 16));
          lds_wr1(lb + KM_CS1 + c * 4, csp);
        }
        const unsigned so = (c8 * 54 + w * 14) * 5 * 1024;
        wait_vm<0>();
        if (L == 0) {
          const rsrc_t rw = make_rsrc(ws + WS_W2, SZ_W2);
          const unsigned vl = lane * 16, vz = lane * 16 + (w < 3 ? 0u : OOB);
          sfor<0, 50>([&](auto N) {
            constexpr int t = 4 + N.v / 5, n = ORD[t] * 5 + N.v % 5;
            wr[t - 4][N.v % 5] = ld16(rw, (ORD[t] < 12 ? vl : vz) + (n & 3) * 1024, so + (n >> 2) * 4096);
            if constexpr (N.v % 5 == 4 && N.v < 49) { sb(); __builtin_amdgcn_s_sleep(RGAP); sb(); }
          });
          sb();
        }
        wait_lgkm0();
        bar();
        bar();
        const u32x4 s2 = make_srd(ws + WS_W2, SZ_W2);
        const unsigned sg = rfl(so);
        asm volatile("s_nop 4" ::: "memory");
        glds_run_sc1<10>(s2, lane * 16, lb + w * 20480, sg);
        glds_run_sc1<10>(s2, lane * 16, lb + w * 20480 + 10240, sg + 4 * 5120);
        wait_vm<0>();
        const unsigned pe = lb + w * 20480 + lane * 16, pl = lb + G4_LATE + w * 15360 + lane * 16;
        auto put = [&](auto T0, auto T1) {
          sfor<T0.v * 5, T1.v * 5>([&](auto N) {
            constexpr int t = N.v / 5, f = N.v % 5;
            *(LDS_AS bf16x8*)(ldsp)(uintptr_t)((g4_late(t) ? pl : pe) + g4_slot(t) + f * 1024) = wr[t - 4][f];
          });
          wait_lgkm0();
        };
        bar();
        put(IC<4>{}, IC<7>{});
        bar();
        put(IC<7>{}, IC<11>{});
        bar();
        put(IC<11>{}, IC<14>{});
        bar();
      }
      if (L) { bar(); break; }
      {
        PHASE_IDS
        IDS_G3
        const u32x4 sd = make_srd(ct < 8 ? opq_p(Dre_) : opq_p(Dim_), D_ * D_ * 2);
        const unsigned vo = (lane >> 3) * 1280 + (((lane & 7) ^ (lane >> 3)) << 4);
        const unsigned so = (ct & 7) * 80 * 1280;
        sfor<0, 10>([&](auto G) { glds<G.v * 1024, G.v * 8 * 1280>(sd, vo, lb + 20 * w * 1024, so + 2 * w * 128); });
        sfor<0, WDPRE>([&](auto G) { glds<G.v * 1024, G.v * 8 * 1280>(sd, vo, lb + (20 * w + 10) * 1024, so + (2 * w + 1) * 128); });
        bar();
        sfor<WDPRE, 10>([&](auto G) { glds<G.v * 1024, G.v * 8 * 1280>(sd, vo, lb + (20 * w + 10) * 1024, so + (2 * w + 1) * 128); });
        sfor<0, 5>([&](auto G) { glds<G.v * 1024, G.v * 8 * 1280>(sd, vo, lb + (140 + w * 5) * 1024, so + (8 + (w >> 1)) * 128 + (w & 1) * 40 * 1280); });
        wait_vm<0>();
        bar();
      }
      {
        PHASE_IDS
        IDS_G2
        const u32x4 sa = make_srd(kq < 2 ? opq_p(Sre_) : opq_p(Sim_), S_ * S_ * 2);
        const unsigned va = (lane >> 3) * 4096 + (((lane & 7) ^ (lane >> 3)) << 4);
        const unsigned soa = (P * 128 + w * 32) * 4096 + (kq & 1) * 2048;
        __builtin_amdgcn_s_sleep(ASLEEP2);
#pragma unroll
        for (int s = 0; s < 3; ++s) {
          const unsigned l0 = G2_LA(0) + s * G2_AS, l1 = G2_LA(1) + s * G2_AS, so = soa + (2 * s + (s & 1)) * 128;
          glds<0, 0>(sa, va, l0, so); glds<1024, 8 * 4096>(sa, va, l0, so);
          glds<0, 16 * 4096>(sa, va, l1, so); glds<1024, 24 * 4096>(sa, va, l1, so);
        }
        bar();
      }
    }
    return;
  }

  {
    const u64 e = __hip_atomic_load(flag_ptr(ws_, FL_E), RLX_AGENT);
    epoch = (u64)rfl((unsigned)e) + ((u64)rfl((unsigned)(e >> 32)) << 32) + 1;
  }
#pragma unroll 1
  for (int L = 0; L < 2; ++L) {
    {
      PHASE_IDS
      IDS_G3
      f32x4 acc[2][5];
      float mu[2][4], rs[2][4], csv[5];
      const float sign = ct < 8 ? 1.f : -1.f;
      const unsigned f5 = FL_5 + (((ct >> 3) * 2 + (P >> 3)) * 4 + ((ct & 7) >> 1)) * 64 + ((P & 7) * 2 + (ct & 1)) * 4 + w;
      if (L == 0) {
        const unsigned ra0 = lb + w * 4096 + i16 * 128 + ((q ^ (lane & 7)) << 4);
        const unsigned rb0 = lb + 16384 + i16 * 128 + ((q ^ (lane & 7)) << 4);
        const f32x4 z4 = {0.f, 0.f, 0.f, 0.f};
        f32x4 cs0 = z4, cs1 = z4, rs_[2] = {z4, z4}, rq_[2] = {z4, z4};
#pragma unroll
        for (int r = 0; r < 2; ++r)
#pragma unroll
          for (int f = 0; f < 5; ++f) acc[r][f] = z4;
        bf16x8 fr[2][8];
        bf16x8 one = __builtin_bit_cast(bf16x8, u32x4{0x3f803f80u, 0x3f803f80u, 0x3f803f80u, 0x3f803f80u});
        auto half = [&](auto KK, unsigned so) {
          constexpr int kk = KK.v;
          const unsigned pa = (ra0 ^ ((kk ^ 1) * 64)) + so, pb = (rb0 ^ ((kk ^ 1) * 64)) + so;
          sfor<0, 16>([&](auto M) {
            constexpr int m = M.v;
            if constexpr (m < 10) mma(acc[m & 1][m >> 1], fr[kk][m & 1], fr[kk][2 + (m >> 1)]);
            else if constexpr (m == 10) mma(cs0, one, fr[kk][7]);
            else if constexpr (m == 11) mma(cs1, one, fr[kk][6]);
            else if constexpr (m < 14) mma(rs_[m - 12], fr[kk][m - 12], one);
            else mma(rq_[m - 14], fr[kk][m - 14], fr[kk][m - 14]);
            constexpr int n = m >> 1;
            if constexpr ((m & 1) == 0) fr[kk ^ 1][n] = n < 2 ? lds_rd(pa + n * 2048) : (n < 7 ? lds_rd(pb + (n - 2) * 2048) : lds_rd(pb + w * 2048));
            sb();
          });
        };
        asm volatile("s_nop 1" : "+v"(cs0), "+v"(cs1), "+v"(rs_[0]), "+v"(rs_[1]), "+v"(rq_[0]), "+v"(rq_[1]), "+v"(one));
        fence_all(acc);
        bar();
        fr[0][0] = lds_rd(ra0); fr[0][1] = lds_rd(ra0 + 2048);
#pragma unroll
        for (int f = 0; f < 5; ++f) fr[0][2 + f] = lds_rd(rb0 + f * 2048);
        fr[0][7] = lds_rd(rb0 + w * 2048);
        sb();
        unsigned so = 0;
#pragma unroll 1
        for (int i = 0; i < 10; ++i) {
          half(IC<0>{}, so);
          wait_lgkm0();
          bar();
          so = so == 4 * K1_STAGE ? 0 : so + K1_STAGE;
          half(IC<1>{}, so);
        }
        fence_all(acc);
        asm volatile("" : "+v"(cs0), "+v"(cs1), "+v"(rs_[0]), "+v"(rs_[1]), "+v"(rq_[0]), "+v"(rq_[1]));
        const unsigned csb = lb + K1_CS;
        if (q == 0) {
          lds_wr1(csb + (w * 16 + i16) * 4, cs0[0]);
          if (w == 0) lds_wr1(csb + (64 + i16) * 4, cs1[0]);
        }
        wait_lgkm0();
        bar();
#pragma unroll
        for (int f = 0; f < 5; ++f) csv[f] = lds_rd1(csb + (f * 16 + i16) * 4);
#pragma unroll
        for (int r = 0; r < 2; ++r)
#pragma unroll
          for (int j = 0; j < 4; ++j) {
            float m_, r_;
            ln_coef(rs_[r][j], __shfl(rq_[r][j], q * 20 + j), m_, r_);
            mu[r][j] = -m_; rs[r][j] = sign * r_;
          }
        if (w == 0) {
          const rsrc_t rc = make_rsrc(ws + WS_CSD, 256 * 320);
          st4(__float_as_uint(q == 0 ? csv[0] : (q == 1 ? csv[1] : (q == 2 ? csv[2] : csv[3]))), rc, lane * 4, bid * 320);
          st4(__float_as_uint(csv[4]), rc, q == 0 ? 256 + lane * 4 : OOB, bid * 320);
        }
        g1_store(acc, mu, rs, csv, make_rsrc(ws + WS_AB1, SZ_AB), P, ct, w, i16, q);
        publish(ws, f5, epoch, lane);
      } else {
        {
          const rsrc_t rc = make_rsrc(ws + WS_CSD, 256 * 320);
#pragma unroll
          for (int f = 0; f < 5; ++f) csv[f] = __uint_as_float(__builtin_amdgcn_raw_buffer_load_b32(rc, (f * 16 + i16) * 4, bid * 320, 16));
        }
        const unsigned h0 = P & 1;
        poll<PD4>(ws, FL_4 + P * 64 + ((i16 >> 3) * 4 + w) * 8 + (i16 & 7), 16, epoch, lane);
        const rsrc_t rx = make_rsrc(ws + WS_X2, SZ_X);
        const unsigned sox0 = (P * 8 + h0 * 4 + w) * 20 * 1024, sox1 = (P * 8 + (h0 ^ 1) * 4 + w) * 20 * 1024;
        bf16x8 a[40];
#define LDA(n) a[n] = ld16(rx, lane * 16 + ((n) & 3) * 1024, ((n) < 20 ? sox0 : sox1) + (((n) % 20) >> 2) * 4096)
        sfor<0, 6>([&](auto N) { LDA(N.v); });
        const rsrc_t rst = make_rsrc(ws + WS_ST2, SZ_ST);
        const unsigned sto = ((lane >> 5) * 4 * S_ + P * 128 + (((lane >> 4) & 1) ^ h0) * 64 + w * 16 + i16) * 8;
        const u32x2 sp0 = ld8u(rst, sto, 0), sp1 = ld8u(rst, sto, S_ * 8), sp2 = ld8u(rst, sto, 2 * S_ * 8), sp3 = ld8u(rst, sto, 3 * S_ * 8);
        sb();
        bar();
        bf16x8 b[2][5];
        const unsigned rb = lb + i16 * 128 + ((q ^ (lane & 7)) << 4);
        const rsrc_t rab = make_rsrc(ws + WS_AB2, SZ_AB);
        const unsigned vo = ((2 * (w & 1) + (q >> 1)) * 16 + i16) * 16 + (q & 1) * 8;
        const unsigned so = ((ct & 7) * 128 + (ct >> 3) * 64 + P * 4 + (w >> 1)) * 5 * 1024;
        auto put = [&](auto H) {
          constexpr int hh = H.v;
#pragma unroll
          for (int f = 0; f < 5; ++f) {
            const f32x2 cs = {csv[f], csv[f]};
            u32x2 y;
#pragma unroll
            for (int e = 0; e < 2; ++e) {
              const f32x2 t = __builtin_elementwise_fma(f32x2{mu[hh][2 * e], mu[hh][2 * e + 1]}, cs, f32x2{acc[hh][f][2 * e], acc[hh][f][2 * e + 1]}) *
                              f32x2{rs[hh][2 * e], rs[hh][2 * e + 1]};
              y[e] = __builtin_bit_cast(unsigned, __builtin_convertvector(t, bf16x2));
            }
            st8(y, rab, vo + (f & 3) * 1024, so + (hh ^ h0) * 10240 + (f >> 2) * 4096);
          }
        };
#pragma unroll
        for (int f = 0; f < 5; ++f) b[0][f] = lds_rd(rb + wd_pos(0) * 1024 + f * 2048);
        sb();
        sfor<0, 40>([&](auto N) {
          constexpr int n = N.v, hh = n / 20, k = n % 20, kn = (k + 1) % 20;
          if constexpr (n == 2 * KP1) {
            float s1 = (__uint_as_float(sp0[0]) + __uint_as_float(sp1[0])) + (__uint_as_float(sp2[0]) + __uint_as_float(sp3[0]));
            float s2 = (__uint_as_float(sp0[1]) + __uint_as_float(sp1[1])) + (__uint_as_float(sp2[1]) + __uint_as_float(sp3[1]));
            auto ta = __builtin_amdgcn_permlane32_swap(__float_as_uint(s1), __float_as_uint(s1), false, false);
            auto tb = __builtin_amdgcn_permlane32_swap(__float_as_uint(s2), __float_as_uint(s2), false, false);
            float m, rr;
            ln_coef(__uint_as_float(ta[0]) + __uint_as_float(ta[1]), __uint_as_float(tb[0]) + __uint_as_float(tb[1]), m, rr);
            m = -m; rr *= sign;
#pragma unroll
            for (int r = 0; r < 2; ++r)
#pragma unroll
              for (int e = 0; e < 4; ++e) { mu[r][e] = __shfl(m, r * 16 + q * 4 + e); rs[r][e] = __shfl(rr, r * 16 + q * 4 + e); }
            asm volatile("" : "+v"(mu[0][0]), "+v"(mu[0][1]), "+v"(mu[0][2]), "+v"(mu[0][3]), "+v"(mu[1][0]), "+v"(mu[1][1]), "+v"(mu[1][2]), "+v"(mu[1][3]),
                              "+v"(rs[0][0]), "+v"(rs[0][1]), "+v"(rs[0][2]), "+v"(rs[0][3]), "+v"(rs[1][0]), "+v"(rs[1][1]), "+v"(rs[1][2]), "+v"(rs[1][3]));
            sb();
          }
          sfor<0, 5>([&](auto F) {
            constexpr int f = F.v;
            if constexpr (k == 0) mma0(acc[hh][f], a[n], b[n & 1][f]); else mma(acc[hh][f], a[n], b[n & 1][f]);
            if constexpr (n + 1 < 40) b[(n + 1) & 1][f] = lds_rd((rb ^ ((kn & 1) * 64)) + wd_pos(kn >> 1) * 1024 + f * 2048);
            if constexpr ((f == 1 || f == 3) && 6 + 2 * n + (f >> 1) < 40) LDA(6 + 2 * n + (f >> 1));
            sb();
          });
          if constexpr (n == 21) { pin_acc(acc[0]); put(IC<0>{}); sb(); }
          if constexpr (n == 20 + PUBG) publish(ws, f5 + 1024, epoch, lane);
        });
        fence_acc(acc[1]);
        bar();
        put(IC<1>{});
        publish(ws, f5 + 2048, epoch, lane);
      }
    }
    {
      PHASE_IDS
      IDS_G2
      const unsigned m = w >> 1, n = w & 1;
      const unsigned ra = lb + G2_A + (m * 64 + i16) * 128 + ((q ^ (lane & 7)) << 4);
      const unsigned rb = lb + n * 10240 + lane * 16;
      const unsigned row = P * 128 + m * 64 + kq * 16 + i16, ct = 2 * c4 + n;
      const TileOff ro = tile_offsets(L == 0, row, ct, q), oo = tile_offsets(false, row, ct, q);
      const rsrc_t rxp = make_rsrc(ws + WS_XP + L * SZ_XP, SZ_XP);
      const unsigned xo = ((P * 4 + c4) * 48 + w) * XSLOT;
      f32x4 acc[4][5];
#pragma unroll
      for (int i = 0; i < 4; ++i)
#pragma unroll
        for (int f = 0; f < 5; ++f) acc[i][f] = f32x4{0.f, 0.f, 0.f, 0.f};
      bf16x8 fa[2][4], fb[2][5];
      unsigned ra1 = ra ^ 64, rb2 = rb + 2 * G2_BS;
      asm volatile("" : "+v"(ra1), "+v"(rb2));
      auto half = [&](auto KK, auto SL) {
        constexpr int kk = KK.v, sl = SL.v;
        sfor<0, 20>([&](auto M) {
          constexpr int mm = M.v;
          mma(acc[mm / 5][mm % 5], fb[kk][mm % 5], fa[kk][mm / 5]);
          if constexpr (mm < 5) fb[kk ^ 1][mm] = lds_rd((sl < 2 ? rb : rb2) + (sl & 1) * G2_BS + (kk ^ 1) * 5120 + mm * 1024);
          else if constexpr (mm < 9) fa[kk ^ 1][mm - 5] = lds_rd((kk ? ra : ra1) + sl * G2_AS + (mm - 5) * 2048);
          sb();
        });
      };
      if (L) poll0(ws, FL_4 + P * 64 + (m * 4 + kq) * 8 + ct, 1, epoch, lane);
      ResTile rt = load_res(make_rsrc(L ? (const void*)(ws + WS_X2) : (const void*)opq_p(x0_), SZ_X), ro);
      fence_all(acc);
      bar();
#pragma unroll
      for (int f = 0; f < 5; ++f) fb[0][f] = lds_rd(rb + f * 1024);
#pragma unroll
      for (int i = 0; i < 4; ++i) fa[0][i] = lds_rd(ra + i * 2048);
      sb();
      auto stage = [&](auto U) {
        half(IC<0>{}, U);
        wait_lgkm0();
        bar();
        half(IC<1>{}, IC<(U.v + 1) & 3>{});
      };
#pragma unroll 1
      for (int g = 0; g < 3; ++g) sfor<0, 4>(stage);
      sfor<0, 3>(stage);
      half(IC<0>{}, IC<3>{});
      wait_lgkm0();
      bar();
      {
        unsigned p[3][10];
        u32x4 y[3][2];
        auto pack = [&](auto N) {
          constexpr int i = N.v / 5, f = N.v % 5;
          asm volatile("" : "+v"(acc[i][f]));
          p[i][2 * f] = pk2(acc[i][f][0], acc[i][f][1]); p[i][2 * f + 1] = pk2(acc[i][f][2], acc[i][f][3]);
          const unsigned o = xo + (((kq + 1 + i) & 3) * 3 + 2 - i) * 4 * XSLOT;
          if constexpr (f == 1) { y[i][0] = u32x4{p[i][0], p[i][1], p[i][2], p[i][3]}; st16_raw(y[i][0], rxp, lane * 16, o); }
          if constexpr (f == 3) { y[i][1] = u32x4{p[i][4], p[i][5], p[i][6], p[i][7]}; st16_raw(y[i][1], rxp, lane * 16 + 1024, o); }
          if constexpr (f == 4) st8(u32x2{p[i][8], p[i][9]}, rxp, lane * 8 + 2048, o);
        };
        sfor<0, 20>([&](auto M) {
          constexpr int mm = M.v;
          mma(acc[mm / 5][mm % 5], fb[1][mm % 5], fa[1][mm / 5]);
          if constexpr (mm >= 8) pack(IC<mm - 8>{});
          sb();
        });
        pack(IC<12>{}); pack(IC<13>{}); pack(IC<14>{});
        st_guard(y[0][0], y[0][1], y[1][0], y[1][1], y[2][0], y[2][1]);
      }
      f32x4 (&v)[5] = acc[3];
      fence_acc(v);
      const unsigned fx = FL_X + ((L * 64 + P * 4 + c4) * 4 + w) * 4;
      publish(ws, fx + kq, epoch, lane);
      poll<PDX>(ws, fx + ((kq + 1 + lane) & 3), 3, epoch, lane);
      const unsigned xi = xo + kq * 12 * XSLOT;
      u32x4 g0[3], g1[3]; u32x2 g2[3];
#pragma unroll
      for (int s = 0; s < 3; ++s) {
        g0[s] = ld16u(rxp, lane * 16, xi + s * 4 * XSLOT);
        g1[s] = ld16u(rxp, lane * 16 + 1024, xi + s * 4 * XSLOT);
        g2[s] = ld8u(rxp, lane * 8 + 2048, xi + s * 4 * XSLOT);
      }
      sb();
#pragma unroll
      for (int s = 0; s < 3; ++s) {
        v[0] += f32x4{bflo(g0[s][0]), bfhi(g0[s][0]), bflo(g0[s][1]), bfhi(g0[s][1])};
        v[1] += f32x4{bflo(g0[s][2]), bfhi(g0[s][2]), bflo(g0[s][3]), bfhi(g0[s][3])};
        v[2] += f32x4{bflo(g1[s][0]), bfhi(g1[s][0]), bflo(g1[s][1]), bfhi(g1[s][1])};
        v[3] += f32x4{bflo(g1[s][2]), bfhi(g1[s][2]), bflo(g1[s][3]), bfhi(g1[s][3])};
        v[4] += f32x4{bflo(g2[s][0]), bfhi(g2[s][0]), bflo(g2[s][1]), bfhi(g2[s][1])};
      }
      finish_tile(v, rt, make_rsrc(ws + (L ? WS_X3 : WS_X1), SZ_X), oo, true, make_rsrc(ws + (L ? WS_ST3 : WS_ST1), SZ_ST),
                  q == 0 ? (ct * S_ + row) * 8 : OOB);
      publish(ws, FL_2 + (L * 16 + P) * 64 + (m * 4 + kq) * 8 + ct, epoch, lane);
    }
    u32x4 y2[2]; u32x2 y1[2];
    {
      PHASE_IDS
      IDS_G3
      typedef __attribute__((ext_vector_type(2))) short s16x2;
      poll<PD2>(ws, FL_2 + (L * 16 + P) * 64 + w * 16 + i16, 16, epoch, lane);
      const rsrc_t rx = make_rsrc(ws + (L ? WS_X3 : WS_X1), SZ_X);
      const rsrc_t rst = make_rsrc(ws + (L ? WS_ST3 : WS_ST1), SZ_ST);
      const unsigned sox = (P * 8 + w * 2) * 20 * 1024;
      const unsigned par = ct & 1;
      bf16x8 a[20][2];
#pragma unroll
      for (int k = 0; k < 3; ++k) { a[k][0] = LDX(0, k); a[k][1] = LDX(1, k); }
      const unsigned sto = (2 * q * S_ + P * 128 + w * 32 + i16) * 8;
      const u32x2 sa0 = ld8u(rst, sto, 0), sa1 = ld8u(rst, sto, S_ * 8), sb0 = ld8u(rst, sto + 128, 0), sb1 = ld8u(rst, sto + 128, S_ * 8);
      float mu[2], rs[2];
      sb();
      bar();
      f32x4 acc[2][4];
      bf16x8 b[2][4];
      const unsigned rb = lb + lane * 16;
#pragma unroll
      for (int f = 0; f < 4; ++f) b[0][f] = lds_rd(rb + f * 1024);
      sb();
      sfor<0, 20>([&](auto K) {
        constexpr int k = K.v;
        const unsigned pb = k + 1 < 20 ? rb + (k + 1) * 4096 : rb + 80 * 1024;
        if constexpr (k == KP3) {
          ln_coef(xsum16_32(__uint_as_float(sa0[0]) + __uint_as_float(sa1[0])), xsum16_32(__uint_as_float(sa0[1]) + __uint_as_float(sa1[1])), mu[0], rs[0]);
          ln_coef(xsum16_32(__uint_as_float(sb0[0]) + __uint_as_float(sb1[0])), xsum16_32(__uint_as_float(sb0[1]) + __uint_as_float(sb1[1])), mu[1], rs[1]);
          asm volatile("" : "+v"(mu[0]), "+v"(mu[1]), "+v"(rs[0]), "+v"(rs[1]));
          sb();
        }
        sfor<0, 8>([&](auto M) {
          constexpr int f = M.v >> 1, r = M.v & 1;
          if constexpr (k == 0) mma0(acc[r][f], b[0][f], a[0][r]); else mma(acc[r][f], b[k & 1][f], a[k][r]);
          if constexpr (r == 0 && (k + 1 < 20 || f < 3)) b[(k + 1) & 1][f] = lds_rd(pb + f * 1024);
          if constexpr (r == 1 && 2 * k + 3 + (f >> 1) < 20) a[2 * k + 3 + (f >> 1)][f & 1] = LDX(f & 1, 2 * k + 3 + (f >> 1));
          sb();
        });
      });
      auto act = [&](const f32x4& c, int r, const f32x4& cs, unsigned (&pk)[2]) {
        const f32x2 nm = {-mu[r], -mu[r]}, r2 = {rs[r], rs[r]};
#pragma unroll
        for (int e = 0; e < 2; ++e) {
          const f32x2 z = __builtin_elementwise_fma(nm, f32x2{cs[2 * e], cs[2 * e + 1]}, f32x2{c[2 * e], c[2 * e + 1]}) * r2;
          const s16x2 hb = __builtin_bit_cast(s16x2, __builtin_convertvector(z, bf16x2));
          pk[e] = __builtin_bit_cast(unsigned, __builtin_elementwise_max(hb, s16x2{0, 0}));
        }
      };
      auto pair = [&](const unsigned (&u)[2], const unsigned (&v)[2]) {
        auto t0 = __builtin_amdgcn_permlane16_swap(u[0], v[0], false, false);
        auto t1 = __builtin_amdgcn_permlane16_swap(u[1], v[1], false, false);
        return u32x4{t0[0], t1[0], t0[1], t1[1]};
      };
      const unsigned csb = lb + KM_CS1 + q * 16;
      f32x4 c1s[4];
#pragma unroll
      for (int f = 0; f < 4; ++f) c1s[f] = lds_rdf(csb + (f + par) * 64);
      asm volatile("s_nop 7\n\ts_nop 1" : "+v"(acc[0][0]), "+v"(acc[0][1]), "+v"(acc[0][2]), "+v"(acc[0][3]),
                                          "+v"(acc[1][0]), "+v"(acc[1][1]), "+v"(acc[1][2]), "+v"(acc[1][3]));
      wait_lgkm0();
      bar();
      {
        const rsrc_t rh = make_rsrc(ws + (L ? WS_H2 : WS_H1), SZ_H);
        const unsigned vo = ((((q & 1) << 1) | (q >> 1)) * 16 + i16) * 16;
        const unsigned so = ((P * 8 + w * 2) * 54 + 7 * (ct >> 1) + 4 * par) * 1024;
        u32x4 y[2][2];
#pragma unroll
        for (int p = 0; p < 2; ++p) {
          const f32x4 c0 = c1s[2 * p], c1 = c1s[2 * p + 1];
#pragma unroll
          for (int r = 0; r < 2; ++r) {
            unsigned u[2], v[2];
            act(acc[r][2 * p], r, c0, u); act(acc[r][2 * p + 1], r, c1, v);
            y[p][r] = pair(u, v);
            st16_raw(y[p][r], rh, p == 1 && ct == 15 ? OOB : vo, so + (r * 54 + p) * 1024);
          }
        }
        st_guard(y[0][0], y[0][1], y[1][0], y[1][1]);
      }
      sb();
      sfor<0, 20>([&](auto K) {
        constexpr int k = K.v;
        const unsigned pb = rb + 80 * 1024 + (k + 1) * 3072;
        sfor<0, 6>([&](auto M) {
          constexpr int f = M.v >> 1, r = M.v & 1;
          if constexpr (k == 0) mma0(acc[r][f], b[0][f], a[0][r]); else mma(acc[r][f], b[k & 1][f], a[k][r]);
          if constexpr (r == 0 && k + 1 < 20) b[(k + 1) & 1][f] = lds_rd(pb + f * 1024);
          sb();
        });
        if constexpr (k == PUB1) publish(ws, FL_3 + (L * 32 + P) * 64 + w * 16 + ct, epoch, lane);
      });
      const f32x4 c0 = lds_rdf(csb + (4 + par) * 64), c1 = lds_rdf(csb + (5 + par) * 64), c2 = lds_rdf(csb + (par ? 0 : 6) * 64);
      asm volatile("s_nop 7\n\ts_nop 1" : "+v"(acc[0][0]), "+v"(acc[0][1]), "+v"(acc[0][2]), "+v"(acc[1][0]), "+v"(acc[1][1]), "+v"(acc[1][2]));
#pragma unroll
      for (int r = 0; r < 2; ++r) {
        unsigned u[2], v[2], s[2];
        act(acc[r][0], r, c0, u); act(acc[r][1], r, c1, v); act(acc[r][2], r, c2, s);
        y2[r] = pair(u, v);
        y1[r] = u32x2{s[0], s[1]};
      }
      asm volatile("" : "+v"(y2[0]), "+v"(y2[1]), "+v"(y1[0]), "+v"(y1[1]));
    }
    {
      PHASE_IDS
      IDS_G4
      const unsigned row = rt64 * 64 + w * 16 + i16;
      const rsrc_t rh = make_rsrc(ws + (L ? WS_H2 : WS_H1), SZ_H);
      const unsigned soh = (rt64 * 4 * 54 + w * 14) * 1024;
      const unsigned vl = lane * 16, vz = lane * 16 + (w < 3 ? 0u : OOB);
      bf16x8 a[14][4], b[2][5];
      unsigned sh[4];
#pragma unroll
      for (int i = 0; i < 4; ++i) sh[i] = soh + ((w + 1 + i) & 3) * 54 * 1024;
#define LDH(t, i) a[t][i] = ld16(rh, (ORD[t] < 12 ? vl : vz) + (ORD[t] & 3) * 1024, sh[i] + (ORD[t] >> 2) * 4096)
      const TileOff ro = tile_offsets(false, row, c8, q), oo = tile_offsets(L != 0, row, c8, q);
      const unsigned f3 = FL_3 + (L * 32 + (rt64 >> 1)) * 64 + ((rt64 & 1) * 2 + (lane & 1)) * 16 + 4 * w + ((lane >> 1) & 3);
      poll0(ws, f3, 8, epoch, lane);
      sfor<0, 8>([&](auto N) { LDH(N.v / 4, N.v % 4); });
      sb();
      {
        const unsigned P = (xcd >> 1) * 4 + (idx >> 3), ct = (xcd & 1) * 8 + (idx & 7), par = ct & 1;
        const unsigned vo = ((((q & 1) << 1) | (q >> 1)) * 16 + i16) * 16;
        const unsigned v1 = ((2 * par + (q >> 1)) * 16 + i16) * 16 + (q & 1) * 8;
        const unsigned so = ((P * 8 + w * 2) * 54 + 7 * (ct >> 1)) * 1024;
#pragma unroll
        for (int r = 0; r < 2; ++r) {
          st16_raw(y2[r], rh, ct == 15 ? OOB : vo, so + (r * 54 + 2 + 4 * par) * 1024);
          st8(y1[r], rh, v1, so + (r * 54 + 3) * 1024);
        }
        st_guard(y2[0], y2[1]);
      }
      ResTile rt;
      u64 fl2;
      const rsrc_t rr = make_rsrc(ws + (L ? WS_X3 : WS_X1), SZ_X);
      sb();
      bar();
      f32x4 acc[4][5];
      const unsigned rbe = lb + w * 20480 + lane * 16, rbl = lb + G4_LATE + w * 15360 + lane * 16;
#pragma unroll
      for (int f = 0; f < 5; ++f) b[0][f] = lds_rd(rbe + f * 1024);
      sb();
      sfor<0, 14>([&](auto T) {
        constexpr int t = T.v;
        constexpr int ta = t < 5 ? (t == 0 ? 2 : t + 3) : (t == 5 ? -1 : (t == 6 ? 8 : (t + 3 < 14 ? t + 3 : -1)));
        if constexpr (t == 3 || t == 6 || t == 10) { wait_lgkm0(); bar(); }
        if constexpr (t == 6) { if (!__all(fl2 == epoch)) poll0(ws, f3 + 16 * 64, 8, epoch, lane); }
        sfor<0, 20>([&](auto M) {
          constexpr int m = M.v, i = m / 5, f = m % 5;
          if constexpr (t == 0) mma0(acc[i][f], b[0][f], a[0][i]); else mma(acc[i][f], b[t & 1][f], a[t][i]);
          if constexpr (t + 1 < 14 && (m & 1) == 1 && m < 10)
            b[(t + 1) & 1][m >> 1] = lds_rd((g4_late(t + 1) ? rbl : rbe) + g4_slot(t + 1) + (m >> 1) * 1024);
          if constexpr (ta >= 0 && (m & 1) == 0 && m < 8) LDH(ta, m >> 1);
          if constexpr (t == 6 && (m & 1) == 0 && m >= 8 && m < 16) LDH(9, (m - 8) >> 1);
          if constexpr (t == 0 && (m & 1) == 0 && m >= 8 && m < 16) LDH(3, (m - 8) >> 1);
          if constexpr (t == 2 && m == 10) rt = load_res(rr, ro);
          if constexpr (t == 4 && m == 18) fl2 = __hip_atomic_load(flag_ptr(ws, f3 + 16 * 64), RLX_AGENT);
          if constexpr (t == 13 && m >= 8) {
            asm volatile("" : "+v"(acc[(m - 8) / 5][(m - 8) % 5]));
            lds_wrf(rbl + (m - 8) * 1024, acc[(m - 8) / 5][(m - 8) % 5]);
          }
          sb();
        });
        if constexpr (t == 2) {
          wait_vm<19>();
          raise(ws, FL_3 + (L * 32 + 16 + (xcd >> 1) * 4 + (idx >> 3)) * 64 + w * 16 + (xcd & 1) * 8 + (idx & 7), epoch, lane);
        }
      });
#pragma unroll
      for (int f = 2; f < 5; ++f) { asm volatile("" : "+v"(acc[2][f])); lds_wrf(rbl + (10 + f) * 1024, acc[2][f]); }
      f32x4 (&v)[5] = acc[3];
      fence_acc(v);
      wait_lgkm0();
      bar();
#pragma unroll
      for (int s = 1; s < 4; ++s) {
        const unsigned src = lb + G4_LATE + ((w + s) & 3) * 15360 + (3 - s) * 5 * 1024 + lane * 16;
#pragma unroll
        for (int f = 0; f < 5; ++f) v[f] += lds_rdf(src + f * 1024);
      }
      finish_tile(v, rt, L ? make_rsrc(opq_p(out_), SZ_X) : make_rsrc(ws + WS_X2, SZ_X), oo, L == 0, make_rsrc(ws + WS_ST2, SZ_ST),
                  q == 0 ? (c8 * S_ + row) * 8 : OOB);
      if (L == 0) publish(ws, FL_4 + (rt64 >> 1) * 64 + ((rt64 & 1) * 4 + w) * 8 + c8, epoch, lane);
    }
  }
  asm volatile(".Lcw_end:");
  if (blockIdx.x == 0 && w0 == 0) raise(ws_, FL_E, epoch, tid0);
}

extern "C" void kernel_launch(void* const* d_in, const int* in_sizes, int n_in, void* d_out, int out_size, void* d_ws,
                              size_t ws_size, hipStream_t stream) {
  char* ws = (char*)d_ws;
  (void)hipFuncSetAttribute((const void*)k_all, hipFuncAttributeMaxDynamicSharedMemorySize, KM_LDS);
  k_all<<<256, 512, KM_LDS, stream>>>((const u16*)d_in[0], (const u16*)d_in[1], (const u16*)d_in[2], (const u16*)d_in[3],
                                     (const u16*)d_in[4], (const u16*)d_in[6], (const u16*)d_in[7], ws, (u16*)d_out);
}
```

```cpp
#define TOUCH_N 6
#include <hip/hip_runtime.h>
#include <hip/hip_bf16.h>
#include <stdint.h>

#define DI __device__ __forceinline__
typedef unsigned short u16;
typedef __attribute__((ext_vector_type(8))) __bf16 bf16x8;
typedef __attribute__((ext_vector_type(2))) __bf16 bf16x2;
typedef __attribute__((ext_vector_type(4))) float f32x4;
typedef __attribute__((ext_vector_type(2))) float f32x2;
typedef __attribute__((ext_vector_type(4))) unsigned u32x4;
typedef __attribute__((ext_vector_type(2))) unsigned u32x2;
typedef __attribute__((ext_vector_type(4))) short s16x4;
#define LDS_AS __attribute__((address_space(3)))
#define GLB_AS __attribute__((address_space(1)))
typedef LDS_AS char* ldsp;
typedef unsigned long long u64;
typedef GLB_AS u64 gu64;
typedef __amdgpu_buffer_rsrc_t rsrc_t;
#define RLX_AGENT __ATOMIC_RELAXED, __HIP_MEMORY_SCOPE_AGENT

constexpr int S_ = 2048, D_ = 640, F_ = 1728;
constexpr float EPS = 1e-5f;
constexpr unsigned OOB = 0x7f000000u;

constexpr unsigned SZ_AB = 8 * 128 * 5 * 1024;
constexpr unsigned SZ_X = 128 * 20 * 1024;
constexpr unsigned SZ_H = 128 * 54 * 1024;
constexpr unsigned SZ_W1 = 16 * 140 * 1024;
constexpr unsigned SZ_W2 = 8 * 54 * 5 * 1024;
constexpr unsigned SZ_ST = 2048 * 64;
constexpr unsigned WS_AB1 = 0;
constexpr unsigned WS_AB2 = WS_AB1 + SZ_AB;
constexpr unsigned WS_X1 = WS_AB2 + SZ_AB;
constexpr unsigned WS_X2 = WS_X1 + SZ_X;
constexpr unsigned WS_X3 = WS_X2 + SZ_X;
constexpr unsigned WS_H1 = WS_X3 + SZ_X;
constexpr unsigned WS_H2 = WS_H1 + SZ_H;
constexpr unsigned WS_W1 = WS_H2 + SZ_H;
constexpr unsigned WS_W2 = WS_W1 + SZ_W1;
constexpr unsigned WS_ST1 = WS_W2 + SZ_W2;
constexpr unsigned WS_ST2 = WS_ST1 + SZ_ST;
constexpr unsigned WS_ST3 = WS_ST2 + SZ_ST;
constexpr unsigned WS_CS1 = WS_ST3 + SZ_ST;
constexpr unsigned WS_CSD = WS_CS1 + 20 * 1728 * 4;
constexpr unsigned XSLOT = 2560;
constexpr unsigned SZ_XP = 256 * 12 * XSLOT;
constexpr unsigned WS_XP = (WS_CSD + 256 * 320 + 4095) & ~4095u;
constexpr unsigned WS_FLG = WS_XP + 2 * SZ_XP;
constexpr unsigned FL_X = 0;
constexpr unsigned FL_2 = FL_X + 2048;
constexpr unsigned FL_3 = FL_2 + 2048;
constexpr unsigned FL_4 = FL_3 + 4096;
constexpr unsigned FL_5 = FL_4 + 1024;
constexpr unsigned FL_W = FL_5 + 3072;
constexpr unsigned FL_E = FL_W + 1088;
constexpr unsigned WS_END = WS_FLG + (FL_E + 1) * 128;

DI u32x4 make_srd(const void* p, unsigned bytes) {
  uint64_t a = (uint64_t)p;
  u32x4 r;
  r[0] = (unsigned)a; r[1] = (unsigned)(a >> 32) & 0xffffu; r[2] = bytes; r[3] = 0x00020000u;
  return r;
}
DI rsrc_t make_rsrc(const void* p, unsigned bytes) {
  return __builtin_amdgcn_make_buffer_rsrc((void*)p, (short)0, (int)bytes, 0x00020000);
}
template <int LOFF, int GOFF>
DI void glds(u32x4 srd, unsigned voff, unsigned ldsb, unsigned soff) {
  unsigned tmp;
  asm volatile("s_add_u32 m0, %1, %2\n\ts_add_u32 %0, %3, %4\n\tbuffer_load_dwordx4 %5, %6, %0 offen lds"
               : "=&s"(tmp)
               : "s"(ldsb), "i"(LOFF), "s"(soff), "i"(GOFF), "v"(voff), "s"(srd)
               : "memory", "scc");
}
template <int N> DI void wait_vm() { asm volatile("s_waitcnt vmcnt(%0)" ::"i"(N) : "memory"); }
DI void wait_lgkm0() { asm volatile("s_waitcnt lgkmcnt(0)" ::: "memory"); }
DI void sb() { __builtin_amdgcn_sched_barrier(0); }
DI void bar() { asm volatile("s_barrier" ::: "memory"); }

DI bf16x8 lds_rd(unsigned addr) { return *(const LDS_AS bf16x8*)(ldsp)(uintptr_t)addr; }
DI f32x4 lds_rdf(unsigned addr) { return *(const LDS_AS f32x4*)(ldsp)(uintptr_t)addr; }
DI void lds_wrf(unsigned addr, f32x4 v) { *(LDS_AS f32x4*)(ldsp)(uintptr_t)addr = v; }
DI float lds_rd1(unsigned addr) { return *(const LDS_AS float*)(ldsp)(uintptr_t)addr; }
DI void lds_wr1(unsigned addr, float v) { *(LDS_AS float*)(ldsp)(uintptr_t)addr = v; }
DI void mma(f32x4& c, const bf16x8& a, const bf16x8& b) {
  asm volatile("v_mfma_f32_16x16x32_bf16 %0, %1, %2, %0" : "+v"(c) : "v"(a), "v"(b));
}
DI void mma0(f32x4& c, const bf16x8& a, const bf16x8& b) {
  asm volatile("v_mfma_f32_16x16x32_bf16 %0, %1, %2, 0" : "=&v"(c) : "v"(a), "v"(b));
}
DI void fence_acc(f32x4 (&a)[5]) { asm volatile("s_nop 7\n\ts_nop 1" : "+v"(a[0]), "+v"(a[1]), "+v"(a[2]), "+v"(a[3]), "+v"(a[4])); }
DI void pin_acc(f32x4 (&a)[5]) { asm volatile("" : "+v"(a[0]), "+v"(a[1]), "+v"(a[2]), "+v"(a[3]), "+v"(a[4])); }
template <int M> DI void fence_all(f32x4 (&a)[M][5]) {
#pragma unroll
  for (int i = 1; i < M; ++i) pin_acc(a[i]);
  fence_acc(a[0]);
#pragma unroll
  for (int i = 1; i < M; ++i) pin_acc(a[i]);
}
DI unsigned pk2(float a, float b) {
  f32x2 v = {a, b};
  return __builtin_bit_cast(unsigned, __builtin_convertvector(v, bf16x2));
}
DI float bflo(unsigned u) { return __uint_as_float(u << 16); }
DI float bfhi(unsigned u) { return __uint_as_float(u & 0xffff0000u); }
DI float dot2(unsigned a, unsigned b, float c) {
  return __builtin_amdgcn_fdot2_f32_bf16(__builtin_bit_cast(bf16x2, a), __builtin_bit_cast(bf16x2, b), c, false);
}
DI float xsum16_32(float x) {
  auto s = __builtin_amdgcn_permlane16_swap(__float_as_uint(x), __float_as_uint(x), false, false);
  x = __uint_as_float(s[0]) + __uint_as_float(s[1]);
  auto t = __builtin_amdgcn_permlane32_swap(__float_as_uint(x), __float_as_uint(x), false, false);
  return __uint_as_float(t[0]) + __uint_as_float(t[1]);
}
template <int V> struct IC { static constexpr int v = V; };
template <int I, int N, class Fn> DI void sfor(Fn&& f) {
  if constexpr (I < N) { f(IC<I>{}); sfor<I + 1, N>(f); }
}
DI unsigned rfl(unsigned v) { return __builtin_amdgcn_readfirstlane(v); }
DI unsigned opq_v(unsigned x) { asm volatile("" : "+v"(x)); return x; }
DI unsigned opq_s(unsigned x) { asm volatile("" : "+s"(x)); return x; }
template <class T> DI T* opq_p(T* x) { asm volatile("" : "+s"(x)); return x; }
DI bf16x8 ld16(rsrc_t r, unsigned voff, unsigned soff) { return __builtin_bit_cast(bf16x8, __builtin_amdgcn_raw_buffer_load_b128(r, voff, soff, 16)); }
DI u32x4 ld16u(rsrc_t r, unsigned voff, unsigned soff) { return __builtin_amdgcn_raw_buffer_load_b128(r, voff, soff, 16); }
DI u32x2 ld8u(rsrc_t r, unsigned voff, unsigned soff) { return __builtin_amdgcn_raw_buffer_load_b64(r, voff, soff, 16); }
DI void st16(u32x4 v, rsrc_t r, unsigned voff, unsigned soff) {
  __builtin_amdgcn_raw_buffer_store_b128(v, r, voff, soff, 16);
  asm volatile("s_nop 1" :: "v"(v) : "memory");
}
DI void st16_raw(u32x4 v, rsrc_t r, unsigned voff, unsigned soff) { __builtin_amdgcn_raw_buffer_store_b128(v, r, voff, soff, 16); }
template <class... T> DI void st_guard(const T&... v) {
  (..., [](const auto& x) { asm volatile("" :: "v"(x) : "memory"); }(v));
  asm volatile("s_nop 1" ::: "memory");
  (..., [](const auto& x) { asm volatile("" :: "v"(x)); }(v));
}
DI void st8(u32x2 v, rsrc_t r, unsigned voff, unsigned soff) { __builtin_amdgcn_raw_buffer_store_b64(v, r, voff, soff, 16); }
DI void st4(unsigned v, rsrc_t r, unsigned voff, unsigned soff) { __builtin_amdgcn_raw_buffer_store_b32(v, r, voff, soff, 16); }

template <int KS> DI unsigned foff(unsigned row, unsigned col) {
  return ((row >> 4) * KS + (col >> 5)) * 1024u + (((col >> 3) & 3) * 16u + (row & 15)) * 16u + (col & 7) * 2u;
}

extern __shared__ __attribute__((aligned(1024))) char smem[];
DI unsigned lds_base() { return (unsigned)(uintptr_t)(ldsp)smem; }

DI void ln_coef(float s1, float s2, float& mu, float& rs) {
  mu = s1 * (1.f / D_);
  rs = __builtin_amdgcn_rsqf(fmaxf(s2 * (1.f / D_) - mu * mu, 0.f) + EPS);
}

DI void g1_store(const f32x4 (&acc)[2][5], const float (&mu)[2][4], const float (&rs)[2][4], const float (&csv)[5], rsrc_t rab,
                 unsigned P, unsigned ct, unsigned w, unsigned i16, unsigned q) {
  const unsigned kstep = (ct >> 3) * 64 + P * 4 + w;
  const unsigned cq = ((q & 1) << 1) | (q >> 1);
  const unsigned so = ((ct & 7) * 128 + kstep) * 5 * 1024;
  const unsigned vo = (cq * 16 + i16) * 16;
  u32x4 y[5];
#pragma unroll
  for (int f = 0; f < 5; ++f) {
    const f32x2 cs = {csv[f], csv[f]};
    unsigned pk[2][2];
#pragma unroll
    for (int r = 0; r < 2; ++r)
#pragma unroll
      for (int e = 0; e < 2; ++e) {
        const f32x2 t = __builtin_elementwise_fma(f32x2{mu[r][2 * e], mu[r][2 * e + 1]}, cs, f32x2{acc[r][f][2 * e], acc[r][f][2 * e + 1]}) *
                        f32x2{rs[r][2 * e], rs[r][2 * e + 1]};
        pk[r][e] = __builtin_bit_cast(unsigned, __builtin_convertvector(t, bf16x2));
      }
    auto t0 = __builtin_amdgcn_permlane16_swap(pk[0][0], pk[1][0], false, false);
    auto t1 = __builtin_amdgcn_permlane16_swap(pk[0][1], pk[1][1], false, false);
    y[f] = u32x4{t0[0], t1[0], t0[1], t1[1]};
    st16_raw(y[f], rab, vo + (f & 3) * 1024, so + (f >> 2) * 4096);
  }
  st_guard(y[0], y[1], y[2], y[3], y[4]);
}

constexpr int K1_STAGE = 26 * 1024;
constexpr int K1_TR = 5 * K1_STAGE;
constexpr int K1_CS = K1_TR + 5 * 4096;

DI void k1_issue(u32x4 sa, u32x4 sbd, unsigned vo, unsigned la, unsigned lbb, unsigned soa, unsigned sob, unsigned w) {
  glds<0, 0>(sa, vo, la, soa);
  glds<1024, 8 * 1280>(sa, vo, la, soa);
  glds<2048, 16 * 1280>(sa, vo, la, soa);
  glds<3072, 24 * 1280>(sa, vo, la, soa);
  glds<0, 0>(sbd, vo, lbb, sob);
  glds<4096, 32 * 1280>(sbd, vo, lbb, sob);
  if (w < 2) glds<8192, 64 * 1280>(sbd, vo, lbb, sob);
}
struct RtUnit { bool first; unsigned ks, g; };
DI RtUnit rt_unit(unsigned u) {
  RtUnit r;
  r.first = u < 540;
  if (r.first) { r.ks = u / 27u; r.g = u - r.ks * 27u; }
  else { const unsigned v = u - 540; r.ks = v / 10u; r.g = v - r.ks * 10u; }
  return r;
}
DI void retile_issue(unsigned u, const u16* w1, const u16* w2, unsigned ldst, unsigned lane) {
  const RtUnit t = rt_unit(u);
  const unsigned ld = t.first ? F_ * 2 : D_ * 2;
  u32x4 srd = make_srd(t.first ? w1 : w2, D_ * F_ * 2);
  const unsigned vo = (lane >> 3) * ld + (lane & 7) * 16;
  const unsigned so = t.ks * 32 * ld + t.g * 128;
  glds<0, 0>(srd, vo, ldst, so);
  glds<1024, 0>(srd, vo, ldst, so + 8 * ld);
  glds<2048, 0>(srd, vo, ldst, so + 16 * ld);
  glds<3072, 0>(srd, vo, ldst, so + 24 * ld);
}
DI unsigned w1_off(unsigned rt, unsigned ks) {
  const unsigned c = rt / 7u, f = rt - c * 7, g = (c & 1) ? (f == 0 ? 6 : f - 1) : f;
  return WS_W1 + (c * 140 + (g < 4 ? ks * 4 + g : 80 + ks * 3 + g - 4)) * 1024;
}
DI void retile_finish(unsigned u, unsigned f0, rsrc_t rws, unsigned ldst, unsigned lane) {
  const RtUnit t = rt_unit(u);
  const unsigned i = lane & 15, q = lane >> 4;
  const unsigned base = ldst + (8 * q + (i >> 2)) * 128 + (i & 3) * 8 + f0 * 32;
  u32x4 v[2];
#pragma unroll
  for (int f = 0; f < 2; ++f) {
    s16x4 lo = __builtin_amdgcn_ds_read_tr16_b64_v4i16((LDS_AS s16x4*)(uintptr_t)(base + f * 32));
    s16x4 hi = __builtin_amdgcn_ds_read_tr16_b64_v4i16((LDS_AS s16x4*)(uintptr_t)(base + f * 32 + 512));
    u32x2 l2 = __builtin_bit_cast(u32x2, lo), h2 = __builtin_bit_cast(u32x2, hi);
    v[f] = u32x4{l2[0], l2[1], h2[0], h2[1]};
  }
  const unsigned rt = t.g * 4 + f0;
  if (t.first) {
    st16(v[0], rws, lane * 16, w1_off(rt, t.ks));
    st16(v[1], rws, lane * 16, w1_off(rt + 1, t.ks));
    float s[2];
#pragma unroll
    for (int f = 0; f < 2; ++f)
      s[f] = xsum16_32(((bflo(v[f][0]) + bfhi(v[f][0])) + (bflo(v[f][1]) + bfhi(v[f][1]))) + ((bflo(v[f][2]) + bfhi(v[f][2])) + (bflo(v[f][3]) + bfhi(v[f][3]))));
    st4(__float_as_uint(q == 0 ? s[0] : s[1]), rws, lane < 32 ? lane * 4 : OOB, WS_CS1 + (t.ks * F_ + rt * 16) * 4);
  } else {
    const unsigned c0 = rt / 5u, c1 = (rt + 1) / 5u;
    st16(v[0], rws, lane * 16, WS_W2 + ((c0 * 54 + t.ks) * 5 + (rt - c0 * 5)) * 1024);
    st16(v[1], rws, lane * 16, WS_W2 + ((c1 * 54 + t.ks) * 5 + (rt + 1 - c1 * 5)) * 1024);
  }
}

constexpr int WS1 = 3, WS2 = 6;
constexpr int PUB1 = 13;
constexpr int PUBG = 11;
constexpr int KP3 = 3, KP1 = 3;
constexpr int RGAP = 10, ASLEEP2 = 40, WDPRE = 6;
constexpr int PD4 = 13, PDX = 15, PD2 = 30, P5DELAY0 = 4, P5DELAY = 1;

constexpr int KM_LDS = 160 * 1024;
constexpr int KM_CS1 = KM_LDS - 1024;
constexpr int G2_A = 80 * 1024, G2_AS = 16 * 1024, G2_BS = 20 * 1024;
constexpr int G4_LATE = 80 * 1024;

DI gu64* flag_ptr(char* ws, unsigned f) { return (gu64*)(ws + WS_FLG + f * 128); }
DI void raise(char* ws, unsigned f, u64 epoch, unsigned lane) {
  if (lane < 16) __hip_atomic_store(flag_ptr(ws, f) + lane, epoch, RLX_AGENT);
}
DI void publish(char* ws, unsigned f, u64 epoch, unsigned lane) {
  wait_vm<0>();
  raise(ws, f, epoch, lane);
}
DI void poll0(char* ws, unsigned f, unsigned n, u64 epoch, unsigned lane) {
  gu64* p = flag_ptr(ws, f);
  bool ok = lane >= n;
#pragma unroll 1
  for (unsigned s = 0; s < (1u << 16); ++s) {
    if (!ok) ok = __hip_atomic_load(p, RLX_AGENT) == epoch;
    if (__all(ok)) break;
    __builtin_amdgcn_s_sleep(1);
  }
  asm volatile("" ::: "memory");
}
template <int DELAY> DI void poll(char* ws, unsigned f, unsigned n, u64 epoch, unsigned lane) {
  __builtin_amdgcn_s_sleep(DELAY);
  poll0(ws, f, n, epoch, lane);
}
DI void warm(unsigned ldump, u32x4 srd, unsigned voff, unsigned soff) {
  asm volatile("s_mov_b32 m0, %0\n\ts_nop 0\n\tbuffer_load_dword %1, %2, %3 offen lds" :: "s"(ldump), "v"(voff), "s"(srd), "s"(soff) : "memory");
}
DI void glds4_sc1(u32x4 srd, unsigned voff, unsigned l, unsigned so) {
  asm volatile("s_mov_b32 m0, %0\n\ts_nop 0\n\tbuffer_load_dwordx4 %1, %2, %3 offen sc1 lds\n\tbuffer_load_dwordx4 %1, %2, %3 offen offset:1024 sc1 lds\n\t"
               "buffer_load_dwordx4 %1, %2, %3 offen offset:2048 sc1 lds\n\tbuffer_load_dwordx4 %1, %2, %3 offen offset:3072 sc1 lds"
               :: "s"(l), "v"(voff), "s"(srd), "s"(so) : "memory");
}
DI void glds1_sc1(u32x4 srd, unsigned voff, unsigned l, unsigned so) {
  asm volatile("s_mov_b32 m0, %0\n\ts_nop 0\n\tbuffer_load_dwordx4 %1, %2, %3 offen sc1 lds" :: "s"(l), "v"(voff), "s"(srd), "s"(so) : "memory");
}
template <int N> DI void glds_run_sc1(u32x4 srd, unsigned voff, unsigned l, unsigned so) {
#pragma unroll
  for (int g = 0; g < N / 4; ++g) glds4_sc1(srd, voff, l + g * 4096, so + g * 4096);
#pragma unroll
  for (int g = N / 4 * 4; g < N; ++g) glds1_sc1(srd, voff, l + g * 1024, so + g * 1024);
}

struct ResTile { u32x4 r0, r1; u32x2 r2; };
struct TileOff { unsigned a0, a1, a2; };
DI TileOff tile_offsets(bool rm, unsigned row, unsigned ct, unsigned q) {
  const unsigned cA = ct * 80 + 16 * (q & 1) + 8 * (q >> 1), cS = ct * 80 + 64 + 4 * q;
  TileOff t;
  if (rm) { t.a0 = (row * D_ + cA) * 2; t.a1 = t.a0 + 64; t.a2 = (row * D_ + cS) * 2; }
  else { t.a0 = foff<20>(row, cA); t.a1 = foff<20>(row, cA + 32); t.a2 = foff<20>(row, cS); }
  asm volatile("" : "+v"(t.a0), "+v"(t.a1), "+v"(t.a2));
  return t;
}
DI ResTile load_res(rsrc_t rr, const TileOff& t) {
  ResTile rt;
  rt.r0 = ld16u(rr, t.a0, 0); rt.r1 = ld16u(rr, t.a1, 0); rt.r2 = ld8u(rr, t.a2, 0);
  return rt;
}
DI void finish_tile(f32x4 (&v)[5], const ResTile& rt, rsrc_t rout, const TileOff& o, bool stats, rsrc_t rst, unsigned sto) {
#pragma unroll
  for (int p = 0; p < 2; ++p)
#pragma unroll
    for (int r = 0; r < 4; ++r) {
      auto t = __builtin_amdgcn_permlane16_swap(__float_as_uint(v[2 * p][r]), __float_as_uint(v[2 * p + 1][r]), false, false);
      v[2 * p][r] = __uint_as_float(t[0]); v[2 * p + 1][r] = __uint_as_float(t[1]);
    }
  u32x4 y0, y1; u32x2 y2;
#pragma unroll
  for (int j = 0; j < 2; ++j) {
    y0[j] = pk2(v[0][2 * j] + bflo(rt.r0[j]), v[0][2 * j + 1] + bfhi(rt.r0[j]));
    y0[2 + j] = pk2(v[1][2 * j] + bflo(rt.r0[2 + j]), v[1][2 * j + 1] + bfhi(rt.r0[2 + j]));
    y1[j] = pk2(v[2][2 * j] + bflo(rt.r1[j]), v[2][2 * j + 1] + bfhi(rt.r1[j]));
    y1[2 + j] = pk2(v[3][2 * j] + bflo(rt.r1[2 + j]), v[3][2 * j + 1] + bfhi(rt.r1[2 + j]));
    y2[j] = pk2(v[4][2 * j] + bflo(rt.r2[j]), v[4][2 * j + 1] + bfhi(rt.r2[j]));
  }
  if (stats) {
    st16_raw(y0, rout, o.a0, 0); st16_raw(y1, rout, o.a1, 0); st8(y2, rout, o.a2, 0);
  } else {
    __builtin_amdgcn_raw_buffer_store_b128(y0, rout, o.a0, 0, 0);
    __builtin_amdgcn_raw_buffer_store_b128(y1, rout, o.a1, 0, 0);
    __builtin_amdgcn_raw_buffer_store_b64(y2, rout, o.a2, 0, 0);
  }
  st_guard(y0, y1);
  if (stats) {
    float s1a = 0.f, s1b = 0.f, s2a = 0.f, s2b = 0.f;
#pragma unroll
    for (int j = 0; j < 4; ++j) {
      s1a = dot2(y0[j], 0x3f803f80u, s1a); s2a = dot2(y0[j], y0[j], s2a);
      s1b = dot2(y1[j], 0x3f803f80u, s1b); s2b = dot2(y1[j], y1[j], s2b);
    }
    s1a = dot2(y2[0], 0x3f803f80u, s1a); s2a = dot2(y2[0], y2[0], s2a);
    s1b = dot2(y2[1], 0x3f803f80u, s1b); s2b = dot2(y2[1], y2[1], s2b);
    const float s1 = xsum16_32(s1a + s1b), s2 = xsum16_32(s2a + s2b);
    st8(u32x2{__float_as_uint(s1), __float_as_uint(s2)}, rst, sto, 0);
  }
}

DI constexpr unsigned wd_pos(int c) { return c < 8 ? 10 * c : 60 + 10 * c; }
constexpr int ORD[14] = {0, 1, 4, 5, 7, 8, 11, 12, 2, 3, 6, 9, 10, 13};
DI constexpr int g4_late(int t) { return (t >= 4 && t < 7) || t >= 11; }
DI constexpr int g4_slot(int t) { return (t < 4 ? t : (t < 7 ? t - 4 : (t < 11 ? t - 7 : t - 11))) * 5120; }

#define PHASE_IDS \
  const unsigned lane = opq_v(tid0) & 63, w = opq_s(w0), bid = opq_s(blockIdx.x); \
  const unsigned xcd = bid & 7, idx = bid >> 3, i16 = lane & 15, q = lane >> 4; \
  char* const ws = opq_p(ws_); \
  (void)xcd; (void)idx; (void)i16; (void)q; (void)ws;
#define IDS_G2 const unsigned kq = xcd & 3, P = (xcd >> 2) * 8 + (idx >> 2), c4 = idx & 3;
#define G2_LA(h) (lb + G2_A + ((w >> 1) * 4 + ((2 * (w & 1) + (h) + 3 - kq) & 3)) * 2048)
#define IDS_G3 const unsigned P = (xcd >> 1) * 4 + (idx >> 3), ct = (xcd & 1) * 8 + (idx & 7);
#define C8_OF ((xcd & 1) * 4 + (idx & 3))
#define IDS_G4 const unsigned rt64 = (xcd >> 1) * 8 + (idx >> 2), c8 = C8_OF;
#define LDX(r, k) ld16(rx, lane * 16 + ((k) & 3) * 1024, sox + (r) * 20480 + ((k) >> 2) * 4096)

__global__ __launch_bounds__(512, 2) void k_all(const u16* x0_, const u16* Sre_, const u16* Sim_, const u16* Dre_, const u16* Dim_,
                                               const u16* w1_, const u16* w2_, char* ws_, u16* out_) {
  const unsigned tid0 = threadIdx.x, wall = rfl(tid0 >> 6), w0 = wall & 3;
  const unsigned lb = lds_base();
  const char* code; unsigned code_len;
  asm volatile("s_getpc_b64 %0\n.Lcw_begin:\n\ts_mov_b32 %1, .Lcw_end-.Lcw_begin" : "=s"(code), "=s"(code_len));
  u64 epoch;

  if (__builtin_expect(wall >= 4, 1)) {
    const unsigned sink = lb + K1_CS + 1024;
    unsigned ep0, ep1;
    asm volatile("global_load_dword %0, %2, %3 sc1\n\tglobal_load_dword %1, %2, %3 offset:4 sc1"
                 : "=&v"(ep0), "=&v"(ep1) : "v"(WS_FLG + FL_E * 128), "s"(ws_) : "memory");
    {
      PHASE_IDS
      IDS_G3
      const u32x4 sa = make_srd(opq_p(x0_), S_ * D_ * 2), sbd = make_srd(ct < 8 ? opq_p(Dre_) : opq_p(Dim_), D_ * D_ * 2);
      const unsigned vo = (lane >> 3) * 1280 + (((lane & 7) ^ (lane >> 3)) << 4);
      const unsigned soa = (P * 128 + w * 32) * 1280, sob = ((ct & 7) * 80 + w * 8) * 1280;
      const unsigned la = lb + w * 4096, lbb = lb + 16384 + w * 1024;
      const unsigned u0 = bid * 4 + w, u1 = 1024 + bid;
      const bool extra = w == 3 && bid < 56;
      const unsigned rot = ((idx & 7) + 3 * (idx >> 3)) % 10u;
      auto ko = [&](unsigned s) { const unsigned c = s + rot; return (c >= 10 ? c - 10 : c) * 128; };
      asm volatile("s_nop 4" ::: "memory");
      k1_issue(sa, sbd, vo, la, lbb, soa + ko(0), sob + ko(0), w);
      k1_issue(sa, sbd, vo, la + K1_STAGE, lbb + K1_STAGE, soa + ko(1), sob + ko(1), w);
      if (w == 3) warm(sink, make_srd(code, code_len), lane < 16 ? idx * 2048 + lane * 128 : OOB, 0);
      wait_vm<6>();
      {
        unsigned lo, hi;
        asm volatile("v_readfirstlane_b32 %0, %2\n\tv_readfirstlane_b32 %1, %3\n\ts_nop 4" : "=s"(lo), "=s"(hi) : "v"(ep0), "v"(ep1));
        epoch = (u64)lo + ((u64)hi << 32) + 1;
      }
      bar();
      k1_issue(sa, sbd, vo, la + 2 * K1_STAGE, lbb + 2 * K1_STAGE, soa + ko(2), sob + ko(2), w);
      const u32x4 ss = make_srd((xcd & 2) ? Sim_ : Sre_, S_ * S_ * 2);
      const unsigned vs = (lane & 31) * 4096 + (lane >> 5) * 128;
      const unsigned sos = ((xcd >> 2) * 1024 + idx * 32) * 4096 + (xcd & 1) * 2048 + w * 256;
      unsigned so = 0;
#pragma unroll 1
      for (int i = 0; i < 10; ++i) {
        if (i == 0) wait_vm<6>(); else if (i == 1) wait_vm<2 * 6>(); else if (i < 6) wait_vm<3 * 6>();
        else if (i == 6) wait_vm<2 * 6 + 4>(); else if (i == 7) wait_vm<6 + 4>(); else wait_vm<4 + (WS2 >= 4 && WS2 < 8) + (WS1 >= 4 && WS1 < 8)>();
        bar();
        if (i == 0) {
          k1_issue(sa, sbd, vo, la + 3 * K1_STAGE, lbb + 3 * K1_STAGE, soa + ko(3), sob + ko(3), w);
          k1_issue(sa, sbd, vo, la + 4 * K1_STAGE, lbb + 4 * K1_STAGE, soa + ko(4), sob + ko(4), w);
        } else if (i == 1) {
          k1_issue(sa, sbd, vo, la, lbb, soa + ko(5), sob + ko(5), w);
          k1_issue(sa, sbd, vo, la + K1_STAGE, lbb + K1_STAGE, soa + ko(6), sob + ko(6), w);
        } else if (i < 5) k1_issue(sa, sbd, vo, la + so, lbb + so, soa + ko(i + 5), sob + ko(i + 5), w);
        so = so == 4 * K1_STAGE ? 0 : so + K1_STAGE;
        if (i == WS1) warm(sink, ss, vs, sos);
        if (i == WS2) warm(sink, ss, vs, sos + 1024);
        if (i == 5) {
          retile_issue(u0, w1_, w2_, lb + K1_TR + w * 4096, lane);
          if (extra) retile_issue(u1, w1_, w2_, lb + K1_TR + 4 * 4096, lane);
        }
      }
      bar();
    }
    bf16x8 wr[10][5];
#pragma unroll
    for (int t = 0; t < 10; ++t)
#pragma unroll
      for (int f = 0; f < 5; ++f) wr[t][f] = bf16x8{};
#pragma unroll 1
    for (int L = 0; L < 2; ++L) {
      {
        PHASE_IDS
        IDS_G2
        const u32x4 sa = make_srd(kq < 2 ? opq_p(Sre_) : opq_p(Sim_), S_ * S_ * 2);
        const u32x4 sb_ = make_srd(ws + (L ? WS_AB2 : WS_AB1), SZ_AB);
        const unsigned va = (lane >> 3) * 4096 + (((lane & 7) ^ (lane >> 3)) << 4), vb = lane * 16;
        const unsigned soa = (P * 128 + w * 32) * 4096 + (kq & 1) * 2048;
        const unsigned sob = ((2 * c4 + (w >> 1)) * 128 + kq * 32 + (w & 1)) * 5 * 1024;
        const unsigned la = lb + G2_A + w * 4096, lbb = lb + w * 5120;
        const unsigned la0 = G2_LA(0), la1 = G2_LA(1);
        const unsigned ct = (xcd & 1) * 8 + (idx & 7);
        auto kc = [&](unsigned s) { return L ? 2 * (s & 7) + ((s ^ (s >> 3)) & 1) : s; };
        asm volatile("s_nop 4" ::: "memory");
        auto issue_a = [&](unsigned l, unsigned so) {
          glds<0, 0>(sa, va, la0 + l, so); glds<1024, 8 * 4096>(sa, va, la0 + l, so);
          glds<0, 16 * 4096>(sa, va, la1 + l, so); glds<1024, 24 * 4096>(sa, va, la1 + l, so);
        };
        if (L == 0) {
#pragma unroll
          for (int s = 0; s < 3; ++s) issue_a(s * G2_AS, soa + s * 128);
          const unsigned u0 = bid * 4 + w, u1 = 1024 + bid;
          const rsrc_t rws = make_rsrc(ws, WS_END);
          wait_vm<12>();
          retile_finish(u0, 0, rws, lb + K1_TR + w * 4096, lane);
          retile_finish(u0, 2, rws, lb + K1_TR + w * 4096, lane);
          if (w == 3 && bid < 56) {
            retile_finish(u1, 0, rws, lb + K1_TR + 4 * 4096, lane);
            retile_finish(u1, 2, rws, lb + K1_TR + 4 * 4096, lane);
          }
          __builtin_amdgcn_s_sleep(P5DELAY0);
        } else {
          issue_a(3 * G2_AS, soa + kc(3) * 128);
          __builtin_amdgcn_s_sleep(P5DELAY);
        }
        const unsigned f5 = FL_5 + ((L * 4 + kq) * 4 + c4) * 64 + ((i16 >> 1) * 2 + (w >> 1)) * 4 + (L ? 2 * (w & 1) + (i16 & 1) : (w & 1) + 2 * (i16 & 1));
        poll0(ws, f5, 16, epoch, lane);
        wait_vm<0>();
        glds_run_sc1<5>(sb_, vb, lbb, sob);
        glds_run_sc1<5>(sb_, vb, lbb + G2_BS, sob + kc(1) * 10240);
        if (L == 0) {
          raise(ws, FL_W + bid * 4 + w, epoch, lane);
          if (w == 3 && bid < 56) raise(ws, FL_W + 1024 + bid, epoch, lane);
          wait_vm<6>();
        } else wait_vm<5>();
        bar();
        glds_run_sc1<5>(sb_, vb, lbb + 2 * G2_BS, sob + kc(2) * 10240);
        if (L == 0) issue_a(3 * G2_AS, soa + kc(3) * 128);
        glds_run_sc1<5>(sb_, vb, lbb + 3 * G2_BS, sob + kc(3) * 10240);
        const u32x4 sw = make_srd(ws + WS_W1, SZ_W1);
        const unsigned sow = ct * 140 * 1024;
        unsigned fw, fw0 = 0, fw1 = 0;
        {
          const unsigned n = w * 42 + (lane < 42 ? lane : 0), k3 = n / 3u, g3 = (7 * ct >> 2) + (n - k3 * 3), n2 = n - 60;
          fw = FL_W + (n < 60 ? k3 * 27 + (g3 < 26 ? g3 : 26) : 540 + (n2 >> 1) * 10 + (5 * C8_OF >> 2) + (n2 & 1));
          if (L) fw = f5 + 1024;
        }
#pragma unroll 1
        for (int s = 0; s < 16; ++s) {
          if (s == 0) wait_vm<10>();
          if (s == 1) wait_vm<14>();
          wait_vm<18>();
          if (s == (L ? 4 : 9)) {
            asm volatile("" : "+v"(fw0), "+v"(fw1));
            if (!__all(fw0 == (unsigned)epoch && fw1 == (unsigned)(epoch >> 32))) poll0(ws, fw, 42, epoch, lane);
          }
          bar();
          const unsigned sl = s & 3;
          if (s < 12) {
            issue_a(sl * G2_AS, soa + kc(s + 4) * 128);
            glds_run_sc1<5>(sb_, vb, lbb + sl * G2_BS, sob + kc(s + 4) * 10240);
            if (s == (L ? 1 : 6))
              asm volatile("global_load_dword %0, %2, %3 sc1\n\tglobal_load_dword %1, %2, %3 offset:4 sc1"
                           : "+v"(fw0), "+v"(fw1) : "v"(WS_FLG + fw * 128), "s"(ws) : "memory");
          } else {
            glds_run_sc1<5>(sw, vb, lbb + sl * G2_BS, sow + w * 5120 + sl * G2_BS);
            if (s < 15) glds_run_sc1<4>(sw, vb, la + sl * G2_AS, sow + G2_A + w * 4096 + sl * G2_AS);
            else glds_run_sc1<3>(sw, vb, lb + G2_A + 3 * G2_AS + w * 3072, sow + G2_A + 3 * G2_AS + w * 3072);
          }
        }
      }
      {
        PHASE_IDS
        IDS_G4
        const unsigned ct = (xcd & 1) * 8 + (idx & 7);
        if (w >= 2) {
          const rsrc_t rc = make_rsrc(ws + WS_CS1, 20 * F_ * 4);
          const unsigned c = (w - 2) * 64 + lane, col = ct * 112 + c;
          const unsigned vo = c < 112 && col < F_ ? col * 4 : OOB;
          float csp = 0.f;
#pragma unroll
          for (int k = 0; k < 20; ++k) csp += __uint_as_float(__builtin_amdgcn_raw_buffer_load_b32(rc, vo, k * F_ * 4, 16));
          lds_wr1(lb + KM_CS1 + c * 4, csp);
        }
        const unsigned so = (c8 * 54 + w * 14) * 5 * 1024;
        wait_vm<0>();
        if (L == 0) {
          const rsrc_t rw = make_rsrc(ws + WS_W2, SZ_W2);
          const unsigned vl = lane * 16, vz = lane * 16 + (w < 3 ? 0u : OOB);
          sfor<0, 50>([&](auto N) {
            constexpr int t = 4 + N.v / 5, n = ORD[t] * 5 + N.v % 5;
            wr[t - 4][N.v % 5] = ld16(rw, (ORD[t] < 12 ? vl : vz) + (n & 3) * 1024, so + (n >> 2) * 4096);
            if constexpr (N.v % 5 == 4 && N.v < 49) { sb(); __builtin_amdgcn_s_sleep(RGAP); sb(); }
          });
          sb();
        }
        wait_lgkm0();
        bar();
        bar();
        const u32x4 s2 = make_srd(ws + WS_W2, SZ_W2);
        const unsigned sg = rfl(so);
        asm volatile("s_nop 4" ::: "memory");
        glds_run_sc1<10>(s2, lane * 16, lb + w * 20480, sg);
        glds_run_sc1<10>(s2, lane * 16, lb + w * 20480 + 10240, sg + 4 * 5120);
        wait_vm<0>();
        const unsigned pe = lb + w * 20480 + lane * 16, pl = lb + G4_LATE + w * 15360 + lane * 16;
        auto put = [&](auto T0, auto T1) {
          sfor<T0.v * 5, T1.v * 5>([&](auto N) {
            constexpr int t = N.v / 5, f = N.v % 5;
            *(LDS_AS bf16x8*)(ldsp)(uintptr_t)((g4_late(t) ? pl : pe) + g4_slot(t) + f * 1024) = wr[t - 4][f];
          });
          wait_lgkm0();
        };
        bar();
        put(IC<4>{}, IC<7>{});
        bar();
        put(IC<7>{}, IC<11>{});
        bar();
        put(IC<11>{}, IC<14>{});
        bar();
      }
      if (L) { bar(); break; }
      {
        PHASE_IDS
        IDS_G3
        const u32x4 sd = make_srd(ct < 8 ? opq_p(Dre_) : opq_p(Dim_), D_ * D_ * 2);
        const unsigned vo = (lane >> 3) * 1280 + (((lane & 7) ^ (lane >> 3)) << 4);
        const unsigned so = (ct & 7) * 80 * 1280;
        sfor<0, 10>([&](auto G) { glds<G.v * 1024, G.v * 8 * 1280>(sd, vo, lb + 20 * w * 1024, so + 2 * w * 128); });
        sfor<0, WDPRE>([&](auto G) { glds<G.v * 1024, G.v * 8 * 1280>(sd, vo, lb + (20 * w + 10) * 1024, so + (2 * w + 1) * 128); });
        bar();
        sfor<WDPRE, 10>([&](auto G) { glds<G.v * 1024, G.v * 8 * 1280>(sd, vo, lb + (20 * w + 10) * 1024, so + (2 * w + 1) * 128); });
        sfor<0, 5>([&](auto G) { glds<G.v * 1024, G.v * 8 * 1280>(sd, vo, lb + (140 + w * 5) * 1024, so + (8 + (w >> 1)) * 128 + (w & 1) * 40 * 1280); });
        wait_vm<0>();
        bar();
      }
      {
        PHASE_IDS
        IDS_G2
        const u32x4 sa = make_srd(kq < 2 ? opq_p(Sre_) : opq_p(Sim_), S_ * S_ * 2);
        const unsigned va = (lane >> 3) * 4096 + (((lane & 7) ^ (lane >> 3)) << 4);
        const unsigned soa = (P * 128 + w * 32) * 4096 + (kq & 1) * 2048;
        __builtin_amdgcn_s_sleep(ASLEEP2);
#pragma unroll
        for (int s = 0; s < 3; ++s) {
          const unsigned l0 = G2_LA(0) + s * G2_AS, l1 = G2_LA(1) + s * G2_AS, so = soa + (2 * s + (s & 1)) * 128;
          glds<0, 0>(sa, va, l0, so); glds<1024, 8 * 4096>(sa, va, l0, so);
          glds<0, 16 * 4096>(sa, va, l1, so); glds<1024, 24 * 4096>(sa, va, l1, so);
        }
        bar();
      }
    }
    return;
  }

  {
    const u64 e = __hip_atomic_load(flag_ptr(ws_, FL_E), RLX_AGENT);
    epoch = (u64)rfl((unsigned)e) + ((u64)rfl((unsigned)(e >> 32)) << 32) + 1;
  }
#pragma unroll 1
  for (int L = 0; L < 2; ++L) {
    {
      PHASE_IDS
      IDS_G3
      f32x4 acc[2][5];
      float mu[2][4], rs[2][4], csv[5];
      const float sign = ct < 8 ? 1.f : -1.f;
      const unsigned f5 = FL_5 + (((ct >> 3) * 2 + (P >> 3)) * 4 + ((ct & 7) >> 1)) * 64 + ((P & 7) * 2 + (ct & 1)) * 4 + w;
      if (L == 0) {
        const unsigned ra0 = lb + w * 4096 + i16 * 128 + ((q ^ (lane & 7)) << 4);
        const unsigned rb0 = lb + 16384 + i16 * 128 + ((q ^ (lane & 7)) << 4);
        const f32x4 z4 = {0.f, 0.f, 0.f, 0.f};
        f32x4 cs0 = z4, cs1 = z4, rs_[2] = {z4, z4}, rq_[2] = {z4, z4};
#pragma unroll
        for (int r = 0; r < 2; ++r)
#pragma unroll
          for (int f = 0; f < 5; ++f) acc[r][f] = z4;
        bf16x8 fr[2][8];
        bf16x8 one = __builtin_bit_cast(bf16x8, u32x4{0x3f803f80u, 0x3f803f80u, 0x3f803f80u, 0x3f803f80u});
        auto half = [&](auto KK, unsigned so) {
          constexpr int kk = KK.v;
          const unsigned pa = (ra0 ^ ((kk ^ 1) * 64)) + so, pb = (rb0 ^ ((kk ^ 1) * 64)) + so;
          sfor<0, 16>([&](auto M) {
            constexpr int m = M.v;
            if constexpr (m < 10) mma(acc[m & 1][m >> 1], fr[kk][m & 1], fr[kk][2 + (m >> 1)]);
            else if constexpr (m == 10) mma(cs0, one, fr[kk][7]);
            else if constexpr (m == 11) mma(cs1, one, fr[kk][6]);
            else if constexpr (m < 14) mma(rs_[m - 12], fr[kk][m - 12], one);
            else mma(rq_[m - 14], fr[kk][m - 14], fr[kk][m - 14]);
            constexpr int n = m >> 1;
            if constexpr ((m & 1) == 0) fr[kk ^ 1][n] = n < 2 ? lds_rd(pa + n * 2048) : (n < 7 ? lds_rd(pb + (n - 2) * 2048) : lds_rd(pb + w * 2048));
            sb();
          });
        };
        asm volatile("s_nop 1" : "+v"(cs0), "+v"(cs1), "+v"(rs_[0]), "+v"(rs_[1]), "+v"(rq_[0]), "+v"(rq_[1]), "+v"(one));
        fence_all(acc);
        if (w == 0 && idx < TOUCH_N) {
          const void* p = idx == 0 ? (const void*)out_ : (idx == 1 ? (const void*)ws : (idx == 2 ? (const void*)Sre_ : (idx == 3 ? (const void*)Sim_ : (idx == 4 ? (const void*)w1_ : (const void*)w2_))));
          const unsigned n = idx == 0 ? SZ_X : (idx == 1 ? WS_END : (idx < 4 ? S_ * S_ * 2 : D_ * F_ * 2));
          asm volatile("s_nop 4" ::: "memory");
          warm(lb + K1_CS + 1024, make_srd(p, n), lane << (idx == 1 ? 20 : (idx == 2 || idx == 3 ? 17 : 16)), 0);
        }
        bar();
        fr[0][0] = lds_rd(ra0); fr[0][1] = lds_rd(ra0 + 2048);
#pragma unroll
        for (int f = 0; f < 5; ++f) fr[0][2 + f] = lds_rd(rb0 + f * 2048);
        fr[0][7] = lds_rd(rb0 + w * 2048);
        sb();
        unsigned so = 0;
#pragma unroll 1
        for (int i = 0; i < 10; ++i) {
          half(IC<0>{}, so);
          wait_lgkm0();
          bar();
          so = so == 4 * K1_STAGE ? 0 : so + K1_STAGE;
          half(IC<1>{}, so);
        }
        fence_all(acc);
        asm volatile("" : "+v"(cs0), "+v"(cs1), "+v"(rs_[0]), "+v"(rs_[1]), "+v"(rq_[0]), "+v"(rq_[1]));
        const unsigned csb = lb + K1_CS;
        if (q == 0) {
          lds_wr1(csb + (w * 16 + i16) * 4, cs0[0]);
          if (w == 0) lds_wr1(csb + (64 + i16) * 4, cs1[0]);
        }
        wait_lgkm0();
        bar();
#pragma unroll
        for (int f = 0; f < 5; ++f) csv[f] = lds_rd1(csb + (f * 16 + i16) * 4);
#pragma unroll
        for (int r = 0; r < 2; ++r)
#pragma unroll
          for (int j = 0; j < 4; ++j) {
            float m_, r_;
            ln_coef(rs_[r][j], __shfl(rq_[r][j], q * 20 + j), m_, r_);
            mu[r][j] = -m_; rs[r][j] = sign * r_;
          }
        if (w == 0) {
          const rsrc_t rc = make_rsrc(ws + WS_CSD, 256 * 320);
          st4(__float_as_uint(q == 0 ? csv[0] : (q == 1 ? csv[1] : (q == 2 ? csv[2] : csv[3]))), rc, lane * 4, bid * 320);
          st4(__float_as_uint(csv[4]), rc, q == 0 ? 256 + lane * 4 : OOB, bid * 320);
        }
        g1_store(acc, mu, rs, csv, make_rsrc(ws + WS_AB1, SZ_AB), P, ct, w, i16, q);
        publish(ws, f5, epoch, lane);
      } else {
        {
          const rsrc_t rc = make_rsrc(ws + WS_CSD, 256 * 320);
#pragma unroll
          for (int f = 0; f < 5; ++f) csv[f] = __uint_as_float(__builtin_amdgcn_raw_buffer_load_b32(rc, (f * 16 + i16) * 4, bid * 320, 16));
        }
        const unsigned h0 = P & 1;
        poll<PD4>(ws, FL_4 + P * 64 + ((i16 >> 3) * 4 + w) * 8 + (i16 & 7), 16, epoch, lane);
        const rsrc_t rx = make_rsrc(ws + WS_X2, SZ_X);
        const unsigned sox0 = (P * 8 + h0 * 4 + w) * 20 * 1024, sox1 = (P * 8 + (h0 ^ 1) * 4 + w) * 20 * 1024;
        bf16x8 a[40];
#define LDA(n) a[n] = ld16(rx, lane * 16 + ((n) & 3) * 1024, ((n) < 20 ? sox0 : sox1) + (((n) % 20) >> 2) * 4096)
        sfor<0, 6>([&](auto N) { LDA(N.v); });
        const rsrc_t rst = make_rsrc(ws + WS_ST2, SZ_ST);
        const unsigned sto = ((lane >> 5) * 4 * S_ + P * 128 + (((lane >> 4) & 1) ^ h0) * 64 + w * 16 + i16) * 8;
        const u32x2 sp0 = ld8u(rst, sto, 0), sp1 = ld8u(rst, sto, S_ * 8), sp2 = ld8u(rst, sto, 2 * S_ * 8), sp3 = ld8u(rst, sto, 3 * S_ * 8);
        sb();
        bar();
        bf16x8 b[2][5];
        const unsigned rb = lb + i16 * 128 + ((q ^ (lane & 7)) << 4);
        const rsrc_t rab = make_rsrc(ws + WS_AB2, SZ_AB);
        const unsigned vo = ((2 * (w & 1) + (q >> 1)) * 16 + i16) * 16 + (q & 1) * 8;
        const unsigned so = ((ct & 7) * 128 + (ct >> 3) * 64 + P * 4 + (w >> 1)) * 5 * 1024;
        auto put = [&](auto H) {
          constexpr int hh = H.v;
#pragma unroll
          for (int f = 0; f < 5; ++f) {
            const f32x2 cs = {csv[f], csv[f]};
            u32x2 y;
#pragma unroll
            for (int e = 0; e < 2; ++e) {
              const f32x2 t = __builtin_elementwise_fma(f32x2{mu[hh][2 * e], mu[hh][2 * e + 1]}, cs, f32x2{acc[hh][f][2 * e], acc[hh][f][2 * e + 1]}) *
                              f32x2{rs[hh][2 * e], rs[hh][2 * e + 1]};
              y[e] = __builtin_bit_cast(unsigned, __builtin_convertvector(t, bf16x2));
            }
            st8(y, rab, vo + (f & 3) * 1024, so + (hh ^ h0) * 10240 + (f >> 2) * 4096);
          }
        };
#pragma unroll
        for (int f = 0; f < 5; ++f) b[0][f] = lds_rd(rb + wd_pos(0) * 1024 + f * 2048);
        sb();
        sfor<0, 40>([&](auto N) {
          constexpr int n = N.v, hh = n / 20, k = n % 20, kn = (k + 1) % 20;
          if constexpr (n == 2 * KP1) {
            float s1 = (__uint_as_float(sp0[0]) + __uint_as_float(sp1[0])) + (__uint_as_float(sp2[0]) + __uint_as_float(sp3[0]));
            float s2 = (__uint_as_float(sp0[1]) + __uint_as_float(sp1[1])) + (__uint_as_float(sp2[1]) + __uint_as_float(sp3[1]));
            auto ta = __builtin_amdgcn_permlane32_swap(__float_as_uint(s1), __float_as_uint(s1), false, false);
            auto tb = __builtin_amdgcn_permlane32_swap(__float_as_uint(s2), __float_as_uint(s2), false, false);
            float m, rr;
            ln_coef(__uint_as_float(ta[0]) + __uint_as_float(ta[1]), __uint_as_float(tb[0]) + __uint_as_float(tb[1]), m, rr);
            m = -m; rr *= sign;
#pragma unroll
            for (int r = 0; r < 2; ++r)
#pragma unroll
              for (int e = 0; e < 4; ++e) { mu[r][e] = __shfl(m, r * 16 + q * 4 + e); rs[r][e] = __shfl(rr, r * 16 + q * 4 + e); }
            asm volatile("" : "+v"(mu[0][0]), "+v"(mu[0][1]), "+v"(mu[0][2]), "+v"(mu[0][3]), "+v"(mu[1][0]), "+v"(mu[1][1]), "+v"(mu[1][2]), "+v"(mu[1][3]),
                              "+v"(rs[0][0]), "+v"(rs[0][1]), "+v"(rs[0][2]), "+v"(rs[0][3]), "+v"(rs[1][0]), "+v"(rs[1][1]), "+v"(rs[1][2]), "+v"(rs[1][3]));
            sb();
          }
          sfor<0, 5>([&](auto F) {
            constexpr int f = F.v;
            if constexpr (k == 0) mma0(acc[hh][f], a[n], b[n & 1][f]); else mma(acc[hh][f], a[n], b[n & 1][f]);
            if constexpr (n + 1 < 40) b[(n + 1) & 1][f] = lds_rd((rb ^ ((kn & 1) * 64)) + wd_pos(kn >> 1) * 1024 + f * 2048);
            if constexpr ((f == 1 || f == 3) && 6 + 2 * n + (f >> 1) < 40) LDA(6 + 2 * n + (f >> 1));
            sb();
          });
          if constexpr (n == 21) { pin_acc(acc[0]); put(IC<0>{}); sb(); }
          if constexpr (n == 20 + PUBG) publish(ws, f5 + 1024, epoch, lane);
        });
        fence_acc(acc[1]);
        bar();
        put(IC<1>{});
        publish(ws, f5 + 2048, epoch, lane);
      }
    }
    {
      PHASE_IDS
      IDS_G2
      const unsigned m = w >> 1, n = w & 1;
      const unsigned ra = lb + G2_A + (m * 64 + i16) * 128 + ((q ^ (lane & 7)) << 4);
      const unsigned rb = lb + n * 10240 + lane * 16;
      const unsigned row = P * 128 + m * 64 + kq * 16 + i16, ct = 2 * c4 + n;
      const TileOff ro = tile_offsets(L == 0, row, ct, q), oo = tile_offsets(false, row, ct, q);
      const rsrc_t rxp = make_rsrc(ws + WS_XP + L * SZ_XP, SZ_XP);
      const unsigned xo = ((P * 4 + c4) * 48 + w) * XSLOT;
      f32x4 acc[4][5];
#pragma unroll
      for (int i = 0; i < 4; ++i)
#pragma unroll
        for (int f = 0; f < 5; ++f) acc[i][f] = f32x4{0.f, 0.f, 0.f, 0.f};
      bf16x8 fa[2][4], fb[2][5];
      unsigned ra1 = ra ^ 64, rb2 = rb + 2 * G2_BS;
      asm volatile("" : "+v"(ra1), "+v"(rb2));
      auto half = [&](auto KK, auto SL) {
        constexpr int kk = KK.v, sl = SL.v;
        sfor<0, 20>([&](auto M) {
          constexpr int mm = M.v;
          mma(acc[mm / 5][mm % 5], fb[kk][mm % 5], fa[kk][mm / 5]);
          if constexpr (mm < 5) fb[kk ^ 1][mm] = lds_rd((sl < 2 ? rb : rb2) + (sl & 1) * G2_BS + (kk ^ 1) * 5120 + mm * 1024);
          else if constexpr (mm < 9) fa[kk ^ 1][mm - 5] = lds_rd((kk ? ra : ra1) + sl * G2_AS + (mm - 5) * 2048);
          sb();
        });
      };
      if (L) poll0(ws, FL_4 + P * 64 + (m * 4 + kq) * 8 + ct, 1, epoch, lane);
      ResTile rt = load_res(make_rsrc(L ? (const void*)(ws + WS_X2) : (const void*)opq_p(x0_), SZ_X), ro);
      fence_all(acc);
      bar();
#pragma unroll
      for (int f = 0; f < 5; ++f) fb[0][f] = lds_rd(rb + f * 1024);
#pragma unroll
      for (int i = 0; i < 4; ++i) fa[0][i] = lds_rd(ra + i * 2048);
      sb();
      auto stage = [&](auto U) {
        half(IC<0>{}, U);
        wait_lgkm0();
        bar();
        half(IC<1>{}, IC<(U.v + 1) & 3>{});
      };
#pragma unroll 1
      for (int g = 0; g < 3; ++g) sfor<0, 4>(stage);
      sfor<0, 3>(stage);
      half(IC<0>{}, IC<3>{});
      wait_lgkm0();
      bar();
      {
        unsigned p[3][10];
        u32x4 y[3][2];
        auto pack = [&](auto N) {
          constexpr int i = N.v / 5, f = N.v % 5;
          asm volatile("" : "+v"(acc[i][f]));
          p[i][2 * f] = pk2(acc[i][f][0], acc[i][f][1]); p[i][2 * f + 1] = pk2(acc[i][f][2], acc[i][f][3]);
          const unsigned o = xo + (((kq + 1 + i) & 3) * 3 + 2 - i) * 4 * XSLOT;
          if constexpr (f == 1) { y[i][0] = u32x4{p[i][0], p[i][1], p[i][2], p[i][3]}; st16_raw(y[i][0], rxp, lane * 16, o); }
          if constexpr (f == 3) { y[i][1] = u32x4{p[i][4], p[i][5], p[i][6], p[i][7]}; st16_raw(y[i][1], rxp, lane * 16 + 1024, o); }
          if constexpr (f == 4) st8(u32x2{p[i][8], p[i][9]}, rxp, lane * 8 + 2048, o);
        };
        sfor<0, 20>([&](auto M) {
          constexpr int mm = M.v;
          mma(acc[mm / 5][mm % 5], fb[1][mm % 5], fa[1][mm / 5]);
          if constexpr (mm >= 8) pack(IC<mm - 8>{});
          sb();
        });
        pack(IC<12>{}); pack(IC<13>{}); pack(IC<14>{});
        st_guard(y[0][0], y[0][1], y[1][0], y[1][1], y[2][0], y[2][1]);
      }
      f32x4 (&v)[5] = acc[3];
      fence_acc(v);
      const unsigned fx = FL_X + ((L * 64 + P * 4 + c4) * 4 + w) * 4;
      publish(ws, fx + kq, epoch, lane);
      poll<PDX>(ws, fx + ((kq + 1 + lane) & 3), 3, epoch, lane);
      const unsigned xi = xo + kq * 12 * XSLOT;
      u32x4 g0[3], g1[3]; u32x2 g2[3];
#pragma unroll
      for (int s = 0; s < 3; ++s) {
        g0[s] = ld16u(rxp, lane * 16, xi + s * 4 * XSLOT);
        g1[s] = ld16u(rxp, lane * 16 + 1024, xi + s * 4 * XSLOT);
        g2[s] = ld8u(rxp, lane * 8 + 2048, xi + s * 4 * XSLOT);
      }
      sb();
#pragma unroll
      for (int s = 0; s < 3; ++s) {
        v[0] += f32x4{bflo(g0[s][0]), bfhi(g0[s][0]), bflo(g0[s][1]), bfhi(g0[s][1])};
        v[1] += f32x4{bflo(g0[s][2]), bfhi(g0[s][2]), bflo(g0[s][3]), bfhi(g0[s][3])};
        v[2] += f32x4{bflo(g1[s][0]), bfhi(g1[s][0]), bflo(g1[s][1]), bfhi(g1[s][1])};
        v[3] += f32x4{bflo(g1[s][2]), bfhi(g1[s][2]), bflo(g1[s][3]), bfhi(g1[s][3])};
        v[4] += f32x4{bflo(g2[s][0]), bfhi(g2[s][0]), bflo(g2[s][1]), bfhi(g2[s][1])};
      }
      finish_tile(v, rt, make_rsrc(ws + (L ? WS_X3 : WS_X1), SZ_X), oo, true, make_rsrc(ws + (L ? WS_ST3 : WS_ST1), SZ_ST),
                  q == 0 ? (ct * S_ + row) * 8 : OOB);
      publish(ws, FL_2 + (L * 16 + P) * 64 + (m * 4 + kq) * 8 + ct, epoch, lane);
    }
    u32x4 y2[2]; u32x2 y1[2];
    {
      PHASE_IDS
      IDS_G3
      typedef __attribute__((ext_vector_type(2))) short s16x2;
      poll<PD2>(ws, FL_2 + (L * 16 + P) * 64 + w * 16 + i16, 16, epoch, lane);
      const rsrc_t rx = make_rsrc(ws + (L ? WS_X3 : WS_X1), SZ_X);
      const rsrc_t rst = make_rsrc(ws + (L ? WS_ST3 : WS_ST1), SZ_ST);
      const unsigned sox = (P * 8 + w * 2) * 20 * 1024;
      const unsigned par = ct & 1;
      bf16x8 a[20][2];
#pragma unroll
      for (int k = 0; k < 3; ++k) { a[k][0] = LDX(0, k); a[k][1] = LDX(1, k); }
      const unsigned sto = (2 * q * S_ + P * 128 + w * 32 + i16) * 8;
      const u32x2 sa0 = ld8u(rst, sto, 0), sa1 = ld8u(rst, sto, S_ * 8), sb0 = ld8u(rst, sto + 128, 0), sb1 = ld8u(rst, sto + 128, S_ * 8);
      float mu[2], rs[2];
      sb();
      bar();
      f32x4 acc[2][4];
      bf16x8 b[2][4];
      const unsigned rb = lb + lane * 16;
#pragma unroll
      for (int f = 0; f < 4; ++f) b[0][f] = lds_rd(rb + f * 1024);
      sb();
      sfor<0, 20>([&](auto K) {
        constexpr int k = K.v;
        const unsigned pb = k + 1 < 20 ? rb + (k + 1) * 4096 : rb + 80 * 1024;
        if constexpr (k == KP3) {
          ln_coef(xsum16_32(__uint_as_float(sa0[0]) + __uint_as_float(sa1[0])), xsum16_32(__uint_as_float(sa0[1]) + __uint_as_float(sa1[1])), mu[0], rs[0]);
          ln_coef(xsum16_32(__uint_as_float(sb0[0]) + __uint_as_float(sb1[0])), xsum16_32(__uint_as_float(sb0[1]) + __uint_as_float(sb1[1])), mu[1], rs[1]);
          asm volatile("" : "+v"(mu[0]), "+v"(mu[1]), "+v"(rs[0]), "+v"(rs[1]));
          sb();
        }
        sfor<0, 8>([&](auto M) {
          constexpr int f = M.v >> 1, r = M.v & 1;
          if constexpr (k == 0) mma0(acc[r][f], b[0][f], a[0][r]); else mma(acc[r][f], b[k & 1][f], a[k][r]);
          if constexpr (r == 0 && (k + 1 < 20 || f < 3)) b[(k + 1) & 1][f] = lds_rd(pb + f * 1024);
          if constexpr (r == 1 && 2 * k + 3 + (f >> 1) < 20) a[2 * k + 3 + (f >> 1)][f & 1] = LDX(f & 1, 2 * k + 3 + (f >> 1));
          sb();
        });
      });
      auto act = [&](const f32x4& c, int r, const f32x4& cs, unsigned (&pk)[2]) {
        const f32x2 nm = {-mu[r], -mu[r]}, r2 = {rs[r], rs[r]};
#pragma unroll
        for (int e = 0; e < 2; ++e) {
          const f32x2 z = __builtin_elementwise_fma(nm, f32x2{cs[2 * e], cs[2 * e + 1]}, f32x2{c[2 * e], c[2 * e + 1]}) * r2;
          const s16x2 hb = __builtin_bit_cast(s16x2, __builtin_convertvector(z, bf16x2));
          pk[e] = __builtin_bit_cast(unsigned, __builtin_elementwise_max(hb, s16x2{0, 0}));
        }
      };
      auto pair = [&](const unsigned (&u)[2], const unsigned (&v)[2]) {
        auto t0 = __builtin_amdgcn_permlane16_swap(u[0], v[0], false, false);
        auto t1 = __builtin_amdgcn_permlane16_swap(u[1], v[1], false, false);
        return u32x4{t0[0], t1[0], t0[1], t1[1]};
      };
      const unsigned csb = lb + KM_CS1 + q * 16;
      f32x4 c1s[4];
#pragma unroll
      for (int f = 0; f < 4; ++f) c1s[f] = lds_rdf(csb + (f + par) * 64);
      asm volatile("s_nop 7\n\ts_nop 1" : "+v"(acc[0][0]), "+v"(acc[0][1]), "+v"(acc[0][2]), "+v"(acc[0][3]),
                                          "+v"(acc[1][0]), "+v"(acc[1][1]), "+v"(acc[1][2]), "+v"(acc[1][3]));
      wait_lgkm0();
      bar();
      {
        const rsrc_t rh = make_rsrc(ws + (L ? WS_H2 : WS_H1), SZ_H);
        const unsigned vo = ((((q & 1) << 1) | (q >> 1)) * 16 + i16) * 16;
        const unsigned so = ((P * 8 + w * 2) * 54 + 7 * (ct >> 1) + 4 * par) * 1024;
        u32x4 y[2][2];
#pragma unroll
        for (int p = 0; p < 2; ++p) {
          const f32x4 c0 = c1s[2 * p], c1 = c1s[2 * p + 1];
#pragma unroll
          for (int r = 0; r < 2; ++r) {
            unsigned u[2], v[2];
            act(acc[r][2 * p], r, c0, u); act(acc[r][2 * p + 1], r, c1, v);
            y[p][r] = pair(u, v);
            st16_raw(y[p][r], rh, p == 1 && ct == 15 ? OOB : vo, so + (r * 54 + p) * 1024);
          }
        }
        st_guard(y[0][0], y[0][1], y[1][0], y[1][1]);
      }
      sb();
      sfor<0, 20>([&](auto K) {
        constexpr int k = K.v;
        const unsigned pb = rb + 80 * 1024 + (k + 1) * 3072;
        sfor<0, 6>([&](auto M) {
          constexpr int f = M.v >> 1, r = M.v & 1;
          if constexpr (k == 0) mma0(acc[r][f], b[0][f], a[0][r]); else mma(acc[r][f], b[k & 1][f], a[k][r]);
          if constexpr (r == 0 && k + 1 < 20) b[(k + 1) & 1][f] = lds_rd(pb + f * 1024);
          sb();
        });
        if constexpr (k == PUB1) publish(ws, FL_3 + (L * 32 + P) * 64 + w * 16 + ct, epoch, lane);
      });
      const f32x4 c0 = lds_rdf(csb + (4 + par) * 64), c1 = lds_rdf(csb + (5 + par) * 64), c2 = lds_rdf(csb + (par ? 0 : 6) * 64);
      asm volatile("s_nop 7\n\ts_nop 1" : "+v"(acc[0][0]), "+v"(acc[0][1]), "+v"(acc[0][2]), "+v"(acc[1][0]), "+v"(acc[1][1]), "+v"(acc[1][2]));
#pragma unroll
      for (int r = 0; r < 2; ++r) {
        unsigned u[2], v[2], s[2];
        act(acc[r][0], r, c0, u); act(acc[r][1], r, c1, v); act(acc[r][2], r, c2, s);
        y2[r] = pair(u, v);
        y1[r] = u32x2{s[0], s[1]};
      }
      asm volatile("" : "+v"(y2[0]), "+v"(y2[1]), "+v"(y1[0]), "+v"(y1[1]));
    }
    {
      PHASE_IDS
      IDS_G4
      const unsigned row = rt64 * 64 + w * 16 + i16;
      const rsrc_t rh = make_rsrc(ws + (L ? WS_H2 : WS_H1), SZ_H);
      const unsigned soh = (rt64 * 4 * 54 + w * 14) * 1024;
      const unsigned vl = lane * 16, vz = lane * 16 + (w < 3 ? 0u : OOB);
      bf16x8 a[14][4], b[2][5];
      unsigned sh[4];
#pragma unroll
      for (int i = 0; i < 4; ++i) sh[i] = soh + ((w + 1 + i) & 3) * 54 * 1024;
#define LDH(t, i) a[t][i] = ld16(rh, (ORD[t] < 12 ? vl : vz) + (ORD[t] & 3) * 1024, sh[i] + (ORD[t] >> 2) * 4096)
      const TileOff ro = tile_offsets(false, row, c8, q), oo = tile_offsets(L != 0, row, c8, q);
      const unsigned f3 = FL_3 + (L * 32 + (rt64 >> 1)) * 64 + ((rt64 & 1) * 2 + (lane & 1)) * 16 + 4 * w + ((lane >> 1) & 3);
      poll0(ws, f3, 8, epoch, lane);
      sfor<0, 8>([&](auto N) { LDH(N.v / 4, N.v % 4); });
      sb();
      {
        const unsigned P = (xcd >> 1) * 4 + (idx >> 3), ct = (xcd & 1) * 8 + (idx & 7), par = ct & 1;
        const unsigned vo = ((((q & 1) << 1) | (q >> 1)) * 16 + i16) * 16;
        const unsigned v1 = ((2 * par + (q >> 1)) * 16 + i16) * 16 + (q & 1) * 8;
        const unsigned so = ((P * 8 + w * 2) * 54 + 7 * (ct >> 1)) * 1024;
#pragma unroll
        for (int r = 0; r < 2; ++r) {
          st16_raw(y2[r], rh, ct == 15 ? OOB : vo, so + (r * 54 + 2 + 4 * par) * 1024);
          st8(y1[r], rh, v1, so + (r * 54 + 3) * 1024);
        }
        st_guard(y2[0], y2[1]);
      }
      ResTile rt;
      u64 fl2;
      const rsrc_t rr = make_rsrc(ws + (L ? WS_X3 : WS_X1), SZ_X);
      sb();
      bar();
      f32x4 acc[4][5];
      const unsigned rbe = lb + w * 20480 + lane * 16, rbl = lb + G4_LATE + w * 15360 + lane * 16;
#pragma unroll
      for (int f = 0; f < 5; ++f) b[0][f] = lds_rd(rbe + f * 1024);
      sb();
      sfor<0, 14>([&](auto T) {
        constexpr int t = T.v;
        constexpr int ta = t < 5 ? (t == 0 ? 2 : t + 3) : (t == 5 ? -1 : (t == 6 ? 8 : (t + 3 < 14 ? t + 3 : -1)));
        if constexpr (t == 3 || t == 6 || t == 10) { wait_lgkm0(); bar(); }
        if constexpr (t == 6) { if (!__all(fl2 == epoch)) poll0(ws, f3 + 16 * 64, 8, epoch, lane); }
        sfor<0, 20>([&](auto M) {
          constexpr int m = M.v, i = m / 5, f = m % 5;
          if constexpr (t == 0) mma0(acc[i][f], b[0][f], a[0][i]); else mma(acc[i][f], b[t & 1][f], a[t][i]);
          if constexpr (t + 1 < 14 && (m & 1) == 1 && m < 10)
            b[(t + 1) & 1][m >> 1] = lds_rd((g4_late(t + 1) ? rbl : rbe) + g4_slot(t + 1) + (m >> 1) * 1024);
          if constexpr (ta >= 0 && (m & 1) == 0 && m < 8) LDH(ta, m >> 1);
          if constexpr (t == 6 && (m & 1) == 0 && m >= 8 && m < 16) LDH(9, (m - 8) >> 1);
          if constexpr (t == 0 && (m & 1) == 0 && m >= 8 && m < 16) LDH(3, (m - 8) >> 1);
          if constexpr (t == 2 && m == 10) rt = load_res(rr, ro);
          if constexpr (t == 4 && m == 18) fl2 = __hip_atomic_load(flag_ptr(ws, f3 + 16 * 64), RLX_AGENT);
          if constexpr (t == 13 && m >= 8) {
            asm volatile("" : "+v"(acc[(m - 8) / 5][(m - 8) % 5]));
            lds_wrf(rbl + (m - 8) * 1024, acc[(m - 8) / 5][(m - 8) % 5]);
          }
          sb();
        });
        if constexpr (t == 2) {
          wait_vm<19>();
          raise(ws, FL_3 + (L * 32 + 16 + (xcd >> 1) * 4 + (idx >> 3)) * 64 + w * 16 + (xcd & 1) * 8 + (idx & 7), epoch, lane);
        }
      });
#pragma unroll
      for (int f = 2; f < 5; ++f) { asm volatile("" : "+v"(acc[2][f])); lds_wrf(rbl + (10 + f) * 1024, acc[2][f]); }
      f32x4 (&v)[5] = acc[3];
      fence_acc(v);
      wait_lgkm0();
      bar();
#pragma unroll
      for (int s = 1; s < 4; ++s) {
        const unsigned src = lb + G4_LATE + ((w + s) & 3) * 15360 + (3 - s) * 5 * 1024 + lane * 16;
#pragma unroll
        for (int f = 0; f < 5; ++f) v[f] += lds_rdf(src + f * 1024);
      }
      finish_tile(v, rt, L ? make_rsrc(opq_p(out_), SZ_X) : make_rsrc(ws + WS_X2, SZ_X), oo, L == 0, make_rsrc(ws + WS_ST2, SZ_ST),
                  q == 0 ? (c8 * S_ + row) * 8 : OOB);
      if (L == 0) publish(ws, FL_4 + (rt64 >> 1) * 64 + ((rt64 & 1) * 4 + w) * 8 + c8, epoch, lane);
    }
  }
  asm volatile(".Lcw_end:");
  if (blockIdx.x == 0 && w0 == 0) raise(ws_, FL_E, epoch, tid0);
}

extern "C" void kernel_launch(void* const* d_in, const int* in_sizes, int n_in, void* d_out, int out_size, void* d_ws,
                              size_t ws_size, hipStream_t stream) {
  char* ws = (char*)d_ws;
  (void)hipFuncSetAttribute((const void*)k_all, hipFuncAttributeMaxDynamicSharedMemorySize, KM_LDS);
  k_all<<<256, 512, KM_LDS, stream>>>((const u16*)d_in[0], (const u16*)d_in[1], (const u16*)d_in[2], (const u16*)d_in[3],
                                     (const u16*)d_in[4], (const u16*)d_in[6], (const u16*)d_in[7], ws, (u16*)d_out);
}
```

```cpp
#include <hip/hip_runtime.h>
#include <hip/hip_bf16.h>
#include <stdint.h>

#define DI __device__ __forceinline__
typedef unsigned short u16;
typedef __attribute__((ext_vector_type(8))) __bf16 bf16x8;
typedef __attribute__((ext_vector_type(2))) __bf16 bf16x2;
typedef __attribute__((ext_vector_type(4))) float f32x4;
typedef __attribute__((ext_vector_type(2))) float f32x2;
typedef __attribute__((ext_vector_type(4))) unsigned u32x4;
typedef __attribute__((ext_vector_type(2))) unsigned u32x2;
typedef __attribute__((ext_vector_type(4))) short s16x4;
#define LDS_AS __attribute__((address_space(3)))
#define GLB_AS __attribute__((address_space(1)))
typedef LDS_AS char* ldsp;
typedef unsigned long long u64;
typedef GLB_AS u64 gu64;
typedef __amdgpu_buffer_rsrc_t rsrc_t;
#define RLX_AGENT __ATOMIC_RELAXED, __HIP_MEMORY_SCOPE_AGENT

constexpr int S_ = 2048, D_ = 640, F_ = 1728;
constexpr float EPS = 1e-5f;
constexpr unsigned OOB = 0x7f000000u;

constexpr unsigned SZ_AB = 8 * 128 * 5 * 1024;
constexpr unsigned SZ_X = 128 * 20 * 1024;
constexpr unsigned SZ_H = 128 * 54 * 1024;
constexpr unsigned SZ_W1 = 16 * 140 * 1024;
constexpr unsigned SZ_W2 = 8 * 54 * 5 * 1024;
constexpr unsigned SZ_ST = 2048 * 64;
constexpr unsigned WS_AB1 = 0;
constexpr unsigned WS_AB2 = WS_AB1 + SZ_AB;
constexpr unsigned WS_X1 = WS_AB2 + SZ_AB;
constexpr unsigned WS_X2 = WS_X1 + SZ_X;
constexpr unsigned WS_X3 = WS_X2 + SZ_X;
constexpr unsigned WS_H1 = WS_X3 + SZ_X;
constexpr unsigned WS_H2 = WS_H1 + SZ_H;
constexpr unsigned WS_W1 = WS_H2 + SZ_H;
constexpr unsigned WS_W2 = WS_W1 + SZ_W1;
constexpr unsigned WS_ST1 = WS_W2 + SZ_W2;
constexpr unsigned WS_ST2 = WS_ST1 + SZ_ST;
constexpr unsigned WS_ST3 = WS_ST2 + SZ_ST;
constexpr unsigned WS_CS1 = WS_ST3 + SZ_ST;
constexpr unsigned WS_CSD = WS_CS1 + 20 * 1728 * 4;
constexpr unsigned XSLOT = 2560;
constexpr unsigned SZ_XP = 256 * 12 * XSLOT;
constexpr unsigned WS_XP = (WS_CSD + 256 * 320 + 4095) & ~4095u;
constexpr unsigned WS_FLG = WS_XP + 2 * SZ_XP;
constexpr unsigned FL_X = 0;
constexpr unsigned FL_2 = FL_X + 2048;
constexpr unsigned FL_3 = FL_2 + 2048;
constexpr unsigned FL_4 = FL_3 + 4096;
constexpr unsigned FL_5 = FL_4 + 1024;
constexpr unsigned FL_W = FL_5 + 3072;
constexpr unsigned FL_E = FL_W + 1088;
constexpr unsigned WS_END = WS_FLG + (FL_E + 1) * 128;

DI u32x4 make_srd(const void* p, unsigned bytes) {
  uint64_t a = (uint64_t)p;
  u32x4 r;
  r[0] = (unsigned)a; r[1] = (unsigned)(a >> 32) & 0xffffu; r[2] = bytes; r[3] = 0x00020000u;
  return r;
}
DI rsrc_t make_rsrc(const void* p, unsigned bytes) {
  return __builtin_amdgcn_make_buffer_rsrc((void*)p, (short)0, (int)bytes, 0x00020000);
}
template <int LOFF, int GOFF>
DI void glds(u32x4 srd, unsigned voff, unsigned ldsb, unsigned soff) {
  unsigned tmp;
  asm volatile("s_add_u32 m0, %1, %2\n\ts_add_u32 %0, %3, %4\n\tbuffer_load_dwordx4 %5, %6, %0 offen lds"
               : "=&s"(tmp)
               : "s"(ldsb), "i"(LOFF), "s"(soff), "i"(GOFF), "v"(voff), "s"(srd)
               : "memory", "scc");
}
template <int N> DI void wait_vm() { asm volatile("s_waitcnt vmcnt(%0)" ::"i"(N) : "memory"); }
DI void wait_lgkm0() { asm volatile("s_waitcnt lgkmcnt(0)" ::: "memory"); }
DI void sb() { __builtin_amdgcn_sched_barrier(0); }
DI void bar() { asm volatile("s_barrier" ::: "memory"); }

DI bf16x8 lds_rd(unsigned addr) { return *(const LDS_AS bf16x8*)(ldsp)(uintptr_t)addr; }
DI f32x4 lds_rdf(unsigned addr) { return *(const LDS_AS f32x4*)(ldsp)(uintptr_t)addr; }
DI void lds_wrf(unsigned addr, f32x4 v) { *(LDS_AS f32x4*)(ldsp)(uintptr_t)addr = v; }
DI float lds_rd1(unsigned addr) { return *(const LDS_AS float*)(ldsp)(uintptr_t)addr; }
DI void lds_wr1(unsigned addr, float v) { *(LDS_AS float*)(ldsp)(uintptr_t)addr = v; }
DI void mma(f32x4& c, const bf16x8& a, const bf16x8& b) {
  asm volatile("v_mfma_f32_16x16x32_bf16 %0, %1, %2, %0" : "+v"(c) : "v"(a), "v"(b));
}
DI void mma0(f32x4& c, const bf16x8& a, const bf16x8& b) {
  asm volatile("v_mfma_f32_16x16x32_bf16 %0, %1, %2, 0" : "=&v"(c) : "v"(a), "v"(b));
}
DI void fence_acc(f32x4 (&a)[5]) { asm volatile("s_nop 7\n\ts_nop 1" : "+v"(a[0]), "+v"(a[1]), "+v"(a[2]), "+v"(a[3]), "+v"(a[4])); }
DI void pin_acc(f32x4 (&a)[5]) { asm volatile("" : "+v"(a[0]), "+v"(a[1]), "+v"(a[2]), "+v"(a[3]), "+v"(a[4])); }
template <int M> DI void fence_all(f32x4 (&a)[M][5]) {
#pragma unroll
  for (int i = 1; i < M; ++i) pin_acc(a[i]);
  fence_acc(a[0]);
#pragma unroll
  for (int i = 1; i < M; ++i) pin_acc(a[i]);
}
DI unsigned pk2(float a, float b) {
  f32x2 v = {a, b};
  return __builtin_bit_cast(unsigned, __builtin_convertvector(v, bf16x2));
}
DI float bflo(unsigned u) { return __uint_as_float(u << 16); }
DI float bfhi(unsigned u) { return __uint_as_float(u & 0xffff0000u); }
DI float dot2(unsigned a, unsigned b, float c) {
  return __builtin_amdgcn_fdot2_f32_bf16(__builtin_bit_cast(bf16x2, a), __builtin_bit_cast(bf16x2, b), c, false);
}
DI float xsum16_32(float x) {
  auto s = __builtin_amdgcn_permlane16_swap(__float_as_uint(x), __float_as_uint(x), false, false);
  x = __uint_as_float(s[0]) + __uint_as_float(s[1]);
  auto t = __builtin_amdgcn_permlane32_swap(__float_as_uint(x), __float_as_uint(x), false, false);
  return __uint_as_float(t[0]) + __uint_as_float(t[1]);
}
template <int V> struct IC { static constexpr int v = V; };
template <int I, int N, class Fn> DI void sfor(Fn&& f) {
  if constexpr (I < N) { f(IC<I>{}); sfor<I + 1, N>(f); }
}
DI unsigned rfl(unsigned v) { return __builtin_amdgcn_readfirstlane(v); }
DI unsigned opq_v(unsigned x) { asm volatile("" : "+v"(x)); return x; }
DI unsigned opq_s(unsigned x) { asm volatile("" : "+s"(x)); return x; }
template <class T> DI T* opq_p(T* x) { asm volatile("" : "+s"(x)); return x; }
DI bf16x8 ld16(rsrc_t r, unsigned voff, unsigned soff) { return __builtin_bit_cast(bf16x8, __builtin_amdgcn_raw_buffer_load_b128(r, voff, soff, 16)); }
DI u32x4 ld16u(rsrc_t r, unsigned voff, unsigned soff) { return __builtin_amdgcn_raw_buffer_load_b128(r, voff, soff, 16); }
DI u32x2 ld8u(rsrc_t r, unsigned voff, unsigned soff) { return __builtin_amdgcn_raw_buffer_load_b64(r, voff, soff, 16); }
DI void st16(u32x4 v, rsrc_t r, unsigned voff, unsigned soff) {
  __builtin_amdgcn_raw_buffer_store_b128(v, r, voff, soff, 16);
  asm volatile("s_nop 1" :: "v"(v) : "memory");
}
DI void st16_raw(u32x4 v, rsrc_t r, unsigned voff, unsigned soff) { __builtin_amdgcn_raw_buffer_store_b128(v, r, voff, soff, 16); }
template <class... T> DI void st_guard(const T&... v) {
  (..., [](const auto& x) { asm volatile("" :: "v"(x) : "memory"); }(v));
  asm volatile("s_nop 1" ::: "memory");
  (..., [](const auto& x) { asm volatile("" :: "v"(x)); }(v));
}
DI void st8(u32x2 v, rsrc_t r, unsigned voff, unsigned soff) { __builtin_amdgcn_raw_buffer_store_b64(v, r, voff, soff, 16); }
DI void st4(unsigned v, rsrc_t r, unsigned voff, unsigned soff) { __builtin_amdgcn_raw_buffer_store_b32(v, r, voff, soff, 16); }

#define ST(i)
#define STAMP_ARGS
#define STAMP_PASS(k)

template <int KS> DI unsigned foff(unsigned row, unsigned col) {
  return ((row >> 4) * KS + (col >> 5)) * 1024u + (((col >> 3) & 3) * 16u + (row & 15)) * 16u + (col & 7) * 2u;
}

extern __shared__ __attribute__((aligned(1024))) char smem[];
DI unsigned lds_base() { return (unsigned)(uintptr_t)(ldsp)smem; }

DI void ln_coef(float s1, float s2, float& mu, float& rs) {
  mu = s1 * (1.f / D_);
  rs = __builtin_amdgcn_rsqf(fmaxf(s2 * (1.f / D_) - mu * mu, 0.f) + EPS);
}

DI void g1_store(const f32x4 (&acc)[2][5], const float (&mu)[2][4], const float (&rs)[2][4], const float (&csv)[5], rsrc_t rab,
                 unsigned P, unsigned ct, unsigned w, unsigned i16, unsigned q) {
  const unsigned kstep = (ct >> 3) * 64 + P * 4 + w;
  const unsigned cq = ((q & 1) << 1) | (q >> 1);
  const unsigned so = ((ct & 7) * 128 + kstep) * 5 * 1024;
  const unsigned vo = (cq * 16 + i16) * 16;
  u32x4 y[5];
#pragma unroll
  for (int f = 0; f < 5; ++f) {
    const f32x2 cs = {csv[f], csv[f]};
    unsigned pk[2][2];
#pragma unroll
    for (int r = 0; r < 2; ++r)
#pragma unroll
      for (int e = 0; e < 2; ++e) {
        const f32x2 t = __builtin_elementwise_fma(f32x2{mu[r][2 * e], mu[r][2 * e + 1]}, cs, f32x2{acc[r][f][2 * e], acc[r][f][2 * e + 1]}) *
                        f32x2{rs[r][2 * e], rs[r][2 * e + 1]};
        pk[r][e] = __builtin_bit_cast(unsigned, __builtin_convertvector(t, bf16x2));
      }
    auto t0 = __builtin_amdgcn_permlane16_swap(pk[0][0], pk[1][0], false, false);
    auto t1 = __builtin_amdgcn_permlane16_swap(pk[0][1], pk[1][1], false, false);
    y[f] = u32x4{t0[0], t1[0], t0[1], t1[1]};
    st16_raw(y[f], rab, vo + (f & 3) * 1024, so + (f >> 2) * 4096);
  }
  st_guard(y[0], y[1], y[2], y[3], y[4]);
}

constexpr int K1_STAGE = 26 * 1024;
constexpr int K1_TR = 5 * K1_STAGE;
constexpr int K1_CS = K1_TR + 5 * 4096;

DI void k1_issue(u32x4 sa, u32x4 sbd, unsigned vo, unsigned la, unsigned lbb, unsigned soa, unsigned sob, unsigned w) {
  glds<0, 0>(sa, vo, la, soa);
  glds<1024, 8 * 1280>(sa, vo, la, soa);
  glds<2048, 16 * 1280>(sa, vo, la, soa);
  glds<3072, 24 * 1280>(sa, vo, la, soa);
  glds<0, 0>(sbd, vo, lbb, sob);
  glds<4096, 32 * 1280>(sbd, vo, lbb, sob);
  if (w < 2) glds<8192, 64 * 1280>(sbd, vo, lbb, sob);
}
struct RtUnit { bool first; unsigned ks, g; };
DI RtUnit rt_unit(unsigned u) {
  RtUnit r;
  r.first = u < 540;
  if (r.first) { r.ks = u / 27u; r.g = u - r.ks * 27u; }
  else { const unsigned v = u - 540; r.ks = v / 10u; r.g = v - r.ks * 10u; }
  return r;
}
DI void retile_issue(unsigned u, const u16* w1, const u16* w2, unsigned ldst, unsigned lane) {
  const RtUnit t = rt_unit(u);
  const unsigned ld = t.first ? F_ * 2 : D_ * 2;
  u32x4 srd = make_srd(t.first ? w1 : w2, D_ * F_ * 2);
  const unsigned vo = (lane >> 3) * ld + (lane & 7) * 16;
  const unsigned so = t.ks * 32 * ld + t.g * 128;
  glds<0, 0>(srd, vo, ldst, so);
  glds<1024, 0>(srd, vo, ldst, so + 8 * ld);
  glds<2048, 0>(srd, vo, ldst, so + 16 * ld);
  glds<3072, 0>(srd, vo, ldst, so + 24 * ld);
}
DI unsigned w1_off(unsigned rt, unsigned ks) {
  const unsigned c = rt / 7u, f = rt - c * 7, g = (c & 1) ? (f == 0 ? 6 : f - 1) : f;
  return WS_W1 + (c * 140 + (g < 4 ? ks * 4 + g : 80 + ks * 3 + g - 4)) * 1024;
}
DI void retile_finish(unsigned u, unsigned f0, rsrc_t rws, unsigned ldst, unsigned lane) {
  const RtUnit t = rt_unit(u);
  const unsigned i = lane & 15, q = lane >> 4;
  const unsigned base = ldst + (8 * q + (i >> 2)) * 128 + (i & 3) * 8 + f0 * 32;
  u32x4 v[2];
#pragma unroll
  for (int f = 0; f < 2; ++f) {
    s16x4 lo = __builtin_amdgcn_ds_read_tr16_b64_v4i16((LDS_AS s16x4*)(uintptr_t)(base + f * 32));
    s16x4 hi = __builtin_amdgcn_ds_read_tr16_b64_v4i16((LDS_AS s16x4*)(uintptr_t)(base + f * 32 + 512));
    u32x2 l2 = __builtin_bit_cast(u32x2, lo), h2 = __builtin_bit_cast(u32x2, hi);
    v[f] = u32x4{l2[0], l2[1], h2[0], h2[1]};
  }
  const unsigned rt = t.g * 4 + f0;
  if (t.first) {
    st16(v[0], rws, lane * 16, w1_off(rt, t.ks));
    st16(v[1], rws, lane * 16, w1_off(rt + 1, t.ks));
    float s[2];
#pragma unroll
    for (int f = 0; f < 2; ++f)
      s[f] = xsum16_32(((bflo(v[f][0]) + bfhi(v[f][0])) + (bflo(v[f][1]) + bfhi(v[f][1]))) + ((bflo(v[f][2]) + bfhi(v[f][2])) + (bflo(v[f][3]) + bfhi(v[f][3]))));
    st4(__float_as_uint(q == 0 ? s[0] : s[1]), rws, lane < 32 ? lane * 4 : OOB, WS_CS1 + (t.ks * F_ + rt * 16) * 4);
  } else {
    const unsigned c0 = rt / 5u, c1 = (rt + 1) / 5u;
    st16(v[0], rws, lane * 16, WS_W2 + ((c0 * 54 + t.ks) * 5 + (rt - c0 * 5)) * 1024);
    st16(v[1], rws, lane * 16, WS_W2 + ((c1 * 54 + t.ks) * 5 + (rt + 1 - c1 * 5)) * 1024);
  }
}

#define P5D 1
#define PUBG 11
#define KP3 3
#define KP1 3
constexpr int WS1 = 4, WS2 = 7, RTI = 2;
constexpr int PUB1 = 13;
constexpr int RGAP = 10, ASLEEP2 = 40, WDPRE = 6;
constexpr int PD4 = 13, PDX = 15, PD2 = 30, P5DELAY0 = 4, P5DELAY = P5D;

constexpr int KM_LDS = 160 * 1024;
constexpr int KM_CS1 = KM_LDS - 1024;
constexpr int G2_A = 80 * 1024, G2_AS = 16 * 1024, G2_BS = 20 * 1024;
constexpr int G4_LATE = 80 * 1024;

DI gu64* flag_ptr(char* ws, unsigned f) { return (gu64*)(ws + WS_FLG + f * 128); }
DI void raise(char* ws, unsigned f, u64 epoch, unsigned lane) {
  if (lane < 16) __hip_atomic_store(flag_ptr(ws, f) + lane, epoch, RLX_AGENT);
}
DI void publish(char* ws, unsigned f, u64 epoch, unsigned lane) {
  wait_vm<0>();
  raise(ws, f, epoch, lane);
}
DI void poll0(char* ws, unsigned f, unsigned n, u64 epoch, unsigned lane) {
  gu64* p = flag_ptr(ws, f);
  bool ok = lane >= n;
#pragma unroll 1
  for (unsigned s = 0; s < (1u << 16); ++s) {
    if (!ok) ok = __hip_atomic_load(p, RLX_AGENT) == epoch;
    if (__all(ok)) break;
    __builtin_amdgcn_s_sleep(1);
  }
  asm volatile("" ::: "memory");
}
template <int DELAY> DI void poll(char* ws, unsigned f, unsigned n, u64 epoch, unsigned lane) {
  __builtin_amdgcn_s_sleep(DELAY);
  poll0(ws, f, n, epoch, lane);
}
DI void warm(unsigned ldump, u32x4 srd, unsigned voff, unsigned soff) {
  asm volatile("s_mov_b32 m0, %0\n\ts_nop 0\n\tbuffer_load_dword %1, %2, %3 offen lds" :: "s"(ldump), "v"(voff), "s"(srd), "s"(soff) : "memory");
}
DI void glds4_sc1(u32x4 srd, unsigned voff, unsigned l, unsigned so) {
  asm volatile("s_mov_b32 m0, %0\n\ts_nop 0\n\tbuffer_load_dwordx4 %1, %2, %3 offen sc1 lds\n\tbuffer_load_dwordx4 %1, %2, %3 offen offset:1024 sc1 lds\n\t"
               "buffer_load_dwordx4 %1, %2, %3 offen offset:2048 sc1 lds\n\tbuffer_load_dwordx4 %1, %2, %3 offen offset:3072 sc1 lds"
               :: "s"(l), "v"(voff), "s"(srd), "s"(so) : "memory");
}
DI void glds1_sc1(u32x4 srd, unsigned voff, unsigned l, unsigned so) {
  asm volatile("s_mov_b32 m0, %0\n\ts_nop 0\n\tbuffer_load_dwordx4 %1, %2, %3 offen sc1 lds" :: "s"(l), "v"(voff), "s"(srd), "s"(so) : "memory");
}
template <int N> DI void glds_run_sc1(u32x4 srd, unsigned voff, unsigned l, unsigned so) {
#pragma unroll
  for (int g = 0; g < N / 4; ++g) glds4_sc1(srd, voff, l + g * 4096, so + g * 4096);
#pragma unroll
  for (int g = N / 4 * 4; g < N; ++g) glds1_sc1(srd, voff, l + g * 1024, so + g * 1024);
}

struct ResTile { u32x4 r0, r1; u32x2 r2; };
struct TileOff { unsigned a0, a1, a2; };
DI TileOff tile_offsets(bool rm, unsigned row, unsigned ct, unsigned q) {
  const unsigned cA = ct * 80 + 16 * (q & 1) + 8 * (q >> 1), cS = ct * 80 + 64 + 4 * q;
  TileOff t;
  if (rm) { t.a0 = (row * D_ + cA) * 2; t.a1 = t.a0 + 64; t.a2 = (row * D_ + cS) * 2; }
  else { t.a0 = foff<20>(row, cA); t.a1 = foff<20>(row, cA + 32); t.a2 = foff<20>(row, cS); }
  asm volatile("" : "+v"(t.a0), "+v"(t.a1), "+v"(t.a2));
  return t;
}
DI ResTile load_res(rsrc_t rr, const TileOff& t) {
  ResTile rt;
  rt.r0 = ld16u(rr, t.a0, 0); rt.r1 = ld16u(rr, t.a1, 0); rt.r2 = ld8u(rr, t.a2, 0);
  return rt;
}
DI void finish_tile(f32x4 (&v)[5], const ResTile& rt, rsrc_t rout, const TileOff& o, bool stats, rsrc_t rst, unsigned sto) {
#pragma unroll
  for (int p = 0; p < 2; ++p)
#pragma unroll
    for (int r = 0; r < 4; ++r) {
      auto t = __builtin_amdgcn_permlane16_swap(__float_as_uint(v[2 * p][r]), __float_as_uint(v[2 * p + 1][r]), false, false);
      v[2 * p][r] = __uint_as_float(t[0]); v[2 * p + 1][r] = __uint_as_float(t[1]);
    }
  u32x4 y0, y1; u32x2 y2;
#pragma unroll
  for (int j = 0; j < 2; ++j) {
    y0[j] = pk2(v[0][2 * j] + bflo(rt.r0[j]), v[0][2 * j + 1] + bfhi(rt.r0[j]));
    y0[2 + j] = pk2(v[1][2 * j] + bflo(rt.r0[2 + j]), v[1][2 * j + 1] + bfhi(rt.r0[2 + j]));
    y1[j] = pk2(v[2][2 * j] + bflo(rt.r1[j]), v[2][2 * j + 1] + bfhi(rt.r1[j]));
    y1[2 + j] = pk2(v[3][2 * j] + bflo(rt.r1[2 + j]), v[3][2 * j + 1] + bfhi(rt.r1[2 + j]));
    y2[j] = pk2(v[4][2 * j] + bflo(rt.r2[j]), v[4][2 * j + 1] + bfhi(rt.r2[j]));
  }
  if (stats) {
    st16_raw(y0, rout, o.a0, 0); st16_raw(y1, rout, o.a1, 0); st8(y2, rout, o.a2, 0);
  } else {
    __builtin_amdgcn_raw_buffer_store_b128(y0, rout, o.a0, 0, 0);
    __builtin_amdgcn_raw_buffer_store_b128(y1, rout, o.a1, 0, 0);
    __builtin_amdgcn_raw_buffer_store_b64(y2, rout, o.a2, 0, 0);
  }
  st_guard(y0, y1);
  if (stats) {
    float s1a = 0.f, s1b = 0.f, s2a = 0.f, s2b = 0.f;
#pragma unroll
    for (int j = 0; j < 4; ++j) {
      s1a = dot2(y0[j], 0x3f803f80u, s1a); s2a = dot2(y0[j], y0[j], s2a);
      s1b = dot2(y1[j], 0x3f803f80u, s1b); s2b = dot2(y1[j], y1[j], s2b);
    }
    s1a = dot2(y2[0], 0x3f803f80u, s1a); s2a = dot2(y2[0], y2[0], s2a);
    s1b = dot2(y2[1], 0x3f803f80u, s1b); s2b = dot2(y2[1], y2[1], s2b);
    const float s1 = xsum16_32(s1a + s1b), s2 = xsum16_32(s2a + s2b);
    st8(u32x2{__float_as_uint(s1), __float_as_uint(s2)}, rst, sto, 0);
  }
}

DI constexpr unsigned wd_pos(int c) { return c < 8 ? 10 * c : 60 + 10 * c; }
constexpr int ORD[14] = {0, 1, 4, 5, 7, 8, 11, 12, 2, 3, 6, 9, 10, 13};
DI constexpr int g4_late(int t) { return (t >= 4 && t < 7) || t >= 11; }
DI constexpr int g4_slot(int t) { return (t < 4 ? t : (t < 7 ? t - 4 : (t < 11 ? t - 7 : t - 11))) * 5120; }

#define PHASE_IDS \
  const unsigned lane = opq_v(tid0) & 63, w = opq_s(w0), bid = opq_s(blockIdx.x); \
  const unsigned xcd = bid & 7, idx = bid >> 3, i16 = lane & 15, q = lane >> 4; \
  char* const ws = opq_p(ws_); \
  (void)xcd; (void)idx; (void)i16; (void)q;
#define IDS_G2 const unsigned kq = xcd & 3, P = (xcd >> 2) * 8 + (idx >> 2), c4 = idx & 3;
#define G2_LA(h) (lb + G2_A + ((w >> 1) * 4 + ((2 * (w & 1) + (h) + 3 - kq) & 3)) * 2048)
#define IDS_G3 const unsigned P = (xcd >> 1) * 4 + (idx >> 3), ct = (xcd & 1) * 8 + (idx & 7);
#define C8_OF ((xcd & 1) * 4 + (idx & 3))
#define IDS_G4 const unsigned rt64 = (xcd >> 1) * 8 + (idx >> 2), c8 = C8_OF;
#define LDX(r, k) ld16(rx, lane * 16 + ((k) & 3) * 1024, sox + (r) * 20480 + ((k) >> 2) * 4096)

__global__ __launch_bounds__(512, 2) void k_all(const u16* x0_, const u16* Sre_, const u16* Sim_, const u16* Dre_, const u16* Dim_,
                                               const u16* w1_, const u16* w2_, char* ws_, u16* out_ STAMP_ARGS) {
  ST(0);
  const unsigned tid0 = threadIdx.x, wall = rfl(tid0 >> 6), w0 = wall & 3;
  const unsigned lb = lds_base();
  const char* code; unsigned code_len;
  asm volatile("s_getpc_b64 %0\n.Lcw_begin:\n\ts_mov_b32 %1, .Lcw_end-.Lcw_begin" : "=s"(code), "=s"(code_len));
  u64 epoch;

  if (__builtin_expect(wall >= 4, 1)) {
    const unsigned sink = lb + K1_CS + 1024;
    unsigned ep0, ep1;
    asm volatile("global_load_dword %0, %2, %3 sc1\n\tglobal_load_dword %1, %2, %3 offset:4 sc1"
                 : "=&v"(ep0), "=&v"(ep1) : "v"(WS_FLG + FL_E * 128), "s"(ws_) : "memory");
    {
      PHASE_IDS
      IDS_G3
      const u32x4 sa = make_srd(opq_p(x0_), S_ * D_ * 2), sbd = make_srd(ct < 8 ? opq_p(Dre_) : opq_p(Dim_), D_ * D_ * 2);
      const unsigned vo = (lane >> 3) * 1280 + (((lane & 7) ^ (lane >> 3)) << 4);
      const unsigned soa = (P * 128 + w * 32) * 1280, sob = ((ct & 7) * 80 + w * 8) * 1280;
      const unsigned la = lb + w * 4096, lbb = lb + 16384 + w * 1024;
      const unsigned u0 = bid * 4 + w, u1 = 1024 + bid;
      const bool extra = w == 3 && bid < 56;
      const unsigned rot = ((idx & 7) + 3 * (idx >> 3)) % 10u;
      auto ko = [&](unsigned s) { const unsigned c = s + rot; return (c >= 10 ? c - 10 : c) * 128; };
      asm volatile("s_nop 4" ::: "memory");
      k1_issue(sa, sbd, vo, la, lbb, soa + ko(0), sob + ko(0), w);
      k1_issue(sa, sbd, vo, la + K1_STAGE, lbb + K1_STAGE, soa + ko(1), sob + ko(1), w);
      ST(8);
      wait_vm<6>();
      {
        unsigned lo, hi;
        asm volatile("v_readfirstlane_b32 %0, %2\n\tv_readfirstlane_b32 %1, %3\n\ts_nop 4" : "=s"(lo), "=s"(hi) : "v"(ep0), "v"(ep1));
        epoch = (u64)lo + ((u64)hi << 32) + 1;
      }
      bar();
      k1_issue(sa, sbd, vo, la + 2 * K1_STAGE, lbb + 2 * K1_STAGE, soa + ko(2), sob + ko(2), w);
      const u32x4 ss = make_srd((xcd & 2) ? Sim_ : Sre_, S_ * S_ * 2);
      const unsigned vs = (lane & 31) * 4096 + (lane >> 5) * 128;
      const unsigned sos = ((xcd >> 2) * 1024 + idx * 32) * 4096 + (xcd & 1) * 2048 + w * 256;
      unsigned so = 0;
#pragma unroll 1
      for (int i = 0; i < 10; ++i) {
        if (i == 0) wait_vm<6>(); else if (i == 1) wait_vm<2 * 6>(); else if (i < 6) wait_vm<3 * 6>();
        else if (i == 6) wait_vm<2 * 6 + (RTI >= 2 && RTI < 6 ? 4 : 0)>(); else if (i == 7) wait_vm<6 + (RTI >= 3 && RTI < 7 ? 4 : 0)>();
        else wait_vm<(RTI >= 4 && RTI < 8 ? 4 : 0) + (WS2 >= 4 && WS2 < 8) + (WS1 >= 4 && WS1 < 8)>();
        bar();
        if (i == 0) {
          k1_issue(sa, sbd, vo, la + 3 * K1_STAGE, lbb + 3 * K1_STAGE, soa + ko(3), sob + ko(3), w);
          k1_issue(sa, sbd, vo, la + 4 * K1_STAGE, lbb + 4 * K1_STAGE, soa + ko(4), sob + ko(4), w);
        } else if (i == 1) {
          k1_issue(sa, sbd, vo, la, lbb, soa + ko(5), sob + ko(5), w);
          k1_issue(sa, sbd, vo, la + K1_STAGE, lbb + K1_STAGE, soa + ko(6), sob + ko(6), w);
        } else if (i < 5) k1_issue(sa, sbd, vo, la + so, lbb + so, soa + ko(i + 5), sob + ko(i + 5), w);
        so = so == 4 * K1_STAGE ? 0 : so + K1_STAGE;
        if (i == WS1) warm(sink, ss, vs, sos);
        if (i == WS2) warm(sink, ss, vs, sos + 1024);
        if (i == RTI) {
          retile_issue(u0, w1_, w2_, lb + K1_TR + w * 4096, lane);
          if (extra) retile_issue(u1, w1_, w2_, lb + K1_TR + 4 * 4096, lane);
        }
      }
      ST(9);
      bar();
    }
    bf16x8 wr[10][5];
#pragma unroll
    for (int t = 0; t < 10; ++t)
#pragma unroll
      for (int f = 0; f < 5; ++f) wr[t][f] = bf16x8{};
#pragma unroll 1
    for (int L = 0; L < 2; ++L) {
      const int sbase = L * 20;
      (void)sbase;
      {
        PHASE_IDS
        IDS_G2
        const u32x4 sa = make_srd(kq < 2 ? opq_p(Sre_) : opq_p(Sim_), S_ * S_ * 2);
        const u32x4 sb_ = make_srd(ws + (L ? WS_AB2 : WS_AB1), SZ_AB);
        const unsigned va = (lane >> 3) * 4096 + (((lane & 7) ^ (lane >> 3)) << 4), vb = lane * 16;
        const unsigned soa = (P * 128 + w * 32) * 4096 + (kq & 1) * 2048;
        const unsigned sob = ((2 * c4 + (w >> 1)) * 128 + kq * 32 + (w & 1)) * 5 * 1024;
        const unsigned la = lb + G2_A + w * 4096, lbb = lb + w * 5120;
        const unsigned la0 = G2_LA(0), la1 = G2_LA(1);
        const unsigned ct = (xcd & 1) * 8 + (idx & 7);
        auto kc = [&](unsigned s) { return L ? 2 * (s & 7) + ((s ^ (s >> 3)) & 1) : s; };
        asm volatile("s_nop 4" ::: "memory");
        auto issue_a = [&](unsigned l, unsigned so) {
          glds<0, 0>(sa, va, la0 + l, so); glds<1024, 8 * 4096>(sa, va, la0 + l, so);
          glds<0, 16 * 4096>(sa, va, la1 + l, so); glds<1024, 24 * 4096>(sa, va, la1 + l, so);
        };
        if (L == 0) {
#pragma unroll
          for (int s = 0; s < 3; ++s) issue_a(s * G2_AS, soa + s * 128);
          const unsigned u0 = bid * 4 + w, u1 = 1024 + bid;
          const rsrc_t rws = make_rsrc(ws, WS_END);
          wait_vm<12>();
          retile_finish(u0, 0, rws, lb + K1_TR + w * 4096, lane);
          retile_finish(u0, 2, rws, lb + K1_TR + w * 4096, lane);
          if (w == 3 && bid < 56) {
            retile_finish(u1, 0, rws, lb + K1_TR + 4 * 4096, lane);
            retile_finish(u1, 2, rws, lb + K1_TR + 4 * 4096, lane);
          }
          __builtin_amdgcn_s_sleep(P5DELAY0);
        } else {
          issue_a(3 * G2_AS, soa + kc(3) * 128);
          __builtin_amdgcn_s_sleep(P5DELAY);
        }
        ST(sbase + 1);
        const unsigned f5 = FL_5 + ((L * 4 + kq) * 4 + c4) * 64 + ((i16 >> 1) * 2 + (w >> 1)) * 4 + (L ? 2 * (w & 1) + (i16 & 1) : (w & 1) + 2 * (i16 & 1));
        poll0(ws, f5, 16, epoch, lane);
        wait_vm<0>();
        ST(sbase + 2);
        glds_run_sc1<5>(sb_, vb, lbb, sob);
        glds_run_sc1<5>(sb_, vb, lbb + G2_BS, sob + kc(1) * 10240);
        if (L == 0) {
          raise(ws, FL_W + bid * 4 + w, epoch, lane);
          if (w == 3 && bid < 56) raise(ws, FL_W + 1024 + bid, epoch, lane);
          wait_vm<6>();
        } else wait_vm<5>();
        bar();
        glds_run_sc1<5>(sb_, vb, lbb + 2 * G2_BS, sob + kc(2) * 10240);
        if (L == 0) issue_a(3 * G2_AS, soa + kc(3) * 128);
        glds_run_sc1<5>(sb_, vb, lbb + 3 * G2_BS, sob + kc(3) * 10240);
        ST(sbase + 3);
        const u32x4 sw = make_srd(ws + WS_W1, SZ_W1);
        const unsigned sow = ct * 140 * 1024;
        unsigned fw, fw0 = 0, fw1 = 0;
        {
          const unsigned n = w * 42 + (lane < 42 ? lane : 0), k3 = n / 3u, g3 = (7 * ct >> 2) + (n - k3 * 3), n2 = n - 60;
          fw = FL_W + (n < 60 ? k3 * 27 + (g3 < 26 ? g3 : 26) : 540 + (n2 >> 1) * 10 + (5 * C8_OF >> 2) + (n2 & 1));
          if (L) fw = f5 + 1024;
        }
#pragma unroll 1
        for (int s = 0; s < 16; ++s) {
          if (s == 0) wait_vm<10>();
          if (s == 1) wait_vm<14>();
          wait_vm<18>();
          if (s == (L ? 4 : 9)) {
            asm volatile("" : "+v"(fw0), "+v"(fw1));
            if (!__all(fw0 == (unsigned)epoch && fw1 == (unsigned)(epoch >> 32))) poll0(ws, fw, 42, epoch, lane);
          }
          bar();
          const unsigned sl = s & 3;
          if (s < 12) {
            issue_a(sl * G2_AS, soa + kc(s + 4) * 128);
            glds_run_sc1<5>(sb_, vb, lbb + sl * G2_BS, sob + kc(s + 4) * 10240);
            if (s == (L ? 1 : 6))
              asm volatile("global_load_dword %0, %2, %3 sc1\n\tglobal_load_dword %1, %2, %3 offset:4 sc1"
                           : "+v"(fw0), "+v"(fw1) : "v"(WS_FLG + fw * 128), "s"(ws) : "memory");
          } else {
            glds_run_sc1<5>(sw, vb, lbb + sl * G2_BS, sow + w * 5120 + sl * G2_BS);
            if (s < 15) glds_run_sc1<4>(sw, vb, la + sl * G2_AS, sow + G2_A + w * 4096 + sl * G2_AS);
            else glds_run_sc1<3>(sw, vb, lb + G2_A + 3 * G2_AS + w * 3072, sow + G2_A + 3 * G2_AS + w * 3072);
          }
        }
        ST(sbase + 4);
      }
      {
        PHASE_IDS
        IDS_G4
        const unsigned ct = (xcd & 1) * 8 + (idx & 7);
        if (w >= 2) {
          const rsrc_t rc = make_rsrc(ws + WS_CS1, 20 * F_ * 4);
          const unsigned c = (w - 2) * 64 + lane, col = ct * 112 + c;
          const unsigned vo = c < 112 && col < F_ ? col * 4 : OOB;
          float csp = 0.f;
#pragma unroll
          for (int k = 0; k < 20; ++k) csp += __uint_as_float(__builtin_amdgcn_raw_buffer_load_b32(rc, vo, k * F_ * 4, 16));
          lds_wr1(lb + KM_CS1 + c * 4, csp);
        }
        const unsigned so = (c8 * 54 + w * 14) * 5 * 1024;
        wait_vm<0>();
        if (L == 0) {
          const rsrc_t rw = make_rsrc(ws + WS_W2, SZ_W2);
          const unsigned vl = lane * 16, vz = lane * 16 + (w < 3 ? 0u : OOB);
          sfor<0, 50>([&](auto N) {
            constexpr int t = 4 + N.v / 5, n = ORD[t] * 5 + N.v % 5;
            wr[t - 4][N.v % 5] = ld16(rw, (ORD[t] < 12 ? vl : vz) + (n & 3) * 1024, so + (n >> 2) * 4096);
            if constexpr (N.v % 5 == 4 && N.v < 49) { sb(); __builtin_amdgcn_s_sleep(RGAP); sb(); }
          });
          sb();
        }
        wait_lgkm0();
        ST(sbase + 5);
        bar();
        bar();
        const u32x4 s2 = make_srd(ws + WS_W2, SZ_W2);
        const unsigned sg = rfl(so);
        asm volatile("s_nop 4" ::: "memory");
        glds_run_sc1<10>(s2, lane * 16, lb + w * 20480, sg);
        glds_run_sc1<10>(s2, lane * 16, lb + w * 20480 + 10240, sg + 4 * 5120);
        wait_vm<0>();
        ST(sbase + 6);
        const unsigned pe = lb + w * 20480 + lane * 16, pl = lb + G4_LATE + w * 15360 + lane * 16;
        auto put = [&](auto T0, auto T1) {
          sfor<T0.v * 5, T1.v * 5>([&](auto N) {
            constexpr int t = N.v / 5, f = N.v % 5;
            *(LDS_AS bf16x8*)(ldsp)(uintptr_t)((g4_late(t) ? pl : pe) + g4_slot(t) + f * 1024) = wr[t - 4][f];
          });
          wait_lgkm0();
        };
        bar();
        put(IC<4>{}, IC<7>{});
        bar();
        put(IC<7>{}, IC<11>{});
        bar();
        put(IC<11>{}, IC<14>{});
        bar();
      }
      if (L) { bar(); break; }
      {
        PHASE_IDS
        IDS_G3
        const u32x4 sd = make_srd(ct < 8 ? opq_p(Dre_) : opq_p(Dim_), D_ * D_ * 2);
        const unsigned vo = (lane >> 3) * 1280 + (((lane & 7) ^ (lane >> 3)) << 4);
        const unsigned so = (ct & 7) * 80 * 1280;
        sfor<0, 10>([&](auto G) { glds<G.v * 1024, G.v * 8 * 1280>(sd, vo, lb + 20 * w * 1024, so + 2 * w * 128); });
        sfor<0, WDPRE>([&](auto G) { glds<G.v * 1024, G.v * 8 * 1280>(sd, vo, lb + (20 * w + 10) * 1024, so + (2 * w + 1) * 128); });
        bar();
        sfor<WDPRE, 10>([&](auto G) { glds<G.v * 1024, G.v * 8 * 1280>(sd, vo, lb + (20 * w + 10) * 1024, so + (2 * w + 1) * 128); });
        sfor<0, 5>([&](auto G) { glds<G.v * 1024, G.v * 8 * 1280>(sd, vo, lb + (140 + w * 5) * 1024, so + (8 + (w >> 1)) * 128 + (w & 1) * 40 * 1280); });
        wait_vm<0>();
        ST(7);
        bar();
      }
      {
        PHASE_IDS
        IDS_G2
        const u32x4 sa = make_srd(kq < 2 ? opq_p(Sre_) : opq_p(Sim_), S_ * S_ * 2);
        const unsigned va = (lane >> 3) * 4096 + (((lane & 7) ^ (lane >> 3)) << 4);
        const unsigned soa = (P * 128 + w * 32) * 4096 + (kq & 1) * 2048;
        __builtin_amdgcn_s_sleep(ASLEEP2);
#pragma unroll
        for (int s = 0; s < 3; ++s) {
          const unsigned l0 = G2_LA(0) + s * G2_AS, l1 = G2_LA(1) + s * G2_AS, so = soa + (2 * s + (s & 1)) * 128;
          glds<0, 0>(sa, va, l0, so); glds<1024, 8 * 4096>(sa, va, l0, so);
          glds<0, 16 * 4096>(sa, va, l1, so); glds<1024, 24 * 4096>(sa, va, l1, so);
        }
        bar();
      }
    }
    return;
  }

  {
    const u64 e = __hip_atomic_load(flag_ptr(ws_, FL_E), RLX_AGENT);
    epoch = (u64)rfl((unsigned)e) + ((u64)rfl((unsigned)(e >> 32)) << 32) + 1;
  }
#pragma unroll 1
  for (int L = 0; L < 2; ++L) {
    const int sbase = L * 20;
    (void)sbase;
    {
      PHASE_IDS
      IDS_G3
      f32x4 acc[2][5];
      float mu[2][4], rs[2][4], csv[5];
      const float sign = ct < 8 ? 1.f : -1.f;
      const unsigned f5 = FL_5 + (((ct >> 3) * 2 + (P >> 3)) * 4 + ((ct & 7) >> 1)) * 64 + ((P & 7) * 2 + (ct & 1)) * 4 + w;
      if (L == 0) {
        const unsigned ra0 = lb + w * 4096 + i16 * 128 + ((q ^ (lane & 7)) << 4);
        const unsigned rb0 = lb + 16384 + i16 * 128 + ((q ^ (lane & 7)) << 4);
        const f32x4 z4 = {0.f, 0.f, 0.f, 0.f};
        f32x4 cs0 = z4, cs1 = z4, rs_[2] = {z4, z4}, rq_[2] = {z4, z4};
#pragma unroll
        for (int r = 0; r < 2; ++r)
#pragma unroll
          for (int f = 0; f < 5; ++f) acc[r][f] = z4;
        bf16x8 fr[2][8];
        bf16x8 one = __builtin_bit_cast(bf16x8, u32x4{0x3f803f80u, 0x3f803f80u, 0x3f803f80u, 0x3f803f80u});
        auto half = [&](auto KK, unsigned so) {
          constexpr int kk = KK.v;
          const unsigned pa = (ra0 ^ ((kk ^ 1) * 64)) + so, pb = (rb0 ^ ((kk ^ 1) * 64)) + so;
          sfor<0, 16>([&](auto M) {
            constexpr int m = M.v;
            if constexpr (m < 10) mma(acc[m & 1][m >> 1], fr[kk][m & 1], fr[kk][2 + (m >> 1)]);
            else if constexpr (m == 10) mma(cs0, one, fr[kk][7]);
            else if constexpr (m == 11) mma(cs1, one, fr[kk][6]);
            else if constexpr (m < 14) mma(rs_[m - 12], fr[kk][m - 12], one);
            else mma(rq_[m - 14], fr[kk][m - 14], fr[kk][m - 14]);
            constexpr int n = m >> 1;
            if constexpr ((m & 1) == 0) fr[kk ^ 1][n] = n < 2 ? lds_rd(pa + n * 2048) : (n < 7 ? lds_rd(pb + (n - 2) * 2048) : lds_rd(pb + w * 2048));
            sb();
          });
        };
        warm(lb + K1_CS + 1024, make_srd(code, code_len), lane < 4 ? (idx * 16 + w * 4 + lane) * 128 : OOB, 0);
        asm volatile("s_nop 1" : "+v"(cs0), "+v"(cs1), "+v"(rs_[0]), "+v"(rs_[1]), "+v"(rq_[0]), "+v"(rq_[1]), "+v"(one));
        fence_all(acc);
        bar();
        ST(38);
        fr[0][0] = lds_rd(ra0); fr[0][1] = lds_rd(ra0 + 2048);
#pragma unroll
        for (int f = 0; f < 5; ++f) fr[0][2 + f] = lds_rd(rb0 + f * 2048);
        fr[0][7] = lds_rd(rb0 + w * 2048);
        sb();
        unsigned so = 0;
#pragma unroll 1
        for (int i = 0; i < 10; ++i) {
          half(IC<0>{}, so);
          wait_lgkm0();
          bar();
          so = so == 4 * K1_STAGE ? 0 : so + K1_STAGE;
          half(IC<1>{}, so);
        }
        fence_all(acc);
        asm volatile("" : "+v"(cs0), "+v"(cs1), "+v"(rs_[0]), "+v"(rs_[1]), "+v"(rq_[0]), "+v"(rq_[1]));
        const unsigned csb = lb + K1_CS;
        if (q == 0) {
          lds_wr1(csb + (w * 16 + i16) * 4, cs0[0]);
          if (w == 0) lds_wr1(csb + (64 + i16) * 4, cs1[0]);
        }
        wait_lgkm0();
        bar();
        ST(18);
#pragma unroll
        for (int f = 0; f < 5; ++f) csv[f] = lds_rd1(csb + (f * 16 + i16) * 4);
#pragma unroll
        for (int r = 0; r < 2; ++r)
#pragma unroll
          for (int j = 0; j < 4; ++j) {
            float m_, r_;
            ln_coef(rs_[r][j], __shfl(rq_[r][j], q * 20 + j), m_, r_);
            mu[r][j] = -m_; rs[r][j] = sign * r_;
          }
        if (w == 0) {
          const rsrc_t rc = make_rsrc(ws + WS_CSD, 256 * 320);
          st4(__float_as_uint(q == 0 ? csv[0] : (q == 1 ? csv[1] : (q == 2 ? csv[2] : csv[3]))), rc, lane * 4, bid * 320);
          st4(__float_as_uint(csv[4]), rc, q == 0 ? 256 + lane * 4 : OOB, bid * 320);
        }
        g1_store(acc, mu, rs, csv, make_rsrc(ws + WS_AB1, SZ_AB), P, ct, w, i16, q);
        ST(19);
        publish(ws, f5, epoch, lane);
      } else {
        {
          const rsrc_t rc = make_rsrc(ws + WS_CSD, 256 * 320);
#pragma unroll
          for (int f = 0; f < 5; ++f) csv[f] = __uint_as_float(__builtin_amdgcn_raw_buffer_load_b32(rc, (f * 16 + i16) * 4, bid * 320, 16));
        }
        const unsigned h0 = P & 1;
        poll<PD4>(ws, FL_4 + P * 64 + ((i16 >> 3) * 4 + w) * 8 + (i16 & 7), 16, epoch, lane);
        ST(17);
        const rsrc_t rx = make_rsrc(ws + WS_X2, SZ_X);
        const unsigned sox0 = (P * 8 + h0 * 4 + w) * 20 * 1024, sox1 = (P * 8 + (h0 ^ 1) * 4 + w) * 20 * 1024;
        bf16x8 a[40];
#define LDA(n) a[n] = ld16(rx, lane * 16 + ((n) & 3) * 1024, ((n) < 20 ? sox0 : sox1) + (((n) % 20) >> 2) * 4096)
        sfor<0, 6>([&](auto N) { LDA(N.v); });
        const rsrc_t rst = make_rsrc(ws + WS_ST2, SZ_ST);
        const unsigned sto = ((lane >> 5) * 4 * S_ + P * 128 + (((lane >> 4) & 1) ^ h0) * 64 + w * 16 + i16) * 8;
        const u32x2 sp0 = ld8u(rst, sto, 0), sp1 = ld8u(rst, sto, S_ * 8), sp2 = ld8u(rst, sto, 2 * S_ * 8), sp3 = ld8u(rst, sto, 3 * S_ * 8);
        sb();
        bar();
        bf16x8 b[2][5];
        const unsigned rb = lb + i16 * 128 + ((q ^ (lane & 7)) << 4);
        const rsrc_t rab = make_rsrc(ws + WS_AB2, SZ_AB);
        const unsigned vo = ((2 * (w & 1) + (q >> 1)) * 16 + i16) * 16 + (q & 1) * 8;
        const unsigned so = ((ct & 7) * 128 + (ct >> 3) * 64 + P * 4 + (w >> 1)) * 5 * 1024;
        auto put = [&](auto H) {
          constexpr int hh = H.v;
#pragma unroll
          for (int f = 0; f < 5; ++f) {
            const f32x2 cs = {csv[f], csv[f]};
            u32x2 y;
#pragma unroll
            for (int e = 0; e < 2; ++e) {
              const f32x2 t = __builtin_elementwise_fma(f32x2{mu[hh][2 * e], mu[hh][2 * e + 1]}, cs, f32x2{acc[hh][f][2 * e], acc[hh][f][2 * e + 1]}) *
                              f32x2{rs[hh][2 * e], rs[hh][2 * e + 1]};
              y[e] = __builtin_bit_cast(unsigned, __builtin_convertvector(t, bf16x2));
            }
            st8(y, rab, vo + (f & 3) * 1024, so + (hh ^ h0) * 10240 + (f >> 2) * 4096);
          }
        };
#pragma unroll
        for (int f = 0; f < 5; ++f) b[0][f] = lds_rd(rb + wd_pos(0) * 1024 + f * 2048);
        sb();
        sfor<0, 40>([&](auto N) {
          constexpr int n = N.v, hh = n / 20, k = n % 20, kn = (k + 1) % 20;
          if constexpr (n == 2 * KP1) {
            float s1 = (__uint_as_float(sp0[0]) + __uint_as_float(sp1[0])) + (__uint_as_float(sp2[0]) + __uint_as_float(sp3[0]));
            float s2 = (__uint_as_float(sp0[1]) + __uint_as_float(sp1[1])) + (__uint_as_float(sp2[1]) + __uint_as_float(sp3[1]));
            auto ta = __builtin_amdgcn_permlane32_swap(__float_as_uint(s1), __float_as_uint(s1), false, false);
            auto tb = __builtin_amdgcn_permlane32_swap(__float_as_uint(s2), __float_as_uint(s2), false, false);
            float m, rr;
            ln_coef(__uint_as_float(ta[0]) + __uint_as_float(ta[1]), __uint_as_float(tb[0]) + __uint_as_float(tb[1]), m, rr);
            m = -m; rr *= sign;
#pragma unroll
            for (int r = 0; r < 2; ++r)
#pragma unroll
              for (int e = 0; e < 4; ++e) { mu[r][e] = __shfl(m, r * 16 + q * 4 + e); rs[r][e] = __shfl(rr, r * 16 + q * 4 + e); }
            asm volatile("" : "+v"(mu[0][0]), "+v"(mu[0][1]), "+v"(mu[0][2]), "+v"(mu[0][3]), "+v"(mu[1][0]), "+v"(mu[1][1]), "+v"(mu[1][2]), "+v"(mu[1][3]),
                              "+v"(rs[0][0]), "+v"(rs[0][1]), "+v"(rs[0][2]), "+v"(rs[0][3]), "+v"(rs[1][0]), "+v"(rs[1][1]), "+v"(rs[1][2]), "+v"(rs[1][3]));
            sb();
          }
          sfor<0, 5>([&](auto F) {
            constexpr int f = F.v;
            if constexpr (k == 0) mma0(acc[hh][f], a[n], b[n & 1][f]); else mma(acc[hh][f], a[n], b[n & 1][f]);
            if constexpr (n + 1 < 40) b[(n + 1) & 1][f] = lds_rd((rb ^ ((kn & 1) * 64)) + wd_pos(kn >> 1) * 1024 + f * 2048);
            if constexpr ((f == 1 || f == 3) && 6 + 2 * n + (f >> 1) < 40) LDA(6 + 2 * n + (f >> 1));
            sb();
          });
          if constexpr (n == 21) { pin_acc(acc[0]); put(IC<0>{}); sb(); }
          if constexpr (n == 20 + PUBG) publish(ws, f5 + 1024, epoch, lane);
          if constexpr (n == 0) ST(40);
          if constexpr (n == 9) ST(41);
          if constexpr (n == 19) ST(42);
        });
        fence_acc(acc[1]);
        bar();
        ST(39);
        put(IC<1>{});
        ST(20);
        publish(ws, f5 + 2048, epoch, lane);
      }
    }
    {
      PHASE_IDS
      IDS_G2
      const unsigned m = w >> 1, n = w & 1;
      const unsigned ra = lb + G2_A + (m * 64 + i16) * 128 + ((q ^ (lane & 7)) << 4);
      const unsigned rb = lb + n * 10240 + lane * 16;
      const unsigned row = P * 128 + m * 64 + kq * 16 + i16, ct = 2 * c4 + n;
      const TileOff ro = tile_offsets(L == 0, row, ct, q), oo = tile_offsets(false, row, ct, q);
      const rsrc_t rxp = make_rsrc(ws + WS_XP + L * SZ_XP, SZ_XP);
      const unsigned xo = ((P * 4 + c4) * 48 + w) * XSLOT;
      f32x4 acc[4][5];
#pragma unroll
      for (int i = 0; i < 4; ++i)
#pragma unroll
        for (int f = 0; f < 5; ++f) acc[i][f] = f32x4{0.f, 0.f, 0.f, 0.f};
      bf16x8 fa[2][4], fb[2][5];
      unsigned ra1 = ra ^ 64, rb2 = rb + 2 * G2_BS;
      asm volatile("" : "+v"(ra1), "+v"(rb2));
      auto half = [&](auto KK, auto SL) {
        constexpr int kk = KK.v, sl = SL.v;
        sfor<0, 20>([&](auto M) {
          constexpr int mm = M.v;
          mma(acc[mm / 5][mm % 5], fb[kk][mm % 5], fa[kk][mm / 5]);
          if constexpr (mm < 5) fb[kk ^ 1][mm] = lds_rd((sl < 2 ? rb : rb2) + (sl & 1) * G2_BS + (kk ^ 1) * 5120 + mm * 1024);
          else if constexpr (mm < 9) fa[kk ^ 1][mm - 5] = lds_rd((kk ? ra : ra1) + sl * G2_AS + (mm - 5) * 2048);
          sb();
        });
      };
      if (L) poll0(ws, FL_4 + P * 64 + (m * 4 + kq) * 8 + ct, 1, epoch, lane);
      ResTile rt = load_res(make_rsrc(L ? (const void*)(ws + WS_X2) : (const void*)opq_p(x0_), SZ_X), ro);
      fence_all(acc);
      bar();
      ST(sbase + 1);
#pragma unroll
      for (int f = 0; f < 5; ++f) fb[0][f] = lds_rd(rb + f * 1024);
#pragma unroll
      for (int i = 0; i < 4; ++i) fa[0][i] = lds_rd(ra + i * 2048);
      sb();
      auto stage = [&](auto U) {
        half(IC<0>{}, U);
        wait_lgkm0();
        bar();
        half(IC<1>{}, IC<(U.v + 1) & 3>{});
      };
#pragma unroll 1
      for (int g = 0; g < 3; ++g) sfor<0, 4>(stage);
      sfor<0, 3>(stage);
      half(IC<0>{}, IC<3>{});
      wait_lgkm0();
      bar();
      {
        unsigned p[3][10];
        u32x4 y[3][2];
        auto pack = [&](auto N) {
          constexpr int i = N.v / 5, f = N.v % 5;
          asm volatile("" : "+v"(acc[i][f]));
          p[i][2 * f] = pk2(acc[i][f][0], acc[i][f][1]); p[i][2 * f + 1] = pk2(acc[i][f][2], acc[i][f][3]);
          const unsigned o = xo + (((kq + 1 + i) & 3) * 3 + 2 - i) * 4 * XSLOT;
          if constexpr (f == 1) { y[i][0] = u32x4{p[i][0], p[i][1], p[i][2], p[i][3]}; st16_raw(y[i][0], rxp, lane * 16, o); }
          if constexpr (f == 3) { y[i][1] = u32x4{p[i][4], p[i][5], p[i][6], p[i][7]}; st16_raw(y[i][1], rxp, lane * 16 + 1024, o); }
          if constexpr (f == 4) st8(u32x2{p[i][8], p[i][9]}, rxp, lane * 8 + 2048, o);
        };
        sfor<0, 20>([&](auto M) {
          constexpr int mm = M.v;
          mma(acc[mm / 5][mm % 5], fb[1][mm % 5], fa[1][mm / 5]);
          if constexpr (mm >= 8) pack(IC<mm - 8>{});
          sb();
        });
        ST(sbase + 2);
        pack(IC<12>{}); pack(IC<13>{}); pack(IC<14>{});
        st_guard(y[0][0], y[0][1], y[1][0], y[1][1], y[2][0], y[2][1]);
      }
      f32x4 (&v)[5] = acc[3];
      fence_acc(v);
      const unsigned fx = FL_X + ((L * 64 + P * 4 + c4) * 4 + w) * 4;
      ST(sbase + 3);
      publish(ws, fx + kq, epoch, lane);
      ST(sbase + 4);
      poll<PDX>(ws, fx + ((kq + 1 + lane) & 3), 3, epoch, lane);
      ST(sbase + 5);
      const unsigned xi = xo + kq * 12 * XSLOT;
      u32x4 g0[3], g1[3]; u32x2 g2[3];
#pragma unroll
      for (int s = 0; s < 3; ++s) {
        g0[s] = ld16u(rxp, lane * 16, xi + s * 4 * XSLOT);
        g1[s] = ld16u(rxp, lane * 16 + 1024, xi + s * 4 * XSLOT);
        g2[s] = ld8u(rxp, lane * 8 + 2048, xi + s * 4 * XSLOT);
      }
      sb();
#pragma unroll
      for (int s = 0; s < 3; ++s) {
        v[0] += f32x4{bflo(g0[s][0]), bfhi(g0[s][0]), bflo(g0[s][1]), bfhi(g0[s][1])};
        v[1] += f32x4{bflo(g0[s][2]), bfhi(g0[s][2]), bflo(g0[s][3]), bfhi(g0[s][3])};
        v[2] += f32x4{bflo(g1[s][0]), bfhi(g1[s][0]), bflo(g1[s][1]), bfhi(g1[s][1])};
        v[3] += f32x4{bflo(g1[s][2]), bfhi(g1[s][2]), bflo(g1[s][3]), bfhi(g1[s][3])};
        v[4] += f32x4{bflo(g2[s][0]), bfhi(g2[s][0]), bflo(g2[s][1]), bfhi(g2[s][1])};
      }
      finish_tile(v, rt, make_rsrc(ws + (L ? WS_X3 : WS_X1), SZ_X), oo, true, make_rsrc(ws + (L ? WS_ST3 : WS_ST1), SZ_ST),
                  q == 0 ? (ct * S_ + row) * 8 : OOB);
      ST(sbase + 6);
      publish(ws, FL_2 + (L * 16 + P) * 64 + (m * 4 + kq) * 8 + ct, epoch, lane);
      ST(sbase + 7);
    }
    u32x4 y2[2]; u32x2 y1[2];
    {
      PHASE_IDS
      IDS_G3
      typedef __attribute__((ext_vector_type(2))) short s16x2;
      poll<PD2>(ws, FL_2 + (L * 16 + P) * 64 + w * 16 + i16, 16, epoch, lane);
      ST(sbase + 8);
      const rsrc_t rx = make_rsrc(ws + (L ? WS_X3 : WS_X1), SZ_X);
      const rsrc_t rst = make_rsrc(ws + (L ? WS_ST3 : WS_ST1), SZ_ST);
      const unsigned sox = (P * 8 + w * 2) * 20 * 1024;
      const unsigned par = ct & 1;
      bf16x8 a[20][2];
#pragma unroll
      for (int k = 0; k < 3; ++k) { a[k][0] = LDX(0, k); a[k][1] = LDX(1, k); }
      const unsigned sto = (2 * q * S_ + P * 128 + w * 32 + i16) * 8;
      const u32x2 sa0 = ld8u(rst, sto, 0), sa1 = ld8u(rst, sto, S_ * 8), sb0 = ld8u(rst, sto + 128, 0), sb1 = ld8u(rst, sto + 128, S_ * 8);
      float mu[2], rs[2];
      sb();
      bar();
      f32x4 acc[2][4];
      bf16x8 b[2][4];
      const unsigned rb = lb + lane * 16;
#pragma unroll
      for (int f = 0; f < 4; ++f) b[0][f] = lds_rd(rb + f * 1024);
      sb();
      sfor<0, 20>([&](auto K) {
        constexpr int k = K.v;
        const unsigned pb = k + 1 < 20 ? rb + (k + 1) * 4096 : rb + 80 * 1024;
        if constexpr (k == KP3) {
          ln_coef(xsum16_32(__uint_as_float(sa0[0]) + __uint_as_float(sa1[0])), xsum16_32(__uint_as_float(sa0[1]) + __uint_as_float(sa1[1])), mu[0], rs[0]);
          ln_coef(xsum16_32(__uint_as_float(sb0[0]) + __uint_as_float(sb1[0])), xsum16_32(__uint_as_float(sb0[1]) + __uint_as_float(sb1[1])), mu[1], rs[1]);
          asm volatile("" : "+v"(mu[0]), "+v"(mu[1]), "+v"(rs[0]), "+v"(rs[1]));
          sb();
        }
        sfor<0, 8>([&](auto M) {
          constexpr int f = M.v >> 1, r = M.v & 1;
          if constexpr (k == 0) mma0(acc[r][f], b[0][f], a[0][r]); else mma(acc[r][f], b[k & 1][f], a[k][r]);
          if constexpr (r == 0 && (k + 1 < 20 || f < 3)) b[(k + 1) & 1][f] = lds_rd(pb + f * 1024);
          if constexpr (r == 1 && 2 * k + 3 + (f >> 1) < 20) a[2 * k + 3 + (f >> 1)][f & 1] = LDX(f & 1, 2 * k + 3 + (f >> 1));
          sb();
        });
        if constexpr (k == 0) ST(sbase + 9);
        if constexpr (k == 3) ST(51 + L * 5);
        if constexpr (k == 7) ST(52 + L * 5);
        if constexpr (k == 11) ST(53 + L * 5);
        if constexpr (k == 15) ST(54 + L * 5);
        if constexpr (k == 19) ST(55 + L * 5);
      });
      auto act = [&](const f32x4& c, int r, const f32x4& cs, unsigned (&pk)[2]) {
        const f32x2 nm = {-mu[r], -mu[r]}, r2 = {rs[r], rs[r]};
#pragma unroll
        for (int e = 0; e < 2; ++e) {
          const f32x2 z = __builtin_elementwise_fma(nm, f32x2{cs[2 * e], cs[2 * e + 1]}, f32x2{c[2 * e], c[2 * e + 1]}) * r2;
          const s16x2 hb = __builtin_bit_cast(s16x2, __builtin_convertvector(z, bf16x2));
          pk[e] = __builtin_bit_cast(unsigned, __builtin_elementwise_max(hb, s16x2{0, 0}));
        }
      };
      auto pair = [&](const unsigned (&u)[2], const unsigned (&v)[2]) {
        auto t0 = __builtin_amdgcn_permlane16_swap(u[0], v[0], false, false);
        auto t1 = __builtin_amdgcn_permlane16_swap(u[1], v[1], false, false);
        return u32x4{t0[0], t1[0], t0[1], t1[1]};
      };
      const unsigned csb = lb + KM_CS1 + q * 16;
      f32x4 c1s[4];
#pragma unroll
      for (int f = 0; f < 4; ++f) c1s[f] = lds_rdf(csb + (f + par) * 64);
      asm volatile("s_nop 7\n\ts_nop 1" : "+v"(acc[0][0]), "+v"(acc[0][1]), "+v"(acc[0][2]), "+v"(acc[0][3]),
                                          "+v"(acc[1][0]), "+v"(acc[1][1]), "+v"(acc[1][2]), "+v"(acc[1][3]));
      wait_lgkm0();
      bar();
      ST(43 + L * 4);
      {
        const rsrc_t rh = make_rsrc(ws + (L ? WS_H2 : WS_H1), SZ_H);
        const unsigned vo = ((((q & 1) << 1) | (q >> 1)) * 16 + i16) * 16;
        const unsigned so = ((P * 8 + w * 2) * 54 + 7 * (ct >> 1) + 4 * par) * 1024;
        u32x4 y[2][2];
#pragma unroll
        for (int p = 0; p < 2; ++p) {
          const f32x4 c0 = c1s[2 * p], c1 = c1s[2 * p + 1];
#pragma unroll
          for (int r = 0; r < 2; ++r) {
            unsigned u[2], v[2];
            act(acc[r][2 * p], r, c0, u); act(acc[r][2 * p + 1], r, c1, v);
            y[p][r] = pair(u, v);
            st16_raw(y[p][r], rh, p == 1 && ct == 15 ? OOB : vo, so + (r * 54 + p) * 1024);
          }
        }
        st_guard(y[0][0], y[0][1], y[1][0], y[1][1]);
      }
      ST(44 + L * 4);
      sb();
      sfor<0, 20>([&](auto K) {
        constexpr int k = K.v;
        const unsigned pb = rb + 80 * 1024 + (k + 1) * 3072;
        sfor<0, 6>([&](auto M) {
          constexpr int f = M.v >> 1, r = M.v & 1;
          if constexpr (k == 0) mma0(acc[r][f], b[0][f], a[0][r]); else mma(acc[r][f], b[k & 1][f], a[k][r]);
          if constexpr (r == 0 && k + 1 < 20) b[(k + 1) & 1][f] = lds_rd(pb + f * 1024);
          sb();
        });
        if constexpr (k == PUB1) publish(ws, FL_3 + (L * 32 + P) * 64 + w * 16 + ct, epoch, lane);
      });
      const f32x4 c0 = lds_rdf(csb + (4 + par) * 64), c1 = lds_rdf(csb + (5 + par) * 64), c2 = lds_rdf(csb + (par ? 0 : 6) * 64);
      asm volatile("s_nop 7\n\ts_nop 1" : "+v"(acc[0][0]), "+v"(acc[0][1]), "+v"(acc[0][2]), "+v"(acc[1][0]), "+v"(acc[1][1]), "+v"(acc[1][2]));
      ST(sbase + 10);
#pragma unroll
      for (int r = 0; r < 2; ++r) {
        unsigned u[2], v[2], s[2];
        act(acc[r][0], r, c0, u); act(acc[r][1], r, c1, v); act(acc[r][2], r, c2, s);
        y2[r] = pair(u, v);
        y1[r] = u32x2{s[0], s[1]};
      }
      asm volatile("" : "+v"(y2[0]), "+v"(y2[1]), "+v"(y1[0]), "+v"(y1[1]));
      ST(sbase + 11);
    }
    {
      PHASE_IDS
      IDS_G4
      const unsigned row = rt64 * 64 + w * 16 + i16;
      const rsrc_t rh = make_rsrc(ws + (L ? WS_H2 : WS_H1), SZ_H);
      const unsigned soh = (rt64 * 4 * 54 + w * 14) * 1024;
      const unsigned vl = lane * 16, vz = lane * 16 + (w < 3 ? 0u : OOB);
      bf16x8 a[14][4], b[2][5];
      unsigned sh[4];
#pragma unroll
      for (int i = 0; i < 4; ++i) sh[i] = soh + ((w + 1 + i) & 3) * 54 * 1024;
#define LDH(t, i) a[t][i] = ld16(rh, (ORD[t] < 12 ? vl : vz) + (ORD[t] & 3) * 1024, sh[i] + (ORD[t] >> 2) * 4096)
      const TileOff ro = tile_offsets(false, row, c8, q), oo = tile_offsets(L != 0, row, c8, q);
      const unsigned f3 = FL_3 + (L * 32 + (rt64 >> 1)) * 64 + ((rt64 & 1) * 2 + (lane & 1)) * 16 + 4 * w + ((lane >> 1) & 3);
      poll0(ws, f3, 8, epoch, lane);
      ST(sbase + 13);
      sfor<0, 8>([&](auto N) { LDH(N.v / 4, N.v % 4); });
      sb();
      {
        const unsigned P = (xcd >> 1) * 4 + (idx >> 3), ct = (xcd & 1) * 8 + (idx & 7), par = ct & 1;
        const unsigned vo = ((((q & 1) << 1) | (q >> 1)) * 16 + i16) * 16;
        const unsigned v1 = ((2 * par + (q >> 1)) * 16 + i16) * 16 + (q & 1) * 8;
        const unsigned so = ((P * 8 + w * 2) * 54 + 7 * (ct >> 1)) * 1024;
#pragma unroll
        for (int r = 0; r < 2; ++r) {
          st16_raw(y2[r], rh, ct == 15 ? OOB : vo, so + (r * 54 + 2 + 4 * par) * 1024);
          st8(y1[r], rh, v1, so + (r * 54 + 3) * 1024);
        }
        st_guard(y2[0], y2[1]);
      }
      ResTile rt;
      u64 fl2;
      const rsrc_t rr = make_rsrc(ws + (L ? WS_X3 : WS_X1), SZ_X);
      sb();
      bar();
      f32x4 acc[4][5];
      const unsigned rbe = lb + w * 20480 + lane * 16, rbl = lb + G4_LATE + w * 15360 + lane * 16;
#pragma unroll
      for (int f = 0; f < 5; ++f) b[0][f] = lds_rd(rbe + f * 1024);
      sb();
      sfor<0, 14>([&](auto T) {
        constexpr int t = T.v;
        constexpr int ta = t < 5 ? (t == 0 ? 2 : t + 3) : (t == 5 ? -1 : (t == 6 ? 8 : (t + 3 < 14 ? t + 3 : -1)));
        if constexpr (t == 3 || t == 6 || t == 10) { wait_lgkm0(); bar(); }
        if constexpr (t == 6) { if (!__all(fl2 == epoch)) poll0(ws, f3 + 16 * 64, 8, epoch, lane); }
        sfor<0, 20>([&](auto M) {
          constexpr int m = M.v, i = m / 5, f = m % 5;
          if constexpr (t == 0) mma0(acc[i][f], b[0][f], a[0][i]); else mma(acc[i][f], b[t & 1][f], a[t][i]);
          if constexpr (t + 1 < 14 && (m & 1) == 1 && m < 10)
            b[(t + 1) & 1][m >> 1] = lds_rd((g4_late(t + 1) ? rbl : rbe) + g4_slot(t + 1) + (m >> 1) * 1024);
          if constexpr (ta >= 0 && (m & 1) == 0 && m < 8) LDH(ta, m >> 1);
          if constexpr (t == 6 && (m & 1) == 0 && m >= 8 && m < 16) LDH(9, (m - 8) >> 1);
          if constexpr (t == 0 && (m & 1) == 0 && m >= 8 && m < 16) LDH(3, (m - 8) >> 1);
          if constexpr (t == 2 && m == 10) rt = load_res(rr, ro);
          if constexpr (t == 4 && m == 18) fl2 = __hip_atomic_load(flag_ptr(ws, f3 + 16 * 64), RLX_AGENT);
          if constexpr (t == 13 && m >= 8) {
            asm volatile("" : "+v"(acc[(m - 8) / 5][(m - 8) % 5]));
            lds_wrf(rbl + (m - 8) * 1024, acc[(m - 8) / 5][(m - 8) % 5]);
          }
          sb();
        });
        if constexpr (t == 2) {
          wait_vm<19>();
          raise(ws, FL_3 + (L * 32 + 16 + (xcd >> 1) * 4 + (idx >> 3)) * 64 + w * 16 + (xcd & 1) * 8 + (idx & 7), epoch, lane);
          ST(sbase + 12);
        }
        if constexpr (t == 0) ST(sbase + 14);
        if constexpr (t == 3) ST(45 + L * 4);
        if constexpr (t == 6) ST(46 + L * 4);
      });
      ST(sbase + 15);
#pragma unroll
      for (int f = 2; f < 5; ++f) { asm volatile("" : "+v"(acc[2][f])); lds_wrf(rbl + (10 + f) * 1024, acc[2][f]); }
      f32x4 (&v)[5] = acc[3];
      fence_acc(v);
      wait_lgkm0();
      bar();
#pragma unroll
      for (int s = 1; s < 4; ++s) {
        const unsigned src = lb + G4_LATE + ((w + s) & 3) * 15360 + (3 - s) * 5 * 1024 + lane * 16;
#pragma unroll
        for (int f = 0; f < 5; ++f) v[f] += lds_rdf(src + f * 1024);
      }
      finish_tile(v, rt, L ? make_rsrc(opq_p(out_), SZ_X) : make_rsrc(ws + WS_X2, SZ_X), oo, L == 0, make_rsrc(ws + WS_ST2, SZ_ST),
                  q == 0 ? (c8 * S_ + row) * 8 : OOB);
      ST(sbase + 16);
      if (L == 0) publish(ws, FL_4 + (rt64 >> 1) * 64 + ((rt64 & 1) * 4 + w) * 8 + c8, epoch, lane);
    }
  }
  asm volatile(".Lcw_end:");
  if (blockIdx.x == 0 && w0 == 0) raise(ws_, FL_E, epoch, tid0);
  ST(37);
}

extern "C" void kernel_launch(void* const* d_in, const int* in_sizes, int n_in, void* d_out, int out_size, void* d_ws,
                              size_t ws_size, hipStream_t stream) {
  char* ws = (char*)d_ws;
  (void)hipFuncSetAttribute((const void*)k_all, hipFuncAttributeMaxDynamicSharedMemorySize, KM_LDS);
  k_all<<<256, 512, KM_LDS, stream>>>((const u16*)d_in[0], (const u16*)d_in[1], (const u16*)d_in[2], (const u16*)d_in[3],
                                     (const u16*)d_in[4], (const u16*)d_in[6], (const u16*)d_in[7], ws, (u16*)d_out STAMP_PASS(0));
}
```

```cpp
#include <hip/hip_runtime.h>
#include <hip/hip_bf16.h>
#include <stdint.h>

#define DI __device__ __forceinline__
typedef unsigned short u16;
typedef __attribute__((ext_vector_type(8))) __bf16 bf16x8;
typedef __attribute__((ext_vector_type(2))) __bf16 bf16x2;
typedef __attribute__((ext_vector_type(4))) float f32x4;
typedef __attribute__((ext_vector_type(2))) float f32x2;
typedef __attribute__((ext_vector_type(4))) unsigned u32x4;
typedef __attribute__((ext_vector_type(2))) unsigned u32x2;
typedef __attribute__((ext_vector_type(4))) short s16x4;
#define LDS_AS __attribute__((address_space(3)))
#define GLB_AS __attribute__((address_space(1)))
typedef LDS_AS char* ldsp;
typedef unsigned long long u64;
typedef GLB_AS u64 gu64;
typedef __amdgpu_buffer_rsrc_t rsrc_t;
#define RLX_AGENT __ATOMIC_RELAXED, __HIP_MEMORY_SCOPE_AGENT

constexpr int S_ = 2048, D_ = 640, F_ = 1728;
constexpr float EPS = 1e-5f;
constexpr unsigned OOB = 0x7f000000u;

constexpr unsigned SZ_AB = 8 * 128 * 5 * 1024;
constexpr unsigned SZ_X = 128 * 20 * 1024;
constexpr unsigned SZ_H = 128 * 54 * 1024;
constexpr unsigned SZ_W1 = 16 * 140 * 1024;
constexpr unsigned SZ_W2 = 8 * 54 * 5 * 1024;
constexpr unsigned SZ_ST = 2048 * 64;
constexpr unsigned WS_AB1 = 0;
constexpr unsigned WS_AB2 = WS_AB1 + SZ_AB;
constexpr unsigned WS_X1 = WS_AB2 + SZ_AB;
constexpr unsigned WS_X2 = WS_X1 + SZ_X;
constexpr unsigned WS_X3 = WS_X2 + SZ_X;
constexpr unsigned WS_H1 = WS_X3 + SZ_X;
constexpr unsigned WS_H2 = WS_H1 + SZ_H;
constexpr unsigned WS_W1 = WS_H2 + SZ_H;
constexpr unsigned WS_W2 = WS_W1 + SZ_W1;
constexpr unsigned WS_ST1 = WS_W2 + SZ_W2;
constexpr unsigned WS_ST2 = WS_ST1 + SZ_ST;
constexpr unsigned WS_ST3 = WS_ST2 + SZ_ST;
constexpr unsigned WS_CS1 = WS_ST3 + SZ_ST;
constexpr unsigned WS_CSD = WS_CS1 + 20 * 1728 * 4;
constexpr unsigned XSLOT = 2560;
constexpr unsigned SZ_XP = 256 * 12 * XSLOT;
constexpr unsigned WS_XP = (WS_CSD + 256 * 320 + 4095) & ~4095u;
constexpr unsigned WS_FLG = WS_XP + 2 * SZ_XP;
constexpr unsigned FL_X = 0;
constexpr unsigned FL_2 = FL_X + 2048;
constexpr unsigned FL_3 = FL_2 + 2048;
constexpr unsigned FL_4 = FL_3 + 4096;
constexpr unsigned FL_5 = FL_4 + 1024;
constexpr unsigned FL_W = FL_5 + 3072;
constexpr unsigned FL_E = FL_W + 1088;
constexpr unsigned WS_END = WS_FLG + (FL_E + 1) * 128;

DI u32x4 make_srd(const void* p, unsigned bytes) {
  uint64_t a = (uint64_t)p;
  u32x4 r;
  r[0] = (unsigned)a; r[1] = (unsigned)(a >> 32) & 0xffffu; r[2] = bytes; r[3] = 0x00020000u;
  return r;
}
DI rsrc_t make_rsrc(const void* p, unsigned bytes) {
  return __builtin_amdgcn_make_buffer_rsrc((void*)p, (short)0, (int)bytes, 0x00020000);
}
template <int LOFF, int GOFF>
DI void glds(u32x4 srd, unsigned voff, unsigned ldsb, unsigned soff) {
  unsigned tmp;
  asm volatile("s_add_u32 m0, %1, %2\n\ts_add_u32 %0, %3, %4\n\tbuffer_load_dwordx4 %5, %6, %0 offen lds"
               : "=&s"(tmp)
               : "s"(ldsb), "i"(LOFF), "s"(soff), "i"(GOFF), "v"(voff), "s"(srd)
               : "memory", "scc");
}
template <int N> DI void wait_vm() { asm volatile("s_waitcnt vmcnt(%0)" ::"i"(N) : "memory"); }
DI void wait_lgkm0() { asm volatile("s_waitcnt lgkmcnt(0)" ::: "memory"); }
DI void sb() { __builtin_amdgcn_sched_barrier(0); }
DI void bar() { asm volatile("s_barrier" ::: "memory"); }

DI bf16x8 lds_rd(unsigned addr) { return *(const LDS_AS bf16x8*)(ldsp)(uintptr_t)addr; }
DI f32x4 lds_rdf(unsigned addr) { return *(const LDS_AS f32x4*)(ldsp)(uintptr_t)addr; }
DI void lds_wrf(unsigned addr, f32x4 v) { *(LDS_AS f32x4*)(ldsp)(uintptr_t)addr = v; }
DI float lds_rd1(unsigned addr) { return *(const LDS_AS float*)(ldsp)(uintptr_t)addr; }
DI void lds_wr1(unsigned addr, float v) { *(LDS_AS float*)(ldsp)(uintptr_t)addr = v; }
DI void mma(f32x4& c, const bf16x8& a, const bf16x8& b) {
  asm volatile("v_mfma_f32_16x16x32_bf16 %0, %1, %2, %0" : "+v"(c) : "v"(a), "v"(b));
}
DI void mma0(f32x4& c, const bf16x8& a, const bf16x8& b) {
  asm volatile("v_mfma_f32_16x16x32_bf16 %0, %1, %2, 0" : "=&v"(c) : "v"(a), "v"(b));
}
DI void fence_acc(f32x4 (&a)[5]) { asm volatile("s_nop 7\n\ts_nop 1" : "+v"(a[0]), "+v"(a[1]), "+v"(a[2]), "+v"(a[3]), "+v"(a[4])); }
DI void pin_acc(f32x4 (&a)[5]) { asm volatile("" : "+v"(a[0]), "+v"(a[1]), "+v"(a[2]), "+v"(a[3]), "+v"(a[4])); }
template <int M> DI void fence_all(f32x4 (&a)[M][5]) {
#pragma unroll
  for (int i = 1; i < M; ++i) pin_acc(a[i]);
  fence_acc(a[0]);
#pragma unroll
  for (int i = 1; i < M; ++i) pin_acc(a[i]);
}
DI unsigned pk2(float a, float b) {
  f32x2 v = {a, b};
  return __builtin_bit_cast(unsigned, __builtin_convertvector(v, bf16x2));
}
DI float bflo(unsigned u) { return __uint_as_float(u << 16); }
DI float bfhi(unsigned u) { return __uint_as_float(u & 0xffff0000u); }
DI float dot2(unsigned a, unsigned b, float c) {
  return __builtin_amdgcn_fdot2_f32_bf16(__builtin_bit_cast(bf16x2, a), __builtin_bit_cast(bf16x2, b), c, false);
}
DI float xsum16_32(float x) {
  auto s = __builtin_amdgcn_permlane16_swap(__float_as_uint(x), __float_as_uint(x), false, false);
  x = __uint_as_float(s[0]) + __uint_as_float(s[1]);
  auto t = __builtin_amdgcn_permlane32_swap(__float_as_uint(x), __float_as_uint(x), false, false);
  return __uint_as_float(t[0]) + __uint_as_float(t[1]);
}
template <int V> struct IC { static constexpr int v = V; };
template <int I, int N, class Fn> DI void sfor(Fn&& f) {
  if constexpr (I < N) { f(IC<I>{}); sfor<I + 1, N>(f); }
}
DI unsigned rfl(unsigned v) { return __builtin_amdgcn_readfirstlane(v); }
DI unsigned opq_v(unsigned x) { asm volatile("" : "+v"(x)); return x; }
DI unsigned opq_s(unsigned x) { asm volatile("" : "+s"(x)); return x; }
template <class T> DI T* opq_p(T* x) { asm volatile("" : "+s"(x)); return x; }
DI bf16x8 ld16(rsrc_t r, unsigned voff, unsigned soff) { return __builtin_bit_cast(bf16x8, __builtin_amdgcn_raw_buffer_load_b128(r, voff, soff, 16)); }
DI u32x4 ld16u(rsrc_t r, unsigned voff, unsigned soff) { return __builtin_amdgcn_raw_buffer_load_b128(r, voff, soff, 16); }
DI u32x2 ld8u(rsrc_t r, unsigned voff, unsigned soff) { return __builtin_amdgcn_raw_buffer_load_b64(r, voff, soff, 16); }
DI void st16(u32x4 v, rsrc_t r, unsigned voff, unsigned soff) {
  __builtin_amdgcn_raw_buffer_store_b128(v, r, voff, soff, 16);
  asm volatile("s_nop 1" :: "v"(v) : "memory");
}
DI void st16_raw(u32x4 v, rsrc_t r, unsigned voff, unsigned soff) { __builtin_amdgcn_raw_buffer_store_b128(v, r, voff, soff, 16); }
template <class... T> DI void st_guard(const T&... v) {
  (..., [](const auto& x) { asm volatile("" :: "v"(x) : "memory"); }(v));
  asm volatile("s_nop 1" ::: "memory");
  (..., [](const auto& x) { asm volatile("" :: "v"(x)); }(v));
}
DI void st8(u32x2 v, rsrc_t r, unsigned voff, unsigned soff) { __builtin_amdgcn_raw_buffer_store_b64(v, r, voff, soff, 16); }
DI void st4(unsigned v, rsrc_t r, unsigned voff, unsigned soff) { __builtin_amdgcn_raw_buffer_store_b32(v, r, voff, soff, 16); }

template <int KS> DI unsigned foff(unsigned row, unsigned col) {
  return ((row >> 4) * KS + (col >> 5)) * 1024u + (((col >> 3) & 3) * 16u + (row & 15)) * 16u + (col & 7) * 2u;
}

extern __shared__ __attribute__((aligned(1024))) char smem[];
DI unsigned lds_base() { return (unsigned)(uintptr_t)(ldsp)smem; }

DI void ln_coef(float s1, float s2, float& mu, float& rs) {
  mu = s1 * (1.f / D_);
  rs = __builtin_amdgcn_rsqf(fmaxf(s2 * (1.f / D_) - mu * mu, 0.f) + EPS);
}

DI void g1_store(const f32x4 (&acc)[2][5], const float (&mu)[2][4], const float (&rs)[2][4], const float (&csv)[5], rsrc_t rab,
                 unsigned P, unsigned ct, unsigned w, unsigned i16, unsigned q) {
  const unsigned kstep = (ct >> 3) * 64 + P * 4 + w;
  const unsigned cq = ((q & 1) << 1) | (q >> 1);
  const unsigned so = ((ct & 7) * 128 + kstep) * 5 * 1024;
  const unsigned vo = (cq * 16 + i16) * 16;
  u32x4 y[5];
#pragma unroll
  for (int f = 0; f < 5; ++f) {
    const f32x2 cs = {csv[f], csv[f]};
    unsigned pk[2][2];
#pragma unroll
    for (int r = 0; r < 2; ++r)
#pragma unroll
      for (int e = 0; e < 2; ++e) {
        const f32x2 t = __builtin_elementwise_fma(f32x2{mu[r][2 * e], mu[r][2 * e + 1]}, cs, f32x2{acc[r][f][2 * e], acc[r][f][2 * e + 1]}) *
                        f32x2{rs[r][2 * e], rs[r][2 * e + 1]};
        pk[r][e] = __builtin_bit_cast(unsigned, __builtin_convertvector(t, bf16x2));
      }
    auto t0 = __builtin_amdgcn_permlane16_swap(pk[0][0], pk[1][0], false, false);
    auto t1 = __builtin_amdgcn_permlane16_swap(pk[0][1], pk[1][1], false, false);
    y[f] = u32x4{t0[0], t1[0], t0[1], t1[1]};
    st16_raw(y[f], rab, vo + (f & 3) * 1024, so + (f >> 2) * 4096);
  }
  st_guard(y[0], y[1], y[2], y[3], y[4]);
}

constexpr int K1_STAGE = 26 * 1024;
constexpr int K1_TR = 5 * K1_STAGE;
constexpr int K1_CS = K1_TR + 5 * 4096;

DI void k1_issue(u32x4 sa, u32x4 sbd, unsigned vo, unsigned la, unsigned lbb, unsigned soa, unsigned sob, unsigned w) {
  glds<0, 0>(sa, vo, la, soa);
  glds<1024, 8 * 1280>(sa, vo, la, soa);
  glds<2048, 16 * 1280>(sa, vo, la, soa);
  glds<3072, 24 * 1280>(sa, vo, la, soa);
  glds<0, 0>(sbd, vo, lbb, sob);
  glds<4096, 32 * 1280>(sbd, vo, lbb, sob);
  if (w < 2) glds<8192, 64 * 1280>(sbd, vo, lbb, sob);
}
struct RtUnit { bool first; unsigned ks, g; };
DI RtUnit rt_unit(unsigned u) {
  RtUnit r;
  r.first = u < 540;
  if (r.first) { r.ks = u / 27u; r.g = u - r.ks * 27u; }
  else { const unsigned v = u - 540; r.ks = v / 10u; r.g = v - r.ks * 10u; }
  return r;
}
DI void retile_issue(unsigned u, const u16* w1, const u16* w2, unsigned ldst, unsigned lane) {
  const RtUnit t = rt_unit(u);
  const unsigned ld = t.first ? F_ * 2 : D_ * 2;
  u32x4 srd = make_srd(t.first ? w1 : w2, D_ * F_ * 2);
  const unsigned vo = (lane >> 3) * ld + (lane & 7) * 16;
  const unsigned so = t.ks * 32 * ld + t.g * 128;
  glds<0, 0>(srd, vo, ldst, so);
  glds<1024, 0>(srd, vo, ldst, so + 8 * ld);
  glds<2048, 0>(srd, vo, ldst, so + 16 * ld);
  glds<3072, 0>(srd, vo, ldst, so + 24 * ld);
}
DI unsigned w1_off(unsigned rt, unsigned ks) {
  const unsigned c = rt / 7u, f = rt - c * 7, g = (c & 1) ? (f == 0 ? 6 : f - 1) : f;
  return WS_W1 + (c * 140 + (g < 4 ? ks * 4 + g : 80 + ks * 3 + g - 4)) * 1024;
}
DI void retile_finish(unsigned u, unsigned f0, rsrc_t rws, unsigned ldst, unsigned lane) {
  const RtUnit t = rt_unit(u);
  const unsigned i = lane & 15, q = lane >> 4;
  const unsigned base = ldst + (8 * q + (i >> 2)) * 128 + (i & 3) * 8 + f0 * 32;
  u32x4 v[2];
#pragma unroll
  for (int f = 0; f < 2; ++f) {
    s16x4 lo = __builtin_amdgcn_ds_read_tr16_b64_v4i16((LDS_AS s16x4*)(uintptr_t)(base + f * 32));
    s16x4 hi = __builtin_amdgcn_ds_read_tr16_b64_v4i16((LDS_AS s16x4*)(uintptr_t)(base + f * 32 + 512));
    u32x2 l2 = __builtin_bit_cast(u32x2, lo), h2 = __builtin_bit_cast(u32x2, hi);
    v[f] = u32x4{l2[0], l2[1], h2[0], h2[1]};
  }
  const unsigned rt = t.g * 4 + f0;
  if (t.first) {
    st16(v[0], rws, lane * 16, w1_off(rt, t.ks));
    st16(v[1], rws, lane * 16, w1_off(rt + 1, t.ks));
    float s[2];
#pragma unroll
    for (int f = 0; f < 2; ++f)
      s[f] = xsum16_32(((bflo(v[f][0]) + bfhi(v[f][0])) + (bflo(v[f][1]) + bfhi(v[f][1]))) + ((bflo(v[f][2]) + bfhi(v[f][2])) + (bflo(v[f][3]) + bfhi(v[f][3]))));
    st4(__float_as_uint(q == 0 ? s[0] : s[1]), rws, lane < 32 ? lane * 4 : OOB, WS_CS1 + (t.ks * F_ + rt * 16) * 4);
  } else {
    const unsigned c0 = rt / 5u, c1 = (rt + 1) / 5u;
    st16(v[0], rws, lane * 16, WS_W2 + ((c0 * 54 + t.ks) * 5 + (rt - c0 * 5)) * 1024);
    st16(v[1], rws, lane * 16, WS_W2 + ((c1 * 54 + t.ks) * 5 + (rt + 1 - c1 * 5)) * 1024);
  }
}

constexpr int WS1 = 4, WS2 = 7, RTI = 2;
constexpr int PUB1 = 13;
constexpr int PUBG = 11;
constexpr int KP3 = 3, KP1 = 3;
constexpr int RGAP = 10, ASLEEP2 = 40, WDPRE = 6;
constexpr int PD4 = 13, PDX = 15, PD2 = 30, P5DELAY0 = 4, P5DELAY = 1;

constexpr int KM_LDS = 160 * 1024;
constexpr int KM_CS1 = KM_LDS - 1024;
constexpr int G2_A = 80 * 1024, G2_AS = 16 * 1024, G2_BS = 20 * 1024;
constexpr int G4_LATE = 80 * 1024;

DI gu64* flag_ptr(char* ws, unsigned f) { return (gu64*)(ws + WS_FLG + f * 128); }
DI void raise(char* ws, unsigned f, u64 epoch, unsigned lane) {
  if (lane < 16) __hip_atomic_store(flag_ptr(ws, f) + lane, epoch, RLX_AGENT);
}
DI void publish(char* ws, unsigned f, u64 epoch, unsigned lane) {
  wait_vm<0>();
  raise(ws, f, epoch, lane);
}
DI void poll0(char* ws, unsigned f, unsigned n, u64 epoch, unsigned lane) {
  gu64* p = flag_ptr(ws, f);
  bool ok = lane >= n;
#pragma unroll 1
  for (unsigned s = 0; s < (1u << 16); ++s) {
    if (!ok) ok = __hip_atomic_load(p, RLX_AGENT) == epoch;
    if (__all(ok)) break;
    __builtin_amdgcn_s_sleep(1);
  }
  asm volatile("" ::: "memory");
}
template <int DELAY> DI void poll(char* ws, unsigned f, unsigned n, u64 epoch, unsigned lane) {
  __builtin_amdgcn_s_sleep(DELAY);
  poll0(ws, f, n, epoch, lane);
}
DI void warm(unsigned ldump, u32x4 srd, unsigned voff, unsigned soff) {
  asm volatile("s_mov_b32 m0, %0\n\ts_nop 0\n\tbuffer_load_dword %1, %2, %3 offen lds" :: "s"(ldump), "v"(voff), "s"(srd), "s"(soff) : "memory");
}
DI void glds4_sc1(u32x4 srd, unsigned voff, unsigned l, unsigned so) {
  asm volatile("s_mov_b32 m0, %0\n\ts_nop 0\n\tbuffer_load_dwordx4 %1, %2, %3 offen sc1 lds\n\tbuffer_load_dwordx4 %1, %2, %3 offen offset:1024 sc1 lds\n\t"
               "buffer_load_dwordx4 %1, %2, %3 offen offset:2048 sc1 lds\n\tbuffer_load_dwordx4 %1, %2, %3 offen offset:3072 sc1 lds"
               :: "s"(l), "v"(voff), "s"(srd), "s"(so) : "memory");
}
DI void glds1_sc1(u32x4 srd, unsigned voff, unsigned l, unsigned so) {
  asm volatile("s_mov_b32 m0, %0\n\ts_nop 0\n\tbuffer_load_dwordx4 %1, %2, %3 offen sc1 lds" :: "s"(l), "v"(voff), "s"(srd), "s"(so) : "memory");
}
template <int N> DI void glds_run_sc1(u32x4 srd, unsigned voff, unsigned l, unsigned so) {
#pragma unroll
  for (int g = 0; g < N / 4; ++g) glds4_sc1(srd, voff, l + g * 4096, so + g * 4096);
#pragma unroll
  for (int g = N / 4 * 4; g < N; ++g) glds1_sc1(srd, voff, l + g * 1024, so + g * 1024);
}

struct ResTile { u32x4 r0, r1; u32x2 r2; };
struct TileOff { unsigned a0, a1, a2; };
DI TileOff tile_offsets(bool rm, unsigned row, unsigned ct, unsigned q) {
  const unsigned cA = ct * 80 + 16 * (q & 1) + 8 * (q >> 1), cS = ct * 80 + 64 + 4 * q;
  TileOff t;
  if (rm) { t.a0 = (row * D_ + cA) * 2; t.a1 = t.a0 + 64; t.a2 = (row * D_ + cS) * 2; }
  else { t.a0 = foff<20>(row, cA); t.a1 = foff<20>(row, cA + 32); t.a2 = foff<20>(row, cS); }
  asm volatile("" : "+v"(t.a0), "+v"(t.a1), "+v"(t.a2));
  return t;
}
DI ResTile load_res(rsrc_t rr, const TileOff& t) {
  ResTile rt;
  rt.r0 = ld16u(rr, t.a0, 0); rt.r1 = ld16u(rr, t.a1, 0); rt.r2 = ld8u(rr, t.a2, 0);
  return rt;
}
DI void finish_tile(f32x4 (&v)[5], const ResTile& rt, rsrc_t rout, const TileOff& o, bool stats, rsrc_t rst, unsigned sto) {
#pragma unroll
  for (int p = 0; p < 2; ++p)
#pragma unroll
    for (int r = 0; r < 4; ++r) {
      auto t = __builtin_amdgcn_permlane16_swap(__float_as_uint(v[2 * p][r]), __float_as_uint(v[2 * p + 1][r]), false, false);
      v[2 * p][r] = __uint_as_float(t[0]); v[2 * p + 1][r] = __uint_as_float(t[1]);
    }
  u32x4 y0, y1; u32x2 y2;
#pragma unroll
  for (int j = 0; j < 2; ++j) {
    y0[j] = pk2(v[0][2 * j] + bflo(rt.r0[j]), v[0][2 * j + 1] + bfhi(rt.r0[j]));
    y0[2 + j] = pk2(v[1][2 * j] + bflo(rt.r0[2 + j]), v[1][2 * j + 1] + bfhi(rt.r0[2 + j]));
    y1[j] = pk2(v[2][2 * j] + bflo(rt.r1[j]), v[2][2 * j + 1] + bfhi(rt.r1[j]));
    y1[2 + j] = pk2(v[3][2 * j] + bflo(rt.r1[2 + j]), v[3][2 * j + 1] + bfhi(rt.r1[2 + j]));
    y2[j] = pk2(v[4][2 * j] + bflo(rt.r2[j]), v[4][2 * j + 1] + bfhi(rt.r2[j]));
  }
  if (stats) {
    st16_raw(y0, rout, o.a0, 0); st16_raw(y1, rout, o.a1, 0); st8(y2, rout, o.a2, 0);
  } else {
    __builtin_amdgcn_raw_buffer_store_b128(y0, rout, o.a0, 0, 0);
    __builtin_amdgcn_raw_buffer_store_b128(y1, rout, o.a1, 0, 0);
    __builtin_amdgcn_raw_buffer_store_b64(y2, rout, o.a2, 0, 0);
  }
  st_guard(y0, y1);
  if (stats) {
    float s1a = 0.f, s1b = 0.f, s2a = 0.f, s2b = 0.f;
#pragma unroll
    for (int j = 0; j < 4; ++j) {
      s1a = dot2(y0[j], 0x3f803f80u, s1a); s2a = dot2(y0[j], y0[j], s2a);
      s1b = dot2(y1[j], 0x3f803f80u, s1b); s2b = dot2(y1[j], y1[j], s2b);
    }
    s1a = dot2(y2[0], 0x3f803f80u, s1a); s2a = dot2(y2[0], y2[0], s2a);
    s1b = dot2(y2[1], 0x3f803f80u, s1b); s2b = dot2(y2[1], y2[1], s2b);
    const float s1 = xsum16_32(s1a + s1b), s2 = xsum16_32(s2a + s2b);
    st8(u32x2{__float_as_uint(s1), __float_as_uint(s2)}, rst, sto, 0);
  }
}

DI constexpr unsigned wd_pos(int c) { return c < 8 ? 10 * c : 60 + 10 * c; }
constexpr int ORD[14] = {0, 1, 4, 5, 7, 8, 11, 12, 2, 3, 6, 9, 10, 13};
DI constexpr int g4_late(int t) { return (t >= 4 && t < 7) || t >= 11; }
DI constexpr int g4_slot(int t) { return (t < 4 ? t : (t < 7 ? t - 4 : (t < 11 ? t - 7 : t - 11))) * 5120; }

#define PHASE_IDS \
  const unsigned lane = opq_v(tid0) & 63, w = opq_s(w0), bid = opq_s(blockIdx.x); \
  const unsigned xcd = bid & 7, idx = bid >> 3, i16 = lane & 15, q = lane >> 4; \
  char* const ws = opq_p(ws_); \
  (void)xcd; (void)idx; (void)i16; (void)q; (void)ws;
#define IDS_G2 const unsigned kq = xcd & 3, P = (xcd >> 2) * 8 + (idx >> 2), c4 = idx & 3;
#define G2_LA(h) (lb + G2_A + ((w >> 1) * 4 + ((2 * (w & 1) + (h) + 3 - kq) & 3)) * 2048)
#define IDS_G3 const unsigned P = (xcd >> 1) * 4 + (idx >> 3), ct = (xcd & 1) * 8 + (idx & 7);
#define C8_OF ((xcd & 1) * 4 + (idx & 3))
#define IDS_G4 const unsigned rt64 = (xcd >> 1) * 8 + (idx >> 2), c8 = C8_OF;
#define LDX(r, k) ld16(rx, lane * 16 + ((k) & 3) * 1024, sox + (r) * 20480 + ((k) >> 2) * 4096)

__global__ __launch_bounds__(512, 2) void k_all(const u16* x0_, const u16* Sre_, const u16* Sim_, const u16* Dre_, const u16* Dim_,
                                               const u16* w1_, const u16* w2_, char* ws_, u16* out_) {
  const unsigned tid0 = threadIdx.x, wall = rfl(tid0 >> 6), w0 = wall & 3;
  const unsigned lb = lds_base();
  const char* code; unsigned code_len;
  asm volatile("s_getpc_b64 %0\n.Lcw_begin:\n\ts_mov_b32 %1, .Lcw_end-.Lcw_begin" : "=s"(code), "=s"(code_len));
  u64 epoch;

  if (__builtin_expect(wall >= 4, 1)) {
    const unsigned sink = lb + K1_CS + 1024;
    unsigned ep0, ep1;
    asm volatile("global_load_dword %0, %2, %3 sc1\n\tglobal_load_dword %1, %2, %3 offset:4 sc1"
                 : "=&v"(ep0), "=&v"(ep1) : "v"(WS_FLG + FL_E * 128), "s"(ws_) : "memory");
    {
      PHASE_IDS
      IDS_G3
      const u32x4 sa = make_srd(opq_p(x0_), S_ * D_ * 2), sbd = make_srd(ct < 8 ? opq_p(Dre_) : opq_p(Dim_), D_ * D_ * 2);
      const unsigned vo = (lane >> 3) * 1280 + (((lane & 7) ^ (lane >> 3)) << 4);
      const unsigned soa = (P * 128 + w * 32) * 1280, sob = ((ct & 7) * 80 + w * 8) * 1280;
      const unsigned la = lb + w * 4096, lbb = lb + 16384 + w * 1024;
      const unsigned u0 = bid * 4 + w, u1 = 1024 + bid;
      const bool extra = w == 3 && bid < 56;
      const unsigned rot = ((idx & 7) + 3 * (idx >> 3)) % 10u;
      auto ko = [&](unsigned s) { const unsigned c = s + rot; return (c >= 10 ? c - 10 : c) * 128; };
      asm volatile("s_nop 4" ::: "memory");
      k1_issue(sa, sbd, vo, la, lbb, soa + ko(0), sob + ko(0), w);
      k1_issue(sa, sbd, vo, la + K1_STAGE, lbb + K1_STAGE, soa + ko(1), sob + ko(1), w);
      wait_vm<6>();
      {
        unsigned lo, hi;
        asm volatile("v_readfirstlane_b32 %0, %2\n\tv_readfirstlane_b32 %1, %3\n\ts_nop 4" : "=s"(lo), "=s"(hi) : "v"(ep0), "v"(ep1));
        epoch = (u64)lo + ((u64)hi << 32) + 1;
      }
      bar();
      k1_issue(sa, sbd, vo, la + 2 * K1_STAGE, lbb + 2 * K1_STAGE, soa + ko(2), sob + ko(2), w);
      const u32x4 ss = make_srd((xcd & 2) ? Sim_ : Sre_, S_ * S_ * 2);
      const unsigned vs = (lane & 31) * 4096 + (lane >> 5) * 128;
      const unsigned sos = ((xcd >> 2) * 1024 + idx * 32) * 4096 + (xcd & 1) * 2048 + w * 256;
      unsigned so = 0;
#pragma unroll 1
      for (int i = 0; i < 10; ++i) {
        if (i == 0) wait_vm<6>(); else if (i == 1) wait_vm<2 * 6>(); else if (i < 6) wait_vm<3 * 6>();
        else if (i == 6) wait_vm<2 * 6 + (RTI >= 2 && RTI < 6 ? 4 : 0)>(); else if (i == 7) wait_vm<6 + (RTI >= 3 && RTI < 7 ? 4 : 0)>();
        else wait_vm<(RTI >= 4 && RTI < 8 ? 4 : 0) + (WS2 >= 4 && WS2 < 8) + (WS1 >= 4 && WS1 < 8)>();
        bar();
        if (i == 0) {
          k1_issue(sa, sbd, vo, la + 3 * K1_STAGE, lbb + 3 * K1_STAGE, soa + ko(3), sob + ko(3), w);
          k1_issue(sa, sbd, vo, la + 4 * K1_STAGE, lbb + 4 * K1_STAGE, soa + ko(4), sob + ko(4), w);
        } else if (i == 1) {
          k1_issue(sa, sbd, vo, la, lbb, soa + ko(5), sob + ko(5), w);
          k1_issue(sa, sbd, vo, la + K1_STAGE, lbb + K1_STAGE, soa + ko(6), sob + ko(6), w);
        } else if (i < 5) k1_issue(sa, sbd, vo, la + so, lbb + so, soa + ko(i + 5), sob + ko(i + 5), w);
        so = so == 4 * K1_STAGE ? 0 : so + K1_STAGE;
        if (i == WS1) warm(sink, ss, vs, sos);
        if (i == WS2) warm(sink, ss, vs, sos + 1024);
        if (i == RTI) {
          retile_issue(u0, w1_, w2_, lb + K1_TR + w * 4096, lane);
          if (extra) retile_issue(u1, w1_, w2_, lb + K1_TR + 4 * 4096, lane);
        }
      }
      bar();
    }
    bf16x8 wr[10][5];
#pragma unroll
    for (int t = 0; t < 10; ++t)
#pragma unroll
      for (int f = 0; f < 5; ++f) wr[t][f] = bf16x8{};
#pragma unroll 1
    for (int L = 0; L < 2; ++L) {
      {
        PHASE_IDS
        IDS_G2
        const u32x4 sa = make_srd(kq < 2 ? opq_p(Sre_) : opq_p(Sim_), S_ * S_ * 2);
        const u32x4 sb_ = make_srd(ws + (L ? WS_AB2 : WS_AB1), SZ_AB);
        const unsigned va = (lane >> 3) * 4096 + (((lane & 7) ^ (lane >> 3)) << 4), vb = lane * 16;
        const unsigned soa = (P * 128 + w * 32) * 4096 + (kq & 1) * 2048;
        const unsigned sob = ((2 * c4 + (w >> 1)) * 128 + kq * 32 + (w & 1)) * 5 * 1024;
        const unsigned la = lb + G2_A + w * 4096, lbb = lb + w * 5120;
        const unsigned la0 = G2_LA(0), la1 = G2_LA(1);
        const unsigned ct = (xcd & 1) * 8 + (idx & 7);
        auto kc = [&](unsigned s) { return L ? 2 * (s & 7) + ((s ^ (s >> 3)) & 1) : s; };
        asm volatile("s_nop 4" ::: "memory");
        auto issue_a = [&](unsigned l, unsigned so) {
          glds<0, 0>(sa, va, la0 + l, so); glds<1024, 8 * 4096>(sa, va, la0 + l, so);
          glds<0, 16 * 4096>(sa, va, la1 + l, so); glds<1024, 24 * 4096>(sa, va, la1 + l, so);
        };
        if (L == 0) {
#pragma unroll
          for (int s = 0; s < 3; ++s) issue_a(s * G2_AS, soa + s * 128);
          const unsigned u0 = bid * 4 + w, u1 = 1024 + bid;
          const rsrc_t rws = make_rsrc(ws, WS_END);
          wait_vm<12>();
          retile_finish(u0, 0, rws, lb + K1_TR + w * 4096, lane);
          retile_finish(u0, 2, rws, lb + K1_TR + w * 4096, lane);
          if (w == 3 && bid < 56) {
            retile_finish(u1, 0, rws, lb + K1_TR + 4 * 4096, lane);
            retile_finish(u1, 2, rws, lb + K1_TR + 4 * 4096, lane);
          }
          __builtin_amdgcn_s_sleep(P5DELAY0);
        } else {
          issue_a(3 * G2_AS, soa + kc(3) * 128);
          __builtin_amdgcn_s_sleep(P5DELAY);
        }
        const unsigned f5 = FL_5 + ((L * 4 + kq) * 4 + c4) * 64 + ((i16 >> 1) * 2 + (w >> 1)) * 4 + (L ? 2 * (w & 1) + (i16 & 1) : (w & 1) + 2 * (i16 & 1));
        poll0(ws, f5, 16, epoch, lane);
        wait_vm<0>();
        glds_run_sc1<5>(sb_, vb, lbb, sob);
        glds_run_sc1<5>(sb_, vb, lbb + G2_BS, sob + kc(1) * 10240);
        if (L == 0) {
          raise(ws, FL_W + bid * 4 + w, epoch, lane);
          if (w == 3 && bid < 56) raise(ws, FL_W + 1024 + bid, epoch, lane);
          wait_vm<6>();
        } else wait_vm<5>();
        bar();
        glds_run_sc1<5>(sb_, vb, lbb + 2 * G2_BS, sob + kc(2) * 10240);
        if (L == 0) issue_a(3 * G2_AS, soa + kc(3) * 128);
        glds_run_sc1<5>(sb_, vb, lbb + 3 * G2_BS, sob + kc(3) * 10240);
        const u32x4 sw = make_srd(ws + WS_W1, SZ_W1);
        const unsigned sow = ct * 140 * 1024;
        unsigned fw, fw0 = 0, fw1 = 0;
        {
          const unsigned n = w * 42 + (lane < 42 ? lane : 0), k3 = n / 3u, g3 = (7 * ct >> 2) + (n - k3 * 3), n2 = n - 60;
          fw = FL_W + (n < 60 ? k3 * 27 + (g3 < 26 ? g3 : 26) : 540 + (n2 >> 1) * 10 + (5 * C8_OF >> 2) + (n2 & 1));
          if (L) fw = f5 + 1024;
        }
#pragma unroll 1
        for (int s = 0; s < 16; ++s) {
          if (s == 0) wait_vm<10>();
          if (s == 1) wait_vm<14>();
          wait_vm<18>();
          if (s == (L ? 4 : 9)) {
            asm volatile("" : "+v"(fw0), "+v"(fw1));
            if (!__all(fw0 == (unsigned)epoch && fw1 == (unsigned)(epoch >> 32))) poll0(ws, fw, 42, epoch, lane);
          }
          bar();
          const unsigned sl = s & 3;
          if (s < 12) {
            issue_a(sl * G2_AS, soa + kc(s + 4) * 128);
            glds_run_sc1<5>(sb_, vb, lbb + sl * G2_BS, sob + kc(s + 4) * 10240);
            if (s == (L ? 1 : 6))
              asm volatile("global_load_dword %0, %2, %3 sc1\n\tglobal_load_dword %1, %2, %3 offset:4 sc1"
                           : "+v"(fw0), "+v"(fw1) : "v"(WS_FLG + fw * 128), "s"(ws) : "memory");
          } else {
            glds_run_sc1<5>(sw, vb, lbb + sl * G2_BS, sow + w * 5120 + sl * G2_BS);
            if (s < 15) glds_run_sc1<4>(sw, vb, la + sl * G2_AS, sow + G2_A + w * 4096 + sl * G2_AS);
            else glds_run_sc1<3>(sw, vb, lb + G2_A + 3 * G2_AS + w * 3072, sow + G2_A + 3 * G2_AS + w * 3072);
          }
        }
      }
      {
        PHASE_IDS
        IDS_G4
        const unsigned ct = (xcd & 1) * 8 + (idx & 7);
        if (w >= 2) {
          const rsrc_t rc = make_rsrc(ws + WS_CS1, 20 * F_ * 4);
          const unsigned c = (w - 2) * 64 + lane, col = ct * 112 + c;
          const unsigned vo = c < 112 && col < F_ ? col * 4 : OOB;
          float csp = 0.f;
#pragma unroll
          for (int k = 0; k < 20; ++k) csp += __uint_as_float(__builtin_amdgcn_raw_buffer_load_b32(rc, vo, k * F_ * 4, 16));
          lds_wr1(lb + KM_CS1 + c * 4, csp);
        }
        const unsigned so = (c8 * 54 + w * 14) * 5 * 1024;
        wait_vm<0>();
        if (L == 0) {
          const rsrc_t rw = make_rsrc(ws + WS_W2, SZ_W2);
          const unsigned vl = lane * 16, vz = lane * 16 + (w < 3 ? 0u : OOB);
          sfor<0, 50>([&](auto N) {
            constexpr int t = 4 + N.v / 5, n = ORD[t] * 5 + N.v % 5;
            wr[t - 4][N.v % 5] = ld16(rw, (ORD[t] < 12 ? vl : vz) + (n & 3) * 1024, so + (n >> 2) * 4096);
            if constexpr (N.v % 5 == 4 && N.v < 49) { sb(); __builtin_amdgcn_s_sleep(RGAP); sb(); }
          });
          sb();
        }
        wait_lgkm0();
        bar();
        bar();
        const u32x4 s2 = make_srd(ws + WS_W2, SZ_W2);
        const unsigned sg = rfl(so);
        asm volatile("s_nop 4" ::: "memory");
        glds_run_sc1<10>(s2, lane * 16, lb + w * 20480, sg);
        glds_run_sc1<10>(s2, lane * 16, lb + w * 20480 + 10240, sg + 4 * 5120);
        wait_vm<0>();
        const unsigned pe = lb + w * 20480 + lane * 16, pl = lb + G4_LATE + w * 15360 + lane * 16;
        auto put = [&](auto T0, auto T1) {
          sfor<T0.v * 5, T1.v * 5>([&](auto N) {
            constexpr int t = N.v / 5, f = N.v % 5;
            *(LDS_AS bf16x8*)(ldsp)(uintptr_t)((g4_late(t) ? pl : pe) + g4_slot(t) + f * 1024) = wr[t - 4][f];
          });
          wait_lgkm0();
        };
        bar();
        put(IC<4>{}, IC<7>{});
        bar();
        put(IC<7>{}, IC<11>{});
        bar();
        put(IC<11>{}, IC<14>{});
        bar();
      }
      if (L) { bar(); break; }
      {
        PHASE_IDS
        IDS_G3
        const u32x4 sd = make_srd(ct < 8 ? opq_p(Dre_) : opq_p(Dim_), D_ * D_ * 2);
        const unsigned vo = (lane >> 3) * 1280 + (((lane & 7) ^ (lane >> 3)) << 4);
        const unsigned so = (ct & 7) * 80 * 1280;
        sfor<0, 10>([&](auto G) { glds<G.v * 1024, G.v * 8 * 1280>(sd, vo, lb + 20 * w * 1024, so + 2 * w * 128); });
        sfor<0, WDPRE>([&](auto G) { glds<G.v * 1024, G.v * 8 * 1280>(sd, vo, lb + (20 * w + 10) * 1024, so + (2 * w + 1) * 128); });
        bar();
        sfor<WDPRE, 10>([&](auto G) { glds<G.v * 1024, G.v * 8 * 1280>(sd, vo, lb + (20 * w + 10) * 1024, so + (2 * w + 1) * 128); });
        sfor<0, 5>([&](auto G) { glds<G.v * 1024, G.v * 8 * 1280>(sd, vo, lb + (140 + w * 5) * 1024, so + (8 + (w >> 1)) * 128 + (w & 1) * 40 * 1280); });
        wait_vm<0>();
        bar();
      }
      {
        PHASE_IDS
        IDS_G2
        const u32x4 sa = make_srd(kq < 2 ? opq_p(Sre_) : opq_p(Sim_), S_ * S_ * 2);
        const unsigned va = (lane >> 3) * 4096 + (((lane & 7) ^ (lane >> 3)) << 4);
        const unsigned soa = (P * 128 + w * 32) * 4096 + (kq & 1) * 2048;
        __builtin_amdgcn_s_sleep(ASLEEP2);
#pragma unroll
        for (int s = 0; s < 3; ++s) {
          const unsigned l0 = G2_LA(0) + s * G2_AS, l1 = G2_LA(1) + s * G2_AS, so = soa + (2 * s + (s & 1)) * 128;
          glds<0, 0>(sa, va, l0, so); glds<1024, 8 * 4096>(sa, va, l0, so);
          glds<0, 16 * 4096>(sa, va, l1, so); glds<1024, 24 * 4096>(sa, va, l1, so);
        }
        bar();
      }
    }
    return;
  }

  {
    const u64 e = __hip_atomic_load(flag_ptr(ws_, FL_E), RLX_AGENT);
    epoch = (u64)rfl((unsigned)e) + ((u64)rfl((unsigned)(e >> 32)) << 32) + 1;
  }
#pragma unroll 1
  for (int L = 0; L < 2; ++L) {
    {
      PHASE_IDS
      IDS_G3
      f32x4 acc[2][5];
      float mu[2][4], rs[2][4], csv[5];
      const float sign = ct < 8 ? 1.f : -1.f;
      const unsigned f5 = FL_5 + (((ct >> 3) * 2 + (P >> 3)) * 4 + ((ct & 7) >> 1)) * 64 + ((P & 7) * 2 + (ct & 1)) * 4 + w;
      if (L == 0) {
        const unsigned ra0 = lb + w * 4096 + i16 * 128 + ((q ^ (lane & 7)) << 4);
        const unsigned rb0 = lb + 16384 + i16 * 128 + ((q ^ (lane & 7)) << 4);
        const f32x4 z4 = {0.f, 0.f, 0.f, 0.f};
        f32x4 cs0 = z4, cs1 = z4, rs_[2] = {z4, z4}, rq_[2] = {z4, z4};
#pragma unroll
        for (int r = 0; r < 2; ++r)
#pragma unroll
          for (int f = 0; f < 5; ++f) acc[r][f] = z4;
        bf16x8 fr[2][8];
        bf16x8 one = __builtin_bit_cast(bf16x8, u32x4{0x3f803f80u, 0x3f803f80u, 0x3f803f80u, 0x3f803f80u});
        auto half = [&](auto KK, unsigned so) {
          constexpr int kk = KK.v;
          const unsigned pa = (ra0 ^ ((kk ^ 1) * 64)) + so, pb = (rb0 ^ ((kk ^ 1) * 64)) + so;
          sfor<0, 16>([&](auto M) {
            constexpr int m = M.v;
            if constexpr (m < 10) mma(acc[m & 1][m >> 1], fr[kk][m & 1], fr[kk][2 + (m >> 1)]);
            else if constexpr (m == 10) mma(cs0, one, fr[kk][7]);
            else if constexpr (m == 11) mma(cs1, one, fr[kk][6]);
            else if constexpr (m < 14) mma(rs_[m - 12], fr[kk][m - 12], one);
            else mma(rq_[m - 14], fr[kk][m - 14], fr[kk][m - 14]);
            constexpr int n = m >> 1;
            if constexpr ((m & 1) == 0) fr[kk ^ 1][n] = n < 2 ? lds_rd(pa + n * 2048) : (n < 7 ? lds_rd(pb + (n - 2) * 2048) : lds_rd(pb + w * 2048));
            sb();
          });
        };
        warm(lb + K1_CS + 1024, make_srd(code, code_len), lane < 4 ? (idx * 16 + w * 4 + lane) * 128 : OOB, 0);
        asm volatile("s_nop 1" : "+v"(cs0), "+v"(cs1), "+v"(rs_[0]), "+v"(rs_[1]), "+v"(rq_[0]), "+v"(rq_[1]), "+v"(one));
        fence_all(acc);
        bar();
        fr[0][0] = lds_rd(ra0); fr[0][1] = lds_rd(ra0 + 2048);
#pragma unroll
        for (int f = 0; f < 5; ++f) fr[0][2 + f] = lds_rd(rb0 + f * 2048);
        fr[0][7] = lds_rd(rb0 + w * 2048);
        sb();
        unsigned so = 0;
#pragma unroll 1
        for (int i = 0; i < 10; ++i) {
          half(IC<0>{}, so);
          wait_lgkm0();
          bar();
          so = so == 4 * K1_STAGE ? 0 : so + K1_STAGE;
          half(IC<1>{}, so);
        }
        fence_all(acc);
        asm volatile("" : "+v"(cs0), "+v"(cs1), "+v"(rs_[0]), "+v"(rs_[1]), "+v"(rq_[0]), "+v"(rq_[1]));
        const unsigned csb = lb + K1_CS;
        if (q == 0) {
          lds_wr1(csb + (w * 16 + i16) * 4, cs0[0]);
          if (w == 0) lds_wr1(csb + (64 + i16) * 4, cs1[0]);
        }
        wait_lgkm0();
        bar();
#pragma unroll
        for (int f = 0; f < 5; ++f) csv[f] = lds_rd1(csb + (f * 16 + i16) * 4);
#pragma unroll
        for (int r = 0; r < 2; ++r)
#pragma unroll
          for (int j = 0; j < 4; ++j) {
            float m_, r_;
            ln_coef(rs_[r][j], __shfl(rq_[r][j], q * 20 + j), m_, r_);
            mu[r][j] = -m_; rs[r][j] = sign * r_;
          }
        if (w == 0) {
          const rsrc_t rc = make_rsrc(ws + WS_CSD, 256 * 320);
          st4(__float_as_uint(q == 0 ? csv[0] : (q == 1 ? csv[1] : (q == 2 ? csv[2] : csv[3]))), rc, lane * 4, bid * 320);
          st4(__float_as_uint(csv[4]), rc, q == 0 ? 256 + lane * 4 : OOB, bid * 320);
        }
        g1_store(acc, mu, rs, csv, make_rsrc(ws + WS_AB1, SZ_AB), P, ct, w, i16, q);
        publish(ws, f5, epoch, lane);
      } else {
        {
          const rsrc_t rc = make_rsrc(ws + WS_CSD, 256 * 320);
#pragma unroll
          for (int f = 0; f < 5; ++f) csv[f] = __uint_as_float(__builtin_amdgcn_raw_buffer_load_b32(rc, (f * 16 + i16) * 4, bid * 320, 16));
        }
        const unsigned h0 = P & 1;
        poll<PD4>(ws, FL_4 + P * 64 + ((i16 >> 3) * 4 + w) * 8 + (i16 & 7), 16, epoch, lane);
        const rsrc_t rx = make_rsrc(ws + WS_X2, SZ_X);
        const unsigned sox0 = (P * 8 + h0 * 4 + w) * 20 * 1024, sox1 = (P * 8 + (h0 ^ 1) * 4 + w) * 20 * 1024;
        bf16x8 a[40];
#define LDA(n) a[n] = ld16(rx, lane * 16 + ((n) & 3) * 1024, ((n) < 20 ? sox0 : sox1) + (((n) % 20) >> 2) * 4096)
        sfor<0, 6>([&](auto N) { LDA(N.v); });
        const rsrc_t rst = make_rsrc(ws + WS_ST2, SZ_ST);
        const unsigned sto = ((lane >> 5) * 4 * S_ + P * 128 + (((lane >> 4) & 1) ^ h0) * 64 + w * 16 + i16) * 8;
        const u32x2 sp0 = ld8u(rst, sto, 0), sp1 = ld8u(rst, sto, S_ * 8), sp2 = ld8u(rst, sto, 2 * S_ * 8), sp3 = ld8u(rst, sto, 3 * S_ * 8);
        sb();
        bar();
        bf16x8 b[2][5];
        const unsigned rb = lb + i16 * 128 + ((q ^ (lane & 7)) << 4);
        const rsrc_t rab = make_rsrc(ws + WS_AB2, SZ_AB);
        const unsigned vo = ((2 * (w & 1) + (q >> 1)) * 16 + i16) * 16 + (q & 1) * 8;
        const unsigned so = ((ct & 7) * 128 + (ct >> 3) * 64 + P * 4 + (w >> 1)) * 5 * 1024;
        auto put = [&](auto H) {
          constexpr int hh = H.v;
#pragma unroll
          for (int f = 0; f < 5; ++f) {
            const f32x2 cs = {csv[f], csv[f]};
            u32x2 y;
#pragma unroll
            for (int e = 0; e < 2; ++e) {
              const f32x2 t = __builtin_elementwise_fma(f32x2{mu[hh][2 * e], mu[hh][2 * e + 1]}, cs, f32x2{acc[hh][f][2 * e], acc[hh][f][2 * e + 1]}) *
                              f32x2{rs[hh][2 * e], rs[hh][2 * e + 1]};
              y[e] = __builtin_bit_cast(unsigned, __builtin_convertvector(t, bf16x2));
            }
            st8(y, rab, vo + (f & 3) * 1024, so + (hh ^ h0) * 10240 + (f >> 2) * 4096);
          }
        };
#pragma unroll
        for (int f = 0; f < 5; ++f) b[0][f] = lds_rd(rb + wd_pos(0) * 1024 + f * 2048);
        sb();
        sfor<0, 40>([&](auto N) {
          constexpr int n = N.v, hh = n / 20, k = n % 20, kn = (k + 1) % 20;
          if constexpr (n == 2 * KP1) {
            float s1 = (__uint_as_float(sp0[0]) + __uint_as_float(sp1[0])) + (__uint_as_float(sp2[0]) + __uint_as_float(sp3[0]));
            float s2 = (__uint_as_float(sp0[1]) + __uint_as_float(sp1[1])) + (__uint_as_float(sp2[1]) + __uint_as_float(sp3[1]));
            auto ta = __builtin_amdgcn_permlane32_swap(__float_as_uint(s1), __float_as_uint(s1), false, false);
            auto tb = __builtin_amdgcn_permlane32_swap(__float_as_uint(s2), __float_as_uint(s2), false, false);
            float m, rr;
            ln_coef(__uint_as_float(ta[0]) + __uint_as_float(ta[1]), __uint_as_float(tb[0]) + __uint_as_float(tb[1]), m, rr);
            m = -m; rr *= sign;
#pragma unroll
            for (int r = 0; r < 2; ++r)
#pragma unroll
              for (int e = 0; e < 4; ++e) { mu[r][e] = __shfl(m, r * 16 + q * 4 + e); rs[r][e] = __shfl(rr, r * 16 + q * 4 + e); }
            asm volatile("" : "+v"(mu[0][0]), "+v"(mu[0][1]), "+v"(mu[0][2]), "+v"(mu[0][3]), "+v"(mu[1][0]), "+v"(mu[1][1]), "+v"(mu[1][2]), "+v"(mu[1][3]),
                              "+v"(rs[0][0]), "+v"(rs[0][1]), "+v"(rs[0][2]), "+v"(rs[0][3]), "+v"(rs[1][0]), "+v"(rs[1][1]), "+v"(rs[1][2]), "+v"(rs[1][3]));
            sb();
          }
          sfor<0, 5>([&](auto F) {
            constexpr int f = F.v;
            if constexpr (k == 0) mma0(acc[hh][f], a[n], b[n & 1][f]); else mma(acc[hh][f], a[n], b[n & 1][f]);
            if constexpr (n + 1 < 40) b[(n + 1) & 1][f] = lds_rd((rb ^ ((kn & 1) * 64)) + wd_pos(kn >> 1) * 1024 + f * 2048);
            if constexpr ((f == 1 || f == 3) && 6 + 2 * n + (f >> 1) < 40) LDA(6 + 2 * n + (f >> 1));
            sb();
          });
          if constexpr (n == 21) { pin_acc(acc[0]); put(IC<0>{}); sb(); }
          if constexpr (n == 20 + PUBG) publish(ws, f5 + 1024, epoch, lane);
        });
        fence_acc(acc[1]);
        bar();
        put(IC<1>{});
        publish(ws, f5 + 2048, epoch, lane);
      }
    }
    {
      PHASE_IDS
      IDS_G2
      const unsigned m = w >> 1, n = w & 1;
      const unsigned ra = lb + G2_A + (m * 64 + i16) * 128 + ((q ^ (lane & 7)) << 4);
      const unsigned rb = lb + n * 10240 + lane * 16;
      const unsigned row = P * 128 + m * 64 + kq * 16 + i16, ct = 2 * c4 + n;
      const TileOff ro = tile_offsets(L == 0, row, ct, q), oo = tile_offsets(false, row, ct, q);
      const rsrc_t rxp = make_rsrc(ws + WS_XP + L * SZ_XP, SZ_XP);
      const unsigned xo = ((P * 4 + c4) * 48 + w) * XSLOT;
      f32x4 acc[4][5];
#pragma unroll
      for (int i = 0; i < 4; ++i)
#pragma unroll
        for (int f = 0; f < 5; ++f) acc[i][f] = f32x4{0.f, 0.f, 0.f, 0.f};
      bf16x8 fa[2][4], fb[2][5];
      unsigned ra1 = ra ^ 64, rb2 = rb + 2 * G2_BS;
      asm volatile("" : "+v"(ra1), "+v"(rb2));
      auto half = [&](auto KK, auto SL) {
        constexpr int kk = KK.v, sl = SL.v;
        sfor<0, 20>([&](auto M) {
          constexpr int mm = M.v;
          mma(acc[mm / 5][mm % 5], fb[kk][mm % 5], fa[kk][mm / 5]);
          if constexpr (mm < 5) fb[kk ^ 1][mm] = lds_rd((sl < 2 ? rb : rb2) + (sl & 1) * G2_BS + (kk ^ 1) * 5120 + mm * 1024);
          else if constexpr (mm < 9) fa[kk ^ 1][mm - 5] = lds_rd((kk ? ra : ra1) + sl * G2_AS + (mm - 5) * 2048);
          sb();
        });
      };
      if (L) poll0(ws, FL_4 + P * 64 + (m * 4 + kq) * 8 + ct, 1, epoch, lane);
      ResTile rt = load_res(make_rsrc(L ? (const void*)(ws + WS_X2) : (const void*)opq_p(x0_), SZ_X), ro);
      fence_all(acc);
      bar();
#pragma unroll
      for (int f = 0; f < 5; ++f) fb[0][f] = lds_rd(rb + f * 1024);
#pragma unroll
      for (int i = 0; i < 4; ++i) fa[0][i] = lds_rd(ra + i * 2048);
      sb();
      auto stage = [&](auto U) {
        half(IC<0>{}, U);
        wait_lgkm0();
        bar();
        half(IC<1>{}, IC<(U.v + 1) & 3>{});
      };
#pragma unroll 1
      for (int g = 0; g < 3; ++g) sfor<0, 4>(stage);
      sfor<0, 3>(stage);
      half(IC<0>{}, IC<3>{});
      wait_lgkm0();
      bar();
      {
        unsigned p[3][10];
        u32x4 y[3][2];
        auto pack = [&](auto N) {
          constexpr int i = N.v / 5, f = N.v % 5;
          asm volatile("" : "+v"(acc[i][f]));
          p[i][2 * f] = pk2(acc[i][f][0], acc[i][f][1]); p[i][2 * f + 1] = pk2(acc[i][f][2], acc[i][f][3]);
          const unsigned o = xo + (((kq + 1 + i) & 3) * 3 + 2 - i) * 4 * XSLOT;
          if constexpr (f == 1) { y[i][0] = u32x4{p[i][0], p[i][1], p[i][2], p[i][3]}; st16_raw(y[i][0], rxp, lane * 16, o); }
          if constexpr (f == 3) { y[i][1] = u32x4{p[i][4], p[i][5], p[i][6], p[i][7]}; st16_raw(y[i][1], rxp, lane * 16 + 1024, o); }
          if constexpr (f == 4) st8(u32x2{p[i][8], p[i][9]}, rxp, lane * 8 + 2048, o);
        };
        sfor<0, 20>([&](auto M) {
          constexpr int mm = M.v;
          mma(acc[mm / 5][mm % 5], fb[1][mm % 5], fa[1][mm / 5]);
          if constexpr (mm >= 8) pack(IC<mm - 8>{});
          sb();
        });
        pack(IC<12>{}); pack(IC<13>{}); pack(IC<14>{});
        st_guard(y[0][0], y[0][1], y[1][0], y[1][1], y[2][0], y[2][1]);
      }
      f32x4 (&v)[5] = acc[3];
      fence_acc(v);
      const unsigned fx = FL_X + ((L * 64 + P * 4 + c4) * 4 + w) * 4;
      publish(ws, fx + kq, epoch, lane);
      poll<PDX>(ws, fx + ((kq + 1 + lane) & 3), 3, epoch, lane);
      const unsigned xi = xo + kq * 12 * XSLOT;
      u32x4 g0[3], g1[3]; u32x2 g2[3];
#pragma unroll
      for (int s = 0; s < 3; ++s) {
        g0[s] = ld16u(rxp, lane * 16, xi + s * 4 * XSLOT);
        g1[s] = ld16u(rxp, lane * 16 + 1024, xi + s * 4 * XSLOT);
        g2[s] = ld8u(rxp, lane * 8 + 2048, xi + s * 4 * XSLOT);
      }
      sb();
#pragma unroll
      for (int s = 0; s < 3; ++s) {
        v[0] += f32x4{bflo(g0[s][0]), bfhi(g0[s][0]), bflo(g0[s][1]), bfhi(g0[s][1])};
        v[1] += f32x4{bflo(g0[s][2]), bfhi(g0[s][2]), bflo(g0[s][3]), bfhi(g0[s][3])};
        v[2] += f32x4{bflo(g1[s][0]), bfhi(g1[s][0]), bflo(g1[s][1]), bfhi(g1[s][1])};
        v[3] += f32x4{bflo(g1[s][2]), bfhi(g1[s][2]), bflo(g1[s][3]), bfhi(g1[s][3])};
        v[4] += f32x4{bflo(g2[s][0]), bfhi(g2[s][0]), bflo(g2[s][1]), bfhi(g2[s][1])};
      }
      finish_tile(v, rt, make_rsrc(ws + (L ? WS_X3 : WS_X1), SZ_X), oo, true, make_rsrc(ws + (L ? WS_ST3 : WS_ST1), SZ_ST),
                  q == 0 ? (ct * S_ + row) * 8 : OOB);
      publish(ws, FL_2 + (L * 16 + P) * 64 + (m * 4 + kq) * 8 + ct, epoch, lane);
    }
    u32x4 y2[2]; u32x2 y1[2];
    {
      PHASE_IDS
      IDS_G3
      typedef __attribute__((ext_vector_type(2))) short s16x2;
      poll<PD2>(ws, FL_2 + (L * 16 + P) * 64 + w * 16 + i16, 16, epoch, lane);
      const rsrc_t rx = make_rsrc(ws + (L ? WS_X3 : WS_X1), SZ_X);
      const rsrc_t rst = make_rsrc(ws + (L ? WS_ST3 : WS_ST1), SZ_ST);
      const unsigned sox = (P * 8 + w * 2) * 20 * 1024;
      const unsigned par = ct & 1;
      bf16x8 a[20][2];
#pragma unroll
      for (int k = 0; k < 3; ++k) { a[k][0] = LDX(0, k); a[k][1] = LDX(1, k); }
      const unsigned sto = (2 * q * S_ + P * 128 + w * 32 + i16) * 8;
      const u32x2 sa0 = ld8u(rst, sto, 0), sa1 = ld8u(rst, sto, S_ * 8), sb0 = ld8u(rst, sto + 128, 0), sb1 = ld8u(rst, sto + 128, S_ * 8);
      float mu[2], rs[2];
      sb();
      bar();
      f32x4 acc[2][4];
      bf16x8 b[2][4];
      const unsigned rb = lb + lane * 16;
#pragma unroll
      for (int f = 0; f < 4; ++f) b[0][f] = lds_rd(rb + f * 1024);
      sb();
      sfor<0, 20>([&](auto K) {
        constexpr int k = K.v;
        const unsigned pb = k + 1 < 20 ? rb + (k + 1) * 4096 : rb + 80 * 1024;
        if constexpr (k == KP3) {
          ln_coef(xsum16_32(__uint_as_float(sa0[0]) + __uint_as_float(sa1[0])), xsum16_32(__uint_as_float(sa0[1]) + __uint_as_float(sa1[1])), mu[0], rs[0]);
          ln_coef(xsum16_32(__uint_as_float(sb0[0]) + __uint_as_float(sb1[0])), xsum16_32(__uint_as_float(sb0[1]) + __uint_as_float(sb1[1])), mu[1], rs[1]);
          asm volatile("" : "+v"(mu[0]), "+v"(mu[1]), "+v"(rs[0]), "+v"(rs[1]));
          sb();
        }
        sfor<0, 8>([&](auto M) {
          constexpr int f = M.v >> 1, r = M.v & 1;
          if constexpr (k == 0) mma0(acc[r][f], b[0][f], a[0][r]); else mma(acc[r][f], b[k & 1][f], a[k][r]);
          if constexpr (r == 0 && (k + 1 < 20 || f < 3)) b[(k + 1) & 1][f] = lds_rd(pb + f * 1024);
          if constexpr (r == 1 && 2 * k + 3 + (f >> 1) < 20) a[2 * k + 3 + (f >> 1)][f & 1] = LDX(f & 1, 2 * k + 3 + (f >> 1));
          sb();
        });
      });
      auto act = [&](const f32x4& c, int r, const f32x4& cs, unsigned (&pk)[2]) {
        const f32x2 nm = {-mu[r], -mu[r]}, r2 = {rs[r], rs[r]};
#pragma unroll
        for (int e = 0; e < 2; ++e) {
          const f32x2 z = __builtin_elementwise_fma(nm, f32x2{cs[2 * e], cs[2 * e + 1]}, f32x2{c[2 * e], c[2 * e + 1]}) * r2;
          const s16x2 hb = __builtin_bit_cast(s16x2, __builtin_convertvector(z, bf16x2));
          pk[e] = __builtin_bit_cast(unsigned, __builtin_elementwise_max(hb, s16x2{0, 0}));
        }
      };
      auto pair = [&](const unsigned (&u)[2], const unsigned (&v)[2]) {
        auto t0 = __builtin_amdgcn_permlane16_swap(u[0], v[0], false, false);
        auto t1 = __builtin_amdgcn_permlane16_swap(u[1], v[1], false, false);
        return u32x4{t0[0], t1[0], t0[1], t1[1]};
      };
      const unsigned csb = lb + KM_CS1 + q * 16;
      f32x4 c1s[4];
#pragma unroll
      for (int f = 0; f < 4; ++f) c1s[f] = lds_rdf(csb + (f + par) * 64);
      asm volatile("s_nop 7\n\ts_nop 1" : "+v"(acc[0][0]), "+v"(acc[0][1]), "+v"(acc[0][2]), "+v"(acc[0][3]),
                                          "+v"(acc[1][0]), "+v"(acc[1][1]), "+v"(acc[1][2]), "+v"(acc[1][3]));
      wait_lgkm0();
      bar();
      {
        const rsrc_t rh = make_rsrc(ws + (L ? WS_H2 : WS_H1), SZ_H);
        const unsigned vo = ((((q & 1) << 1) | (q >> 1)) * 16 + i16) * 16;
        const unsigned so = ((P * 8 + w * 2) * 54 + 7 * (ct >> 1) + 4 * par) * 1024;
        u32x4 y[2][2];
#pragma unroll
        for (int p = 0; p < 2; ++p) {
          const f32x4 c0 = c1s[2 * p], c1 = c1s[2 * p + 1];
#pragma unroll
          for (int r = 0; r < 2; ++r) {
            unsigned u[2], v[2];
            act(acc[r][2 * p], r, c0, u); act(acc[r][2 * p + 1], r, c1, v);
            y[p][r] = pair(u, v);
            st16_raw(y[p][r], rh, p == 1 && ct == 15 ? OOB : vo, so + (r * 54 + p) * 1024);
          }
        }
        st_guard(y[0][0], y[0][1], y[1][0], y[1][1]);
      }
      sb();
      sfor<0, 20>([&](auto K) {
        constexpr int k = K.v;
        const unsigned pb = rb + 80 * 1024 + (k + 1) * 3072;
        sfor<0, 6>([&](auto M) {
          constexpr int f = M.v >> 1, r = M.v & 1;
          if constexpr (k == 0) mma0(acc[r][f], b[0][f], a[0][r]); else mma(acc[r][f], b[k & 1][f], a[k][r]);
          if constexpr (r == 0 && k + 1 < 20) b[(k + 1) & 1][f] = lds_rd(pb + f * 1024);
          sb();
        });
        if constexpr (k == PUB1) publish(ws, FL_3 + (L * 32 + P) * 64 + w * 16 + ct, epoch, lane);
      });
      const f32x4 c0 = lds_rdf(csb + (4 + par) * 64), c1 = lds_rdf(csb + (5 + par) * 64), c2 = lds_rdf(csb + (par ? 0 : 6) * 64);
      asm volatile("s_nop 7\n\ts_nop 1" : "+v"(acc[0][0]), "+v"(acc[0][1]), "+v"(acc[0][2]), "+v"(acc[1][0]), "+v"(acc[1][1]), "+v"(acc[1][2]));
#pragma unroll
      for (int r = 0; r < 2; ++r) {
        unsigned u[2], v[2], s[2];
        act(acc[r][0], r, c0, u); act(acc[r][1], r, c1, v); act(acc[r][2], r, c2, s);
        y2[r] = pair(u, v);
        y1[r] = u32x2{s[0], s[1]};
      }
      asm volatile("" : "+v"(y2[0]), "+v"(y2[1]), "+v"(y1[0]), "+v"(y1[1]));
    }
    {
      PHASE_IDS
      IDS_G4
      const unsigned row = rt64 * 64 + w * 16 + i16;
      const rsrc_t rh = make_rsrc(ws + (L ? WS_H2 : WS_H1), SZ_H);
      const unsigned soh = (rt64 * 4 * 54 + w * 14) * 1024;
      const unsigned vl = lane * 16, vz = lane * 16 + (w < 3 ? 0u : OOB);
      bf16x8 a[14][4], b[2][5];
      unsigned sh[4];
#pragma unroll
      for (int i = 0; i < 4; ++i) sh[i] = soh + ((w + 1 + i) & 3) * 54 * 1024;
#define LDH(t, i) a[t][i] = ld16(rh, (ORD[t] < 12 ? vl : vz) + (ORD[t] & 3) * 1024, sh[i] + (ORD[t] >> 2) * 4096)
      const TileOff ro = tile_offsets(false, row, c8, q), oo = tile_offsets(L != 0, row, c8, q);
      const unsigned f3 = FL_3 + (L * 32 + (rt64 >> 1)) * 64 + ((rt64 & 1) * 2 + (lane & 1)) * 16 + 4 * w + ((lane >> 1) & 3);
      poll0(ws, f3, 8, epoch, lane);
      sfor<0, 8>([&](auto N) { LDH(N.v / 4, N.v % 4); });
      sb();
      {
        const unsigned P = (xcd >> 1) * 4 + (idx >> 3), ct = (xcd & 1) * 8 + (idx & 7), par = ct & 1;
        const unsigned vo = ((((q & 1) << 1) | (q >> 1)) * 16 + i16) * 16;
        const unsigned v1 = ((2 * par + (q >> 1)) * 16 + i16) * 16 + (q & 1) * 8;
        const unsigned so = ((P * 8 + w * 2) * 54 + 7 * (ct >> 1)) * 1024;
#pragma unroll
        for (int r = 0; r < 2; ++r) {
          st16_raw(y2[r], rh, ct == 15 ? OOB : vo, so + (r * 54 + 2 + 4 * par) * 1024);
          st8(y1[r], rh, v1, so + (r * 54 + 3) * 1024);
        }
        st_guard(y2[0], y2[1]);
      }
      ResTile rt;
      u64 fl2;
      const rsrc_t rr = make_rsrc(ws + (L ? WS_X3 : WS_X1), SZ_X);
      sb();
      bar();
      f32x4 acc[4][5];
      const unsigned rbe = lb + w * 20480 + lane * 16, rbl = lb + G4_LATE + w * 15360 + lane * 16;
#pragma unroll
      for (int f = 0; f < 5; ++f) b[0][f] = lds_rd(rbe + f * 1024);
      sb();
      sfor<0, 14>([&](auto T) {
        constexpr int t = T.v;
        constexpr int ta = t < 5 ? (t == 0 ? 2 : t + 3) : (t == 5 ? -1 : (t == 6 ? 8 : (t + 3 < 14 ? t + 3 : -1)));
        if constexpr (t == 3 || t == 6 || t == 10) { wait_lgkm0(); bar(); }
        if constexpr (t == 6) { if (!__all(fl2 == epoch)) poll0(ws, f3 + 16 * 64, 8, epoch, lane); }
        sfor<0, 20>([&](auto M) {
          constexpr int m = M.v, i = m / 5, f = m % 5;
          if constexpr (t == 0) mma0(acc[i][f], b[0][f], a[0][i]); else mma(acc[i][f], b[t & 1][f], a[t][i]);
          if constexpr (t + 1 < 14 && (m & 1) == 1 && m < 10)
            b[(t + 1) & 1][m >> 1] = lds_rd((g4_late(t + 1) ? rbl : rbe) + g4_slot(t + 1) + (m >> 1) * 1024);
          if constexpr (ta >= 0 && (m & 1) == 0 && m < 8) LDH(ta, m >> 1);
          if constexpr (t == 6 && (m & 1) == 0 && m >= 8 && m < 16) LDH(9, (m - 8) >> 1);
          if constexpr (t == 0 && (m & 1) == 0 && m >= 8 && m < 16) LDH(3, (m - 8) >> 1);
          if constexpr (t == 2 && m == 10) rt = load_res(rr, ro);
          if constexpr (t == 4 && m == 18) fl2 = __hip_atomic_load(flag_ptr(ws, f3 + 16 * 64), RLX_AGENT);
          if constexpr (t == 13 && m >= 8) {
            asm volatile("" : "+v"(acc[(m - 8) / 5][(m - 8) % 5]));
            lds_wrf(rbl + (m - 8) * 1024, acc[(m - 8) / 5][(m - 8) % 5]);
          }
          sb();
        });
        if constexpr (t == 2) {
          wait_vm<19>();
          raise(ws, FL_3 + (L * 32 + 16 + (xcd >> 1) * 4 + (idx >> 3)) * 64 + w * 16 + (xcd & 1) * 8 + (idx & 7), epoch, lane);
        }
      });
#pragma unroll
      for (int f = 2; f < 5; ++f) { asm volatile("" : "+v"(acc[2][f])); lds_wrf(rbl + (10 + f) * 1024, acc[2][f]); }
      f32x4 (&v)[5] = acc[3];
      fence_acc(v);
      wait_lgkm0();
      bar();
#pragma unroll
      for (int s = 1; s < 4; ++s) {
        const unsigned src = lb + G4_LATE + ((w + s) & 3) * 15360 + (3 - s) * 5 * 1024 + lane * 16;
#pragma unroll
        for (int f = 0; f < 5; ++f) v[f] += lds_rdf(src + f * 1024);
      }
      finish_tile(v, rt, L ? make_rsrc(opq_p(out_), SZ_X) : make_rsrc(ws + WS_X2, SZ_X), oo, L == 0, make_rsrc(ws + WS_ST2, SZ_ST),
                  q == 0 ? (c8 * S_ + row) * 8 : OOB);
      if (L == 0) publish(ws, FL_4 + (rt64 >> 1) * 64 + ((rt64 & 1) * 4 + w) * 8 + c8, epoch, lane);
    }
  }
  asm volatile(".Lcw_end:");
  if (blockIdx.x == 0 && w0 == 0) raise(ws_, FL_E, epoch, tid0);
}

extern "C" void kernel_launch(void* const* d_in, const int* in_sizes, int n_in, void* d_out, int out_size, void* d_ws,
                              size_t ws_size, hipStream_t stream) {
  char* ws = (char*)d_ws;
  (void)hipFuncSetAttribute((const void*)k_all, hipFuncAttributeMaxDynamicSharedMemorySize, KM_LDS);
  k_all<<<256, 512, KM_LDS, stream>>>((const u16*)d_in[0], (const u16*)d_in[1], (const u16*)d_in[2], (const u16*)d_in[3],
                                     (const u16*)d_in[4], (const u16*)d_in[6], (const u16*)d_in[7], ws, (u16*)d_out);
}
```

```cpp
#define LG 256
#define LB 512
#define LL 163840
#define BIGV 1
#define MULT 0
#include <hip/hip_runtime.h>
#include <hip/hip_bf16.h>
#include <stdint.h>

#define DI __device__ __forceinline__
typedef unsigned short u16;
typedef __attribute__((ext_vector_type(8))) __bf16 bf16x8;
typedef __attribute__((ext_vector_type(2))) __bf16 bf16x2;
typedef __attribute__((ext_vector_type(4))) float f32x4;
typedef __attribute__((ext_vector_type(2))) float f32x2;
typedef __attribute__((ext_vector_type(4))) unsigned u32x4;
typedef __attribute__((ext_vector_type(2))) unsigned u32x2;
typedef __attribute__((ext_vector_type(4))) short s16x4;
#define LDS_AS __attribute__((address_space(3)))
#define GLB_AS __attribute__((address_space(1)))
typedef LDS_AS char* ldsp;
typedef unsigned long long u64;
typedef GLB_AS u64 gu64;
typedef __amdgpu_buffer_rsrc_t rsrc_t;
#define RLX_AGENT __ATOMIC_RELAXED, __HIP_MEMORY_SCOPE_AGENT

constexpr int S_ = 2048, D_ = 640, F_ = 1728;
constexpr float EPS = 1e-5f;
constexpr unsigned OOB = 0x7f000000u;

constexpr unsigned SZ_AB = 8 * 128 * 5 * 1024;
constexpr unsigned SZ_X = 128 * 20 * 1024;
constexpr unsigned SZ_H = 128 * 54 * 1024;
constexpr unsigned SZ_W1 = 16 * 140 * 1024;
constexpr unsigned SZ_W2 = 8 * 54 * 5 * 1024;
constexpr unsigned SZ_ST = 2048 * 64;
constexpr unsigned WS_AB1 = 0;
constexpr unsigned WS_AB2 = WS_AB1 + SZ_AB;
constexpr unsigned WS_X1 = WS_AB2 + SZ_AB;
constexpr unsigned WS_X2 = WS_X1 + SZ_X;
constexpr unsigned WS_X3 = WS_X2 + SZ_X;
constexpr unsigned WS_H1 = WS_X3 + SZ_X;
constexpr unsigned WS_H2 = WS_H1 + SZ_H;
constexpr unsigned WS_W1 = WS_H2 + SZ_H;
constexpr unsigned WS_W2 = WS_W1 + SZ_W1;
constexpr unsigned WS_ST1 = WS_W2 + SZ_W2;
constexpr unsigned WS_ST2 = WS_ST1 + SZ_ST;
constexpr unsigned WS_ST3 = WS_ST2 + SZ_ST;
constexpr unsigned WS_CS1 = WS_ST3 + SZ_ST;
constexpr unsigned WS_CSD = WS_CS1 + 20 * 1728 * 4;
constexpr unsigned XSLOT = 2560;
constexpr unsigned SZ_XP = 256 * 12 * XSLOT;
constexpr unsigned WS_XP = (WS_CSD + 256 * 320 + 4095) & ~4095u;
constexpr unsigned WS_FLG = WS_XP + 2 * SZ_XP;
constexpr unsigned FL_X = 0;
constexpr unsigned FL_2 = FL_X + 2048;
constexpr unsigned FL_3 = FL_2 + 2048;
constexpr unsigned FL_4 = FL_3 + 4096;
constexpr unsigned FL_5 = FL_4 + 1024;
constexpr unsigned FL_W = FL_5 + 3072;
constexpr unsigned FL_E = FL_W + 1088;
constexpr unsigned WS_END = WS_FLG + (FL_E + 1) * 128;

DI u32x4 make_srd(const void* p, unsigned bytes) {
  uint64_t a = (uint64_t)p;
  u32x4 r;
  r[0] = (unsigned)a; r[1] = (unsigned)(a >> 32) & 0xffffu; r[2] = bytes; r[3] = 0x00020000u;
  return r;
}
DI rsrc_t make_rsrc(const void* p, unsigned bytes) {
  return __builtin_amdgcn_make_buffer_rsrc((void*)p, (short)0, (int)bytes, 0x00020000);
}
template <int LOFF, int GOFF>
DI void glds(u32x4 srd, unsigned voff, unsigned ldsb, unsigned soff) {
  unsigned tmp;
  asm volatile("s_add_u32 m0, %1, %2\n\ts_add_u32 %0, %3, %4\n\tbuffer_load_dwordx4 %5, %6, %0 offen lds"
               : "=&s"(tmp)
               : "s"(ldsb), "i"(LOFF), "s"(soff), "i"(GOFF), "v"(voff), "s"(srd)
               : "memory", "scc");
}
template <int N> DI void wait_vm() { asm volatile("s_waitcnt vmcnt(%0)" ::"i"(N) : "memory"); }
DI void wait_lgkm0() { asm volatile("s_waitcnt lgkmcnt(0)" ::: "memory"); }
DI void sb() { __builtin_amdgcn_sched_barrier(0); }
DI void bar() { asm volatile("s_barrier" ::: "memory"); }

DI bf16x8 lds_rd(unsigned addr) { return *(const LDS_AS bf16x8*)(ldsp)(uintptr_t)addr; }
DI f32x4 lds_rdf(unsigned addr) { return *(const LDS_AS f32x4*)(ldsp)(uintptr_t)addr; }
DI void lds_wrf(unsigned addr, f32x4 v) { *(LDS_AS f32x4*)(ldsp)(uintptr_t)addr = v; }
DI float lds_rd1(unsigned addr) { return *(const LDS_AS float*)(ldsp)(uintptr_t)addr; }
DI void lds_wr1(unsigned addr, float v) { *(LDS_AS float*)(ldsp)(uintptr_t)addr = v; }
DI void mma(f32x4& c, const bf16x8& a, const bf16x8& b) {
  asm volatile("v_mfma_f32_16x16x32_bf16 %0, %1, %2, %0" : "+v"(c) : "v"(a), "v"(b));
}
DI void mma0(f32x4& c, const bf16x8& a, const bf16x8& b) {
  asm volatile("v_mfma_f32_16x16x32_bf16 %0, %1, %2, 0" : "=&v"(c) : "v"(a), "v"(b));
}
DI void fence_acc(f32x4 (&a)[5]) { asm volatile("s_nop 7\n\ts_nop 1" : "+v"(a[0]), "+v"(a[1]), "+v"(a[2]), "+v"(a[3]), "+v"(a[4])); }
DI void pin_acc(f32x4 (&a)[5]) { asm volatile("" : "+v"(a[0]), "+v"(a[1]), "+v"(a[2]), "+v"(a[3]), "+v"(a[4])); }
template <int M> DI void fence_all(f32x4 (&a)[M][5]) {
#pragma unroll
  for (int i = 1; i < M; ++i) pin_acc(a[i]);
  fence_acc(a[0]);
#pragma unroll
  for (int i = 1; i < M; ++i) pin_acc(a[i]);
}
DI unsigned pk2(float a, float b) {
  f32x2 v = {a, b};
  return __builtin_bit_cast(unsigned, __builtin_convertvector(v, bf16x2));
}
DI float bflo(unsigned u) { return __uint_as_float(u << 16); }
DI float bfhi(unsigned u) { return __uint_as_float(u & 0xffff0000u); }
DI float dot2(unsigned a, unsigned b, float c) {
  return __builtin_amdgcn_fdot2_f32_bf16(__builtin_bit_cast(bf16x2, a), __builtin_bit_cast(bf16x2, b), c, false);
}
DI float xsum16_32(float x) {
  auto s = __builtin_amdgcn_permlane16_swap(__float_as_uint(x), __float_as_uint(x), false, false);
  x = __uint_as_float(s[0]) + __uint_as_float(s[1]);
  auto t = __builtin_amdgcn_permlane32_swap(__float_as_uint(x), __float_as_uint(x), false, false);
  return __uint_as_float(t[0]) + __uint_as_float(t[1]);
}
template <int V> struct IC { static constexpr int v = V; };
template <int I, int N, class Fn> DI void sfor(Fn&& f) {
  if constexpr (I < N) { f(IC<I>{}); sfor<I + 1, N>(f); }
}
DI unsigned rfl(unsigned v) { return __builtin_amdgcn_readfirstlane(v); }
DI unsigned opq_v(unsigned x) { asm volatile("" : "+v"(x)); return x; }
DI unsigned opq_s(unsigned x) { asm volatile("" : "+s"(x)); return x; }
template <class T> DI T* opq_p(T* x) { asm volatile("" : "+s"(x)); return x; }
DI bf16x8 ld16(rsrc_t r, unsigned voff, unsigned soff) { return __builtin_bit_cast(bf16x8, __builtin_amdgcn_raw_buffer_load_b128(r, voff, soff, 16)); }
DI u32x4 ld16u(rsrc_t r, unsigned voff, unsigned soff) { return __builtin_amdgcn_raw_buffer_load_b128(r, voff, soff, 16); }
DI u32x2 ld8u(rsrc_t r, unsigned voff, unsigned soff) { return __builtin_amdgcn_raw_buffer_load_b64(r, voff, soff, 16); }
DI void st16(u32x4 v, rsrc_t r, unsigned voff, unsigned soff) {
  __builtin_amdgcn_raw_buffer_store_b128(v, r, voff, soff, 16);
  asm volatile("s_nop 1" :: "v"(v) : "memory");
}
DI void st16_raw(u32x4 v, rsrc_t r, unsigned voff, unsigned soff) { __builtin_amdgcn_raw_buffer_store_b128(v, r, voff, soff, 16); }
template <class... T> DI void st_guard(const T&... v) {
  (..., [](const auto& x) { asm volatile("" :: "v"(x) : "memory"); }(v));
  asm volatile("s_nop 1" ::: "memory");
  (..., [](const auto& x) { asm volatile("" :: "v"(x)); }(v));
}
DI void st8(u32x2 v, rsrc_t r, unsigned voff, unsigned soff) { __builtin_amdgcn_raw_buffer_store_b64(v, r, voff, soff, 16); }
DI void st4(unsigned v, rsrc_t r, unsigned voff, unsigned soff) { __builtin_amdgcn_raw_buffer_store_b32(v, r, voff, soff, 16); }

template <int KS> DI unsigned foff(unsigned row, unsigned col) {
  return ((row >> 4) * KS + (col >> 5)) * 1024u + (((col >> 3) & 3) * 16u + (row & 15)) * 16u + (col & 7) * 2u;
}

extern __shared__ __attribute__((aligned(1024))) char smem[];
DI unsigned lds_base() { return (unsigned)(uintptr_t)(ldsp)smem; }

DI void ln_coef(float s1, float s2, float& mu, float& rs) {
  mu = s1 * (1.f / D_);
  rs = __builtin_amdgcn_rsqf(fmaxf(s2 * (1.f / D_) - mu * mu, 0.f) + EPS);
}

DI void g1_store(const f32x4 (&acc)[2][5], const float (&mu)[2][4], const float (&rs)[2][4], const float (&csv)[5], rsrc_t rab,
                 unsigned P, unsigned ct, unsigned w, unsigned i16, unsigned q) {
  const unsigned kstep = (ct >> 3) * 64 + P * 4 + w;
  const unsigned cq = ((q & 1) << 1) | (q >> 1);
  const unsigned so = ((ct & 7) * 128 + kstep) * 5 * 1024;
  const unsigned vo = (cq * 16 + i16) * 16;
  u32x4 y[5];
#pragma unroll
  for (int f = 0; f < 5; ++f) {
    const f32x2 cs = {csv[f], csv[f]};
    unsigned pk[2][2];
#pragma unroll
    for (int r = 0; r < 2; ++r)
#pragma unroll
      for (int e = 0; e < 2; ++e) {
        const f32x2 t = __builtin_elementwise_fma(f32x2{mu[r][2 * e], mu[r][2 * e + 1]}, cs, f32x2{acc[r][f][2 * e], acc[r][f][2 * e + 1]}) *
                        f32x2{rs[r][2 * e], rs[r][2 * e + 1]};
        pk[r][e] = __builtin_bit_cast(unsigned, __builtin_convertvector(t, bf16x2));
      }
    auto t0 = __builtin_amdgcn_permlane16_swap(pk[0][0], pk[1][0], false, false);
    auto t1 = __builtin_amdgcn_permlane16_swap(pk[0][1], pk[1][1], false, false);
    y[f] = u32x4{t0[0], t1[0], t0[1], t1[1]};
    st16_raw(y[f], rab, vo + (f & 3) * 1024, so + (f >> 2) * 4096);
  }
  st_guard(y[0], y[1], y[2], y[3], y[4]);
}

constexpr int K1_STAGE = 26 * 1024;
constexpr int K1_TR = 5 * K1_STAGE;
constexpr int K1_CS = K1_TR + 5 * 4096;

DI void k1_issue(u32x4 sa, u32x4 sbd, unsigned vo, unsigned la, unsigned lbb, unsigned soa, unsigned sob, unsigned w) {
  glds<0, 0>(sa, vo, la, soa);
  glds<1024, 8 * 1280>(sa, vo, la, soa);
  glds<2048, 16 * 1280>(sa, vo, la, soa);
  glds<3072, 24 * 1280>(sa, vo, la, soa);
  glds<0, 0>(sbd, vo, lbb, sob);
  glds<4096, 32 * 1280>(sbd, vo, lbb, sob);
  if (w < 2) glds<8192, 64 * 1280>(sbd, vo, lbb, sob);
}
struct RtUnit { bool first; unsigned ks, g; };
DI RtUnit rt_unit(unsigned u) {
  RtUnit r;
  r.first = u < 540;
  if (r.first) { r.ks = u / 27u; r.g = u - r.ks * 27u; }
  else { const unsigned v = u - 540; r.ks = v / 10u; r.g = v - r.ks * 10u; }
  return r;
}
DI void retile_issue(unsigned u, const u16* w1, const u16* w2, unsigned ldst, unsigned lane) {
  const RtUnit t = rt_unit(u);
  const unsigned ld = t.first ? F_ * 2 : D_ * 2;
  u32x4 srd = make_srd(t.first ? w1 : w2, D_ * F_ * 2);
  const unsigned vo = (lane >> 3) * ld + (lane & 7) * 16;
  const unsigned so = t.ks * 32 * ld + t.g * 128;
  glds<0, 0>(srd, vo, ldst, so);
  glds<1024, 0>(srd, vo, ldst, so + 8 * ld);
  glds<2048, 0>(srd, vo, ldst, so + 16 * ld);
  glds<3072, 0>(srd, vo, ldst, so + 24 * ld);
}
DI unsigned w1_off(unsigned rt, unsigned ks) {
  const unsigned c = rt / 7u, f = rt - c * 7, g = (c & 1) ? (f == 0 ? 6 : f - 1) : f;
  return WS_W1 + (c * 140 + (g < 4 ? ks * 4 + g : 80 + ks * 3 + g - 4)) * 1024;
}
DI void retile_finish(unsigned u, unsigned f0, rsrc_t rws, unsigned ldst, unsigned lane) {
  const RtUnit t = rt_unit(u);
  const unsigned i = lane & 15, q = lane >> 4;
  const unsigned base = ldst + (8 * q + (i >> 2)) * 128 + (i & 3) * 8 + f0 * 32;
  u32x4 v[2];
#pragma unroll
  for (int f = 0; f < 2; ++f) {
    s16x4 lo = __builtin_amdgcn_ds_read_tr16_b64_v4i16((LDS_AS s16x4*)(uintptr_t)(base + f * 32));
    s16x4 hi = __builtin_amdgcn_ds_read_tr16_b64_v4i16((LDS_AS s16x4*)(uintptr_t)(base + f * 32 + 512));
    u32x2 l2 = __builtin_bit_cast(u32x2, lo), h2 = __builtin_bit_cast(u32x2, hi);
    v[f] = u32x4{l2[0], l2[1], h2[0], h2[1]};
  }
  const unsigned rt = t.g * 4 + f0;
  if (t.first) {
    st16(v[0], rws, lane * 16, w1_off(rt, t.ks));
    st16(v[1], rws, lane * 16, w1_off(rt + 1, t.ks));
    float s[2];
#pragma unroll
    for (int f = 0; f < 2; ++f)
      s[f] = xsum16_32(((bflo(v[f][0]) + bfhi(v[f][0])) + (bflo(v[f][1]) + bfhi(v[f][1]))) + ((bflo(v[f][2]) + bfhi(v[f][2])) + (bflo(v[f][3]) + bfhi(v[f][3]))));
    st4(__float_as_uint(q == 0 ? s[0] : s[1]), rws, lane < 32 ? lane * 4 : OOB, WS_CS1 + (t.ks * F_ + rt * 16) * 4);
  } else {
    const unsigned c0 = rt / 5u, c1 = (rt + 1) / 5u;
    st16(v[0], rws, lane * 16, WS_W2 + ((c0 * 54 + t.ks) * 5 + (rt - c0 * 5)) * 1024);
    st16(v[1], rws, lane * 16, WS_W2 + ((c1 * 54 + t.ks) * 5 + (rt + 1 - c1 * 5)) * 1024);
  }
}

constexpr int WS1 = 4, WS2 = 7, RTI = 2;
constexpr int PUB1 = 13;
constexpr int PUBG = 11;
constexpr int KP3 = 3, KP1 = 3;
constexpr int RGAP = 10, ASLEEP2 = 40, WDPRE = 6;
constexpr int PD4 = 13, PDX = 15, PD2 = 30, P5DELAY0 = 4, P5DELAY = 1;

constexpr int KM_LDS = 160 * 1024;
constexpr int KM_CS1 = KM_LDS - 1024;
constexpr int G2_A = 80 * 1024, G2_AS = 16 * 1024, G2_BS = 20 * 1024;
constexpr int G4_LATE = 80 * 1024;

DI gu64* flag_ptr(char* ws, unsigned f) { return (gu64*)(ws + WS_FLG + f * 128); }
DI void raise(char* ws, unsigned f, u64 epoch, unsigned lane) {
  if (lane < 16) __hip_atomic_store(flag_ptr(ws, f) + lane, epoch, RLX_AGENT);
}
DI void publish(char* ws, unsigned f, u64 epoch, unsigned lane) {
  wait_vm<0>();
  raise(ws, f, epoch, lane);
}
DI void poll0(char* ws, unsigned f, unsigned n, u64 epoch, unsigned lane) {
  gu64* p = flag_ptr(ws, f);
  bool ok = lane >= n;
#pragma unroll 1
  for (unsigned s = 0; s < (1u << 16); ++s) {
    if (!ok) ok = __hip_atomic_load(p, RLX_AGENT) == epoch;
    if (__all(ok)) break;
    __builtin_amdgcn_s_sleep(1);
  }
  asm volatile("" ::: "memory");
}
template <int DELAY> DI void poll(char* ws, unsigned f, unsigned n, u64 epoch, unsigned lane) {
  __builtin_amdgcn_s_sleep(DELAY);
  poll0(ws, f, n, epoch, lane);
}
DI void warm(unsigned ldump, u32x4 srd, unsigned voff, unsigned soff) {
  asm volatile("s_mov_b32 m0, %0\n\ts_nop 0\n\tbuffer_load_dword %1, %2, %3 offen lds" :: "s"(ldump), "v"(voff), "s"(srd), "s"(soff) : "memory");
}
DI void glds4_sc1(u32x4 srd, unsigned voff, unsigned l, unsigned so) {
  asm volatile("s_mov_b32 m0, %0\n\ts_nop 0\n\tbuffer_load_dwordx4 %1, %2, %3 offen sc1 lds\n\tbuffer_load_dwordx4 %1, %2, %3 offen offset:1024 sc1 lds\n\t"
               "buffer_load_dwordx4 %1, %2, %3 offen offset:2048 sc1 lds\n\tbuffer_load_dwordx4 %1, %2, %3 offen offset:3072 sc1 lds"
               :: "s"(l), "v"(voff), "s"(srd), "s"(so) : "memory");
}
DI void glds1_sc1(u32x4 srd, unsigned voff, unsigned l, unsigned so) {
  asm volatile("s_mov_b32 m0, %0\n\ts_nop 0\n\tbuffer_load_dwordx4 %1, %2, %3 offen sc1 lds" :: "s"(l), "v"(voff), "s"(srd), "s"(so) : "memory");
}
template <int N> DI void glds_run_sc1(u32x4 srd, unsigned voff, unsigned l, unsigned so) {
#pragma unroll
  for (int g = 0; g < N / 4; ++g) glds4_sc1(srd, voff, l + g * 4096, so + g * 4096);
#pragma unroll
  for (int g = N / 4 * 4; g < N; ++g) glds1_sc1(srd, voff, l + g * 1024, so + g * 1024);
}

struct ResTile { u32x4 r0, r1; u32x2 r2; };
struct TileOff { unsigned a0, a1, a2; };
DI TileOff tile_offsets(bool rm, unsigned row, unsigned ct, unsigned q) {
  const unsigned cA = ct * 80 + 16 * (q & 1) + 8 * (q >> 1), cS = ct * 80 + 64 + 4 * q;
  TileOff t;
  if (rm) { t.a0 = (row * D_ + cA) * 2; t.a1 = t.a0 + 64; t.a2 = (row * D_ + cS) * 2; }
  else { t.a0 = foff<20>(row, cA); t.a1 = foff<20>(row, cA + 32); t.a2 = foff<20>(row, cS); }
  asm volatile("" : "+v"(t.a0), "+v"(t.a1), "+v"(t.a2));
  return t;
}
DI ResTile load_res(rsrc_t rr, const TileOff& t) {
  ResTile rt;
  rt.r0 = ld16u(rr, t.a0, 0); rt.r1 = ld16u(rr, t.a1, 0); rt.r2 = ld8u(rr, t.a2, 0);
  return rt;
}
DI void finish_tile(f32x4 (&v)[5], const ResTile& rt, rsrc_t rout, const TileOff& o, bool stats, rsrc_t rst, unsigned sto) {
#pragma unroll
  for (int p = 0; p < 2; ++p)
#pragma unroll
    for (int r = 0; r < 4; ++r) {
      auto t = __builtin_amdgcn_permlane16_swap(__float_as_uint(v[2 * p][r]), __float_as_uint(v[2 * p + 1][r]), false, false);
      v[2 * p][r] = __uint_as_float(t[0]); v[2 * p + 1][r] = __uint_as_float(t[1]);
    }
  u32x4 y0, y1; u32x2 y2;
#pragma unroll
  for (int j = 0; j < 2; ++j) {
    y0[j] = pk2(v[0][2 * j] + bflo(rt.r0[j]), v[0][2 * j + 1] + bfhi(rt.r0[j]));
    y0[2 + j] = pk2(v[1][2 * j] + bflo(rt.r0[2 + j]), v[1][2 * j + 1] + bfhi(rt.r0[2 + j]));
    y1[j] = pk2(v[2][2 * j] + bflo(rt.r1[j]), v[2][2 * j + 1] + bfhi(rt.r1[j]));
    y1[2 + j] = pk2(v[3][2 * j] + bflo(rt.r1[2 + j]), v[3][2 * j + 1] + bfhi(rt.r1[2 + j]));
    y2[j] = pk2(v[4][2 * j] + bflo(rt.r2[j]), v[4][2 * j + 1] + bfhi(rt.r2[j]));
  }
  if (stats) {
    st16_raw(y0, rout, o.a0, 0); st16_raw(y1, rout, o.a1, 0); st8(y2, rout, o.a2, 0);
  } else {
    __builtin_amdgcn_raw_buffer_store_b128(y0, rout, o.a0, 0, 0);
    __builtin_amdgcn_raw_buffer_store_b128(y1, rout, o.a1, 0, 0);
    __builtin_amdgcn_raw_buffer_store_b64(y2, rout, o.a2, 0, 0);
  }
  st_guard(y0, y1);
  if (stats) {
    float s1a = 0.f, s1b = 0.f, s2a = 0.f, s2b = 0.f;
#pragma unroll
    for (int j = 0; j < 4; ++j) {
      s1a = dot2(y0[j], 0x3f803f80u, s1a); s2a = dot2(y0[j], y0[j], s2a);
      s1b = dot2(y1[j], 0x3f803f80u, s1b); s2b = dot2(y1[j], y1[j], s2b);
    }
    s1a = dot2(y2[0], 0x3f803f80u, s1a); s2a = dot2(y2[0], y2[0], s2a);
    s1b = dot2(y2[1], 0x3f803f80u, s1b); s2b = dot2(y2[1], y2[1], s2b);
    const float s1 = xsum16_32(s1a + s1b), s2 = xsum16_32(s2a + s2b);
    st8(u32x2{__float_as_uint(s1), __float_as_uint(s2)}, rst, sto, 0);
  }
}

DI constexpr unsigned wd_pos(int c) { return c < 8 ? 10 * c : 60 + 10 * c; }
constexpr int ORD[14] = {0, 1, 4, 5, 7, 8, 11, 12, 2, 3, 6, 9, 10, 13};
DI constexpr int g4_late(int t) { return (t >= 4 && t < 7) || t >= 11; }
DI constexpr int g4_slot(int t) { return (t < 4 ? t : (t < 7 ? t - 4 : (t < 11 ? t - 7 : t - 11))) * 5120; }

#define PHASE_IDS \
  const unsigned lane = opq_v(tid0) & 63, w = opq_s(w0), bid = opq_s(blockIdx.x); \
  const unsigned xcd = bid & 7, idx = bid >> 3, i16 = lane & 15, q = lane >> 4; \
  char* const ws = opq_p(ws_); \
  (void)xcd; (void)idx; (void)i16; (void)q; (void)ws;
#define IDS_G2 const unsigned kq = xcd & 3, P = (xcd >> 2) * 8 + (idx >> 2), c4 = idx & 3;
#define G2_LA(h) (lb + G2_A + ((w >> 1) * 4 + ((2 * (w & 1) + (h) + 3 - kq) & 3)) * 2048)
#define IDS_G3 const unsigned P = (xcd >> 1) * 4 + (idx >> 3), ct = (xcd & 1) * 8 + (idx & 7);
#define C8_OF ((xcd & 1) * 4 + (idx & 3))
#define IDS_G4 const unsigned rt64 = (xcd >> 1) * 8 + (idx >> 2), c8 = C8_OF;
#define LDX(r, k) ld16(rx, lane * 16 + ((k) & 3) * 1024, sox + (r) * 20480 + ((k) >> 2) * 4096)

__global__ __launch_bounds__(512, 2) void k_all(const u16* x0_, const u16* Sre_, const u16* Sim_, const u16* Dre_, const u16* Dim_,
                                               const u16* w1_, const u16* w2_, char* ws_, u16* out_) {
  const unsigned tid0 = threadIdx.x, wall = rfl(tid0 >> 6), w0 = wall & 3;
  const unsigned lb = lds_base();
  const char* code; unsigned code_len;
  asm volatile("s_getpc_b64 %0\n.Lcw_begin:\n\ts_mov_b32 %1, .Lcw_end-.Lcw_begin" : "=s"(code), "=s"(code_len));
  u64 epoch;

  if (__builtin_expect(wall >= 4, 1)) {
    const unsigned sink = lb + K1_CS + 1024;
    unsigned ep0, ep1;
    asm volatile("global_load_dword %0, %2, %3 sc1\n\tglobal_load_dword %1, %2, %3 offset:4 sc1"
                 : "=&v"(ep0), "=&v"(ep1) : "v"(WS_FLG + FL_E * 128), "s"(ws_) : "memory");
    {
      PHASE_IDS
      IDS_G3
      const u32x4 sa = make_srd(opq_p(x0_), S_ * D_ * 2), sbd = make_srd(ct < 8 ? opq_p(Dre_) : opq_p(Dim_), D_ * D_ * 2);
      const unsigned vo = (lane >> 3) * 1280 + (((lane & 7) ^ (lane >> 3)) << 4);
      const unsigned soa = (P * 128 + w * 32) * 1280, sob = ((ct & 7) * 80 + w * 8) * 1280;
      const unsigned la = lb + w * 4096, lbb = lb + 16384 + w * 1024;
      const unsigned u0 = bid * 4 + w, u1 = 1024 + bid;
      const bool extra = w == 3 && bid < 56;
      const unsigned rot = ((idx & 7) + 3 * (idx >> 3)) % 10u;
      auto ko = [&](unsigned s) { const unsigned c = s + rot; return (c >= 10 ? c - 10 : c) * 128; };
      asm volatile("s_nop 4" ::: "memory");
      k1_issue(sa, sbd, vo, la, lbb, soa + ko(0), sob + ko(0), w);
      k1_issue(sa, sbd, vo, la + K1_STAGE, lbb + K1_STAGE, soa + ko(1), sob + ko(1), w);
      wait_vm<6>();
      {
        unsigned lo, hi;
        asm volatile("v_readfirstlane_b32 %0, %2\n\tv_readfirstlane_b32 %1, %3\n\ts_nop 4" : "=s"(lo), "=s"(hi) : "v"(ep0), "v"(ep1));
        epoch = (u64)lo + ((u64)hi << 32) + 1;
      }
      bar();
      k1_issue(sa, sbd, vo, la + 2 * K1_STAGE, lbb + 2 * K1_STAGE, soa + ko(2), sob + ko(2), w);
      const u32x4 ss = make_srd((xcd & 2) ? Sim_ : Sre_, S_ * S_ * 2);
      const unsigned vs = (lane & 31) * 4096 + (lane >> 5) * 128;
      const unsigned sos = ((xcd >> 2) * 1024 + idx * 32) * 4096 + (xcd & 1) * 2048 + w * 256;
      unsigned so = 0;
#pragma unroll 1
      for (int i = 0; i < 10; ++i) {
        if (i == 0) wait_vm<6>(); else if (i == 1) wait_vm<2 * 6>(); else if (i < 6) wait_vm<3 * 6>();
        else if (i == 6) wait_vm<2 * 6 + (RTI >= 2 && RTI < 6 ? 4 : 0)>(); else if (i == 7) wait_vm<6 + (RTI >= 3 && RTI < 7 ? 4 : 0)>();
        else wait_vm<(RTI >= 4 && RTI < 8 ? 4 : 0) + (WS2 >= 4 && WS2 < 8) + (WS1 >= 4 && WS1 < 8)>();
        bar();
        if (i == 0) {
          k1_issue(sa, sbd, vo, la + 3 * K1_STAGE, lbb + 3 * K1_STAGE, soa + ko(3), sob + ko(3), w);
          k1_issue(sa, sbd, vo, la + 4 * K1_STAGE, lbb + 4 * K1_STAGE, soa + ko(4), sob + ko(4), w);
        } else if (i == 1) {
          k1_issue(sa, sbd, vo, la, lbb, soa + ko(5), sob + ko(5), w);
          k1_issue(sa, sbd, vo, la + K1_STAGE, lbb + K1_STAGE, soa + ko(6), sob + ko(6), w);
        } else if (i < 5) k1_issue(sa, sbd, vo, la + so, lbb + so, soa + ko(i + 5), sob + ko(i + 5), w);
        so = so == 4 * K1_STAGE ? 0 : so + K1_STAGE;
        if (i == WS1) warm(sink, ss, vs, sos);
        if (i == WS2) warm(sink, ss, vs, sos + 1024);
        if (i == RTI) {
          retile_issue(u0, w1_, w2_, lb + K1_TR + w * 4096, lane);
          if (extra) retile_issue(u1, w1_, w2_, lb + K1_TR + 4 * 4096, lane);
        }
      }
      bar();
    }
    bf16x8 wr[10][5];
#pragma unroll
    for (int t = 0; t < 10; ++t)
#pragma unroll
      for (int f = 0; f < 5; ++f) wr[t][f] = bf16x8{};
#pragma unroll 1
    for (int L = 0; L < 2; ++L) {
      {
        PHASE_IDS
        IDS_G2
        const u32x4 sa = make_srd(kq < 2 ? opq_p(Sre_) : opq_p(Sim_), S_ * S_ * 2);
        const u32x4 sb_ = make_srd(ws + (L ? WS_AB2 : WS_AB1), SZ_AB);
        const unsigned va = (lane >> 3) * 4096 + (((lane & 7) ^ (lane >> 3)) << 4), vb = lane * 16;
        const unsigned soa = (P * 128 + w * 32) * 4096 + (kq & 1) * 2048;
        const unsigned sob = ((2 * c4 + (w >> 1)) * 128 + kq * 32 + (w & 1)) * 5 * 1024;
        const unsigned la = lb + G2_A + w * 4096, lbb = lb + w * 5120;
        const unsigned la0 = G2_LA(0), la1 = G2_LA(1);
        const unsigned ct = (xcd & 1) * 8 + (idx & 7);
        auto kc = [&](unsigned s) { return L ? 2 * (s & 7) + ((s ^ (s >> 3)) & 1) : s; };
        asm volatile("s_nop 4" ::: "memory");
        auto issue_a = [&](unsigned l, unsigned so) {
          glds<0, 0>(sa, va, la0 + l, so); glds<1024, 8 * 4096>(sa, va, la0 + l, so);
          glds<0, 16 * 4096>(sa, va, la1 + l, so); glds<1024, 24 * 4096>(sa, va, la1 + l, so);
        };
        if (L == 0) {
#pragma unroll
          for (int s = 0; s < 3; ++s) issue_a(s * G2_AS, soa + s * 128);
          const unsigned u0 = bid * 4 + w, u1 = 1024 + bid;
          const rsrc_t rws = make_rsrc(ws, WS_END);
          wait_vm<12>();
          retile_finish(u0, 0, rws, lb + K1_TR + w * 4096, lane);
          retile_finish(u0, 2, rws, lb + K1_TR + w * 4096, lane);
          if (w == 3 && bid < 56) {
            retile_finish(u1, 0, rws, lb + K1_TR + 4 * 4096, lane);
            retile_finish(u1, 2, rws, lb + K1_TR + 4 * 4096, lane);
          }
          __builtin_amdgcn_s_sleep(P5DELAY0);
        } else {
          issue_a(3 * G2_AS, soa + kc(3) * 128);
          __builtin_amdgcn_s_sleep(P5DELAY);
        }
        const unsigned f5 = FL_5 + ((L * 4 + kq) * 4 + c4) * 64 + ((i16 >> 1) * 2 + (w >> 1)) * 4 + (L ? 2 * (w & 1) + (i16 & 1) : (w & 1) + 2 * (i16 & 1));
        poll0(ws, f5, 16, epoch, lane);
        wait_vm<0>();
        glds_run_sc1<5>(sb_, vb, lbb, sob);
        glds_run_sc1<5>(sb_, vb, lbb + G2_BS, sob + kc(1) * 10240);
        if (L == 0) {
          raise(ws, FL_W + bid * 4 + w, epoch, lane);
          if (w == 3 && bid < 56) raise(ws, FL_W + 1024 + bid, epoch, lane);
          wait_vm<6>();
        } else wait_vm<5>();
        bar();
        glds_run_sc1<5>(sb_, vb, lbb + 2 * G2_BS, sob + kc(2) * 10240);
        if (L == 0) issue_a(3 * G2_AS, soa + kc(3) * 128);
        glds_run_sc1<5>(sb_, vb, lbb + 3 * G2_BS, sob + kc(3) * 10240);
        const u32x4 sw = make_srd(ws + WS_W1, SZ_W1);
        const unsigned sow = ct * 140 * 1024;
        unsigned fw, fw0 = 0, fw1 = 0;
        {
          const unsigned n = w * 42 + (lane < 42 ? lane : 0), k3 = n / 3u, g3 = (7 * ct >> 2) + (n - k3 * 3), n2 = n - 60;
          fw = FL_W + (n < 60 ? k3 * 27 + (g3 < 26 ? g3 : 26) : 540 + (n2 >> 1) * 10 + (5 * C8_OF >> 2) + (n2 & 1));
          if (L) fw = f5 + 1024;
        }
#pragma unroll 1
        for (int s = 0; s < 16; ++s) {
          if (s == 0) wait_vm<10>();
          if (s == 1) wait_vm<14>();
          wait_vm<18>();
          if (s == (L ? 4 : 9)) {
            asm volatile("" : "+v"(fw0), "+v"(fw1));
            if (!__all(fw0 == (unsigned)epoch && fw1 == (unsigned)(epoch >> 32))) poll0(ws, fw, 42, epoch, lane);
          }
          bar();
          const unsigned sl = s & 3;
          if (s < 12) {
            issue_a(sl * G2_AS, soa + kc(s + 4) * 128);
            glds_run_sc1<5>(sb_, vb, lbb + sl * G2_BS, sob + kc(s + 4) * 10240);
            if (s == (L ? 1 : 6))
              asm volatile("global_load_dword %0, %2, %3 sc1\n\tglobal_load_dword %1, %2, %3 offset:4 sc1"
                           : "+v"(fw0), "+v"(fw1) : "v"(WS_FLG + fw * 128), "s"(ws) : "memory");
          } else {
            glds_run_sc1<5>(sw, vb, lbb + sl * G2_BS, sow + w * 5120 + sl * G2_BS);
            if (s < 15) glds_run_sc1<4>(sw, vb, la + sl * G2_AS, sow + G2_A + w * 4096 + sl * G2_AS);
            else glds_run_sc1<3>(sw, vb, lb + G2_A + 3 * G2_AS + w * 3072, sow + G2_A + 3 * G2_AS + w * 3072);
          }
        }
      }
      {
        PHASE_IDS
        IDS_G4
        const unsigned ct = (xcd & 1) * 8 + (idx & 7);
        if (w >= 2) {
          const rsrc_t rc = make_rsrc(ws + WS_CS1, 20 * F_ * 4);
          const unsigned c = (w - 2) * 64 + lane, col = ct * 112 + c;
          const unsigned vo = c < 112 && col < F_ ? col * 4 : OOB;
          float csp = 0.f;
#pragma unroll
          for (int k = 0; k < 20; ++k) csp += __uint_as_float(__builtin_amdgcn_raw_buffer_load_b32(rc, vo, k * F_ * 4, 16));
          lds_wr1(lb + KM_CS1 + c * 4, csp);
        }
        const unsigned so = (c8 * 54 + w * 14) * 5 * 1024;
        wait_vm<0>();
        if (L == 0) {
          const rsrc_t rw = make_rsrc(ws + WS_W2, SZ_W2);
          const unsigned vl = lane * 16, vz = lane * 16 + (w < 3 ? 0u : OOB);
          sfor<0, 50>([&](auto N) {
            constexpr int t = 4 + N.v / 5, n = ORD[t] * 5 + N.v % 5;
            wr[t - 4][N.v % 5] = ld16(rw, (ORD[t] < 12 ? vl : vz) + (n & 3) * 1024, so + (n >> 2) * 4096);
            if constexpr (N.v % 5 == 4 && N.v < 49) { sb(); __builtin_amdgcn_s_sleep(RGAP); sb(); }
          });
          sb();
        }
        wait_lgkm0();
        bar();
        bar();
        const u32x4 s2 = make_srd(ws + WS_W2, SZ_W2);
        const unsigned sg = rfl(so);
        asm volatile("s_nop 4" ::: "memory");
        glds_run_sc1<10>(s2, lane * 16, lb + w * 20480, sg);
        glds_run_sc1<10>(s2, lane * 16, lb + w * 20480 + 10240, sg + 4 * 5120);
        wait_vm<0>();
        const unsigned pe = lb + w * 20480 + lane * 16, pl = lb + G4_LATE + w * 15360 + lane * 16;
        auto put = [&](auto T0, auto T1) {
          sfor<T0.v * 5, T1.v * 5>([&](auto N) {
            constexpr int t = N.v / 5, f = N.v % 5;
            *(LDS_AS bf16x8*)(ldsp)(uintptr_t)((g4_late(t) ? pl : pe) + g4_slot(t) + f * 1024) = wr[t - 4][f];
          });
          wait_lgkm0();
        };
        bar();
        put(IC<4>{}, IC<7>{});
        bar();
        put(IC<7>{}, IC<11>{});
        bar();
        put(IC<11>{}, IC<14>{});
        bar();
      }
      if (L) { bar(); break; }
      {
        PHASE_IDS
        IDS_G3
        const u32x4 sd = make_srd(ct < 8 ? opq_p(Dre_) : opq_p(Dim_), D_ * D_ * 2);
        const unsigned vo = (lane >> 3) * 1280 + (((lane & 7) ^ (lane >> 3)) << 4);
        const unsigned so = (ct & 7) * 80 * 1280;
        sfor<0, 10>([&](auto G) { glds<G.v * 1024, G.v * 8 * 1280>(sd, vo, lb + 20 * w * 1024, so + 2 * w * 128); });
        sfor<0, WDPRE>([&](auto G) { glds<G.v * 1024, G.v * 8 * 1280>(sd, vo, lb + (20 * w + 10) * 1024, so + (2 * w + 1) * 128); });
        bar();
        sfor<WDPRE, 10>([&](auto G) { glds<G.v * 1024, G.v * 8 * 1280>(sd, vo, lb + (20 * w + 10) * 1024, so + (2 * w + 1) * 128); });
        sfor<0, 5>([&](auto G) { glds<G.v * 1024, G.v * 8 * 1280>(sd, vo, lb + (140 + w * 5) * 1024, so + (8 + (w >> 1)) * 128 + (w & 1) * 40 * 1280); });
        wait_vm<0>();
        bar();
      }
      {
        PHASE_IDS
        IDS_G2
        const u32x4 sa = make_srd(kq < 2 ? opq_p(Sre_) : opq_p(Sim_), S_ * S_ * 2);
        const unsigned va = (lane >> 3) * 4096 + (((lane & 7) ^ (lane >> 3)) << 4);
        const unsigned soa = (P * 128 + w * 32) * 4096 + (kq & 1) * 2048;
        __builtin_amdgcn_s_sleep(ASLEEP2);
#pragma unroll
        for (int s = 0; s < 3; ++s) {
          const unsigned l0 = G2_LA(0) + s * G2_AS, l1 = G2_LA(1) + s * G2_AS, so = soa + (2 * s + (s & 1)) * 128;
          glds<0, 0>(sa, va, l0, so); glds<1024, 8 * 4096>(sa, va, l0, so);
          glds<0, 16 * 4096>(sa, va, l1, so); glds<1024, 24 * 4096>(sa, va, l1, so);
        }
        bar();
      }
    }
    return;
  }

  {
    const u64 e = __hip_atomic_load(flag_ptr(ws_, FL_E), RLX_AGENT);
    epoch = (u64)rfl((unsigned)e) + ((u64)rfl((unsigned)(e >> 32)) << 32) + 1;
  }
#pragma unroll 1
  for (int L = 0; L < 2; ++L) {
    {
      PHASE_IDS
      IDS_G3
      f32x4 acc[2][5];
      float mu[2][4], rs[2][4], csv[5];
      const float sign = ct < 8 ? 1.f : -1.f;
      const unsigned f5 = FL_5 + (((ct >> 3) * 2 + (P >> 3)) * 4 + ((ct & 7) >> 1)) * 64 + ((P & 7) * 2 + (ct & 1)) * 4 + w;
      if (L == 0) {
        const unsigned ra0 = lb + w * 4096 + i16 * 128 + ((q ^ (lane & 7)) << 4);
        const unsigned rb0 = lb + 16384 + i16 * 128 + ((q ^ (lane & 7)) << 4);
        const f32x4 z4 = {0.f, 0.f, 0.f, 0.f};
        f32x4 cs0 = z4, cs1 = z4, rs_[2] = {z4, z4}, rq_[2] = {z4, z4};
#pragma unroll
        for (int r = 0; r < 2; ++r)
#pragma unroll
          for (int f = 0; f < 5; ++f) acc[r][f] = z4;
        bf16x8 fr[2][8];
        bf16x8 one = __builtin_bit_cast(bf16x8, u32x4{0x3f803f80u, 0x3f803f80u, 0x3f803f80u, 0x3f803f80u});
        auto half = [&](auto KK, unsigned so) {
          constexpr int kk = KK.v;
          const unsigned pa = (ra0 ^ ((kk ^ 1) * 64)) + so, pb = (rb0 ^ ((kk ^ 1) * 64)) + so;
          sfor<0, 16>([&](auto M) {
            constexpr int m = M.v;
            if constexpr (m < 10) mma(acc[m & 1][m >> 1], fr[kk][m & 1], fr[kk][2 + (m >> 1)]);
            else if constexpr (m == 10) mma(cs0, one, fr[kk][7]);
            else if constexpr (m == 11) mma(cs1, one, fr[kk][6]);
            else if constexpr (m < 14) mma(rs_[m - 12], fr[kk][m - 12], one);
            else mma(rq_[m - 14], fr[kk][m - 14], fr[kk][m - 14]);
            constexpr int n = m >> 1;
            if constexpr ((m & 1) == 0) fr[kk ^ 1][n] = n < 2 ? lds_rd(pa + n * 2048) : (n < 7 ? lds_rd(pb + (n - 2) * 2048) : lds_rd(pb + w * 2048));
            sb();
          });
        };
        warm(lb + K1_CS + 1024, make_srd(code, code_len), lane < 4 ? (idx * 16 + w * 4 + lane) * 128 : OOB, 0);
        asm volatile("s_nop 1" : "+v"(cs0), "+v"(cs1), "+v"(rs_[0]), "+v"(rs_[1]), "+v"(rq_[0]), "+v"(rq_[1]), "+v"(one));
        fence_all(acc);
        bar();
        fr[0][0] = lds_rd(ra0); fr[0][1] = lds_rd(ra0 + 2048);
#pragma unroll
        for (int f = 0; f < 5; ++f) fr[0][2 + f] = lds_rd(rb0 + f * 2048);
        fr[0][7] = lds_rd(rb0 + w * 2048);
        sb();
        unsigned so = 0;
#pragma unroll 1
        for (int i = 0; i < 10; ++i) {
          half(IC<0>{}, so);
          wait_lgkm0();
          bar();
          so = so == 4 * K1_STAGE ? 0 : so + K1_STAGE;
          half(IC<1>{}, so);
        }
        fence_all(acc);
        asm volatile("" : "+v"(cs0), "+v"(cs1), "+v"(rs_[0]), "+v"(rs_[1]), "+v"(rq_[0]), "+v"(rq_[1]));
        const unsigned csb = lb + K1_CS;
        if (q == 0) {
          lds_wr1(csb + (w * 16 + i16) * 4, cs0[0]);
          if (w == 0) lds_wr1(csb + (64 + i16) * 4, cs1[0]);
        }
        wait_lgkm0();
        bar();
#pragma unroll
        for (int f = 0; f < 5; ++f) csv[f] = lds_rd1(csb + (f * 16 + i16) * 4);
#pragma unroll
        for (int r = 0; r < 2; ++r)
#pragma unroll
          for (int j = 0; j < 4; ++j) {
            float m_, r_;
            ln_coef(rs_[r][j], __shfl(rq_[r][j], q * 20 + j), m_, r_);
            mu[r][j] = -m_; rs[r][j] = sign * r_;
          }
        if (w == 0) {
          const rsrc_t rc = make_rsrc(ws + WS_CSD, 256 * 320);
          st4(__float_as_uint(q == 0 ? csv[0] : (q == 1 ? csv[1] : (q == 2 ? csv[2] : csv[3]))), rc, lane * 4, bid * 320);
          st4(__float_as_uint(csv[4]), rc, q == 0 ? 256 + lane * 4 : OOB, bid * 320);
        }
        g1_store(acc, mu, rs, csv, make_rsrc(ws + WS_AB1, SZ_AB), P, ct, w, i16, q);
        publish(ws, f5, epoch, lane);
      } else {
        {
          const rsrc_t rc = make_rsrc(ws + WS_CSD, 256 * 320);
#pragma unroll
          for (int f = 0; f < 5; ++f) csv[f] = __uint_as_float(__builtin_amdgcn_raw_buffer_load_b32(rc, (f * 16 + i16) * 4, bid * 320, 16));
        }
        const unsigned h0 = P & 1;
        poll<PD4>(ws, FL_4 + P * 64 + ((i16 >> 3) * 4 + w) * 8 + (i16 & 7), 16, epoch, lane);
        const rsrc_t rx = make_rsrc(ws + WS_X2, SZ_X);
        const unsigned sox0 = (P * 8 + h0 * 4 + w) * 20 * 1024, sox1 = (P * 8 + (h0 ^ 1) * 4 + w) * 20 * 1024;
        bf16x8 a[40];
#define LDA(n) a[n] = ld16(rx, lane * 16 + ((n) & 3) * 1024, ((n) < 20 ? sox0 : sox1) + (((n) % 20) >> 2) * 4096)
        sfor<0, 6>([&](auto N) { LDA(N.v); });
        const rsrc_t rst = make_rsrc(ws + WS_ST2, SZ_ST);
        const unsigned sto = ((lane >> 5) * 4 * S_ + P * 128 + (((lane >> 4) & 1) ^ h0) * 64 + w * 16 + i16) * 8;
        const u32x2 sp0 = ld8u(rst, sto, 0), sp1 = ld8u(rst, sto, S_ * 8), sp2 = ld8u(rst, sto, 2 * S_ * 8), sp3 = ld8u(rst, sto, 3 * S_ * 8);
        sb();
        bar();
        bf16x8 b[2][5];
        const unsigned rb = lb + i16 * 128 + ((q ^ (lane & 7)) << 4);
        const rsrc_t rab = make_rsrc(ws + WS_AB2, SZ_AB);
        const unsigned vo = ((2 * (w & 1) + (q >> 1)) * 16 + i16) * 16 + (q & 1) * 8;
        const unsigned so = ((ct & 7) * 128 + (ct >> 3) * 64 + P * 4 + (w >> 1)) * 5 * 1024;
        auto put = [&](auto H) {
          constexpr int hh = H.v;
#pragma unroll
          for (int f = 0; f < 5; ++f) {
            const f32x2 cs = {csv[f], csv[f]};
            u32x2 y;
#pragma unroll
            for (int e = 0; e < 2; ++e) {
              const f32x2 t = __builtin_elementwise_fma(f32x2{mu[hh][2 * e], mu[hh][2 * e + 1]}, cs, f32x2{acc[hh][f][2 * e], acc[hh][f][2 * e + 1]}) *
                              f32x2{rs[hh][2 * e], rs[hh][2 * e + 1]};
              y[e] = __builtin_bit_cast(unsigned, __builtin_convertvector(t, bf16x2));
            }
            st8(y, rab, vo + (f & 3) * 1024, so + (hh ^ h0) * 10240 + (f >> 2) * 4096);
          }
        };
#pragma unroll
        for (int f = 0; f < 5; ++f) b[0][f] = lds_rd(rb + wd_pos(0) * 1024 + f * 2048);
        sb();
        sfor<0, 40>([&](auto N) {
          constexpr int n = N.v, hh = n / 20, k = n % 20, kn = (k + 1) % 20;
          if constexpr (n == 2 * KP1) {
            float s1 = (__uint_as_float(sp0[0]) + __uint_as_float(sp1[0])) + (__uint_as_float(sp2[0]) + __uint_as_float(sp3[0]));
            float s2 = (__uint_as_float(sp0[1]) + __uint_as_float(sp1[1])) + (__uint_as_float(sp2[1]) + __uint_as_float(sp3[1]));
            auto ta = __builtin_amdgcn_permlane32_swap(__float_as_uint(s1), __float_as_uint(s1), false, false);
            auto tb = __builtin_amdgcn_permlane32_swap(__float_as_uint(s2), __float_as_uint(s2), false, false);
            float m, rr;
            ln_coef(__uint_as_float(ta[0]) + __uint_as_float(ta[1]), __uint_as_float(tb[0]) + __uint_as_float(tb[1]), m, rr);
            m = -m; rr *= sign;
#pragma unroll
            for (int r = 0; r < 2; ++r)
#pragma unroll
              for (int e = 0; e < 4; ++e) { mu[r][e] = __shfl(m, r * 16 + q * 4 + e); rs[r][e] = __shfl(rr, r * 16 + q * 4 + e); }
            asm volatile("" : "+v"(mu[0][0]), "+v"(mu[0][1]), "+v"(mu[0][2]), "+v"(mu[0][3]), "+v"(mu[1][0]), "+v"(mu[1][1]), "+v"(mu[1][2]), "+v"(mu[1][3]),
                              "+v"(rs[0][0]), "+v"(rs[0][1]), "+v"(rs[0][2]), "+v"(rs[0][3]), "+v"(rs[1][0]), "+v"(rs[1][1]), "+v"(rs[1][2]), "+v"(rs[1][3]));
            sb();
          }
          sfor<0, 5>([&](auto F) {
            constexpr int f = F.v;
            if constexpr (k == 0) mma0(acc[hh][f], a[n], b[n & 1][f]); else mma(acc[hh][f], a[n], b[n & 1][f]);
            if constexpr (n + 1 < 40) b[(n + 1) & 1][f] = lds_rd((rb ^ ((kn & 1) * 64)) + wd_pos(kn >> 1) * 1024 + f * 2048);
            if constexpr ((f == 1 || f == 3) && 6 + 2 * n + (f >> 1) < 40) LDA(6 + 2 * n + (f >> 1));
            sb();
          });
          if constexpr (n == 21) { pin_acc(acc[0]); put(IC<0>{}); sb(); }
          if constexpr (n == 20 + PUBG) publish(ws, f5 + 1024, epoch, lane);
        });
        fence_acc(acc[1]);
        bar();
        put(IC<1>{});
        publish(ws, f5 + 2048, epoch, lane);
      }
    }
    {
      PHASE_IDS
      IDS_G2
      const unsigned m = w >> 1, n = w & 1;
      const unsigned ra = lb + G2_A + (m * 64 + i16) * 128 + ((q ^ (lane & 7)) << 4);
      const unsigned rb = lb + n * 10240 + lane * 16;
      const unsigned row = P * 128 + m * 64 + kq * 16 + i16, ct = 2 * c4 + n;
      const TileOff ro = tile_offsets(L == 0, row, ct, q), oo = tile_offsets(false, row, ct, q);
      const rsrc_t rxp = make_rsrc(ws + WS_XP + L * SZ_XP, SZ_XP);
      const unsigned xo = ((P * 4 + c4) * 48 + w) * XSLOT;
      f32x4 acc[4][5];
#pragma unroll
      for (int i = 0; i < 4; ++i)
#pragma unroll
        for (int f = 0; f < 5; ++f) acc[i][f] = f32x4{0.f, 0.f, 0.f, 0.f};
      bf16x8 fa[2][4], fb[2][5];
      unsigned ra1 = ra ^ 64, rb2 = rb + 2 * G2_BS;
      asm volatile("" : "+v"(ra1), "+v"(rb2));
      auto half = [&](auto KK, auto SL) {
        constexpr int kk = KK.v, sl = SL.v;
        sfor<0, 20>([&](auto M) {
          constexpr int mm = M.v;
          mma(acc[mm / 5][mm % 5], fb[kk][mm % 5], fa[kk][mm / 5]);
          if constexpr (mm < 5) fb[kk ^ 1][mm] = lds_rd((sl < 2 ? rb : rb2) + (sl & 1) * G2_BS + (kk ^ 1) * 5120 + mm * 1024);
          else if constexpr (mm < 9) fa[kk ^ 1][mm - 5] = lds_rd((kk ? ra : ra1) + sl * G2_AS + (mm - 5) * 2048);
          sb();
        });
      };
      if (L) poll0(ws, FL_4 + P * 64 + (m * 4 + kq) * 8 + ct, 1, epoch, lane);
      ResTile rt = load_res(make_rsrc(L ? (const void*)(ws + WS_X2) : (const void*)opq_p(x0_), SZ_X), ro);
      fence_all(acc);
      bar();
#pragma unroll
      for (int f = 0; f < 5; ++f) fb[0][f] = lds_rd(rb + f * 1024);
#pragma unroll
      for (int i = 0; i < 4; ++i) fa[0][i] = lds_rd(ra + i * 2048);
      sb();
      auto stage = [&](auto U) {
        half(IC<0>{}, U);
        wait_lgkm0();
        bar();
        half(IC<1>{}, IC<(U.v + 1) & 3>{});
      };
#pragma unroll 1
      for (int g = 0; g < 3; ++g) sfor<0, 4>(stage);
      sfor<0, 3>(stage);
      half(IC<0>{}, IC<3>{});
      wait_lgkm0();
      bar();
      {
        unsigned p[3][10];
        u32x4 y[3][2];
        auto pack = [&](auto N) {
          constexpr int i = N.v / 5, f = N.v % 5;
          asm volatile("" : "+v"(acc[i][f]));
          p[i][2 * f] = pk2(acc[i][f][0], acc[i][f][1]); p[i][2 * f + 1] = pk2(acc[i][f][2], acc[i][f][3]);
          const unsigned o = xo + (((kq + 1 + i) & 3) * 3 + 2 - i) * 4 * XSLOT;
          if constexpr (f == 1) { y[i][0] = u32x4{p[i][0], p[i][1], p[i][2], p[i][3]}; st16_raw(y[i][0], rxp, lane * 16, o); }
          if constexpr (f == 3) { y[i][1] = u32x4{p[i][4], p[i][5], p[i][6], p[i][7]}; st16_raw(y[i][1], rxp, lane * 16 + 1024, o); }
          if constexpr (f == 4) st8(u32x2{p[i][8], p[i][9]}, rxp, lane * 8 + 2048, o);
        };
        sfor<0, 20>([&](auto M) {
          constexpr int mm = M.v;
          mma(acc[mm / 5][mm % 5], fb[1][mm % 5], fa[1][mm / 5]);
          if constexpr (mm >= 8) pack(IC<mm - 8>{});
          sb();
        });
        pack(IC<12>{}); pack(IC<13>{}); pack(IC<14>{});
        st_guard(y[0][0], y[0][1], y[1][0], y[1][1], y[2][0], y[2][1]);
      }
      f32x4 (&v)[5] = acc[3];
      fence_acc(v);
      const unsigned fx = FL_X + ((L * 64 + P * 4 + c4) * 4 + w) * 4;
      publish(ws, fx + kq, epoch, lane);
      poll<PDX>(ws, fx + ((kq + 1 + lane) & 3), 3, epoch, lane);
      const unsigned xi = xo + kq * 12 * XSLOT;
      u32x4 g0[3], g1[3]; u32x2 g2[3];
#pragma unroll
      for (int s = 0; s < 3; ++s) {
        g0[s] = ld16u(rxp, lane * 16, xi + s * 4 * XSLOT);
        g1[s] = ld16u(rxp, lane * 16 + 1024, xi + s * 4 * XSLOT);
        g2[s] = ld8u(rxp, lane * 8 + 2048, xi + s * 4 * XSLOT);
      }
      sb();
#pragma unroll
      for (int s = 0; s < 3; ++s) {
        v[0] += f32x4{bflo(g0[s][0]), bfhi(g0[s][0]), bflo(g0[s][1]), bfhi(g0[s][1])};
        v[1] += f32x4{bflo(g0[s][2]), bfhi(g0[s][2]), bflo(g0[s][3]), bfhi(g0[s][3])};
        v[2] += f32x4{bflo(g1[s][0]), bfhi(g1[s][0]), bflo(g1[s][1]), bfhi(g1[s][1])};
        v[3] += f32x4{bflo(g1[s][2]), bfhi(g1[s][2]), bflo(g1[s][3]), bfhi(g1[s][3])};
        v[4] += f32x4{bflo(g2[s][0]), bfhi(g2[s][0]), bflo(g2[s][1]), bfhi(g2[s][1])};
      }
      finish_tile(v, rt, make_rsrc(ws + (L ? WS_X3 : WS_X1), SZ_X), oo, true, make_rsrc(ws + (L ? WS_ST3 : WS_ST1), SZ_ST),
                  q == 0 ? (ct * S_ + row) * 8 : OOB);
      publish(ws, FL_2 + (L * 16 + P) * 64 + (m * 4 + kq) * 8 + ct, epoch, lane);
    }
    u32x4 y2[2]; u32x2 y1[2];
    {
      PHASE_IDS
      IDS_G3
      typedef __attribute__((ext_vector_type(2))) short s16x2;
      poll<PD2>(ws, FL_2 + (L * 16 + P) * 64 + w * 16 + i16, 16, epoch, lane);
      const rsrc_t rx = make_rsrc(ws + (L ? WS_X3 : WS_X1), SZ_X);
      const rsrc_t rst = make_rsrc(ws + (L ? WS_ST3 : WS_ST1), SZ_ST);
      const unsigned sox = (P * 8 + w * 2) * 20 * 1024;
      const unsigned par = ct & 1;
      bf16x8 a[20][2];
#pragma unroll
      for (int k = 0; k < 3; ++k) { a[k][0] = LDX(0, k); a[k][1] = LDX(1, k); }
      const unsigned sto = (2 * q * S_ + P * 128 + w * 32 + i16) * 8;
      const u32x2 sa0 = ld8u(rst, sto, 0), sa1 = ld8u(rst, sto, S_ * 8), sb0 = ld8u(rst, sto + 128, 0), sb1 = ld8u(rst, sto + 128, S_ * 8);
      float mu[2], rs[2];
      sb();
      bar();
      f32x4 acc[2][4];
      bf16x8 b[2][4];
      const unsigned rb = lb + lane * 16;
#pragma unroll
      for (int f = 0; f < 4; ++f) b[0][f] = lds_rd(rb + f * 1024);
      sb();
      sfor<0, 20>([&](auto K) {
        constexpr int k = K.v;
        const unsigned pb = k + 1 < 20 ? rb + (k + 1) * 4096 : rb + 80 * 1024;
        if constexpr (k == KP3) {
          ln_coef(xsum16_32(__uint_as_float(sa0[0]) + __uint_as_float(sa1[0])), xsum16_32(__uint_as_float(sa0[1]) + __uint_as_float(sa1[1])), mu[0], rs[0]);
          ln_coef(xsum16_32(__uint_as_float(sb0[0]) + __uint_as_float(sb1[0])), xsum16_32(__uint_as_float(sb0[1]) + __uint_as_float(sb1[1])), mu[1], rs[1]);
          asm volatile("" : "+v"(mu[0]), "+v"(mu[1]), "+v"(rs[0]), "+v"(rs[1]));
          sb();
        }
        sfor<0, 8>([&](auto M) {
          constexpr int f = M.v >> 1, r = M.v & 1;
          if constexpr (k == 0) mma0(acc[r][f], b[0][f], a[0][r]); else mma(acc[r][f], b[k & 1][f], a[k][r]);
          if constexpr (r == 0 && (k + 1 < 20 || f < 3)) b[(k + 1) & 1][f] = lds_rd(pb + f * 1024);
          if constexpr (r == 1 && 2 * k + 3 + (f >> 1) < 20) a[2 * k + 3 + (f >> 1)][f & 1] = LDX(f & 1, 2 * k + 3 + (f >> 1));
          sb();
        });
      });
      auto act = [&](const f32x4& c, int r, const f32x4& cs, unsigned (&pk)[2]) {
        const f32x2 nm = {-mu[r], -mu[r]}, r2 = {rs[r], rs[r]};
#pragma unroll
        for (int e = 0; e < 2; ++e) {
          const f32x2 z = __builtin_elementwise_fma(nm, f32x2{cs[2 * e], cs[2 * e + 1]}, f32x2{c[2 * e], c[2 * e + 1]}) * r2;
          const s16x2 hb = __builtin_bit_cast(s16x2, __builtin_convertvector(z, bf16x2));
          pk[e] = __builtin_bit_cast(unsigned, __builtin_elementwise_max(hb, s16x2{0, 0}));
        }
      };
      auto pair = [&](const unsigned (&u)[2], const unsigned (&v)[2]) {
        auto t0 = __builtin_amdgcn_permlane16_swap(u[0], v[0], false, false);
        auto t1 = __builtin_amdgcn_permlane16_swap(u[1], v[1], false, false);
        return u32x4{t0[0], t1[0], t0[1], t1[1]};
      };
      const unsigned csb = lb + KM_CS1 + q * 16;
      f32x4 c1s[4];
#pragma unroll
      for (int f = 0; f < 4; ++f) c1s[f] = lds_rdf(csb + (f + par) * 64);
      asm volatile("s_nop 7\n\ts_nop 1" : "+v"(acc[0][0]), "+v"(acc[0][1]), "+v"(acc[0][2]), "+v"(acc[0][3]),
                                          "+v"(acc[1][0]), "+v"(acc[1][1]), "+v"(acc[1][2]), "+v"(acc[1][3]));
      wait_lgkm0();
      bar();
      {
        const rsrc_t rh = make_rsrc(ws + (L ? WS_H2 : WS_H1), SZ_H);
        const unsigned vo = ((((q & 1) << 1) | (q >> 1)) * 16 + i16) * 16;
        const unsigned so = ((P * 8 + w * 2) * 54 + 7 * (ct >> 1) + 4 * par) * 1024;
        u32x4 y[2][2];
#pragma unroll
        for (int p = 0; p < 2; ++p) {
          const f32x4 c0 = c1s[2 * p], c1 = c1s[2 * p + 1];
#pragma unroll
          for (int r = 0; r < 2; ++r) {
            unsigned u[2], v[2];
            act(acc[r][2 * p], r, c0, u); act(acc[r][2 * p + 1], r, c1, v);
            y[p][r] = pair(u, v);
            st16_raw(y[p][r], rh, p == 1 && ct == 15 ? OOB : vo, so + (r * 54 + p) * 1024);
          }
        }
        st_guard(y[0][0], y[0][1], y[1][0], y[1][1]);
      }
      sb();
      sfor<0, 20>([&](auto K) {
        constexpr int k = K.v;
        const unsigned pb = rb + 80 * 1024 + (k + 1) * 3072;
        sfor<0, 6>([&](auto M) {
          constexpr int f = M.v >> 1, r = M.v & 1;
          if constexpr (k == 0) mma0(acc[r][f], b[0][f], a[0][r]); else mma(acc[r][f], b[k & 1][f], a[k][r]);
          if constexpr (r == 0 && k + 1 < 20) b[(k + 1) & 1][f] = lds_rd(pb + f * 1024);
          sb();
        });
        if constexpr (k == PUB1) publish(ws, FL_3 + (L * 32 + P) * 64 + w * 16 + ct, epoch, lane);
      });
      const f32x4 c0 = lds_rdf(csb + (4 + par) * 64), c1 = lds_rdf(csb + (5 + par) * 64), c2 = lds_rdf(csb + (par ? 0 : 6) * 64);
      asm volatile("s_nop 7\n\ts_nop 1" : "+v"(acc[0][0]), "+v"(acc[0][1]), "+v"(acc[0][2]), "+v"(acc[1][0]), "+v"(acc[1][1]), "+v"(acc[1][2]));
#pragma unroll
      for (int r = 0; r < 2; ++r) {
        unsigned u[2], v[2], s[2];
        act(acc[r][0], r, c0, u); act(acc[r][1], r, c1, v); act(acc[r][2], r, c2, s);
        y2[r] = pair(u, v);
        y1[r] = u32x2{s[0], s[1]};
      }
      asm volatile("" : "+v"(y2[0]), "+v"(y2[1]), "+v"(y1[0]), "+v"(y1[1]));
    }
    {
      PHASE_IDS
      IDS_G4
      const unsigned row = rt64 * 64 + w * 16 + i16;
      const rsrc_t rh = make_rsrc(ws + (L ? WS_H2 : WS_H1), SZ_H);
      const unsigned soh = (rt64 * 4 * 54 + w * 14) * 1024;
      const unsigned vl = lane * 16, vz = lane * 16 + (w < 3 ? 0u : OOB);
      bf16x8 a[14][4], b[2][5];
      unsigned sh[4];
#pragma unroll
      for (int i = 0; i < 4; ++i) sh[i] = soh + ((w + 1 + i) & 3) * 54 * 1024;
#define LDH(t, i) a[t][i] = ld16(rh, (ORD[t] < 12 ? vl : vz) + (ORD[t] & 3) * 1024, sh[i] + (ORD[t] >> 2) * 4096)
      const TileOff ro = tile_offsets(false, row, c8, q), oo = tile_offsets(L != 0, row, c8, q);
      const unsigned f3 = FL_3 + (L * 32 + (rt64 >> 1)) * 64 + ((rt64 & 1) * 2 + (lane & 1)) * 16 + 4 * w + ((lane >> 1) & 3);
      poll0(ws, f3, 8, epoch, lane);
      sfor<0, 8>([&](auto N) { LDH(N.v / 4, N.v % 4); });
      sb();
      {
        const unsigned P = (xcd >> 1) * 4 + (idx >> 3), ct = (xcd & 1) * 8 + (idx & 7), par = ct & 1;
        const unsigned vo = ((((q & 1) << 1) | (q >> 1)) * 16 + i16) * 16;
        const unsigned v1 = ((2 * par + (q >> 1)) * 16 + i16) * 16 + (q & 1) * 8;
        const unsigned so = ((P * 8 + w * 2) * 54 + 7 * (ct >> 1)) * 1024;
#pragma unroll
        for (int r = 0; r < 2; ++r) {
          st16_raw(y2[r], rh, ct == 15 ? OOB : vo, so + (r * 54 + 2 + 4 * par) * 1024);
          st8(y1[r], rh, v1, so + (r * 54 + 3) * 1024);
        }
        st_guard(y2[0], y2[1]);
      }
      ResTile rt;
      u64 fl2;
      const rsrc_t rr = make_rsrc(ws + (L ? WS_X3 : WS_X1), SZ_X);
      sb();
      bar();
      f32x4 acc[4][5];
      const unsigned rbe = lb + w * 20480 + lane * 16, rbl = lb + G4_LATE + w * 15360 + lane * 16;
#pragma unroll
      for (int f = 0; f < 5; ++f) b[0][f] = lds_rd(rbe + f * 1024);
      sb();
      sfor<0, 14>([&](auto T) {
        constexpr int t = T.v;
        constexpr int ta = t < 5 ? (t == 0 ? 2 : t + 3) : (t == 5 ? -1 : (t == 6 ? 8 : (t + 3 < 14 ? t + 3 : -1)));
        if constexpr (t == 3 || t == 6 || t == 10) { wait_lgkm0(); bar(); }
        if constexpr (t == 6) { if (!__all(fl2 == epoch)) poll0(ws, f3 + 16 * 64, 8, epoch, lane); }
        sfor<0, 20>([&](auto M) {
          constexpr int m = M.v, i = m / 5, f = m % 5;
          if constexpr (t == 0) mma0(acc[i][f], b[0][f], a[0][i]); else mma(acc[i][f], b[t & 1][f], a[t][i]);
          if constexpr (t + 1 < 14 && (m & 1) == 1 && m < 10)
            b[(t + 1) & 1][m >> 1] = lds_rd((g4_late(t + 1) ? rbl : rbe) + g4_slot(t + 1) + (m >> 1) * 1024);
          if constexpr (ta >= 0 && (m & 1) == 0 && m < 8) LDH(ta, m >> 1);
          if constexpr (t == 6 && (m & 1) == 0 && m >= 8 && m < 16) LDH(9, (m - 8) >> 1);
          if constexpr (t == 0 && (m & 1) == 0 && m >= 8 && m < 16) LDH(3, (m - 8) >> 1);
          if constexpr (t == 2 && m == 10) rt = load_res(rr, ro);
          if constexpr (t == 4 && m == 18) fl2 = __hip_atomic_load(flag_ptr(ws, f3 + 16 * 64), RLX_AGENT);
          if constexpr (t == 13 && m >= 8) {
            asm volatile("" : "+v"(acc[(m - 8) / 5][(m - 8) % 5]));
            lds_wrf(rbl + (m - 8) * 1024, acc[(m - 8) / 5][(m - 8) % 5]);
          }
          sb();
        });
        if constexpr (t == 2) {
          wait_vm<19>();
          raise(ws, FL_3 + (L * 32 + 16 + (xcd >> 1) * 4 + (idx >> 3)) * 64 + w * 16 + (xcd & 1) * 8 + (idx & 7), epoch, lane);
        }
      });
#pragma unroll
      for (int f = 2; f < 5; ++f) { asm volatile("" : "+v"(acc[2][f])); lds_wrf(rbl + (10 + f) * 1024, acc[2][f]); }
      f32x4 (&v)[5] = acc[3];
      fence_acc(v);
      wait_lgkm0();
      bar();
#pragma unroll
      for (int s = 1; s < 4; ++s) {
        const unsigned src = lb + G4_LATE + ((w + s) & 3) * 15360 + (3 - s) * 5 * 1024 + lane * 16;
#pragma unroll
        for (int f = 0; f < 5; ++f) v[f] += lds_rdf(src + f * 1024);
      }
      finish_tile(v, rt, L ? make_rsrc(opq_p(out_), SZ_X) : make_rsrc(ws + WS_X2, SZ_X), oo, L == 0, make_rsrc(ws + WS_ST2, SZ_ST),
                  q == 0 ? (c8 * S_ + row) * 8 : OOB);
      if (L == 0) publish(ws, FL_4 + (rt64 >> 1) * 64 + ((rt64 & 1) * 4 + w) * 8 + c8, epoch, lane);
    }
  }
  asm volatile(".Lcw_end:");
  if (blockIdx.x == 0 && w0 == 0) raise(ws_, FL_E, epoch, tid0);
  if (MULT && blockIdx.x == 5 && w0 == 0) {
    unsigned t5 = (unsigned)__builtin_amdgcn_s_memrealtime();
    unsigned ta = rfl(__builtin_amdgcn_raw_buffer_load_b32(make_rsrc(ws_ + WS_END, 4096), 0, 0, 16));
    unsigned d = (t5 - ta) * MULT;
    if (d > 100000) d = 100000;
    while ((unsigned)__builtin_amdgcn_s_memrealtime() - t5 < d) __builtin_amdgcn_s_sleep(1);
  }
}
__global__ __launch_bounds__(512, 2) void k_lead(char* ws_) {
  unsigned t = (unsigned)__builtin_amdgcn_s_memrealtime();
  if (BIGV) asm volatile("v_mov_b32 v250, 0" ::: "v250");
  if (blockIdx.x == 0 && threadIdx.x < 64) st4(t, make_rsrc(ws_ + WS_END, 4096), threadIdx.x == 0 ? 0 : OOB, 0);
}

extern "C" void kernel_launch(void* const* d_in, const int* in_sizes, int n_in, void* d_out, int out_size, void* d_ws,
                              size_t ws_size, hipStream_t stream) {
  char* ws = (char*)d_ws;
  (void)hipFuncSetAttribute((const void*)k_all, hipFuncAttributeMaxDynamicSharedMemorySize, KM_LDS);
  (void)hipFuncSetAttribute((const void*)k_lead, hipFuncAttributeMaxDynamicSharedMemorySize, KM_LDS);
  k_lead<<<LG, LB, LL, stream>>>(ws);
  k_all<<<256, 512, KM_LDS, stream>>>((const u16*)d_in[0], (const u16*)d_in[1], (const u16*)d_in[2], (const u16*)d_in[3],
                                     (const u16*)d_in[4], (const u16*)d_in[6], (const u16*)d_in[7], ws, (u16*)d_out);
}
```

```cpp
#include <hip/hip_runtime.h>
#include <hip/hip_bf16.h>
#include <stdint.h>

#define DI __device__ __forceinline__
typedef unsigned short u16;
typedef __attribute__((ext_vector_type(8))) __bf16 bf16x8;
typedef __attribute__((ext_vector_type(2))) __bf16 bf16x2;
typedef __attribute__((ext_vector_type(4))) float f32x4;
typedef __attribute__((ext_vector_type(2))) float f32x2;
typedef __attribute__((ext_vector_type(4))) unsigned u32x4;
typedef __attribute__((ext_vector_type(2))) unsigned u32x2;
typedef __attribute__((ext_vector_type(4))) short s16x4;
#define LDS_AS __attribute__((address_space(3)))
#define GLB_AS __attribute__((address_space(1)))
typedef LDS_AS char* ldsp;
typedef unsigned long long u64;
typedef GLB_AS u64 gu64;
typedef __amdgpu_buffer_rsrc_t rsrc_t;
#define RLX_AGENT __ATOMIC_RELAXED, __HIP_MEMORY_SCOPE_AGENT

constexpr int S_ = 2048, D_ = 640, F_ = 1728;
constexpr float EPS = 1e-5f;
constexpr unsigned OOB = 0x7f000000u;

constexpr unsigned SZ_AB = 8 * 128 * 5 * 1024;
constexpr unsigned SZ_X = 128 * 20 * 1024;
constexpr unsigned SZ_H = 128 * 54 * 1024;
constexpr unsigned SZ_W1 = 16 * 140 * 1024;
constexpr unsigned SZ_W2 = 8 * 54 * 5 * 1024;
constexpr unsigned SZ_ST = 2048 * 64;
constexpr unsigned WS_AB1 = 0;
constexpr unsigned WS_AB2 = WS_AB1 + SZ_AB;
constexpr unsigned WS_X1 = WS_AB2 + SZ_AB;
constexpr unsigned WS_X2 = WS_X1 + SZ_X;
constexpr unsigned WS_X3 = WS_X2 + SZ_X;
constexpr unsigned WS_H1 = WS_X3 + SZ_X;
constexpr unsigned WS_H2 = WS_H1 + SZ_H;
constexpr unsigned WS_W1 = WS_H2 + SZ_H;
constexpr unsigned WS_W2 = WS_W1 + SZ_W1;
constexpr unsigned WS_ST1 = WS_W2 + SZ_W2;
constexpr unsigned WS_ST2 = WS_ST1 + SZ_ST;
constexpr unsigned WS_ST3 = WS_ST2 + SZ_ST;
constexpr unsigned WS_CS1 = WS_ST3 + SZ_ST;
constexpr unsigned WS_CSD = WS_CS1 + 20 * 1728 * 4;
constexpr unsigned XSLOT = 2560;
constexpr unsigned SZ_XP = 256 * 12 * XSLOT;
constexpr unsigned WS_XP = (WS_CSD + 256 * 320 + 4095) & ~4095u;
constexpr unsigned WS_FLG = WS_XP + 2 * SZ_XP;
constexpr unsigned FL_X = 0;
constexpr unsigned FL_2 = FL_X + 2048;
constexpr unsigned FL_3 = FL_2 + 2048;
constexpr unsigned FL_4 = FL_3 + 4096;
constexpr unsigned FL_5 = FL_4 + 1024;
constexpr unsigned FL_W = FL_5 + 3072;
constexpr unsigned FL_E = FL_W + 1088;
constexpr unsigned WS_END = WS_FLG + (FL_E + 1) * 128;

DI u32x4 make_srd(const void* p, unsigned bytes) {
  uint64_t a = (uint64_t)p;
  u32x4 r;
  r[0] = (unsigned)a; r[1] = (unsigned)(a >> 32) & 0xffffu; r[2] = bytes; r[3] = 0x00020000u;
  return r;
}
DI rsrc_t make_rsrc(const void* p, unsigned bytes) {
  return __builtin_amdgcn_make_buffer_rsrc((void*)p, (short)0, (int)bytes, 0x00020000);
}
template <int LOFF, int GOFF>
DI void glds(u32x4 srd, unsigned voff, unsigned ldsb, unsigned soff) {
  unsigned tmp;
  asm volatile("s_add_u32 m0, %1, %2\n\ts_add_u32 %0, %3, %4\n\tbuffer_load_dwordx4 %5, %6, %0 offen lds"
               : "=&s"(tmp)
               : "s"(ldsb), "i"(LOFF), "s"(soff), "i"(GOFF), "v"(voff), "s"(srd)
               : "memory", "scc");
}
template <int N> DI void wait_vm() { asm volatile("s_waitcnt vmcnt(%0)" ::"i"(N) : "memory"); }
DI void wait_lgkm0() { asm volatile("s_waitcnt lgkmcnt(0)" ::: "memory"); }
DI void sb() { __builtin_amdgcn_sched_barrier(0); }
DI void bar() { asm volatile("s_barrier" ::: "memory"); }

DI bf16x8 lds_rd(unsigned addr) { return *(const LDS_AS bf16x8*)(ldsp)(uintptr_t)addr; }
DI f32x4 lds_rdf(unsigned addr) { return *(const LDS_AS f32x4*)(ldsp)(uintptr_t)addr; }
DI void lds_wrf(unsigned addr, f32x4 v) { *(LDS_AS f32x4*)(ldsp)(uintptr_t)addr = v; }
DI float lds_rd1(unsigned addr) { return *(const LDS_AS float*)(ldsp)(uintptr_t)addr; }
DI void lds_wr1(unsigned addr, float v) { *(LDS_AS float*)(ldsp)(uintptr_t)addr = v; }
DI void mma(f32x4& c, const bf16x8& a, const bf16x8& b) {
  asm volatile("v_mfma_f32_16x16x32_bf16 %0, %1, %2, %0" : "+v"(c) : "v"(a), "v"(b));
}
DI void mma0(f32x4& c, const bf16x8& a, const bf16x8& b) {
  asm volatile("v_mfma_f32_16x16x32_bf16 %0, %1, %2, 0" : "=&v"(c) : "v"(a), "v"(b));
}
DI void fence_acc(f32x4 (&a)[5]) { asm volatile("s_nop 7\n\ts_nop 1" : "+v"(a[0]), "+v"(a[1]), "+v"(a[2]), "+v"(a[3]), "+v"(a[4])); }
DI void pin_acc(f32x4 (&a)[5]) { asm volatile("" : "+v"(a[0]), "+v"(a[1]), "+v"(a[2]), "+v"(a[3]), "+v"(a[4])); }
template <int M> DI void fence_all(f32x4 (&a)[M][5]) {
#pragma unroll
  for (int i = 1; i < M; ++i) pin_acc(a[i]);
  fence_acc(a[0]);
#pragma unroll
  for (int i = 1; i < M; ++i) pin_acc(a[i]);
}
DI unsigned pk2(float a, float b) {
  f32x2 v = {a, b};
  return __builtin_bit_cast(unsigned, __builtin_convertvector(v, bf16x2));
}
DI float bflo(unsigned u) { return __uint_as_float(u << 16); }
DI float bfhi(unsigned u) { return __uint_as_float(u & 0xffff0000u); }
DI float dot2(unsigned a, unsigned b, float c) {
  return __builtin_amdgcn_fdot2_f32_bf16(__builtin_bit_cast(bf16x2, a), __builtin_bit_cast(bf16x2, b), c, false);
}
DI float xsum16_32(float x) {
  auto s = __builtin_amdgcn_permlane16_swap(__float_as_uint(x), __float_as_uint(x), false, false);
  x = __uint_as_float(s[0]) + __uint_as_float(s[1]);
  auto t = __builtin_amdgcn_permlane32_swap(__float_as_uint(x), __float_as_uint(x), false, false);
  return __uint_as_float(t[0]) + __uint_as_float(t[1]);
}
template <int V> struct IC { static constexpr int v = V; };
template <int I, int N, class Fn> DI void sfor(Fn&& f) {
  if constexpr (I < N) { f(IC<I>{}); sfor<I + 1, N>(f); }
}
DI unsigned rfl(unsigned v) { return __builtin_amdgcn_readfirstlane(v); }
DI unsigned opq_v(unsigned x) { asm volatile("" : "+v"(x)); return x; }
DI unsigned opq_s(unsigned x) { asm volatile("" : "+s"(x)); return x; }
template <class T> DI T* opq_p(T* x) { asm volatile("" : "+s"(x)); return x; }
DI bf16x8 ld16(rsrc_t r, unsigned voff, unsigned soff) { return __builtin_bit_cast(bf16x8, __builtin_amdgcn_raw_buffer_load_b128(r, voff, soff, 16)); }
DI u32x4 ld16u(rsrc_t r, unsigned voff, unsigned soff) { return __builtin_amdgcn_raw_buffer_load_b128(r, voff, soff, 16); }
DI u32x2 ld8u(rsrc_t r, unsigned voff, unsigned soff) { return __builtin_amdgcn_raw_buffer_load_b64(r, voff, soff, 16); }
DI void st16(u32x4 v, rsrc_t r, unsigned voff, unsigned soff) {
  __builtin_amdgcn_raw_buffer_store_b128(v, r, voff, soff, 16);
  asm volatile("s_nop 1" :: "v"(v) : "memory");
}
DI void st16_raw(u32x4 v, rsrc_t r, unsigned voff, unsigned soff) { __builtin_amdgcn_raw_buffer_store_b128(v, r, voff, soff, 16); }
template <class... T> DI void st_guard(const T&... v) {
  (..., [](const auto& x) { asm volatile("" :: "v"(x) : "memory"); }(v));
  asm volatile("s_nop 1" ::: "memory");
  (..., [](const auto& x) { asm volatile("" :: "v"(x)); }(v));
}
DI void st8(u32x2 v, rsrc_t r, unsigned voff, unsigned soff) { __builtin_amdgcn_raw_buffer_store_b64(v, r, voff, soff, 16); }
DI void st4(unsigned v, rsrc_t r, unsigned voff, unsigned soff) { __builtin_amdgcn_raw_buffer_store_b32(v, r, voff, soff, 16); }

template <int KS> DI unsigned foff(unsigned row, unsigned col) {
  return ((row >> 4) * KS + (col >> 5)) * 1024u + (((col >> 3) & 3) * 16u + (row & 15)) * 16u + (col & 7) * 2u;
}

extern __shared__ __attribute__((aligned(1024))) char smem[];
DI unsigned lds_base() { return (unsigned)(uintptr_t)(ldsp)smem; }

DI void ln_coef(float s1, float s2, float& mu, float& rs) {
  mu = s1 * (1.f / D_);
  rs = __builtin_amdgcn_rsqf(fmaxf(s2 * (1.f / D_) - mu * mu, 0.f) + EPS);
}

DI void g1_store(const f32x4 (&acc)[2][5], const float (&mu)[2][4], const float (&rs)[2][4], const float (&csv)[5], rsrc_t rab,
                 unsigned P, unsigned ct, unsigned w, unsigned i16, unsigned q) {
  const unsigned kstep = (ct >> 3) * 64 + P * 4 + w;
  const unsigned cq = ((q & 1) << 1) | (q >> 1);
  const unsigned so = ((ct & 7) * 128 + kstep) * 5 * 1024;
  const unsigned vo = (cq * 16 + i16) * 16;
  u32x4 y[5];
#pragma unroll
  for (int f = 0; f < 5; ++f) {
    const f32x2 cs = {csv[f], csv[f]};
    unsigned pk[2][2];
#pragma unroll
    for (int r = 0; r < 2; ++r)
#pragma unroll
      for (int e = 0; e < 2; ++e) {
        const f32x2 t = __builtin_elementwise_fma(f32x2{mu[r][2 * e], mu[r][2 * e + 1]}, cs, f32x2{acc[r][f][2 * e], acc[r][f][2 * e + 1]}) *
                        f32x2{rs[r][2 * e], rs[r][2 * e + 1]};
        pk[r][e] = __builtin_bit_cast(unsigned, __builtin_convertvector(t, bf16x2));
      }
    auto t0 = __builtin_amdgcn_permlane16_swap(pk[0][0], pk[1][0], false, false);
    auto t1 = __builtin_amdgcn_permlane16_swap(pk[0][1], pk[1][1], false, false);
    y[f] = u32x4{t0[0], t1[0], t0[1], t1[1]};
    st16_raw(y[f], rab, vo + (f & 3) * 1024, so + (f >> 2) * 4096);
  }
  st_guard(y[0], y[1], y[2], y[3], y[4]);
}

constexpr int K1_STAGE = 26 * 1024;
constexpr int K1_TR = 5 * K1_STAGE;
constexpr int K1_CS = K1_TR + 5 * 4096;

DI void k1_issue(u32x4 sa, u32x4 sbd, unsigned vo, unsigned la, unsigned lbb, unsigned soa, unsigned sob, unsigned w) {
  glds<0, 0>(sa, vo, la, soa);
  glds<1024, 8 * 1280>(sa, vo, la, soa);
  glds<2048, 16 * 1280>(sa, vo, la, soa);
  glds<3072, 24 * 1280>(sa, vo, la, soa);
  glds<0, 0>(sbd, vo, lbb, sob);
  glds<4096, 32 * 1280>(sbd, vo, lbb, sob);
  if (w < 2) glds<8192, 64 * 1280>(sbd, vo, lbb, sob);
}
struct RtUnit { bool first; unsigned ks, g; };
DI RtUnit rt_unit(unsigned u) {
  RtUnit r;
  r.first = u < 540;
  if (r.first) { r.ks = u / 27u; r.g = u - r.ks * 27u; }
  else { const unsigned v = u - 540; r.ks = v / 10u; r.g = v - r.ks * 10u; }
  return r;
}
DI void retile_issue(unsigned u, const u16* w1, const u16* w2, unsigned ldst, unsigned lane) {
  const RtUnit t = rt_unit(u);
  const unsigned ld = t.first ? F_ * 2 : D_ * 2;
  u32x4 srd = make_srd(t.first ? w1 : w2, D_ * F_ * 2);
  const unsigned vo = (lane >> 3) * ld + (lane & 7) * 16;
  const unsigned so = t.ks * 32 * ld + t.g * 128;
  glds<0, 0>(srd, vo, ldst, so);
  glds<1024, 0>(srd, vo, ldst, so + 8 * ld);
  glds<2048, 0>(srd, vo, ldst, so + 16 * ld);
  glds<3072, 0>(srd, vo, ldst, so + 24 * ld);
}
DI unsigned w1_off(unsigned rt, unsigned ks) {
  const unsigned c = rt / 7u, f = rt - c * 7, g = (c & 1) ? (f == 0 ? 6 : f - 1) : f;
  return WS_W1 + (c * 140 + (g < 4 ? ks * 4 + g : 80 + ks * 3 + g - 4)) * 1024;
}
DI void retile_finish(unsigned u, unsigned f0, rsrc_t rws, unsigned ldst, unsigned lane) {
  const RtUnit t = rt_unit(u);
  const unsigned i = lane & 15, q = lane >> 4;
  const unsigned base = ldst + (8 * q + (i >> 2)) * 128 + (i & 3) * 8 + f0 * 32;
  u32x4 v[2];
#pragma unroll
  for (int f = 0; f < 2; ++f) {
    s16x4 lo = __builtin_amdgcn_ds_read_tr16_b64_v4i16((LDS_AS s16x4*)(uintptr_t)(base + f * 32));
    s16x4 hi = __builtin_amdgcn_ds_read_tr16_b64_v4i16((LDS_AS s16x4*)(uintptr_t)(base + f * 32 + 512));
    u32x2 l2 = __builtin_bit_cast(u32x2, lo), h2 = __builtin_bit_cast(u32x2, hi);
    v[f] = u32x4{l2[0], l2[1], h2[0], h2[1]};
  }
  const unsigned rt = t.g * 4 + f0;
  if (t.first) {
    st16(v[0], rws, lane * 16, w1_off(rt, t.ks));
    st16(v[1], rws, lane * 16, w1_off(rt + 1, t.ks));
    float s[2];
#pragma unroll
    for (int f = 0; f < 2; ++f)
      s[f] = xsum16_32(((bflo(v[f][0]) + bfhi(v[f][0])) + (bflo(v[f][1]) + bfhi(v[f][1]))) + ((bflo(v[f][2]) + bfhi(v[f][2])) + (bflo(v[f][3]) + bfhi(v[f][3]))));
    st4(__float_as_uint(q == 0 ? s[0] : s[1]), rws, lane < 32 ? lane * 4 : OOB, WS_CS1 + (t.ks * F_ + rt * 16) * 4);
  } else {
    const unsigned c0 = rt / 5u, c1 = (rt + 1) / 5u;
    st16(v[0], rws, lane * 16, WS_W2 + ((c0 * 54 + t.ks) * 5 + (rt - c0 * 5)) * 1024);
    st16(v[1], rws, lane * 16, WS_W2 + ((c1 * 54 + t.ks) * 5 + (rt + 1 - c1 * 5)) * 1024);
  }
}

constexpr int WS1 = 4, WS2 = 7, RTI = 2;
constexpr int PUB1 = 13;
constexpr int PUBG = 11;
constexpr int KP3 = 3, KP1 = 3;
constexpr int RGAP = 10, ASLEEP2 = 40, WDPRE = 6;
constexpr int PD4 = 13, PDX = 15, PD2 = 30, P5DELAY0 = 4, P5DELAY = 1;

constexpr int KM_LDS = 160 * 1024;
constexpr int KM_CS1 = KM_LDS - 1024;
constexpr int G2_A = 80 * 1024, G2_AS = 16 * 1024, G2_BS = 20 * 1024;
constexpr int G4_LATE = 80 * 1024;

DI gu64* flag_ptr(char* ws, unsigned f) { return (gu64*)(ws + WS_FLG + f * 128); }
DI void raise(char* ws, unsigned f, u64 epoch, unsigned lane) {
  if (lane < 16) __hip_atomic_store(flag_ptr(ws, f) + lane, epoch, RLX_AGENT);
}
DI void publish(char* ws, unsigned f, u64 epoch, unsigned lane) {
  wait_vm<0>();
  raise(ws, f, epoch, lane);
}
DI void poll0(char* ws, unsigned f, unsigned n, u64 epoch, unsigned lane) {
  gu64* p = flag_ptr(ws, f);
  bool ok = lane >= n;
#pragma unroll 1
  for (unsigned s = 0; s < (1u << 18); ++s) {
    if (!ok) ok = __hip_atomic_load(p, RLX_AGENT) == epoch;
    if (__all(ok)) break;
    __builtin_amdgcn_s_sleep(1);
  }
  asm volatile("" ::: "memory");
}
template <int DELAY> DI void poll(char* ws, unsigned f, unsigned n, u64 epoch, unsigned lane) {
  __builtin_amdgcn_s_sleep(DELAY);
  poll0(ws, f, n, epoch, lane);
}
DI void warm(unsigned ldump, u32x4 srd, unsigned voff, unsigned soff) {
  asm volatile("s_mov_b32 m0, %0\n\ts_nop 0\n\tbuffer_load_dword %1, %2, %3 offen lds" :: "s"(ldump), "v"(voff), "s"(srd), "s"(soff) : "memory");
}
DI void glds4_sc1(u32x4 srd, unsigned voff, unsigned l, unsigned so) {
  asm volatile("s_mov_b32 m0, %0\n\ts_nop 0\n\tbuffer_load_dwordx4 %1, %2, %3 offen sc1 lds\n\tbuffer_load_dwordx4 %1, %2, %3 offen offset:1024 sc1 lds\n\t"
               "buffer_load_dwordx4 %1, %2, %3 offen offset:2048 sc1 lds\n\tbuffer_load_dwordx4 %1, %2, %3 offen offset:3072 sc1 lds"
               :: "s"(l), "v"(voff), "s"(srd), "s"(so) : "memory");
}
DI void glds1_sc1(u32x4 srd, unsigned voff, unsigned l, unsigned so) {
  asm volatile("s_mov_b32 m0, %0\n\ts_nop 0\n\tbuffer_load_dwordx4 %1, %2, %3 offen sc1 lds" :: "s"(l), "v"(voff), "s"(srd), "s"(so) : "memory");
}
template <int N> DI void glds_run_sc1(u32x4 srd, unsigned voff, unsigned l, unsigned so) {
#pragma unroll
  for (int g = 0; g < N / 4; ++g) glds4_sc1(srd, voff, l + g * 4096, so + g * 4096);
#pragma unroll
  for (int g = N / 4 * 4; g < N; ++g) glds1_sc1(srd, voff, l + g * 1024, so + g * 1024);
}

struct ResTile { u32x4 r0, r1; u32x2 r2; };
struct TileOff { unsigned a0, a1, a2; };
DI TileOff tile_offsets(bool rm, unsigned row, unsigned ct, unsigned q) {
  const unsigned cA = ct * 80 + 16 * (q & 1) + 8 * (q >> 1), cS = ct * 80 + 64 + 4 * q;
  TileOff t;
  if (rm) { t.a0 = (row * D_ + cA) * 2; t.a1 = t.a0 + 64; t.a2 = (row * D_ + cS) * 2; }
  else { t.a0 = foff<20>(row, cA); t.a1 = foff<20>(row, cA + 32); t.a2 = foff<20>(row, cS); }
  asm volatile("" : "+v"(t.a0), "+v"(t.a1), "+v"(t.a2));
  return t;
}
DI ResTile load_res(rsrc_t rr, const TileOff& t) {
  ResTile rt;
  rt.r0 = ld16u(rr, t.a0, 0); rt.r1 = ld16u(rr, t.a1, 0); rt.r2 = ld8u(rr, t.a2, 0);
  return rt;
}
DI void finish_tile(f32x4 (&v)[5], const ResTile& rt, rsrc_t rout, const TileOff& o, bool stats, rsrc_t rst, unsigned sto) {
#pragma unroll
  for (int p = 0; p < 2; ++p)
#pragma unroll
    for (int r = 0; r < 4; ++r) {
      auto t = __builtin_amdgcn_permlane16_swap(__float_as_uint(v[2 * p][r]), __float_as_uint(v[2 * p + 1][r]), false, false);
      v[2 * p][r] = __uint_as_float(t[0]); v[2 * p + 1][r] = __uint_as_float(t[1]);
    }
  u32x4 y0, y1; u32x2 y2;
#pragma unroll
  for (int j = 0; j < 2; ++j) {
    y0[j] = pk2(v[0][2 * j] + bflo(rt.r0[j]), v[0][2 * j + 1] + bfhi(rt.r0[j]));
    y0[2 + j] = pk2(v[1][2 * j] + bflo(rt.r0[2 + j]), v[1][2 * j + 1] + bfhi(rt.r0[2 + j]));
    y1[j] = pk2(v[2][2 * j] + bflo(rt.r1[j]), v[2][2 * j + 1] + bfhi(rt.r1[j]));
    y1[2 + j] = pk2(v[3][2 * j] + bflo(rt.r1[2 + j]), v[3][2 * j + 1] + bfhi(rt.r1[2 + j]));
    y2[j] = pk2(v[4][2 * j] + bflo(rt.r2[j]), v[4][2 * j + 1] + bfhi(rt.r2[j]));
  }
  if (stats) {
    st16_raw(y0, rout, o.a0, 0); st16_raw(y1, rout, o.a1, 0); st8(y2, rout, o.a2, 0);
  } else {
    __builtin_amdgcn_raw_buffer_store_b128(y0, rout, o.a0, 0, 0);
    __builtin_amdgcn_raw_buffer_store_b128(y1, rout, o.a1, 0, 0);
    __builtin_amdgcn_raw_buffer_store_b64(y2, rout, o.a2, 0, 0);
  }
  st_guard(y0, y1);
  if (stats) {
    float s1a = 0.f, s1b = 0.f, s2a = 0.f, s2b = 0.f;
#pragma unroll
    for (int j = 0; j < 4; ++j) {
      s1a = dot2(y0[j], 0x3f803f80u, s1a); s2a = dot2(y0[j], y0[j], s2a);
      s1b = dot2(y1[j], 0x3f803f80u, s1b); s2b = dot2(y1[j], y1[j], s2b);
    }
    s1a = dot2(y2[0], 0x3f803f80u, s1a); s2a = dot2(y2[0], y2[0], s2a);
    s1b = dot2(y2[1], 0x3f803f80u, s1b); s2b = dot2(y2[1], y2[1], s2b);
    const float s1 = xsum16_32(s1a + s1b), s2 = xsum16_32(s2a + s2b);
    st8(u32x2{__float_as_uint(s1), __float_as_uint(s2)}, rst, sto, 0);
  }
}

DI constexpr unsigned wd_pos(int c) { return c < 8 ? 10 * c : 60 + 10 * c; }
constexpr int ORD[14] = {0, 1, 4, 5, 7, 8, 11, 12, 2, 3, 6, 9, 10, 13};
DI constexpr int g4_late(int t) { return (t >= 4 && t < 7) || t >= 11; }
DI constexpr int g4_slot(int t) { return (t < 4 ? t : (t < 7 ? t - 4 : (t < 11 ? t - 7 : t - 11))) * 5120; }

#define PHASE_IDS \
  const unsigned lane = opq_v(tid0) & 63, w = opq_s(w0), bid = opq_s(blockIdx.x); \
  const unsigned xcd = bid & 7, idx = bid >> 3, i16 = lane & 15, q = lane >> 4; \
  char* const ws = opq_p(ws_); \
  (void)xcd; (void)idx; (void)i16; (void)q; (void)ws;
#define IDS_G2 const unsigned kq = xcd & 3, P = (xcd >> 2) * 8 + (idx >> 2), c4 = idx & 3;
#define G2_LA(h) (lb + G2_A + ((w >> 1) * 4 + ((2 * (w & 1) + (h) + 3 - kq) & 3)) * 2048)
#define IDS_G3 const unsigned P = (xcd >> 1) * 4 + (idx >> 3), ct = (xcd & 1) * 8 + (idx & 7);
#define C8_OF ((xcd & 1) * 4 + (idx & 3))
#define IDS_G4 const unsigned rt64 = (xcd >> 1) * 8 + (idx >> 2), c8 = C8_OF;
#define LDX(r, k) ld16(rx, lane * 16 + ((k) & 3) * 1024, sox + (r) * 20480 + ((k) >> 2) * 4096)

__global__ __launch_bounds__(512, 2) void k_all(const u16* x0_, const u16* Sre_, const u16* Sim_, const u16* Dre_, const u16* Dim_,
                                               const u16* w1_, const u16* w2_, char* ws_, u16* out_) {
  const unsigned tid0 = threadIdx.x, wall = rfl(tid0 >> 6), w0 = wall & 3;
  const unsigned lb = lds_base();
  const char* code; unsigned code_len;
  asm volatile("s_getpc_b64 %0\n.Lcw_begin:\n\ts_mov_b32 %1, .Lcw_end-.Lcw_begin" : "=s"(code), "=s"(code_len));
  u64 epoch;

  if (__builtin_expect(wall >= 4, 1)) {
    const unsigned sink = lb + K1_CS + 1024;
    unsigned ep0, ep1;
    asm volatile("global_load_dword %0, %2, %3 sc1\n\tglobal_load_dword %1, %2, %3 offset:4 sc1"
                 : "=&v"(ep0), "=&v"(ep1) : "v"(WS_FLG + FL_E * 128), "s"(ws_) : "memory");
    {
      PHASE_IDS
      IDS_G3
      const u32x4 sa = make_srd(opq_p(x0_), S_ * D_ * 2), sbd = make_srd(ct < 8 ? opq_p(Dre_) : opq_p(Dim_), D_ * D_ * 2);
      const unsigned vo = (lane >> 3) * 1280 + (((lane & 7) ^ (lane >> 3)) << 4);
      const unsigned soa = (P * 128 + w * 32) * 1280, sob = ((ct & 7) * 80 + w * 8) * 1280;
      const unsigned la = lb + w * 4096, lbb = lb + 16384 + w * 1024;
      const unsigned u0 = bid * 4 + w, u1 = 1024 + bid;
      const bool extra = w == 3 && bid < 56;
      const unsigned rot = ((idx & 7) + 3 * (idx >> 3)) % 10u;
      auto ko = [&](unsigned s) { const unsigned c = s + rot; return (c >= 10 ? c - 10 : c) * 128; };
      asm volatile("s_nop 4" ::: "memory");
      k1_issue(sa, sbd, vo, la, lbb, soa + ko(0), sob + ko(0), w);
      k1_issue(sa, sbd, vo, la + K1_STAGE, lbb + K1_STAGE, soa + ko(1), sob + ko(1), w);
      wait_vm<6>();
      {
        unsigned lo, hi;
        asm volatile("v_readfirstlane_b32 %0, %2\n\tv_readfirstlane_b32 %1, %3\n\ts_nop 4" : "=s"(lo), "=s"(hi) : "v"(ep0), "v"(ep1));
        epoch = (u64)lo + ((u64)hi << 32) + 1;
      }
      bar();
      k1_issue(sa, sbd, vo, la + 2 * K1_STAGE, lbb + 2 * K1_STAGE, soa + ko(2), sob + ko(2), w);
      const u32x4 ss = make_srd((xcd & 2) ? Sim_ : Sre_, S_ * S_ * 2);
      const unsigned vs = (lane & 31) * 4096 + (lane >> 5) * 128;
      const unsigned sos = ((xcd >> 2) * 1024 + idx * 32) * 4096 + (xcd & 1) * 2048 + w * 256;
      unsigned so = 0;
#pragma unroll 1
      for (int i = 0; i < 10; ++i) {
        if (i == 0) wait_vm<6>(); else if (i == 1) wait_vm<2 * 6>(); else if (i < 6) wait_vm<3 * 6>();
        else if (i == 6) wait_vm<2 * 6 + (RTI >= 2 && RTI < 6 ? 4 : 0)>(); else if (i == 7) wait_vm<6 + (RTI >= 3 && RTI < 7 ? 4 : 0)>();
        else wait_vm<(RTI >= 4 && RTI < 8 ? 4 : 0) + (WS2 >= 4 && WS2 < 8) + (WS1 >= 4 && WS1 < 8)>();
        bar();
        if (i == 0) {
          k1_issue(sa, sbd, vo, la + 3 * K1_STAGE, lbb + 3 * K1_STAGE, soa + ko(3), sob + ko(3), w);
          k1_issue(sa, sbd, vo, la + 4 * K1_STAGE, lbb + 4 * K1_STAGE, soa + ko(4), sob + ko(4), w);
        } else if (i == 1) {
          k1_issue(sa, sbd, vo, la, lbb, soa + ko(5), sob + ko(5), w);
          k1_issue(sa, sbd, vo, la + K1_STAGE, lbb + K1_STAGE, soa + ko(6), sob + ko(6), w);
        } else if (i < 5) k1_issue(sa, sbd, vo, la + so, lbb + so, soa + ko(i + 5), sob + ko(i + 5), w);
        so = so == 4 * K1_STAGE ? 0 : so + K1_STAGE;
        if (i == WS1) warm(sink, ss, vs, sos);
        if (i == WS2) warm(sink, ss, vs, sos + 1024);
        if (i == RTI) {
          retile_issue(u0, w1_, w2_, lb + K1_TR + w * 4096, lane);
          if (extra) retile_issue(u1, w1_, w2_, lb + K1_TR + 4 * 4096, lane);
        }
      }
      bar();
    }
    bf16x8 wr[10][5];
#pragma unroll
    for (int t = 0; t < 10; ++t)
#pragma unroll
      for (int f = 0; f < 5; ++f) wr[t][f] = bf16x8{};
#pragma unroll 1
    for (int L = 0; L < 2; ++L) {
      {
        PHASE_IDS
        IDS_G2
        const u32x4 sa = make_srd(kq < 2 ? opq_p(Sre_) : opq_p(Sim_), S_ * S_ * 2);
        const u32x4 sb_ = make_srd(ws + (L ? WS_AB2 : WS_AB1), SZ_AB);
        const unsigned va = (lane >> 3) * 4096 + (((lane & 7) ^ (lane >> 3)) << 4), vb = lane * 16;
        const unsigned soa = (P * 128 + w * 32) * 4096 + (kq & 1) * 2048;
        const unsigned sob = ((2 * c4 + (w >> 1)) * 128 + kq * 32 + (w & 1)) * 5 * 1024;
        const unsigned la = lb + G2_A + w * 4096, lbb = lb + w * 5120;
        const unsigned la0 = G2_LA(0), la1 = G2_LA(1);
        const unsigned ct = (xcd & 1) * 8 + (idx & 7);
        auto kc = [&](unsigned s) { return L ? 2 * (s & 7) + ((s ^ (s >> 3)) & 1) : s; };
        asm volatile("s_nop 4" ::: "memory");
        auto issue_a = [&](unsigned l, unsigned so) {
          glds<0, 0>(sa, va, la0 + l, so); glds<1024, 8 * 4096>(sa, va, la0 + l, so);
          glds<0, 16 * 4096>(sa, va, la1 + l, so); glds<1024, 24 * 4096>(sa, va, la1 + l, so);
        };
        if (L == 0) {
#pragma unroll
          for (int s = 0; s < 3; ++s) issue_a(s * G2_AS, soa + s * 128);
          const unsigned u0 = bid * 4 + w, u1 = 1024 + bid;
          const rsrc_t rws = make_rsrc(ws, WS_END);
          wait_vm<12>();
          retile_finish(u0, 0, rws, lb + K1_TR + w * 4096, lane);
          retile_finish(u0, 2, rws, lb + K1_TR + w * 4096, lane);
          if (w == 3 && bid < 56) {
            retile_finish(u1, 0, rws, lb + K1_TR + 4 * 4096, lane);
            retile_finish(u1, 2, rws, lb + K1_TR + 4 * 4096, lane);
          }
          __builtin_amdgcn_s_sleep(P5DELAY0);
        } else {
          issue_a(3 * G2_AS, soa + kc(3) * 128);
          __builtin_amdgcn_s_sleep(P5DELAY);
        }
        const unsigned f5 = FL_5 + ((L * 4 + kq) * 4 + c4) * 64 + ((i16 >> 1) * 2 + (w >> 1)) * 4 + (L ? 2 * (w & 1) + (i16 & 1) : (w & 1) + 2 * (i16 & 1));
        poll0(ws, f5, 16, epoch, lane);
        wait_vm<0>();
        glds_run_sc1<5>(sb_, vb, lbb, sob);
        glds_run_sc1<5>(sb_, vb, lbb + G2_BS, sob + kc(1) * 10240);
        if (L == 0) {
          raise(ws, FL_W + bid * 4 + w, epoch, lane);
          if (w == 3 && bid < 56) raise(ws, FL_W + 1024 + bid, epoch, lane);
          wait_vm<6>();
        } else wait_vm<5>();
        bar();
        glds_run_sc1<5>(sb_, vb, lbb + 2 * G2_BS, sob + kc(2) * 10240);
        if (L == 0) issue_a(3 * G2_AS, soa + kc(3) * 128);
        glds_run_sc1<5>(sb_, vb, lbb + 3 * G2_BS, sob + kc(3) * 10240);
        const u32x4 sw = make_srd(ws + WS_W1, SZ_W1);
        const unsigned sow = ct * 140 * 1024;
        unsigned fw, fw0 = 0, fw1 = 0;
        {
          const unsigned n = w * 42 + (lane < 42 ? lane : 0), k3 = n / 3u, g3 = (7 * ct >> 2) + (n - k3 * 3), n2 = n - 60;
          fw = FL_W + (n < 60 ? k3 * 27 + (g3 < 26 ? g3 : 26) : 540 + (n2 >> 1) * 10 + (5 * C8_OF >> 2) + (n2 & 1));
          if (L) fw = f5 + 1024;
        }
#pragma unroll 1
        for (int s = 0; s < 16; ++s) {
          if (s == 0) wait_vm<10>();
          if (s == 1) wait_vm<14>();
          wait_vm<18>();
          if (s == (L ? 4 : 9)) {
            asm volatile("" : "+v"(fw0), "+v"(fw1));
            if (!__all(fw0 == (unsigned)epoch && fw1 == (unsigned)(epoch >> 32))) poll0(ws, fw, 42, epoch, lane);
          }
          bar();
          const unsigned sl = s & 3;
          if (s < 12) {
            issue_a(sl * G2_AS, soa + kc(s + 4) * 128);
            glds_run_sc1<5>(sb_, vb, lbb + sl * G2_BS, sob + kc(s + 4) * 10240);
            if (s == (L ? 1 : 6))
              asm volatile("global_load_dword %0, %2, %3 sc1\n\tglobal_load_dword %1, %2, %3 offset:4 sc1"
                           : "+v"(fw0), "+v"(fw1) : "v"(WS_FLG + fw * 128), "s"(ws) : "memory");
          } else {
            glds_run_sc1<5>(sw, vb, lbb + sl * G2_BS, sow + w * 5120 + sl * G2_BS);
            if (s < 15) glds_run_sc1<4>(sw, vb, la + sl * G2_AS, sow + G2_A + w * 4096 + sl * G2_AS);
            else glds_run_sc1<3>(sw, vb, lb + G2_A + 3 * G2_AS + w * 3072, sow + G2_A + 3 * G2_AS + w * 3072);
          }
        }
      }
      {
        PHASE_IDS
        IDS_G4
        const unsigned ct = (xcd & 1) * 8 + (idx & 7);
        if (w >= 2) {
          const rsrc_t rc = make_rsrc(ws + WS_CS1, 20 * F_ * 4);
          const unsigned c = (w - 2) * 64 + lane, col = ct * 112 + c;
          const unsigned vo = c < 112 && col < F_ ? col * 4 : OOB;
          float csp = 0.f;
#pragma unroll
          for (int k = 0; k < 20; ++k) csp += __uint_as_float(__builtin_amdgcn_raw_buffer_load_b32(rc, vo, k * F_ * 4, 16));
          lds_wr1(lb + KM_CS1 + c * 4, csp);
        }
        const unsigned so = (c8 * 54 + w * 14) * 5 * 1024;
        wait_vm<0>();
        if (L == 0) {
          const rsrc_t rw = make_rsrc(ws + WS_W2, SZ_W2);
          const unsigned vl = lane * 16, vz = lane * 16 + (w < 3 ? 0u : OOB);
          sfor<0, 50>([&](auto N) {
            constexpr int t = 4 + N.v / 5, n = ORD[t] * 5 + N.v % 5;
            wr[t - 4][N.v % 5] = ld16(rw, (ORD[t] < 12 ? vl : vz) + (n & 3) * 1024, so + (n >> 2) * 4096);
            if constexpr (N.v % 5 == 4 && N.v < 49) { sb(); __builtin_amdgcn_s_sleep(RGAP); sb(); }
          });
          sb();
        }
        wait_lgkm0();
        bar();
        bar();
        const u32x4 s2 = make_srd(ws + WS_W2, SZ_W2);
        const unsigned sg = rfl(so);
        asm volatile("s_nop 4" ::: "memory");
        glds_run_sc1<10>(s2, lane * 16, lb + w * 20480, sg);
        glds_run_sc1<10>(s2, lane * 16, lb + w * 20480 + 10240, sg + 4 * 5120);
        wait_vm<0>();
        const unsigned pe = lb + w * 20480 + lane * 16, pl = lb + G4_LATE + w * 15360 + lane * 16;
        auto put = [&](auto T0, auto T1) {
          sfor<T0.v * 5, T1.v * 5>([&](auto N) {
            constexpr int t = N.v / 5, f = N.v % 5;
            *(LDS_AS bf16x8*)(ldsp)(uintptr_t)((g4_late(t) ? pl : pe) + g4_slot(t) + f * 1024) = wr[t - 4][f];
          });
          wait_lgkm0();
        };
        bar();
        put(IC<4>{}, IC<7>{});
        bar();
        put(IC<7>{}, IC<11>{});
        bar();
        put(IC<11>{}, IC<14>{});
        bar();
      }
      if (L) { bar(); break; }
      {
        PHASE_IDS
        IDS_G3
        const u32x4 sd = make_srd(ct < 8 ? opq_p(Dre_) : opq_p(Dim_), D_ * D_ * 2);
        const unsigned vo = (lane >> 3) * 1280 + (((lane & 7) ^ (lane >> 3)) << 4);
        const unsigned so = (ct & 7) * 80 * 1280;
        sfor<0, 10>([&](auto G) { glds<G.v * 1024, G.v * 8 * 1280>(sd, vo, lb + 20 * w * 1024, so + 2 * w * 128); });
        sfor<0, WDPRE>([&](auto G) { glds<G.v * 1024, G.v * 8 * 1280>(sd, vo, lb + (20 * w + 10) * 1024, so + (2 * w + 1) * 128); });
        bar();
        sfor<WDPRE, 10>([&](auto G) { glds<G.v * 1024, G.v * 8 * 1280>(sd, vo, lb + (20 * w + 10) * 1024, so + (2 * w + 1) * 128); });
        sfor<0, 5>([&](auto G) { glds<G.v * 1024, G.v * 8 * 1280>(sd, vo, lb + (140 + w * 5) * 1024, so + (8 + (w >> 1)) * 128 + (w & 1) * 40 * 1280); });
        wait_vm<0>();
        bar();
      }
      {
        PHASE_IDS
        IDS_G2
        const u32x4 sa = make_srd(kq < 2 ? opq_p(Sre_) : opq_p(Sim_), S_ * S_ * 2);
        const unsigned va = (lane >> 3) * 4096 + (((lane & 7) ^ (lane >> 3)) << 4);
        const unsigned soa = (P * 128 + w * 32) * 4096 + (kq & 1) * 2048;
        __builtin_amdgcn_s_sleep(ASLEEP2);
#pragma unroll
        for (int s = 0; s < 3; ++s) {
          const unsigned l0 = G2_LA(0) + s * G2_AS, l1 = G2_LA(1) + s * G2_AS, so = soa + (2 * s + (s & 1)) * 128;
          glds<0, 0>(sa, va, l0, so); glds<1024, 8 * 4096>(sa, va, l0, so);
          glds<0, 16 * 4096>(sa, va, l1, so); glds<1024, 24 * 4096>(sa, va, l1, so);
        }
        bar();
      }
    }
    return;
  }

  {
    const u64 e = __hip_atomic_load(flag_ptr(ws_, FL_E), RLX_AGENT);
    epoch = (u64)rfl((unsigned)e) + ((u64)rfl((unsigned)(e >> 32)) << 32) + 1;
  }
#pragma unroll 1
  for (int L = 0; L < 2; ++L) {
    {
      PHASE_IDS
      IDS_G3
      f32x4 acc[2][5];
      float mu[2][4], rs[2][4], csv[5];
      const float sign = ct < 8 ? 1.f : -1.f;
      const unsigned f5 = FL_5 + (((ct >> 3) * 2 + (P >> 3)) * 4 + ((ct & 7) >> 1)) * 64 + ((P & 7) * 2 + (ct & 1)) * 4 + w;
      if (L == 0) {
        const unsigned ra0 = lb + w * 4096 + i16 * 128 + ((q ^ (lane & 7)) << 4);
        const unsigned rb0 = lb + 16384 + i16 * 128 + ((q ^ (lane & 7)) << 4);
        const f32x4 z4 = {0.f, 0.f, 0.f, 0.f};
        f32x4 cs0 = z4, cs1 = z4, rs_[2] = {z4, z4}, rq_[2] = {z4, z4};
#pragma unroll
        for (int r = 0; r < 2; ++r)
#pragma unroll
          for (int f = 0; f < 5; ++f) acc[r][f] = z4;
        bf16x8 fr[2][8];
        bf16x8 one = __builtin_bit_cast(bf16x8, u32x4{0x3f803f80u, 0x3f803f80u, 0x3f803f80u, 0x3f803f80u});
        auto half = [&](auto KK, unsigned so) {
          constexpr int kk = KK.v;
          const unsigned pa = (ra0 ^ ((kk ^ 1) * 64)) + so, pb = (rb0 ^ ((kk ^ 1) * 64)) + so;
          sfor<0, 16>([&](auto M) {
            constexpr int m = M.v;
            if constexpr (m < 10) mma(acc[m & 1][m >> 1], fr[kk][m & 1], fr[kk][2 + (m >> 1)]);
            else if constexpr (m == 10) mma(cs0, one, fr[kk][7]);
            else if constexpr (m == 11) mma(cs1, one, fr[kk][6]);
            else if constexpr (m < 14) mma(rs_[m - 12], fr[kk][m - 12], one);
            else mma(rq_[m - 14], fr[kk][m - 14], fr[kk][m - 14]);
            constexpr int n = m >> 1;
            if constexpr ((m & 1) == 0) fr[kk ^ 1][n] = n < 2 ? lds_rd(pa + n * 2048) : (n < 7 ? lds_rd(pb + (n - 2) * 2048) : lds_rd(pb + w * 2048));
            sb();
          });
        };
        warm(lb + K1_CS + 1024, make_srd(code, code_len), lane < 4 ? (idx * 16 + w * 4 + lane) * 128 : OOB, 0);
        asm volatile("s_nop 1" : "+v"(cs0), "+v"(cs1), "+v"(rs_[0]), "+v"(rs_[1]), "+v"(rq_[0]), "+v"(rq_[1]), "+v"(one));
        fence_all(acc);
        bar();
        fr[0][0] = lds_rd(ra0); fr[0][1] = lds_rd(ra0 + 2048);
#pragma unroll
        for (int f = 0; f < 5; ++f) fr[0][2 + f] = lds_rd(rb0 + f * 2048);
        fr[0][7] = lds_rd(rb0 + w * 2048);
        sb();
        unsigned so = 0;
#pragma unroll 1
        for (int i = 0; i < 10; ++i) {
          half(IC<0>{}, so);
          wait_lgkm0();
          bar();
          so = so == 4 * K1_STAGE ? 0 : so + K1_STAGE;
          half(IC<1>{}, so);
        }
        fence_all(acc);
        asm volatile("" : "+v"(cs0), "+v"(cs1), "+v"(rs_[0]), "+v"(rs_[1]), "+v"(rq_[0]), "+v"(rq_[1]));
        const unsigned csb = lb + K1_CS;
        if (q == 0) {
          lds_wr1(csb + (w * 16 + i16) * 4, cs0[0]);
          if (w == 0) lds_wr1(csb + (64 + i16) * 4, cs1[0]);
        }
        wait_lgkm0();
        bar();
#pragma unroll
        for (int f = 0; f < 5; ++f) csv[f] = lds_rd1(csb + (f * 16 + i16) * 4);
#pragma unroll
        for (int r = 0; r < 2; ++r)
#pragma unroll
          for (int j = 0; j < 4; ++j) {
            float m_, r_;
            ln_coef(rs_[r][j], __shfl(rq_[r][j], q * 20 + j), m_, r_);
            mu[r][j] = -m_; rs[r][j] = sign * r_;
          }
        if (w == 0) {
          const rsrc_t rc = make_rsrc(ws + WS_CSD, 256 * 320);
          st4(__float_as_uint(q == 0 ? csv[0] : (q == 1 ? csv[1] : (q == 2 ? csv[2] : csv[3]))), rc, lane * 4, bid * 320);
          st4(__float_as_uint(csv[4]), rc, q == 0 ? 256 + lane * 4 : OOB, bid * 320);
        }
        g1_store(acc, mu, rs, csv, make_rsrc(ws + WS_AB1, SZ_AB), P, ct, w, i16, q);
        publish(ws, f5, epoch, lane);
      } else {
        {
          const rsrc_t rc = make_rsrc(ws + WS_CSD, 256 * 320);
#pragma unroll
          for (int f = 0; f < 5; ++f) csv[f] = __uint_as_float(__builtin_amdgcn_raw_buffer_load_b32(rc, (f * 16 + i16) * 4, bid * 320, 16));
        }
        const unsigned h0 = P & 1;
        poll<PD4>(ws, FL_4 + P * 64 + ((i16 >> 3) * 4 + w) * 8 + (i16 & 7), 16, epoch, lane);
        const rsrc_t rx = make_rsrc(ws + WS_X2, SZ_X);
        const unsigned sox0 = (P * 8 + h0 * 4 + w) * 20 * 1024, sox1 = (P * 8 + (h0 ^ 1) * 4 + w) * 20 * 1024;
        bf16x8 a[40];
#define LDA(n) a[n] = ld16(rx, lane * 16 + ((n) & 3) * 1024, ((n) < 20 ? sox0 : sox1) + (((n) % 20) >> 2) * 4096)
        sfor<0, 6>([&](auto N) { LDA(N.v); });
        const rsrc_t rst = make_rsrc(ws + WS_ST2, SZ_ST);
        const unsigned sto = ((lane >> 5) * 4 * S_ + P * 128 + (((lane >> 4) & 1) ^ h0) * 64 + w * 16 + i16) * 8;
        const u32x2 sp0 = ld8u(rst, sto, 0), sp1 = ld8u(rst, sto, S_ * 8), sp2 = ld8u(rst, sto, 2 * S_ * 8), sp3 = ld8u(rst, sto, 3 * S_ * 8);
        sb();
        bar();
        bf16x8 b[2][5];
        const unsigned rb = lb + i16 * 128 + ((q ^ (lane & 7)) << 4);
        const rsrc_t rab = make_rsrc(ws + WS_AB2, SZ_AB);
        const unsigned vo = ((2 * (w & 1) + (q >> 1)) * 16 + i16) * 16 + (q & 1) * 8;
        const unsigned so = ((ct & 7) * 128 + (ct >> 3) * 64 + P * 4 + (w >> 1)) * 5 * 1024;
        auto put = [&](auto H) {
          constexpr int hh = H.v;
#pragma unroll
          for (int f = 0; f < 5; ++f) {
            const f32x2 cs = {csv[f], csv[f]};
            u32x2 y;
#pragma unroll
            for (int e = 0; e < 2; ++e) {
              const f32x2 t = __builtin_elementwise_fma(f32x2{mu[hh][2 * e], mu[hh][2 * e + 1]}, cs, f32x2{acc[hh][f][2 * e], acc[hh][f][2 * e + 1]}) *
                              f32x2{rs[hh][2 * e], rs[hh][2 * e + 1]};
              y[e] = __builtin_bit_cast(unsigned, __builtin_convertvector(t, bf16x2));
            }
            st8(y, rab, vo + (f & 3) * 1024, so + (hh ^ h0) * 10240 + (f >> 2) * 4096);
          }
        };
#pragma unroll
        for (int f = 0; f < 5; ++f) b[0][f] = lds_rd(rb + wd_pos(0) * 1024 + f * 2048);
        sb();
        sfor<0, 40>([&](auto N) {
          constexpr int n = N.v, hh = n / 20, k = n % 20, kn = (k + 1) % 20;
          if constexpr (n == 2 * KP1) {
            float s1 = (__uint_as_float(sp0[0]) + __uint_as_float(sp1[0])) + (__uint_as_float(sp2[0]) + __uint_as_float(sp3[0]));
            float s2 = (__uint_as_float(sp0[1]) + __uint_as_float(sp1[1])) + (__uint_as_float(sp2[1]) + __uint_as_float(sp3[1]));
            auto ta = __builtin_amdgcn_permlane32_swap(__float_as_uint(s1), __float_as_uint(s1), false, false);
            auto tb = __builtin_amdgcn_permlane32_swap(__float_as_uint(s2), __float_as_uint(s2), false, false);
            float m, rr;
            ln_coef(__uint_as_float(ta[0]) + __uint_as_float(ta[1]), __uint_as_float(tb[0]) + __uint_as_float(tb[1]), m, rr);
            m = -m; rr *= sign;
#pragma unroll
            for (int r = 0; r < 2; ++r)
#pragma unroll
              for (int e = 0; e < 4; ++e) { mu[r][e] = __shfl(m, r * 16 + q * 4 + e); rs[r][e] = __shfl(rr, r * 16 + q * 4 + e); }
            asm volatile("" : "+v"(mu[0][0]), "+v"(mu[0][1]), "+v"(mu[0][2]), "+v"(mu[0][3]), "+v"(mu[1][0]), "+v"(mu[1][1]), "+v"(mu[1][2]), "+v"(mu[1][3]),
                              "+v"(rs[0][0]), "+v"(rs[0][1]), "+v"(rs[0][2]), "+v"(rs[0][3]), "+v"(rs[1][0]), "+v"(rs[1][1]), "+v"(rs[1][2]), "+v"(rs[1][3]));
            sb();
          }
          sfor<0, 5>([&](auto F) {
            constexpr int f = F.v;
            if constexpr (k == 0) mma0(acc[hh][f], a[n], b[n & 1][f]); else mma(acc[hh][f], a[n], b[n & 1][f]);
            if constexpr (n + 1 < 40) b[(n + 1) & 1][f] = lds_rd((rb ^ ((kn & 1) * 64)) + wd_pos(kn >> 1) * 1024 + f * 2048);
            if constexpr ((f == 1 || f == 3) && 6 + 2 * n + (f >> 1) < 40) LDA(6 + 2 * n + (f >> 1));
            sb();
          });
          if constexpr (n == 21) { pin_acc(acc[0]); put(IC<0>{}); sb(); }
          if constexpr (n == 20 + PUBG) publish(ws, f5 + 1024, epoch, lane);
        });
        fence_acc(acc[1]);
        bar();
        put(IC<1>{});
        publish(ws, f5 + 2048, epoch, lane);
      }
    }
    {
      PHASE_IDS
      IDS_G2
      const unsigned m = w >> 1, n = w & 1;
      const unsigned ra = lb + G2_A + (m * 64 + i16) * 128 + ((q ^ (lane & 7)) << 4);
      const unsigned rb = lb + n * 10240 + lane * 16;
      const unsigned row = P * 128 + m * 64 + kq * 16 + i16, ct = 2 * c4 + n;
      const TileOff ro = tile_offsets(L == 0, row, ct, q), oo = tile_offsets(false, row, ct, q);
      const rsrc_t rxp = make_rsrc(ws + WS_XP + L * SZ_XP, SZ_XP);
      const unsigned xo = ((P * 4 + c4) * 48 + w) * XSLOT;
      f32x4 acc[4][5];
#pragma unroll
      for (int i = 0; i < 4; ++i)
#pragma unroll
        for (int f = 0; f < 5; ++f) acc[i][f] = f32x4{0.f, 0.f, 0.f, 0.f};
      bf16x8 fa[2][4], fb[2][5];
      unsigned ra1 = ra ^ 64, rb2 = rb + 2 * G2_BS;
      asm volatile("" : "+v"(ra1), "+v"(rb2));
      auto half = [&](auto KK, auto SL) {
        constexpr int kk = KK.v, sl = SL.v;
        sfor<0, 20>([&](auto M) {
          constexpr int mm = M.v;
          mma(acc[mm / 5][mm % 5], fb[kk][mm % 5], fa[kk][mm / 5]);
          if constexpr (mm < 5) fb[kk ^ 1][mm] = lds_rd((sl < 2 ? rb : rb2) + (sl & 1) * G2_BS + (kk ^ 1) * 5120 + mm * 1024);
          else if constexpr (mm < 9) fa[kk ^ 1][mm - 5] = lds_rd((kk ? ra : ra1) + sl * G2_AS + (mm - 5) * 2048);
          sb();
        });
      };
      if (L) poll0(ws, FL_4 + P * 64 + (m * 4 + kq) * 8 + ct, 1, epoch, lane);
      ResTile rt = load_res(make_rsrc(L ? (const void*)(ws + WS_X2) : (const void*)opq_p(x0_), SZ_X), ro);
      fence_all(acc);
      bar();
#pragma unroll
      for (int f = 0; f < 5; ++f) fb[0][f] = lds_rd(rb + f * 1024);
#pragma unroll
      for (int i = 0; i < 4; ++i) fa[0][i] = lds_rd(ra + i * 2048);
      sb();
      auto stage = [&](auto U) {
        half(IC<0>{}, U);
        wait_lgkm0();
        bar();
        half(IC<1>{}, IC<(U.v + 1) & 3>{});
      };
#pragma unroll 1
      for (int g = 0; g < 3; ++g) sfor<0, 4>(stage);
      sfor<0, 3>(stage);
      half(IC<0>{}, IC<3>{});
      wait_lgkm0();
      bar();
      {
        unsigned p[3][10];
        u32x4 y[3][2];
        auto pack = [&](auto N) {
          constexpr int i = N.v / 5, f = N.v % 5;
          asm volatile("" : "+v"(acc[i][f]));
          p[i][2 * f] = pk2(acc[i][f][0], acc[i][f][1]); p[i][2 * f + 1] = pk2(acc[i][f][2], acc[i][f][3]);
          const unsigned o = xo + (((kq + 1 + i) & 3) * 3 + 2 - i) * 4 * XSLOT;
          if constexpr (f == 1) { y[i][0] = u32x4{p[i][0], p[i][1], p[i][2], p[i][3]}; st16_raw(y[i][0], rxp, lane * 16, o); }
          if constexpr (f == 3) { y[i][1] = u32x4{p[i][4], p[i][5], p[i][6], p[i][7]}; st16_raw(y[i][1], rxp, lane * 16 + 1024, o); }
          if constexpr (f == 4) st8(u32x2{p[i][8], p[i][9]}, rxp, lane * 8 + 2048, o);
        };
        sfor<0, 20>([&](auto M) {
          constexpr int mm = M.v;
          mma(acc[mm / 5][mm % 5], fb[1][mm % 5], fa[1][mm / 5]);
          if constexpr (mm >= 8) pack(IC<mm - 8>{});
          sb();
        });
        pack(IC<12>{}); pack(IC<13>{}); pack(IC<14>{});
        st_guard(y[0][0], y[0][1], y[1][0], y[1][1], y[2][0], y[2][1]);
      }
      f32x4 (&v)[5] = acc[3];
      fence_acc(v);
      const unsigned fx = FL_X + ((L * 64 + P * 4 + c4) * 4 + w) * 4;
      publish(ws, fx + kq, epoch, lane);
      poll<PDX>(ws, fx + ((kq + 1 + lane) & 3), 3, epoch, lane);
      const unsigned xi = xo + kq * 12 * XSLOT;
      u32x4 g0[3], g1[3]; u32x2 g2[3];
#pragma unroll
      for (int s = 0; s < 3; ++s) {
        g0[s] = ld16u(rxp, lane * 16, xi + s * 4 * XSLOT);
        g1[s] = ld16u(rxp, lane * 16 + 1024, xi + s * 4 * XSLOT);
        g2[s] = ld8u(rxp, lane * 8 + 2048, xi + s * 4 * XSLOT);
      }
      sb();
#pragma unroll
      for (int s = 0; s < 3; ++s) {
        v[0] += f32x4{bflo(g0[s][0]), bfhi(g0[s][0]), bflo(g0[s][1]), bfhi(g0[s][1])};
        v[1] += f32x4{bflo(g0[s][2]), bfhi(g0[s][2]), bflo(g0[s][3]), bfhi(g0[s][3])};
        v[2] += f32x4{bflo(g1[s][0]), bfhi(g1[s][0]), bflo(g1[s][1]), bfhi(g1[s][1])};
        v[3] += f32x4{bflo(g1[s][2]), bfhi(g1[s][2]), bflo(g1[s][3]), bfhi(g1[s][3])};
        v[4] += f32x4{bflo(g2[s][0]), bfhi(g2[s][0]), bflo(g2[s][1]), bfhi(g2[s][1])};
      }
      finish_tile(v, rt, make_rsrc(ws + (L ? WS_X3 : WS_X1), SZ_X), oo, true, make_rsrc(ws + (L ? WS_ST3 : WS_ST1), SZ_ST),
                  q == 0 ? (ct * S_ + row) * 8 : OOB);
      publish(ws, FL_2 + (L * 16 + P) * 64 + (m * 4 + kq) * 8 + ct, epoch, lane);
    }
    u32x4 y2[2]; u32x2 y1[2];
    {
      PHASE_IDS
      IDS_G3
      typedef __attribute__((ext_vector_type(2))) short s16x2;
      poll<PD2>(ws, FL_2 + (L * 16 + P) * 64 + w * 16 + i16, 16, epoch, lane);
      const rsrc_t rx = make_rsrc(ws + (L ? WS_X3 : WS_X1), SZ_X);
      const rsrc_t rst = make_rsrc(ws + (L ? WS_ST3 : WS_ST1), SZ_ST);
      const unsigned sox = (P * 8 + w * 2) * 20 * 1024;
      const unsigned par = ct & 1;
      bf16x8 a[20][2];
#pragma unroll
      for (int k = 0; k < 3; ++k) { a[k][0] = LDX(0, k); a[k][1] = LDX(1, k); }
      const unsigned sto = (2 * q * S_ + P * 128 + w * 32 + i16) * 8;
      const u32x2 sa0 = ld8u(rst, sto, 0), sa1 = ld8u(rst, sto, S_ * 8), sb0 = ld8u(rst, sto + 128, 0), sb1 = ld8u(rst, sto + 128, S_ * 8);
      float mu[2], rs[2];
      sb();
      bar();
      f32x4 acc[2][4];
      bf16x8 b[2][4];
      const unsigned rb = lb + lane * 16;
#pragma unroll
      for (int f = 0; f < 4; ++f) b[0][f] = lds_rd(rb + f * 1024);
      sb();
      sfor<0, 20>([&](auto K) {
        constexpr int k = K.v;
        const unsigned pb = k + 1 < 20 ? rb + (k + 1) * 4096 : rb + 80 * 1024;
        if constexpr (k == KP3) {
          ln_coef(xsum16_32(__uint_as_float(sa0[0]) + __uint_as_float(sa1[0])), xsum16_32(__uint_as_float(sa0[1]) + __uint_as_float(sa1[1])), mu[0], rs[0]);
          ln_coef(xsum16_32(__uint_as_float(sb0[0]) + __uint_as_float(sb1[0])), xsum16_32(__uint_as_float(sb0[1]) + __uint_as_float(sb1[1])), mu[1], rs[1]);
          asm volatile("" : "+v"(mu[0]), "+v"(mu[1]), "+v"(rs[0]), "+v"(rs[1]));
          sb();
        }
        sfor<0, 8>([&](auto M) {
          constexpr int f = M.v >> 1, r = M.v & 1;
          if constexpr (k == 0) mma0(acc[r][f], b[0][f], a[0][r]); else mma(acc[r][f], b[k & 1][f], a[k][r]);
          if constexpr (r == 0 && (k + 1 < 20 || f < 3)) b[(k + 1) & 1][f] = lds_rd(pb + f * 1024);
          if constexpr (r == 1 && 2 * k + 3 + (f >> 1) < 20) a[2 * k + 3 + (f >> 1)][f & 1] = LDX(f & 1, 2 * k + 3 + (f >> 1));
          sb();
        });
      });
      auto act = [&](const f32x4& c, int r, const f32x4& cs, unsigned (&pk)[2]) {
        const f32x2 nm = {-mu[r], -mu[r]}, r2 = {rs[r], rs[r]};
#pragma unroll
        for (int e = 0; e < 2; ++e) {
          const f32x2 z = __builtin_elementwise_fma(nm, f32x2{cs[2 * e], cs[2 * e + 1]}, f32x2{c[2 * e], c[2 * e + 1]}) * r2;
          const s16x2 hb = __builtin_bit_cast(s16x2, __builtin_convertvector(z, bf16x2));
          pk[e] = __builtin_bit_cast(unsigned, __builtin_elementwise_max(hb, s16x2{0, 0}));
        }
      };
      auto pair = [&](const unsigned (&u)[2], const unsigned (&v)[2]) {
        auto t0 = __builtin_amdgcn_permlane16_swap(u[0], v[0], false, false);
        auto t1 = __builtin_amdgcn_permlane16_swap(u[1], v[1], false, false);
        return u32x4{t0[0], t1[0], t0[1], t1[1]};
      };
      const unsigned csb = lb + KM_CS1 + q * 16;
      f32x4 c1s[4];
#pragma unroll
      for (int f = 0; f < 4; ++f) c1s[f] = lds_rdf(csb + (f + par) * 64);
      asm volatile("s_nop 7\n\ts_nop 1" : "+v"(acc[0][0]), "+v"(acc[0][1]), "+v"(acc[0][2]), "+v"(acc[0][3]),
                                          "+v"(acc[1][0]), "+v"(acc[1][1]), "+v"(acc[1][2]), "+v"(acc[1][3]));
      wait_lgkm0();
      bar();
      {
        const rsrc_t rh = make_rsrc(ws + (L ? WS_H2 : WS_H1), SZ_H);
        const unsigned vo = ((((q & 1) << 1) | (q >> 1)) * 16 + i16) * 16;
        const unsigned so = ((P * 8 + w * 2) * 54 + 7 * (ct >> 1) + 4 * par) * 1024;
        u32x4 y[2][2];
#pragma unroll
        for (int p = 0; p < 2; ++p) {
          const f32x4 c0 = c1s[2 * p], c1 = c1s[2 * p + 1];
#pragma unroll
          for (int r = 0; r < 2; ++r) {
            unsigned u[2], v[2];
            act(acc[r][2 * p], r, c0, u); act(acc[r][2 * p + 1], r, c1, v);
            y[p][r] = pair(u, v);
            st16_raw(y[p][r], rh, p == 1 && ct == 15 ? OOB : vo, so + (r * 54 + p) * 1024);
          }
        }
        st_guard(y[0][0], y[0][1], y[1][0], y[1][1]);
      }
      sb();
      sfor<0, 20>([&](auto K) {
        constexpr int k = K.v;
        const unsigned pb = rb + 80 * 1024 + (k + 1) * 3072;
        sfor<0, 6>([&](auto M) {
          constexpr int f = M.v >> 1, r = M.v & 1;
          if constexpr (k == 0) mma0(acc[r][f], b[0][f], a[0][r]); else mma(acc[r][f], b[k & 1][f], a[k][r]);
          if constexpr (r == 0 && k + 1 < 20) b[(k + 1) & 1][f] = lds_rd(pb + f * 1024);
          sb();
        });
        if constexpr (k == PUB1) publish(ws, FL_3 + (L * 32 + P) * 64 + w * 16 + ct, epoch, lane);
      });
      const f32x4 c0 = lds_rdf(csb + (4 + par) * 64), c1 = lds_rdf(csb + (5 + par) * 64), c2 = lds_rdf(csb + (par ? 0 : 6) * 64);
      asm volatile("s_nop 7\n\ts_nop 1" : "+v"(acc[0][0]), "+v"(acc[0][1]), "+v"(acc[0][2]), "+v"(acc[1][0]), "+v"(acc[1][1]), "+v"(acc[1][2]));
#pragma unroll
      for (int r = 0; r < 2; ++r) {
        unsigned u[2], v[2], s[2];
        act(acc[r][0], r, c0, u); act(acc[r][1], r, c1, v); act(acc[r][2], r, c2, s);
        y2[r] = pair(u, v);
        y1[r] = u32x2{s[0], s[1]};
      }
      asm volatile("" : "+v"(y2[0]), "+v"(y2[1]), "+v"(y1[0]), "+v"(y1[1]));
    }
    {
      PHASE_IDS
      IDS_G4
      const unsigned row = rt64 * 64 + w * 16 + i16;
      const rsrc_t rh = make_rsrc(ws + (L ? WS_H2 : WS_H1), SZ_H);
      const unsigned soh = (rt64 * 4 * 54 + w * 14) * 1024;
      const unsigned vl = lane * 16, vz = lane * 16 + (w < 3 ? 0u : OOB);
      bf16x8 a[14][4], b[2][5];
      unsigned sh[4];
#pragma unroll
      for (int i = 0; i < 4; ++i) sh[i] = soh + ((w + 1 + i) & 3) * 54 * 1024;
#define LDH(t, i) a[t][i] = ld16(rh, (ORD[t] < 12 ? vl : vz) + (ORD[t] & 3) * 1024, sh[i] + (ORD[t] >> 2) * 4096)
      const TileOff ro = tile_offsets(false, row, c8, q), oo = tile_offsets(L != 0, row, c8, q);
      const unsigned f3 = FL_3 + (L * 32 + (rt64 >> 1)) * 64 + ((rt64 & 1) * 2 + (lane & 1)) * 16 + 4 * w + ((lane >> 1) & 3);
      poll0(ws, f3, 8, epoch, lane);
      sfor<0, 8>([&](auto N) { LDH(N.v / 4, N.v % 4); });
      sb();
      {
        const unsigned P = (xcd >> 1) * 4 + (idx >> 3), ct = (xcd & 1) * 8 + (idx & 7), par = ct & 1;
        const unsigned vo = ((((q & 1) << 1) | (q >> 1)) * 16 + i16) * 16;
        const unsigned v1 = ((2 * par + (q >> 1)) * 16 + i16) * 16 + (q & 1) * 8;
        const unsigned so = ((P * 8 + w * 2) * 54 + 7 * (ct >> 1)) * 1024;
#pragma unroll
        for (int r = 0; r < 2; ++r) {
          st16_raw(y2[r], rh, ct == 15 ? OOB : vo, so + (r * 54 + 2 + 4 * par) * 1024);
          st8(y1[r], rh, v1, so + (r * 54 + 3) * 1024);
        }
        st_guard(y2[0], y2[1]);
      }
      ResTile rt;
      u64 fl2;
      const rsrc_t rr = make_rsrc(ws + (L ? WS_X3 : WS_X1), SZ_X);
      sb();
      bar();
      f32x4 acc[4][5];
      const unsigned rbe = lb + w * 20480 + lane * 16, rbl = lb + G4_LATE + w * 15360 + lane * 16;
#pragma unroll
      for (int f = 0; f < 5; ++f) b[0][f] = lds_rd(rbe + f * 1024);
      sb();
      sfor<0, 14>([&](auto T) {
        constexpr int t = T.v;
        constexpr int ta = t < 5 ? (t == 0 ? 2 : t + 3) : (t == 5 ? -1 : (t == 6 ? 8 : (t + 3 < 14 ? t + 3 : -1)));
        if constexpr (t == 3 || t == 6 || t == 10) { wait_lgkm0(); bar(); }
        if constexpr (t == 6) { if (!__all(fl2 == epoch)) poll0(ws, f3 + 16 * 64, 8, epoch, lane); }
        sfor<0, 20>([&](auto M) {
          constexpr int m = M.v, i = m / 5, f = m % 5;
          if constexpr (t == 0) mma0(acc[i][f], b[0][f], a[0][i]); else mma(acc[i][f], b[t & 1][f], a[t][i]);
          if constexpr (t + 1 < 14 && (m & 1) == 1 && m < 10)
            b[(t + 1) & 1][m >> 1] = lds_rd((g4_late(t + 1) ? rbl : rbe) + g4_slot(t + 1) + (m >> 1) * 1024);
          if constexpr (ta >= 0 && (m & 1) == 0 && m < 8) LDH(ta, m >> 1);
          if constexpr (t == 6 && (m & 1) == 0 && m >= 8 && m < 16) LDH(9, (m - 8) >> 1);
          if constexpr (t == 0 && (m & 1) == 0 && m >= 8 && m < 16) LDH(3, (m - 8) >> 1);
          if constexpr (t == 2 && m == 10) rt = load_res(rr, ro);
          if constexpr (t == 4 && m == 18) fl2 = __hip_atomic_load(flag_ptr(ws, f3 + 16 * 64), RLX_AGENT);
          if constexpr (t == 13 && m >= 8) {
            asm volatile("" : "+v"(acc[(m - 8) / 5][(m - 8) % 5]));
            lds_wrf(rbl + (m - 8) * 1024, acc[(m - 8) / 5][(m - 8) % 5]);
          }
          sb();
        });
        if constexpr (t == 2) {
          wait_vm<19>();
          raise(ws, FL_3 + (L * 32 + 16 + (xcd >> 1) * 4 + (idx >> 3)) * 64 + w * 16 + (xcd & 1) * 8 + (idx & 7), epoch, lane);
        }
      });
#pragma unroll
      for (int f = 2; f < 5; ++f) { asm volatile("" : "+v"(acc[2][f])); lds_wrf(rbl + (10 + f) * 1024, acc[2][f]); }
      f32x4 (&v)[5] = acc[3];
      fence_acc(v);
      wait_lgkm0();
      bar();
#pragma unroll
      for (int s = 1; s < 4; ++s) {
        const unsigned src = lb + G4_LATE + ((w + s) & 3) * 15360 + (3 - s) * 5 * 1024 + lane * 16;
#pragma unroll
        for (int f = 0; f < 5; ++f) v[f] += lds_rdf(src + f * 1024);
      }
      finish_tile(v, rt, L ? make_rsrc(opq_p(out_), SZ_X) : make_rsrc(ws + WS_X2, SZ_X), oo, L == 0, make_rsrc(ws + WS_ST2, SZ_ST),
                  q == 0 ? (c8 * S_ + row) * 8 : OOB);
      if (L == 0) publish(ws, FL_4 + (rt64 >> 1) * 64 + ((rt64 & 1) * 4 + w) * 8 + c8, epoch, lane);
    }
  }
  asm volatile(".Lcw_end:");
  if (blockIdx.x == 0 && w0 == 0) raise(ws_, FL_E, epoch, tid0);
}

extern "C" void kernel_launch(void* const* d_in, const int* in_sizes, int n_in, void* d_out, int out_size, void* d_ws,
                              size_t ws_size, hipStream_t stream) {
  char* ws = (char*)d_ws;
  (void)hipFuncSetAttribute((const void*)k_all, hipFuncAttributeMaxDynamicSharedMemorySize, KM_LDS);
  k_all<<<256, 512, KM_LDS, stream>>>((const u16*)d_in[0], (const u16*)d_in[1], (const u16*)d_in[2], (const u16*)d_in[3],
                                     (const u16*)d_in[4], (const u16*)d_in[6], (const u16*)d_in[7], ws, (u16*)d_out);
}
```
